```python
import torch

def _softmax(x, axis=-1):
    return torch.softmax(x, dim=axis)
def _gelu(x):
    return torch.nn.functional.gelu(x, approximate="tanh")
def _silu(x):
    return torch.nn.functional.silu(x)
def _sigmoid(x):
    return torch.sigmoid(x)
def _layernorm(x, axis=-1, eps=1e-5):
    m = x.mean(dim=axis, keepdim=True)
    v = x.var(dim=axis, keepdim=True, unbiased=False)
    return (x - m) / torch.sqrt(v + eps)
def _scatter_add(tgt, idx, src, axis=0):
    return tgt.clone().index_add_(axis, idx, src)
def _segment_sum(src, seg, n):
    out = torch.zeros((n,) + src.shape[1:], dtype=src.dtype)
    return out.index_add_(0, seg, src)
def _pad(x, pw):
    flat = tuple(v for pair in reversed(pw) for v in pair)
    return torch.nn.functional.pad(x, flat)

kernel_name = 'sequential_preLN__linear_kernel_hedgehog__moe_arch_mod__prompt'

def setup_inputs():

    _w = (lambda a: (lambda t: t.to(torch.bfloat16) if t.is_floating_point() else t)(torch.as_tensor(a)))
    def _A_setup():
        B, S, D, H = 1, 2048, 1024, 32
        Dh = D // H
        return {
            "x": _w((lambda *s: torch.randn(*s, dtype=torch.bfloat16) * 0.05)(B, S, D)),
            "wqkv": _w((lambda *s: torch.randn(*s, dtype=torch.bfloat16) * 0.2)(D, 3 * D)),
            "w_phi": _w((lambda *s: torch.randn(*s, dtype=torch.bfloat16) * 0.2)(H, Dh, Dh) * (Dh ** -0.5)),
            "wo": _w((lambda *s: torch.randn(*s, dtype=torch.bfloat16) * 0.05)(D, D)),
        }
    _A_in = _A_setup()

    def _B_setup():
        B, S, D, F, C = 1, 2048, 1024, 3584, 1
        return {
            "x": _w((lambda *s: torch.randn(*s, dtype=torch.bfloat16) * 0.05)(B, S, D)),
            "w_r": _w((lambda *s: torch.randn(*s, dtype=torch.bfloat16) * 0.1)(D)),
            "w_gate": _w((lambda *s: torch.randn(*s, dtype=torch.bfloat16) * 0.05)(D, F)),
            "w_up": _w((lambda *s: torch.randn(*s, dtype=torch.bfloat16) * 0.05)(D, F)),
            "w_down": _w((lambda *s: torch.randn(*s, dtype=torch.bfloat16) * 0.05)(F, D)),
        }
    _B_in = _B_setup()

    return {
        "A_x": _A_in["x"],
        "A_wqkv": _A_in["wqkv"],
        "A_w_phi": _A_in["w_phi"],
        "A_wo": _A_in["wo"],
        "B_x": _B_in["x"],
        "B_w_r": _B_in["w_r"],
        "B_w_gate": _B_in["w_gate"],
        "B_w_up": _B_in["w_up"],
        "B_w_down": _B_in["w_down"],
    }

def reference(A_x, A_wqkv, A_w_phi, A_wo, B_x, B_w_r, B_w_gate, B_w_up, B_w_down):
    def _A_ref(x, wqkv, w_phi, wo):
        B, S, D = x.shape
        H = 32; Dh = D // H
        qkv = (lambda x, *d: x.permute(*d))((x @ wqkv).reshape(B, S, 3, H, Dh), 2, 0, 3, 1, 4)
        q, k, v = qkv[0], qkv[1], qkv[2]
        pq = torch.einsum("bhsd,hdf->bhsf", q, w_phi)
        pk = torch.einsum("bhsd,hdf->bhsf", k, w_phi)
        phi_q = torch.concatenate([_softmax(pq, axis=-1), _softmax(-pq, axis=-1)], axis=-1)
        phi_k = torch.concatenate([_softmax(pk, axis=-1), _softmax(-pk, axis=-1)], axis=-1)
        kv = torch.einsum("bhsf,bhsd->bhfd", phi_k, v)
        z  = torch.einsum("bhsf->bhf", phi_k)
        num = torch.einsum("bhqf,bhfd->bhqd", phi_q, kv)
        den = torch.einsum("bhqf,bhf->bhq", phi_q, z)[..., None] + 1e-6
        o = num / den
        return (lambda x, *d: x.permute(*d))(o, 0, 2, 1, 3).reshape(B, S, D) @ wo

    def _B_ref(x, w_r, w_gate, w_up, w_down):
        B, S, D = x.shape
        C = 1
        r = x @ w_r
        idx = torch.argsort(-r, axis=-1)[:, :C]
        idx_e = (lambda x, n, axis: torch.repeat_interleave(x, n, dim=axis))(idx.reshape(B, C, 1), D, axis=2)
        x_sel = (lambda x, i, axis: torch.take_along_dim(x, i, dim=axis))(x, idx_e, axis=1)
        g = _silu(x_sel @ w_gate)
        u = x_sel @ w_up
        y = (g * u) @ w_down
        r_sel = _sigmoid((lambda x, i, axis: torch.take_along_dim(x, i, dim=axis))(r, idx, axis=1))
        y = y * r_sel.reshape(B, C, 1)
        b_off = (torch.arange(B) * S).reshape(B, 1)
        flat_idx = (idx + b_off).reshape(B * C)
        zero = (x * 0.0).reshape(B * S, D)
        upd = _scatter_add(zero, flat_idx, y.reshape(B * C, D), axis=0)
        return x + upd.reshape(B, S, D)

    x = A_x
    h = x + _A_ref(_layernorm(x, axis=-1), A_wqkv, A_w_phi, A_wo)
    h2 = h + _B_ref(_layernorm(h, axis=-1), B_w_r, B_w_gate, B_w_up, B_w_down)
    return h2
```

```cpp
#include <hip/hip_runtime.h>
#include <stdint.h>
#include <utility>

typedef unsigned short u16;
typedef unsigned long long u64;
typedef short bf16x8 __attribute__((ext_vector_type(8)));
typedef short s16x4 __attribute__((ext_vector_type(4)));
typedef float f32x4 __attribute__((ext_vector_type(4)));
typedef float f32x2 __attribute__((ext_vector_type(2)));
typedef __bf16 bf16x2 __attribute__((ext_vector_type(2)));
typedef unsigned v4u __attribute__((ext_vector_type(4)));
typedef unsigned v2u __attribute__((ext_vector_type(2)));
#define LAS __attribute__((address_space(3)))
#define GAS __attribute__((address_space(1)))
#define CAS __attribute__((address_space(4)))
typedef GAS u64 gu64;
typedef GAS unsigned gu32;
typedef __amdgpu_buffer_rsrc_t rsrc_t;
#define DI __device__ __forceinline__
#define RLX_AGENT __ATOMIC_RELAXED, __HIP_MEMORY_SCOPE_AGENT
#define BAR() __builtin_amdgcn_s_barrier()
#define SCHED() __builtin_amdgcn_sched_barrier(0)
#define SGB(m, n) __builtin_amdgcn_sched_group_barrier((m), (n), 0)
#define WAITL0() asm volatile("s_waitcnt lgkmcnt(0)" ::: "memory")
#define WL0() __builtin_amdgcn_s_waitcnt(0xc07f)
#define MFMA(a, b, c) __builtin_amdgcn_mfma_f32_16x16x32_bf16((a), (b), (c), 0, 0, 0)
#define ORDER_BEHIND_POLL() __builtin_amdgcn_fence(__ATOMIC_ACQUIRE, "wavefront")
#define AUX_SC1 16
template <int N> DI void waitv() { asm volatile("s_waitcnt vmcnt(%0)" ::"i"(N) : "memory"); }

#define WS_EPOCH 0
#define WS_G0 0x1000
#define WS_G1 0x8000
#define WS_F4 0x78000
#define WS_GC 0x70000
#define WS_G3 0x80000
#define WS_GZ 0xC0000
#define SENT 0xFFFFFFFFu
#define WS_TL 0x100000
#define WS_G5 0x100000
#define WS_G8 0x180000
#define WS_G9 0x1A0000
#define WS_GK 0x1B4000
#define WS_C 0x1C0000
#define WS_P2 0x200000
#define P2_STRIDE 0x2180
#define WS_P4 0x700000
#define WS_USED 0xB00000
#define WS_STAMP 0x1000000

#define STAGE_B 40960
#define LDS_BYTES 163840
#define L_WO 32768
#define L_MB 161792
#define L_WPHI (L_MB - 8192)
#define L_SD (L_MB + 512)
#define L_CS (L_MB + 1024)
#define L_V L_WO
#define L_KH (L_WO + 65536)
#define L_XB 0
#define L_STASH (L_WO + 1024)
#define L_RED 4096

#define SPIN_CAP 20000u
#define DN0 3
#define DNP 1
#define DN1 8
#define S3 12
#define S3H 10
#define S4 20
#define S7 10
#define W_AUX 2
#define LOG2E 1.4426950408889634f

DI unsigned rfl(unsigned x) { return __builtin_amdgcn_readfirstlane(x); }
DI float bf2f(unsigned u) { return __uint_as_float(u << 16); }
DI float bflo(unsigned u) { return __uint_as_float(u << 16); }
DI float bfhi(unsigned u) { return __uint_as_float(u & 0xffff0000u); }
DI unsigned pk2(float lo, float hi) { f32x2 v = {lo, hi}; return __builtin_bit_cast(unsigned, __builtin_convertvector(v, bf16x2)); }
DI unsigned nsent(unsigned x) { return __builtin_elementwise_min(x, SENT - 1); }
DI unsigned umx(unsigned a, unsigned b) { return __builtin_elementwise_max(a, b); }
DI float rbf(float f) { return bflo(pk2(f, f) & 0xffffu); }
DI bf16x8 mk8(unsigned a, unsigned b, unsigned c, unsigned d) { v4u v = {a, b, c, d}; return __builtin_bit_cast(bf16x8, v); }
DI void dma16(unsigned ldsaddr, unsigned voff, rsrc_t rs, unsigned soff) {
    asm volatile("s_mov_b32 m0, %0\n\ts_nop 0\n\tbuffer_load_dwordx4 %1, %2, %3 offen lds" ::"s"(ldsaddr), "v"(voff), "s"(rs), "s"(soff) : "memory");
}
DI void dma16x4(unsigned ldsaddr, const unsigned* v, rsrc_t rs, unsigned soff) {
    asm volatile("s_mov_b32 m0, %0\n\ts_nop 0\n\tbuffer_load_dwordx4 %1, %5, %6 offen lds\n\tbuffer_load_dwordx4 %2, %5, %6 offen offset:1024 lds\n\t"
                 "buffer_load_dwordx4 %3, %5, %6 offen offset:2048 lds\n\tbuffer_load_dwordx4 %4, %5, %6 offen offset:3072 lds"
                 ::"s"(ldsaddr), "v"(v[0]), "v"(v[1]), "v"(v[2]), "v"(v[3]), "s"(rs), "s"(soff) : "memory");
}
DI void touch(unsigned voff, rsrc_t rs, unsigned soff) { asm volatile("buffer_load_dword v255, %0, %1, %2 offen" ::"v"(voff), "s"(rs), "s"(soff) : "memory", "v255"); }
DI v4u ld16_asm(unsigned voff, rsrc_t rs, unsigned soff) {
    v4u r; asm volatile("buffer_load_dwordx4 %0, %1, %2, %3 offen" : "=v"(r) : "v"(voff), "s"(rs), "s"(soff) : "memory"); return r;
}
#define PIN(x) asm volatile("" : "+v"(x))
DI s16x4 tr_read(unsigned a) { return __builtin_amdgcn_ds_read_tr16_b64_v4i16((LAS s16x4*)(a)); }
DI bf16x8 cat8(s16x4 a, s16x4 b) { return __builtin_shufflevector(a, b, 0, 1, 2, 3, 4, 5, 6, 7); }
DI bf16x8 lds128(unsigned a) { return *(const LAS bf16x8*)(a); }
DI f32x4 ldsf4(unsigned a) { return *(const LAS f32x4*)(a); }
DI float ldsf(unsigned a) { return *(const LAS float*)(a); }
DI unsigned ldsu(unsigned a) { return *(const LAS unsigned*)(a); }
DI void stsf4(unsigned a, f32x4 v) { *(LAS f32x4*)(a) = v; }
DI void stsf(unsigned a, float v) { *(LAS float*)(a) = v; }
DI void stsu(unsigned a, unsigned v) { *(LAS unsigned*)(a) = v; }
template <int C> DI float dppf(float x) { return __uint_as_float(__builtin_amdgcn_update_dpp(0, __float_as_uint(x), C, 0xf, 0xf, true)); }
template <int C> DI unsigned dppu(unsigned x) { return __builtin_amdgcn_update_dpp(0, x, C, 0xf, 0xf, true); }
DI float max2f(float a, float b) { float r; asm("v_max_f32_e32 %0, %1, %2" : "=v"(r) : "v"(a), "v"(b)); return r; }
DI float max3f(float a, float b, float c) { float r; asm("v_max3_f32 %0, %1, %2, %3" : "=v"(r) : "v"(a), "v"(b), "v"(c)); return r; }
DI float row_sum(float x) { x += dppf<0x128>(x); x += dppf<0x124>(x); x += dppf<0x122>(x); x += dppf<0x121>(x); return x; }
DI float xrow_sum(float x) {
    auto a = __builtin_amdgcn_permlane16_swap(__float_as_uint(x), __float_as_uint(x), false, false);
    x = __uint_as_float(a[0]) + __uint_as_float(a[1]);
    auto b = __builtin_amdgcn_permlane32_swap(__float_as_uint(x), __float_as_uint(x), false, false);
    return __uint_as_float(b[0]) + __uint_as_float(b[1]);
}
DI float wave_sum(float x) { return xrow_sum(row_sum(x)); }
DI unsigned wave_maxu(unsigned x) {
    x = umx(x, dppu<0x128>(x)); x = umx(x, dppu<0x124>(x)); x = umx(x, dppu<0x122>(x)); x = umx(x, dppu<0x121>(x));
    auto a = __builtin_amdgcn_permlane16_swap(x, x, false, false);
    x = umx(a[0], a[1]);
    auto b = __builtin_amdgcn_permlane32_swap(x, x, false, false);
    return umx(b[0], b[1]);
}
DI float nmax3f(float a, float b, float c) { float r; asm("v_max3_f32 %0, -%1, -%2, -%3" : "=v"(r) : "v"(a), "v"(b), "v"(c)); return r; }
DI float nmax2f(float a, float b) { float r; asm("v_max_f32_e64 %0, -%1, -%2" : "=v"(r) : "v"(a), "v"(b)); return r; }
DI float mx2(float a, float b) { return __builtin_amdgcn_fmed3f(a, b, __builtin_inff()); }
template <bool MAX> DI float xrow_scatter(float a, float b, float c, float d) {
    const auto s = __builtin_amdgcn_permlane16_swap(__float_as_uint(a), __float_as_uint(b), false, false);
    const auto t = __builtin_amdgcn_permlane16_swap(__float_as_uint(c), __float_as_uint(d), false, false);
    const float u = MAX ? mx2(__uint_as_float(s[0]), __uint_as_float(s[1])) : __uint_as_float(s[0]) + __uint_as_float(s[1]);
    const float v = MAX ? mx2(__uint_as_float(t[0]), __uint_as_float(t[1])) : __uint_as_float(t[0]) + __uint_as_float(t[1]);
    const auto r = __builtin_amdgcn_permlane32_swap(__float_as_uint(u), __float_as_uint(v), false, false);
    return MAX ? mx2(__uint_as_float(r[0]), __uint_as_float(r[1])) : __uint_as_float(r[0]) + __uint_as_float(r[1]);
}
DI void xrow_gather(float w, float (&o)[4]) {
    const auto r = __builtin_amdgcn_permlane32_swap(__float_as_uint(w), __float_as_uint(w), false, false);
    const auto s = __builtin_amdgcn_permlane16_swap(r[0], r[0], false, false);
    const auto t = __builtin_amdgcn_permlane16_swap(r[1], r[1], false, false);
    o[0] = __uint_as_float(s[0]); o[1] = __uint_as_float(s[1]); o[2] = __uint_as_float(t[0]); o[3] = __uint_as_float(t[1]);
}
struct NoHook { DI void operator()(int, float&) const {} DI void operator()(int, float&, float&) const {} };
template <bool MUL, class H = NoHook> DI void softmax2(const f32x4 (&p)[2][2], float rs0, float rs1, bool hi, bf16x8 (&out)[4], H hook = H()) {
    float m[4];
#pragma unroll
    for (int i = 0; i < 2; i++) {
        const f32x4 &p0 = p[i][0], &p1 = p[i][1];
        m[2 * i] = max3f(max3f(p0[0], p0[1], p0[2]), max3f(p0[3], p1[0], p1[1]), max2f(p1[2], p1[3]));
        m[2 * i + 1] = max3f(nmax3f(p0[0], p0[1], p0[2]), nmax3f(p0[3], p1[0], p1[1]), nmax2f(p1[2], p1[3]));
    }
    asm volatile("s_nop 1" : "+v"(m[0]), "+v"(m[1]), "+v"(m[2]), "+v"(m[3]));
    hook(1, m[3]);
    const float rsr = hi ? rs1 : rs0;
    float a[4];
    xrow_gather(xrow_scatter<true>(m[0], m[1], m[2], m[3]) * (rsr * -LOG2E), a);
    f32x2 e[4][4];
    float s[4];
#pragma unroll
    for (int k = 0; k < 4; k++) {
        if (k == 2) hook(2, a[2], s[1]);
        const float sc = ((k >> 1) ? rs1 : rs0) * ((k & 1) ? -LOG2E : LOG2E);
#pragma unroll
        for (int j = 0; j < 4; j++) {
            const f32x4& q = p[k >> 1][j >> 1];
            const f32x2 t = __builtin_elementwise_fma((f32x2){q[2 * (j & 1)], q[2 * (j & 1) + 1]}, (f32x2){sc, sc}, (f32x2){a[k], a[k]});
            e[k][j] = (f32x2){__builtin_amdgcn_exp2f(t[0]), __builtin_amdgcn_exp2f(t[1])};
        }
        const f32x2 h = (e[k][0] + e[k][1]) + (e[k][2] + e[k][3]);
        s[k] = h[0] + h[1];
        if (k == 3) hook(3, s[k]);
    }
    float iv = __builtin_amdgcn_rcpf(xrow_scatter<false>(s[0], s[1], s[2], s[3]));
    if (MUL) iv *= rsr;
    xrow_gather(iv, a);
#pragma unroll
    for (int k = 0; k < 4; k++) {
        if (k == 2) hook(4, a[2]);
        const f32x2 c = {a[k], a[k]};
        const f32x2 x0 = e[k][0] * c, x1 = e[k][1] * c, x2 = e[k][2] * c, x3 = e[k][3] * c;
        out[k] = mk8(pk2(x0[0], x0[1]), pk2(x1[0], x1[1]), pk2(x2[0], x2[1]), pk2(x3[0], x3[1]));
    }
}
DI float dot2(unsigned a, unsigned b, float c) {
    return __builtin_amdgcn_fdot2_f32_bf16(__builtin_bit_cast(bf16x2, a), __builtin_bit_cast(bf16x2, b), c, false);
}
DI void st_gran(char* ws, unsigned off, unsigned tag, unsigned val) {
    __hip_atomic_store((gu64*)(ws + off), ((u64)tag << 32) | val, RLX_AGENT);
}
DI v2u ldg(rsrc_t rs, unsigned voff, unsigned soff) { return __builtin_amdgcn_raw_buffer_load_b64(rs, voff, soff, AUX_SC1); }
#define RARE(c) __builtin_expect(!!(c), 0)
#define FRESH() asm volatile("" ::: "memory")
template <int N> DI void sweep(rsrc_t rs, unsigned voff, unsigned soff, unsigned S, unsigned tag, unsigned (&v)[N]) {
#pragma nounroll
    for (unsigned spins = 0;;) {
        unsigned bad = 0;
        FRESH();
#pragma unroll
        for (int k = 0; k < N; ++k) { const v2u x = ldg(rs, voff, soff + k * S); v[k] = x[0]; bad |= x[1] ^ tag; }
        if (__all(bad == 0)) break;
        if (++spins > SPIN_CAP) break;
        __builtin_amdgcn_s_sleep(1);
    }
}
DI unsigned wmax(unsigned x) { return x; }
DI unsigned wmax(v2u x) { return umx(x[0], x[1]); }
DI unsigned wmax(v4u x) { return umx(umx(x[0], x[1]), umx(x[2], x[3])); }
template <int N, int SL = 0, class T, class F> DI void sweepz(F ld, T (&v)[N]) {
#pragma nounroll
    for (unsigned spins = 0;;) {
        FRESH();
        unsigned m = 0;
#pragma unroll
        for (int k = 0; k < N; ++k) v[k] = ld(k);
#pragma unroll
        for (int k = 0; k < N; ++k) m = umx(m, wmax(v[k]));
        if (__all(m != SENT)) break;
        if (++spins > SPIN_CAP) break;
        if (SL) __builtin_amdgcn_s_sleep(SL);
    }
}
DI unsigned f2s(float x) { return nsent(__float_as_uint(x)); }
#define OOB 0x4000000u
#define LD16(v, s) __builtin_amdgcn_raw_buffer_load_b128(rsWS, (v), (s), AUX_SC1)
#define LD4(v, s) __builtin_amdgcn_raw_buffer_load_b32(rsWS, (v), (s), AUX_SC1)
#define ST16(x, v, s) __builtin_amdgcn_raw_buffer_store_b128((x), rsWS, (v), (s), AUX_SC1)
#define ST4(x, v, s) __builtin_amdgcn_raw_buffer_store_b32((x), rsWS, (v), (s), AUX_SC1)
#define SLEEP(n) __builtin_amdgcn_s_sleep(n)
DI void poll16(rsrc_t rs, unsigned off, unsigned tag, int l) {
#pragma nounroll
    for (unsigned spins = 0; spins < SPIN_CAP; spins++) {
        FRESH();
        const v2u x = ldg(rs, (l & 15) * 8, off);
        if (__all(x[1] == tag && x[0] == (tag ^ 0x5bd1e995u))) break;
    }
    ORDER_BEHIND_POLL();
}
#define STAMPR(i, role)
#define STAMP(i)
#define STAMPW(i, ww)

#define STAMPW2(i, a, b) STAMPW(i, a); STAMPW(i, b)
#define RINIT()
#define RDUMP()
#define ARR_DECL
#define KATTR
#define ARR_MARK(i)
#define ARR_END()
template <class F, int... Is> DI void static_for_impl(F&& f, std::integer_sequence<int, Is...>) { (f(std::integral_constant<int, Is>{}), ...); }
template <int N, class F> DI void static_for(F&& f) { static_for_impl(f, std::make_integer_sequence<int, N>{}); }

DI s16x4 bq(unsigned b0, unsigned b1, int j, int i) {
    const int ni = (j + 2) % 6;
    return tr_read(((ni & 1) ? b1 : b0) + (ni >> 1) * 8192 + (i >> 1) * 4096 + (i & 1) * 512);
}
template <class F> DI void group(f32x4 (&acc)[4][6], const bf16x8 (&A)[2][4], const bf16x8 (&B)[2], int j, F gap) {
    const int ni = (j + 2) % 6;
#pragma unroll
    for (int i = 0; i < 8; i++) {
        const int mi = i >> 1, ks = i & 1;
        if (ni < 4) asm volatile("v_mfma_f32_16x16x32_bf16 %0, %1, %2, %0" : "+v"(acc[mi][ni]) : "v"(B[ks]), "v"(A[ks][mi]));
        else asm volatile("v_mfma_f32_16x16x32_bf16 %0, %1, %2, %0" : "+v"(acc[mi][ni]) : "v"(A[ks][mi]), "v"(B[ks]));
        gap(i);
        SCHED();
    }
}
DI void img_write2(const unsigned (&ia)[4], const bf16x8 (&f)[4]) {
#pragma unroll
    for (int k = 0; k < 4; k++) {
        const v4u u = __builtin_bit_cast(v4u, f[k]);
        *(LAS v2u*)(ia[2 * (k & 1)] + (k >> 1) * 2048) = (v2u){u[0], u[1]};
        *(LAS v2u*)(ia[2 * (k & 1) + 1] + (k >> 1) * 2048) = (v2u){u[2], u[3]};
    }
}
DI float lds_bf(unsigned a) { return bf2f(*(const LAS u16*)(a)); }
#define MFMA_SETTLE2(p) asm volatile("s_nop 7\n\ts_nop 1" : "+v"(p[0][0]), "+v"(p[0][1]), "+v"(p[1][0]), "+v"(p[1][1]))

__global__ KATTR __launch_bounds__(512, 2) void fused_kernel(const u16* __restrict__ X, const u16* __restrict__ WQKV, const u16* __restrict__ WPHI,
                                                       const u16* __restrict__ WO, const u16* __restrict__ WR, const u16* __restrict__ WG,
                                                       const u16* __restrict__ WU, const u16* __restrict__ WD, u16* __restrict__ OUT,
                                                       char* __restrict__ ws) {
    extern __shared__ __attribute__((aligned(16))) char lds[];
    u64 pc0; unsigned clen;
    const int tid = threadIdx.x, l = tid & 63;
    asm volatile("s_getpc_b64 %0\n.Lfk_beg:\n\ts_lshr_b32 %1, %3, 3\n\tv_and_b32 v255, 0x1e0, %2\n\tv_add_lshl_u32 v255, v255, %1, 7\n\ts_mov_b32 %1, .Lfk_end-.Lfk_beg\n\t"
                 "v_min_u32 v255, %1, v255\n\tglobal_load_dword v255, v255, %0" : "=&s"(pc0), "=&s"(clen) : "v"(tid), "s"(blockIdx.x) : "v255", "scc");
    SCHED();
    const unsigned w = rfl(tid >> 6);
    const unsigned bid = blockIdx.x, xcd = bid & 7, idx = bid >> 3;
    const unsigned rt = (xcd & 1) * 8 + (idx & 7), hp = (xcd >> 1) * 4 + (idx >> 3);
    const unsigned i16 = idx & 15, rt2 = xcd * 2 + (idx >> 4), ct = i16 == 4 ? 11 : i16 == 11 ? 4 : i16;
    const unsigned hq = (hp + 4) & 15;
    const unsigned rot = (rt >> 2) * 4;
    const unsigned rot2 = (2 * (rt2 + ct)) & 31;
    const unsigned ldsb = (unsigned)(size_t)lds;
    const int g = l >> 4, q4 = (l & 15) >> 2, p4 = l & 3, t16 = l & 15;
    const unsigned kr = l >> 3;
    asm volatile("" ::"s"(X), "s"(WQKV), "s"(WPHI), "s"(WO), "s"(WR), "s"(WG), "s"(WU), "s"(WD), "s"(OUT), "s"(ws));
    unsigned tag;
#define READ_TAG() do { SCHED(); tag = *(const volatile CAS unsigned*)(ws + WS_EPOCH) + 1u; asm volatile("" : "+s"(tag)); } while (0)
    const rsrc_t rsWS = __builtin_amdgcn_make_buffer_rsrc((void*)ws, 0, WS_USED, 0x00020000);
    const rsrc_t rsX = __builtin_amdgcn_make_buffer_rsrc((void*)X, 0, 2048 * 1024 * 2, 0x00020000);
    const rsrc_t rsW = __builtin_amdgcn_make_buffer_rsrc((void*)WQKV, 0, 1024 * 3072 * 2, 0x00020000);
    const unsigned vA = (kr * 1024 + (((l & 7) ^ kr) * 8)) * 2;
    const unsigned vB0 = (kr * 3072 + (((l & 7) ^ (((kr >> 1) & 1) << 1)) * 8)) * 2;
    const unsigned vB1 = (kr * 3072 + (((l & 7) ^ ((((kr >> 1) & 1) | 2) << 1)) * 8)) * 2;
    const unsigned srow1 = rt * 128 + hp * 8 + (w & 3) * 2;
    v4u xr[4];
    if (w < 4) {
#pragma unroll
        for (int i = 0; i < 4; i++) xr[i] = ld16_asm(l * 16, rsX, srow1 * 2048 + i * 1024);
    }
    {
        const unsigned vBw = (w & 1) ? vB1 : vB0;
#pragma unroll
        for (int i = 0; i < 2; i++) dma16(ldsb + (w * 2 + i) * 1024, vA, rsX, (rt * 128 + w * 16 + i * 8) * 2048 + rot * 128);
#pragma unroll
        for (int i = 0; i < 3; i++) dma16(ldsb + 16384 + i * 8192 + w * 1024, vBw, rsW, (rot * 64 + w * 8) * 6144 + (i ? hp : hq) * 128 + i * 2048);
    }
    const f32x4 zero4 = {0.f, 0.f, 0.f, 0.f};
    ARR_DECL;
    const bf16x8 ones = mk8(0x3F803F80u, 0x3F803F80u, 0x3F803F80u, 0x3F803F80u);
    const unsigned lw = w & 3;
    const bool ffn = (ct & 7) != 3;
    const unsigned fb = !ffn ? 255u : rt2 * 14 + (i16 < 3 ? i16 : i16 - 2);
    const unsigned cg = (fb + 5) & 15, kc = fb >> 4;
    const unsigned kq = fb / 56, cg1 = fb % 56, cg1u = (cg1 + 3) % 56;
    v4u wreg[24];
    RINIT();
    STAMPW2(0, 0, 4);

    const rsrc_t rsWO = __builtin_amdgcn_make_buffer_rsrc((void*)WO, 0, 1024 * 1024 * 2, 0x00020000);
    const unsigned vWO = kr * 2048 + (((l & 7) ^ (((kr >> 1) & 3) << 1)) * 16);
    unsigned vwo[4];
    auto wo_so = [&](unsigned b) { return (((2 * b & 30) + (b >> 4) + rot2) & 31) * 65536 + ct * 128; };
    const unsigned fw = (((t16 >> 1) & 3) << 2) | (((t16 >> 3) & 1) << 1) | (t16 & 1);
    unsigned ad;
    unsigned ia[4];
    bf16x8 sdv[4];
    auto pre_addr = [&]() {
#pragma unroll
        for (int k = 0; k < 4; k++) { ia[k] = ldsb + L_XB + lw * 8192 + (w >> 2) * 4096 + t16 * 128 + (((4 * k + g) ^ fw) * 8); PIN(ia[k]); }
        const unsigned rr = 4 * g + q4, fr = (((rr >> 1) & 3) << 2) | (((rr >> 3) & 1) << 1) | (rr & 1);
        ad = ldsb + L_XB + (w & 1) * 16384 + rr * 128 + ((((w >> 1) * 4 + p4) ^ fr) * 8);
        PIN(ad);
    };
    auto sd_reads = [&]() {
        unsigned sa = ldsb + L_SD + g * 8;
        PIN(sa);
#pragma unroll
        for (int s = 0; s < 4; s++) {
            const v2u a = *(const LAS v2u*)(sa + s * 64), b = *(const LAS v2u*)(sa + s * 64 + 32);
            sdv[s] = mk8(a[0], a[1], b[0], b[1]);
        }
    };
    bf16x8 v0[4], v1[4];
    auto v_reads = [&]() {
        unsigned vd = ldsb + L_V + (w & 1) * 8192 + l * 16;
        PIN(vd);
#pragma unroll
        for (int s = 0; s < 4; s++) { v0[s] = lds128(vd + s * 2048); v1[s] = lds128(vd + s * 2048 + 1024); }
    };
    auto kv_tile = [&](auto hook6) {
        const unsigned h2 = w & 1, ft = w >> 1;
        WAITL0();
        ARR_MARK(18);
        BAR();
        SCHED();
        bf16x8 pf[4];
#pragma unroll
        for (int s = 0; s < 4; s++) pf[s] = cat8(tr_read(ad + s * 4096), tr_read(ad + s * 4096 + 2048));
        SCHED();
        hook6();
        SCHED();
        f32x4 k0 = zero4, k1 = zero4, kz = zero4;
#pragma unroll
        for (int s = 0; s < 4; s++) { k0 = MFMA(pf[s], v0[s], k0); k1 = MFMA(pf[s], v1[s], k1); }
#pragma unroll
        for (int s = 0; s < 4; s++) kz = MFMA(pf[s], sdv[s], kz);
        const unsigned pbase = WS_P2 + (hp * 16 + rt) * P2_STRIDE;
        const v4u o0 = {nsent(pk2(k0[0], k0[1])), nsent(pk2(k0[2], k0[3])), nsent(pk2(k1[0], k1[1])), nsent(pk2(k1[2], k1[3]))};
        __builtin_amdgcn_raw_buffer_store_b128(o0, rsWS, l * 16, pbase + h2 * 4096 + ft * 1024, AUX_SC1);
        asm volatile("s_nop 1" :: "v"(o0));
        __builtin_amdgcn_raw_buffer_store_b64((v2u){nsent(pk2(kz[0], kz[1])), nsent(pk2(kz[2], kz[3]))}, rsWS, t16 == 0 ? g * 8 : OOB,
                                              pbase + 8192 + h2 * 128 + ft * 32, AUX_SC1);
        SCHED();
        ARR_MARK(19);
        BAR();
    };

    const bool zjob = lw == 0 && (rt & 7) == 0;
    v4u pp[4];
    auto pp_ld = [&](int k) {
        return LD16(kr * P2_STRIDE + (l & 7) * 16, WS_P2 + (hp * 16 + (k & 1) * 8) * P2_STRIDE +
                    (k < 2 ? (rt >> 2) * 2048 + (lw & 1) * 1024 + (rt & 3) * 256 + (lw >> 1) * 128 : 8192 + (rt >> 3) * 128 + (zjob ? 0u : OOB)));
    };
    auto kv_reduce = [&]() {
        SLEEP(2);
        sweepz<4, 2>(pp_ld, pp);
        STAMPW(25, 0);
        auto red = [&](const v4u* p) {
            float lo = xrow_scatter<false>(bflo(p[0][0]) + bflo(p[1][0]), bflo(p[0][1]) + bflo(p[1][1]), bflo(p[0][2]) + bflo(p[1][2]), bflo(p[0][3]) + bflo(p[1][3]));
            float hi = xrow_scatter<false>(bfhi(p[0][0]) + bfhi(p[1][0]), bfhi(p[0][1]) + bfhi(p[1][1]), bfhi(p[0][2]) + bfhi(p[1][2]), bfhi(p[0][3]) + bfhi(p[1][3]));
            lo += dppf<0x128>(lo); hi += dppf<0x128>(hi);
            return nsent(pk2(lo, hi));
        };
        __builtin_amdgcn_raw_buffer_store_b32(red(pp), rsWS, (l & 8) ? OOB : (l & 7) * 16 + g * 4,
                                              WS_G3 + (hp * 4 + (rt >> 2)) * 2048 + (lw & 1) * 1024 + (rt & 3) * 256 + (lw >> 1) * 128, AUX_SC1);
        if (RARE(zjob))
            __builtin_amdgcn_raw_buffer_store_b32(red(pp + 2), rsWS, (l & 8) ? OOB : ((l >> 2) & 1) * 64 + ((l & 1) * 2 + (g >> 1)) * 16 + ((l >> 1) & 1) * 8 + (g & 1) * 4,
                                                  WS_GZ + (hp * 2 + (rt >> 3)) * 128, AUX_SC1);
    };

    const unsigned half = w >> 2;
    const unsigned R2 = rt2 * 128 + lw * 32 + half * 16 + t16;

    auto dloads = [&](int i0, int i1) {
        const rsrc_t rsD = __builtin_amdgcn_make_buffer_rsrc((void*)WD, 0, 1024 * 3584 * 2, 0x00020000);
#pragma unroll
        for (int i = i0; i < i1; i++)
            wreg[16 + i] = __builtin_amdgcn_raw_buffer_load_b128(rsD, kr * 4096 + (l & 7) * 16, (kc * 256 + lw * 64 + (i >> 1) * 16 + (i & 1)) * 2048 + cg * 128, W_AUX);
        FRESH();
    };
    v4u xres[2], wrv[2];
    auto res_loads = [&]() {
#pragma unroll
        for (int i = 0; i < 2; i++) {
            xres[i] = *(const v4u*)(X + (size_t)R2 * 1024 + ct * 64 + i * 32 + 8 * g);
            wrv[i] = *(const v4u*)(WR + ct * 64 + i * 32 + 8 * g);
        }
        FRESH();
    };

    if (w < 4) {
        const unsigned wm = w & 1, hh = w >> 1;
        f32x4 acc[4][6];
        bf16x8 wf[2], wfq[2];
        float mbt[4], rst[4], csv[2];
        f32x4 mbv[4], csq[2], csk[2];
        unsigned vgc = WS_GC + (l < 32 ? hp : hq) * 256 + (l & 31) * 8;
        PIN(vgc);
        v2u gc;
#pragma unroll
        for (int i = 0; i < 4; i++)
#pragma unroll
            for (int j = 0; j < 6; j++) { acc[i][j] = zero4; PIN(acc[i][j]); }
        {
            const unsigned aoff0 = ldsb + (wm * 64 + t16) * 128 + (((0 + g) ^ (l & 7)) * 16);
            const unsigned aoff1 = ldsb + (wm * 64 + t16) * 128 + (((4 + g) ^ (l & 7)) * 16);
            const unsigned sk = ((q4 >> 1) & 1) | ((g & 1) << 1);
            const unsigned boffA = ldsb + 16384 + (8 * g + q4) * 128 + (((2 * hh) ^ sk) * 32) + p4 * 8;
            const unsigned boffB = ldsb + 16384 + (8 * g + q4) * 128 + (((2 * hh + 1) ^ sk) * 32) + p4 * 8;
            bf16x8 A[2][4], B[3][2];
            s16x4 lo;
            unsigned a0[3], a1[3], bA[3], bB[3];
#pragma unroll
            for (int i = 0; i < 3; i++) {
                a0[i] = aoff0 + i * STAGE_B; a1[i] = aoff1 + i * STAGE_B; bA[i] = boffA + i * STAGE_B; bB[i] = boffB + i * STAGE_B;
                PIN(a0[i]); PIN(a1[i]); PIN(bA[i]); PIN(bB[i]);
            }
            unsigned aT = ldsb + L_MB + (wm * 64 + t16) * 4, aS = ldsb + L_SD + (wm * 64 + t16) * 2, aM = ldsb + L_MB + wm * 256 + g * 16;
            unsigned aC = ldsb + L_CS + hh * 128 + g * 16, aV = ldsb + L_CS + (128 + hh * 32 + t16) * 4;
            unsigned aW = ldsb + L_WPHI + hh * 2048 + (4 * g + q4) * 64 + p4 * 8, aK = ldsb + L_KH + w * 2048 + l * 16;
            PIN(aT); PIN(aS); PIN(aM); PIN(aC); PIN(aV); PIN(aW); PIN(aK);
            pre_addr();
            READ_TAG();
            waitv<5>();
            PIN(xr[0]); PIN(xr[1]); PIN(xr[2]); PIN(xr[3]);
            {
                float s[2], q[2], x0[2];
#pragma unroll
                for (int rr = 0; rr < 2; rr++) {
                    x0[rr] = bflo(rfl(xr[2 * rr][0]));
                    s[rr] = 0.f; q[rr] = 0.f;
#pragma unroll
                    for (int i = 0; i < 4; i++) {
                        const float a = bflo(xr[2 * rr][i]) - x0[rr], b = bfhi(xr[2 * rr][i]) - x0[rr];
                        const float c = bflo(xr[2 * rr + 1][i]) - x0[rr], d = bfhi(xr[2 * rr + 1][i]) - x0[rr];
                        s[rr] += (a + b) + (c + d);
                        q[rr] += (a * a + b * b) + (c * c + d * d);
                    }
                }
                const float sq = row_sum(xrow_scatter<false>(s[0], s[1], q[0], q[1]));
#define RLF(x, n) __uint_as_float(__builtin_amdgcn_readlane(__float_as_uint(x), (n)))
                const float r0 = RLF(sq, 0), r1 = RLF(sq, 16), r2 = RLF(sq, 32), r3 = RLF(sq, 48);
                const float sm = (l ? r1 : r0) * (1.0f / 1024.0f), qm = (l ? r3 : r2) * (1.0f / 1024.0f);
                const float var = fmaxf(qm - sm * sm, 0.f);
                if (l < 2) st_gran(ws, WS_G0 + (srow1 + l) * 8, tag, pk2((l ? x0[1] : x0[0]) + sm, sqrtf(rbf(rbf(var) + 1e-5f))));
            }
            waitv<1>();
            WAITL0();
            ARR_MARK(0);
            BAR();
            SCHED();
            STAMPW(1, 0);
            auto rdB = [&](int u, int j, int i) {
                if (i & 1) B[j % 3][i >> 1] = cat8(lo, bq(bA[u], bB[u], j, i)); else lo = bq(bA[u], bB[u], j, i);
            };
            auto rdA = [&](int u, int i) { A[i & 1][i >> 1] = lds128(((i & 1) ? a1[u] : a0[u]) + (i >> 1) * 2048); };
            auto head = [&](int u) {
                group(acc, A, B[0], 0, [&](int i) { rdB(u, 1 + (i >> 2), i & 3); });
                group(acc, A, B[1], 1, [&](int i) { if (i < 4) rdB(u, 3, i); });
                group(acc, A, B[2], 2, [&](int i) { if (i < 4) rdB(u, 4, i); });
                group(acc, A, B[0], 3, [&](int i) { if (i < 4) rdB(u, 5, i); });
                WL0();
            };
#pragma unroll
            for (int i = 0; i < 4; i++) rdB(0, 0, i);
#pragma unroll
            for (int i = 0; i < 8; i++) rdA(0, i);
            SCHED();
#pragma nounroll
            for (unsigned t = 0; t < 5; t++) {
#pragma unroll
                for (int u = 0; u < 3; u++) {
                    head(u);
                    ARR_MARK(1 + t * 3 + u);
                    BAR();
                    SCHED();
                    group(acc, A, B[1], 4, [&](int i) { if (i < 4) rdB((u + 1) % 3, 0, i); });
                    group(acc, A, B[2], 5, [&](int i) { rdA((u + 1) % 3, i); });
                }
            }
            unsigned kw[4][4];
            f32x2 kx;
            auto kpiece = [&](int n) {
                if (n > 0) { kw[((n - 1 >> 2) + 2) & 3][(n - 1) & 3] = pk2(kx[0], kx[1]); PIN(kw[((n - 1 >> 2) + 2) & 3][(n - 1) & 3]); }
                if (n < 16) {
                    const int mi = ((n >> 2) + 2) & 3, j = n & 3;
                    const f32x4 &c = acc[mi][2 + (j >> 1)], &s_ = csk[j >> 1];
                    float x0 = __builtin_fmaf(mbt[mi], s_[2 * (j & 1)], c[2 * (j & 1)]); PIN(x0);
                    float x1 = __builtin_fmaf(mbt[mi], s_[2 * (j & 1) + 1], c[2 * (j & 1) + 1]); PIN(x1);
                    kx = (f32x2){x0, x1};
                }
                if (n == 5 || n == 9) *(LAS v4u*)(aK + (n == 9) * 1024) = (v4u){kw[2 + (n == 9)][0], kw[2 + (n == 9)][1], kw[2 + (n == 9)][2], kw[2 + (n == 9)][3]};
            };
            group(acc, A, B[0], 0, [&](int i) { rdB(0, 1 + (i >> 2), i & 3); });
            group(acc, A, B[1], 1, [&](int i) {
                if (i < 4) { rdB(0, 3, i); mbt[(i + 2) & 3] = ldsf(aT + ((i + 2) & 3) * 64); }
                else if (i < 6) csk[i - 4] = ldsf4(aC + 256 + (i - 4) * 64);
                else wf[i - 6] = cat8(tr_read(aW + (i - 6) * 32), tr_read(aW + (i - 6) * 32 + 1024));
            });
            group(acc, A, B[2], 2, [&](int i) { if (i < 4) rdB(0, 4, i); else mbv[i - 4] = ldsf4(aM + (i - 4) * 64); kpiece(i); });
            group(acc, A, B[0], 3, [&](int i) { if (i < 4) rdB(0, 5, i); if (i == 2 || i == 3) csv[i - 2] = ldsf(aV + (i - 2) * 64); kpiece(8 + i); });
            WL0();
            ARR_MARK(16);
            BAR();
            SCHED();
            kpiece(16);
            f32x4 p[2][2];
#pragma unroll
            for (int i = 0; i < 4; i++) p[i >> 1][i & 1] = MFMA(wf[i & 1], mk8(kw[i >> 1][0], kw[i >> 1][1], kw[i >> 1][2], kw[i >> 1][3]), zero4);
            SCHED();
            STAMPW(2, 0);
#pragma unroll
            for (int mi = 0; mi < 4; mi++) rst[mi] = lds_bf(aS + mi * 32);
            gc = ldg(rsWS, vgc, 0);
#pragma unroll
            for (int a = 0; a < 2; a++)
#pragma unroll
                for (int dt = 0; dt < 2; dt++) {
                    const f32x4 x0 = acc[2 * a][4 + dt] + mbv[2 * a] * csv[dt], x1 = acc[2 * a + 1][4 + dt] + mbv[2 * a + 1] * csv[dt];
                    *(LAS v4u*)(aK + (L_V - L_KH) + w * 2048 + (a * 2 + dt) * 1024) = (v4u){pk2(x0[0], x0[1]), pk2(x0[2], x0[3]), pk2(x1[0], x1[1]), pk2(x1[2], x1[3])};
                }
            SCHED();
            rst[0] = __builtin_amdgcn_rcpf(rst[0]); rst[1] = __builtin_amdgcn_rcpf(rst[1]);
            bf16x8 f[4];
            struct HookC {
                decltype(v_reads)& vr; decltype(sd_reads)& sr;
                DI void operator()(int k, float& x) const { if (k == 1) { PIN(x); WAITL0(); ARR_MARK(17); BAR(); PIN(x); } if (k == 3) { PIN(x); vr(); sr(); PIN(x); } }
                DI void operator()(int, float&, float&) const {}
            };
            softmax2<true>(p, rst[0], rst[1], g >= 2, f, HookC{v_reads, sd_reads});
            img_write2(ia, f);
#pragma unroll
            for (int ft = 0; ft < 2; ft++) wfq[ft] = cat8(tr_read(aW + ft * 32 + 4096), tr_read(aW + ft * 32 + 5120));
            csq[0] = ldsf4(aC); csq[1] = ldsf4(aC + 64);
            STAMPW(3, 0);
            kv_tile([]() {});
            STAMPW(4, 0);
            SCHED();
            group(acc, A, B[1], 4, [&](int i) { if (i == 2 || i == 3) { rst[i] = __builtin_amdgcn_rcpf(rst[i]); PIN(rst[i]); } });
            group(acc, A, B[2], 5, [&](int) {});
            asm volatile("s_nop 7\n\ts_nop 1");
            SCHED();
        }
        bf16x8 phq[2][4];
        v4u kg[6];
        auto kg_ld = [&](int k) { return k < 4 ? LD16(l * 16, WS_G3 + (hq * 2 + hh) * 4096 + k * 1024) : LD16(g * 16, WS_GZ + (hq * 2 + hh) * 128 + (k - 4) * 64); };
        {
            f32x4 pq[2][2][2];
#pragma unroll
            for (int mi = 0; mi < 4; mi++) {
                const f32x4 x0 = acc[mi][0] + mbt[mi] * csq[0], x1 = acc[mi][1] + mbt[mi] * csq[1];
                const bf16x8 qf = mk8(pk2(x0[0], x0[1]), pk2(x0[2], x0[3]), pk2(x1[0], x1[1]), pk2(x1[2], x1[3]));
                pq[mi >> 1][mi & 1][0] = MFMA(wfq[0], qf, zero4); pq[mi >> 1][mi & 1][1] = MFMA(wfq[1], qf, zero4);
            }
            MFMA_SETTLE2(pq[0]); MFMA_SETTLE2(pq[1]);
            if (RARE(!__all(gc[1] == tag))) { unsigned c[1]; sweep<1>(rsWS, vgc, 0, 0, tag, c); }
            ORDER_BEHIND_POLL();
            softmax2<false>(pq[0], rst[0], rst[1], g >= 2, phq[0]);
#pragma unroll
            for (int i = 0; i < 4; i++) PIN(phq[0][i]);
            SCHED();
            kv_reduce();
            SCHED();
            res_loads();
            if (DNP == 3) { stsu(ldsb + w * 4, 0); WAITL0(); }
            SCHED();
            BAR();
            SCHED();
            softmax2<false>(pq[1], rst[2], rst[3], g >= 2, phq[1]);
#pragma unroll
            for (int i = 0; i < 4; i++) { PIN(phq[0][i]); PIN(phq[1][i]); }
        }
        STAMPW(5, 0);
        STAMPW(26, 0);
        v2u fv = {tag ^ 0x5bd1e995u, tag};
        unsigned sF = WS_F4 + ((rt * 4 + w) * 16 + hq) * 8, vF = l == 0 ? 0 : OOB;
        {
            unsigned vO = l * 16, sO = WS_P4 + ((rt * 8 + wm * 4) * 32 + hq * 2 + hh) * 1024;
            asm volatile("" : "+s"(sO), "+s"(sF));
            PIN(fv); PIN(vF);
            SLEEP(16);
            sweepz<6, 6>(kg_ld, kg);
            STAMPW(6, 0);
            bf16x8 kvA[2][2], zA[2];
            v4u ov[4];
#pragma unroll
            for (int s = 0; s < 2; s++) {
#pragma unroll
                for (int dt = 0; dt < 2; dt++) kvA[s][dt] = mk8(kg[2 * s][2 * dt], kg[2 * s][2 * dt + 1], kg[2 * s + 1][2 * dt], kg[2 * s + 1][2 * dt + 1]);
                zA[s] = __builtin_bit_cast(bf16x8, kg[4 + s]);
            }
#define PH(mi, s) phq[(mi) >> 1][2 * ((mi) & 1) + (s)]
            f32x4 dn[4], n0[4], n1[4];
            float id[4];
#pragma unroll
            for (int mi = 0; mi < 4; mi++) dn[mi] = MFMA(zA[0], PH(mi, 0), zero4);
#pragma unroll
            for (int mi = 0; mi < 4; mi++) dn[mi] = MFMA(zA[1], PH(mi, 1), dn[mi]);
            SCHED();
#pragma unroll
            for (int mi = 0; mi <= 4; mi++) {
                if (mi < 4) {
                    n0[mi] = MFMA(kvA[0][0], PH(mi, 0), zero4); n1[mi] = MFMA(kvA[0][1], PH(mi, 0), zero4);
                    n0[mi] = MFMA(kvA[1][0], PH(mi, 1), n0[mi]); n1[mi] = MFMA(kvA[1][1], PH(mi, 1), n1[mi]);
                    id[mi] = __builtin_amdgcn_rcpf(rbf(rbf(dn[mi][0]) + 1e-6f));
                }
                if (mi > 0) {
                    const f32x4 a = n0[mi - 1] * id[mi - 1], b = n1[mi - 1] * id[mi - 1];
                    ov[mi - 1] = (v4u){pk2(a[0], a[1]), pk2(a[2], a[3]), pk2(b[0], b[1]), pk2(b[2], b[3])};
                    ST16(ov[mi - 1], vO + (mi - 1) * 32768, sO);
                }
                if (mi < 4) { SGB(0x8, 1); SGB(0x2, 6); SGB(0x8, 1); SGB(0x2, 3); SGB(0x8, 1); SGB(0x2, 3); SGB(0x8, 1); SGB(0x2, 4); }
                SCHED();
            }
#pragma unroll
            for (int mi = 0; mi < 4; mi++) PIN(ov[mi]);
        }
        if (DNP == 3) { stsu(ldsb + w * 4, 1); SCHED(); }
        waitv<0>();
        __builtin_amdgcn_raw_buffer_store_b64(fv, rsWS, vF, sF, AUX_SC1);
        STAMPW(7, 0);
    } else {
        const unsigned pw = lw & 1;
        const rsrc_t rsG = __builtin_amdgcn_make_buffer_rsrc((void*)WG, 0, 1024 * 3584 * 2, 0x00020000);
        const rsrc_t rsU = __builtin_amdgcn_make_buffer_rsrc((void*)WU, 0, 1024 * 3584 * 2, 0x00020000);
        unsigned vwl = kr * 57344 + (l & 7) * 16;
        unsigned sg = (kq * 256 + lw * 64) * 7168 + cg1 * 128, su = (kq * 256 + lw * 64) * 7168 + cg1u * 128;
        auto wloads = [&](int n0, int n1) {
#pragma unroll
            for (int n = n0; n < n1; n++)
                asm volatile("buffer_load_dwordx4 %0, %1, %2, %3 offen nt" : "=v"(wreg[n]) : "v"(vwl), "s"(n < 8 ? rsG : rsU), "s"((n < 8 ? sg : su) + (n & 7) * 7168));
        };
        auto dl_asm = [&]() {
            const rsrc_t rsD = __builtin_amdgcn_make_buffer_rsrc((void*)WD, 0, 1024 * 3584 * 2, 0x00020000);
#pragma unroll
            for (int i = 0; i < DN0; i++)
                asm volatile("buffer_load_dwordx4 %0, %1, %2, %3 offen nt" : "=v"(wreg[16 + i]) : "v"(kr * 4096 + (l & 7) * 16), "s"(rsD),
                             "s"((kc * 256 + lw * 64 + (i >> 1) * 16 + (i & 1)) * 2048 + cg * 128));
        };
        auto repack = [&](unsigned gI) {
            const unsigned s2 = (2 * g + (q4 >> 1)) & 3;
            bf16x8 f[4];
#pragma unroll
            for (int nt = 0; nt < 4; nt++) {
                const unsigned a = ldsb + L_WO + gI * 4096 + (4 * g + q4) * 128 + ((((nt >> 1) * 2 + (p4 >> 1)) ^ s2) * 32) + (p4 & 1) * 16 + (nt & 1) * 8;
                f[nt] = cat8(tr_read(a), tr_read(a + 2048));
            }
            WAITL0();
#pragma unroll
            for (int nt = 0; nt < 4; nt++) *(LAS bf16x8*)(ldsb + L_WO + gI * 4096 + nt * 1024 + l * 16) = f[nt];
        };
        unsigned aK, aW, aS;
        auto k_pre = [&]() {
            pre_addr();
            aK = ldsb + L_KH + lw * 2048 + l * 16; aW = ldsb + L_WPHI + (lw >> 1) * 2048 + (4 * g + q4) * 64 + p4 * 8; aS = ldsb + L_SD + (pw * 64 + 32 + t16) * 2;
            PIN(aK); PIN(aW); PIN(aS);
        };
        auto kfeat = [&](auto hook) {
            bf16x8 wf[2];
            float rst[2];
#pragma unroll
            for (int ft = 0; ft < 2; ft++) {
                wf[ft] = cat8(tr_read(aW + ft * 32), tr_read(aW + ft * 32 + 1024));
                rst[ft] = __builtin_amdgcn_rcpf(lds_bf(aS + ft * 32));
            }
            sd_reads();
            PIN(wf[0]); PIN(wf[1]); PIN(rst[0]); PIN(rst[1]);
            SCHED();
            ARR_MARK(16);
            BAR();
            SCHED();
            const bf16x8 kf0 = lds128(aK), kf1 = lds128(aK + 1024);
            SCHED();
            hook(-1, rst[0]);
            hook(0, rst[0]);
            SCHED();
            f32x4 p[2][2];
            p[0][0] = MFMA(wf[0], kf0, zero4); p[0][1] = MFMA(wf[1], kf0, zero4);
            p[1][0] = MFMA(wf[0], kf1, zero4); p[1][1] = MFMA(wf[1], kf1, zero4);
            MFMA_SETTLE2(p);
            bf16x8 f[4];
            softmax2<true>(p, rst[0], rst[1], g >= 2, f, hook);
            img_write2(ia, f);
            SCHED();
            hook(5, rst[0]);
            SCHED();
            STAMPW2(3, 4, 7);
            kv_tile([&]() { hook(6, rst[0]); });
            STAMPW2(4, 4, 7);
        };
        auto wblk = [&](int k) { return k == 0 ? 22 + lw : k == 1 ? (lw < 3 ? 26 + lw : 4) : k < 6 ? 4 * k - 3 + lw : k == 6 ? lw : (lw < 3 ? 29 + lw : 21); };
        auto wo_init = [&]() {
#pragma unroll
            for (int i = 0; i < 4; i++) { vwo[i] = vWO + i * (16384 - 1024); PIN(vwo[i]); }
        };
        unsigned hs[8];
        auto wl_pre = [&]() { asm volatile("" : "+s"(sg), "+s"(su)); PIN(vwl); };
        auto wo_pre = [&]() {
            wo_init();
#pragma unroll
            for (int k = 0; k < 8; k++) { hs[k] = wo_so(wblk(k)); asm volatile("" : "+s"(hs[k])); }
        };
        auto wo_rng = [&](int k0, int k1) {
#pragma unroll
            for (int k = k0; k < k1; k++) dma16x4(ldsb + L_WO + wblk(k) * 4096, vwo, rsWO, hs[k]);
        };
        auto pf = [&](int i) {
            if (i == 0) { wloads(0, 6); SCHED(); if (lw < 2) wo_pre(); SCHED(); }
            if (i < 3 && !ffn) wo_rng(i, i + 1);
            if (i == 1 || i == 2) wloads(6 * i, i == 1 ? 12 : 16);
#define DHK 4
            if (DNP == 2 && i == DHK) dl_asm();
        };
        struct Hook {
            decltype(pf)& f; decltype(v_reads)& vr;
            DI void operator()(int k, float& x) const { PIN(x); if (k == 1) { ARR_MARK(17); BAR(); } if (k == 3) vr(); f(3 + k); PIN(x); }
            DI void operator()(int k, float& x, float& y) const { PIN(y); f(3 + k); PIN(x); }
        };
        auto tail16 = [&]() {
            kfeat(Hook{pf, v_reads});
            if (ffn) wo_rng(0, 3);
            wo_rng(3, 4);
            static_for<4>([&](auto kc_) {
                constexpr int k = decltype(kc_)::value;
                waitv<4 * (3 - k)>();
                repack(wblk(k));
            });
            BAR();
            res_loads();
            wo_rng(4, 8);
            static_for<4>([&](auto kc_) {
                constexpr int k = decltype(kc_)::value + 4;
                waitv<4 * (7 - k)>();
                repack(wblk(k));
            });
            waitv<0>();
#pragma unroll
            for (int n = 0; n < 16 + (DNP == 2 ? DN0 : 0); n++) PIN(wreg[n]);
            PIN(xres[0]); PIN(xres[1]); PIN(wrv[0]); PIN(wrv[1]);
        };
        auto vT23 = [&]() {
            const unsigned n = (idx * 2 + pw) * 64 + l, r = n / 12, c = n % 12;
            return idx < 24 ? ((r & 127) + (r >> 7) * 256 + 128) * 6144 + (c >> 2) * 2048 + (((xcd >> 1) * 4 + (c >> 2 ? 0 : 4) + (c & 3)) & 15) * 128 : OOB;
        };
        if (lw < 2) {
            const unsigned sA = (rt * 128 + lw * 64) * 2048, sB = lw * 32 * 6144 + hp * 128, sBq = lw * 32 * 6144 + hq * 128;
            unsigned buf = 1;
            unsigned va[4], vb[4];
#pragma unroll
            for (int i = 0; i < 4; i++) { va[i] = vA + i * (16384 - 1024); vb[i] = ((i & 1) ? vB1 : vB0) + i * (49152 - 1024); PIN(va[i]); PIN(vb[i]); }
            auto stage = [&](unsigned s, unsigned bf, int i0, int i1) {
                const unsigned kt = (s + rot) & 15;
                const unsigned lb = ldsb + bf * STAGE_B;
                const unsigned sa = sA + kt * 128, sb = kt * 393216;
                if (i0 <= 0 && 0 < i1) dma16x4(lb + lw * 8192, va, rsX, sa);
                if (i0 <= 1 && 1 < i1) dma16x4(lb + lw * 8192 + 4096, va, rsX, sa + 65536);
                if (i0 <= 2 && 2 < i1) dma16x4(lb + 16384 + 8192 + lw * 4096, vb, rsW, sb + sB + 2048);
                if (i0 <= 3 && 3 < i1) dma16x4(lb + 16384 + 16384 + lw * 4096, vb, rsW, sb + sB + 4096);
                if (i0 <= 4 && 4 < i1) dma16x4(lb + 16384 + lw * 4096, vb, rsW, sb + sBq);
            };
            stage(1, 1, 0, 4);
            SCHED();
            unsigned vt = vT23();
            PIN(vt);
            wl_pre(); k_pre();
            waitv<16>(); ARR_MARK(0); BAR();
            stage(1, 1, 4, 5);
            touch(vt, rsW, (xcd & 1) * 512 * 6144);
            waitv<1>(); ARR_MARK(1); BAR();
            stage(2, 2, 0, 5); stage(3, 0, 0, 3);
            waitv<12>(); ARR_MARK(2); BAR();
            stage(3, 0, 3, 5); stage(4, 1, 0, 4);
            waitv<16>(); ARR_MARK(3); BAR();
            stage(4, 1, 4, 5);
            buf = 2;
#pragma nounroll
            for (unsigned s = 5; s < 16; s++) {
                if (s > 5) { waitv<20>(); ARR_MARK(s - 2); BAR(); }
                stage(s, buf, 0, 5);
                buf = buf == 2 ? 0 : buf + 1;
            }
            waitv<20>(); ARR_MARK(14); BAR();
            pf(0);
            if (ffn) waitv<6>(); else waitv<10>();
            ARR_MARK(15); BAR();
            pf(1);
            tail16();
            READ_TAG();
        } else {
            SCHED();
            unsigned v3;
            const unsigned h3 = (xcd >> 1) * 4 + 3, kv3 = !(xcd & 2), wv = idx * 2 + pw;
            auto line3 = [&](unsigned per, unsigned rows, auto rowf) {
                const unsigned n = wv * (kv3 ? per : (per + 2) / 3) + l, r3 = kv3 ? n / 3 : n, c3 = kv3 ? n % 3 : 0;
                return (idx < 24 && l < (kv3 ? per : (per + 2) / 3) && r3 < rows) ? rowf(r3) * 6144 + c3 * 2048 + ((c3 ? h3 : h3 + 4) & 15) * 128 : OOB;
            };
            v3 = line3(32, 512, [](unsigned r) { return r; });
            touch(line3(16, 256, [](unsigned r) { return (r & 127) + (r >> 7) * 256; }), rsW, (xcd & 1) * 512 * 6144);
            {
                const rsrc_t rsP = __builtin_amdgcn_make_buffer_rsrc((void*)WPHI, 0, 32 * 1024 * 2, 0x00020000);
#pragma unroll
                for (int i = 0; i < 4; i++) dma16(ldsb + L_WPHI + pw * 4096 + i * 1024, l * 16, rsP, (pw ? hq : hp) * 4096 + i * 1024);
            }
            v4u sv = {SENT, SENT, SENT, SENT};
            unsigned vc4 = l < 16 ? WS_G3 + (hp * 4 + (rt >> 2)) * 2048 + pw * 1024 + (rt & 3) * 256 + l * 16
                         : l < 24 ? WS_P2 + (hp * 16 + rt) * P2_STRIDE + 8192 + pw * 128 + (l - 16) * 16
                         : (l < 32 && pw == 0 && (rt & 7) == 0) ? WS_GZ + (hp * 2 + (rt >> 3)) * 128 + (l - 24) * 16 : OOB;
            auto clears = [&](int i0, int i1) {
#pragma unroll
                for (int i = i0; i < i1; i++) {
                    if (i < 4) ST16(sv, l * 16, WS_P2 + (hp * 16 + rt) * P2_STRIDE + pw * 4096 + i * 1024);
                    if (i == 4) ST16(sv, vc4, 0);
                    if (i > 4) ST16(sv, l * 16, WS_TL + (bid * 4 + pw * 2 + i - 5) * 1024);
                }
                FRESH();
            };
            PIN(vc4); PIN(sv);
            unsigned ca = ldsb + 16384 + pw * 8192 + (8 * g + q4) * 128 + (((rt & 3) ^ (((q4 >> 1) & 1) | ((g & 1) << 1))) * 32) + p4 * 8;
            PIN(ca);
            bf16x8 cf[4];
            f32x4 cd[2] = {zero4, zero4};
            auto cs_read = [&](int st) {
#pragma unroll
                for (int i = 0; i < 4; i++) cf[i] = cat8(tr_read(ca + (st % 3) * STAGE_B + i * 4096), tr_read(ca + (st % 3) * STAGE_B + i * 4096 + 512));
                SCHED();
            };
            READ_TAG();
            waitv<5>();
            ARR_MARK(0);
            BAR();
            SCHED();
            cs_read(0);
            FRESH();
            const rsrc_t rsC = __builtin_amdgcn_make_buffer_rsrc((void*)pc0, 0, clen, 0x00020000);
            auto code_pf = [&]() { touch(l < 6 ? l * 128 : OOB, rsC, (idx * 2 + pw) * 768); };
            v4u gl[4];
            float s0 = 0.f, s1 = 0.f;
            unsigned bad;
            v2u gs;
            unsigned vgl = (l < 48 ? l : 0) * 16;
            unsigned aw0 = ldsb + L_CS + pw * 384 + (vgl >> 1), aw1 = ldsb + L_MB + (pw * 64 + l) * 4, aw2 = ldsb + L_SD + (pw * 64 + l) * 2;
            static_for<15>([&](auto tc) {
                constexpr int t = decltype(tc)::value;
                WAITL0();
                ARR_MARK(t + 1);
                BAR();
                SCHED();
                if constexpr (t < 4) {
                    __builtin_amdgcn_s_setprio(2);
#pragma unroll
                    for (int i = 0; i < 4; i++) cd[i & 1] = MFMA(ones, cf[(i & 1) * 2 + (i >> 1)], cd[i & 1]);
                    SCHED();
                    __builtin_amdgcn_s_setprio(0);
                    if constexpr (t < 3) cs_read(t + 1);
                    else __builtin_amdgcn_raw_buffer_store_b64((v2u){__float_as_uint(g ? cd[1][0] : cd[0][0]), tag}, rsWS, (g == 1 || g + pw == 0) ? g * 512 + t16 * 8 : OOB,
                                                               WS_G1 + ((hp * 4 + (rt >> 2)) * 192 + pw * 64 + (rt & 3) * 16) * 8, AUX_SC1);
                }
                if constexpr (t == 0) touch(v3, rsW, (~xcd & 1) * 512 * 6144);
                if constexpr (t == 12) code_pf();
                if constexpr (t == 8) {
                    waitv<0>();
                    __builtin_amdgcn_raw_buffer_store_b64((v2u){0u, tag}, rsWS, l == 0 ? 0 : OOB, WS_GC + ((hp * 2 + pw) * 16 + rt) * 8, AUX_SC1);
                }
                if constexpr (t == 8 || t == 9) {
#pragma unroll
                    for (int r = (t - 8) * 2; r < (t - 8) * 2 + 2; r++)
                        asm volatile("buffer_load_dwordx4 %0, %1, %2, %3 offen sc1" : "=v"(gl[r]) : "v"(vgl), "s"(rsWS), "s"(WS_G1 + ((hp * 4 + r) * 192 + pw * 96) * 8) : "memory");
                }
                if constexpr (t == 6) {
                    asm volatile("buffer_load_dwordx2 %0, %1, %2, %3 offen sc1" : "=v"(gs) : "v"(l * 8), "s"(rsWS), "s"(WS_G0 + (rt * 128 + pw * 64) * 8) : "memory");
                }
                if constexpr (t == 11) {
                    waitv<0>();
                    PIN(gs);
                    bad = gs[1] ^ tag;
#pragma unroll
                    for (int r = 0; r < 4; r++) {
                        PIN(gl[r]);
                        bad |= (gl[r][1] ^ tag) | (gl[r][3] ^ tag);
                        s0 += __uint_as_float(gl[r][0]); s1 += __uint_as_float(gl[r][2]);
                    }
                    PIN(s0); PIN(s1); PIN(bad);
                }
                if constexpr (t == 12) {
                    if (RARE(!__all(bad == 0))) {
                        s0 = 0.f; s1 = 0.f;
#pragma nounroll
                        for (unsigned r = 0; r < 4; r++) {
                            unsigned a[2];
                            sweep<2>(rsWS, vgl, WS_G1 + ((hp * 4 + r) * 192 + pw * 96) * 8, 8, tag, a);
                            s0 += __uint_as_float(a[0]); s1 += __uint_as_float(a[1]);
                        }
                        unsigned c[1];
                        sweep<1>(rsWS, l * 8, WS_G0 + (rt * 128 + pw * 64) * 8, 0, tag, c);
                        gs[0] = c[0];
                    }
                    *(LAS f32x2*)(aw0) = (f32x2){-s0, -s1};
                    stsf(aw1, bflo(gs[0]));
                    *(LAS u16*)(aw2) = (u16)(gs[0] >> 16);
                    SCHED();
                }
                if constexpr (t == 5) k_pre();
                if constexpr (t == 6) { wl_pre(); wo_pre(); }
                if constexpr (t == 7) { PIN(vgl); PIN(aw0); PIN(aw1); PIN(aw2); }
                clears(t < 7 ? t : 7, t < 7 ? t + 1 : 7);
                if constexpr (t == 13) pf(0);
                if constexpr (t == 14) pf(1);
            });
            tail16();
        }
        WAITL0();
        STAMPW2(7, 4, 7);
    }
    const unsigned sFl = WS_F4 + (rt2 * 4 + (lw >> 1) + 2 * half) * 128;
    if (DNP == 0 && half) { SCHED(); dloads(0, DN0); SCHED(); }
    if (DNP == 3 && half) {
#pragma nounroll
        for (unsigned spins = 0; spins < 16 * SPIN_CAP; spins++) {
            const v4u f = *(const volatile LAS v4u*)ldsb;
            if (rfl(f[0] & f[1] & f[2] & f[3])) break;
            SLEEP(1);
        }
        SCHED(); dloads(0, DN0); SCHED();
    }
    ARR_MARK(20);
    BAR();
    ARR_END();
    if (DNP == 1 && half) { SCHED(); dloads(0, DN0); SCHED(); }
    const unsigned wb = ldsb + L_WO + half * 65536 + l * 16;
    bf16x8 bb[2][4];
#pragma unroll
    for (int nt = 0; nt < 4; nt++) { bb[0][nt] = lds128(wb + nt * 1024); PIN(bb[0][nt]); }
    unsigned so[16], va0 = l * 16 + half * 32768, va1 = l * 16 + (half ^ 1) * 32768;
    unsigned aXw = ldsb + L_XB + w * 4096 + l * 16, aXr = ldsb + L_XB + (w ^ 4) * 4096 + l * 16, sG5 = WS_G5 + ((rt2 * 8 + lw * 2 + half) * 16 + ct) * 256;
    unsigned vG5 = g == q4 ? t16 * 16 : OOB;
#pragma unroll
    for (int st = 0; st < 16; st++) { so[st] = WS_P4 + (rt2 * 8 + lw * 2) * 32768 + ((2 * st + half + rot2) & 31) * 1024; asm volatile("" : "+s"(so[st])); }
    PIN(va0); PIN(va1); PIN(aXw); PIN(aXr); PIN(vG5);
    asm volatile("" : "+s"(sG5));
    unsigned wp1[2][4][8];
    if (w >= 4) {
#pragma unroll
        for (int ip = 0; ip < 4; ip++)
#pragma unroll
            for (int c = 0; c < 4; c++) {
#pragma unroll
                for (int m = 0; m < 2; m++) {
                    const unsigned a = wreg[m * 8 + 2 * ip][c], b = wreg[m * 8 + 2 * ip + 1][c];
                    wp1[m][ip][2 * c] = __builtin_amdgcn_perm(b, a, 0x05040100);
                    wp1[m][ip][2 * c + 1] = __builtin_amdgcn_perm(b, a, 0x07060302);
                }
            }
#pragma unroll
        for (int ip = 0; ip < 4; ip++)
#pragma unroll
            for (int c = 0; c < 8; c++) { PIN(wp1[0][ip][c]); PIN(wp1[1][ip][c]); }
    }
    if (half) SLEEP(S3H); else SLEEP(S3);
    poll16(rsWS, sFl, tag, l);
    FRESH();
    SCHED();
    STAMPW2(8, 0, 4);
    unsigned swc;
    f32x4 acc2[2][4];
    bf16x8 sop[2];
    {
        auto aload = [&](int st, int m2) { return LD16(m2 ? va1 : va0, so[st]); };
        constexpr int PD = 2;
        v4u af[PD][2];
#pragma unroll
        for (int i = 0; i < PD; i++) {
#pragma unroll
            for (int m2 = 1; m2 >= 0; m2--) af[i][m2] = aload(i, m2);
            SCHED();
        }
#pragma unroll
        for (int nt = 0; nt < 4; nt++) {
            const unsigned x0 = xres[nt >> 1][2 * (nt & 1)], x1 = xres[nt >> 1][2 * (nt & 1) + 1];
            acc2[0][nt] = (f32x4){bflo(x0), bfhi(x0), bflo(x1), bfhi(x1)};
            acc2[1][nt] = zero4;
        }
        unsigned m0 = p4 == 0 ? 0x3F803F80u : 0u, m1 = p4 == 1 ? ~0u : 0u;
        PIN(m0); PIN(m1);
#pragma unroll
        for (int i = 0; i < 2; i++) {
            v4u u;
#pragma unroll
            for (int j = 0; j < 4; j++) u[j] = (wrv[i][j] & m1) | m0;
            sop[i] = __builtin_bit_cast(bf16x8, u);
            PIN(sop[i]);
        }
        {
            float sw = 0.f;
#pragma unroll
            for (int i = 0; i < 8; i++) sw = dot2(wrv[i >> 2][i & 3], 0x3F803F80u, sw);
            swc = f2s(xrow_sum(sw));
            PIN(swc);
        }
        SCHED();
#pragma unroll
        for (int st = 0; st < 16; st++) {
            if (st + 1 < 16) {
#pragma unroll
                for (int nt = 0; nt < 4; nt++)
                    bb[(st + 1) % (2)][nt] = lds128(wb + (st + 1) * 4096 + nt * 1024);
            }
#pragma unroll
            for (int mm = 0; mm < 2; mm++) {
                const int m2 = mm ^ 1;
                const bf16x8 a = __builtin_bit_cast(bf16x8, af[st % PD][m2]);
#pragma unroll
                for (int nt = 0; nt < 4; nt++) acc2[m2][nt] = MFMA(bb[st % (2)][nt], a, acc2[m2][nt]);
            }
            if (st + PD < 16) {
#pragma unroll
                for (int m2 = 1; m2 >= 0; m2--) af[st % PD][m2] = aload(PD + st, m2);
            }
#pragma unroll
            for (int i = 0; i < 4; i++) { SGB(0x8, 1); SGB(0x100, 1); }
            SCHED();
        }
    }
    STAMPW2(9, 0, 4);
#pragma unroll
    for (int nt = 0; nt < 4; nt++) stsf4(aXw + nt * 1024, acc2[1][nt]);
    SCHED();
    if (half) dloads(DN0, DN1);
    SCHED();
    WAITL0();
    if (half) __builtin_amdgcn_s_setprio(1);
    BAR();
    SCHED();
    v4u hpk[2];
    {
        f32x4 pa[4];
#pragma unroll
        for (int nt = 0; nt < 4; nt++) pa[nt] = ldsf4(aXr + nt * 1024);
        SCHED();
#pragma unroll
        for (int i = 0; i < 8; i++) {
            const f32x4 a = acc2[0][i >> 1] + pa[i >> 1];
            hpk[i >> 2][i & 3] = pk2(a[2 * (i & 1)], a[2 * (i & 1) + 1]);
        }
        const bf16x8 h0 = __builtin_bit_cast(bf16x8, hpk[0]), h1 = __builtin_bit_cast(bf16x8, hpk[1]);
        f32x4 gq = MFMA(h0, h0, zero4), sd = MFMA(sop[0], h0, zero4);
        gq = MFMA(h1, h1, gq); sd = MFMA(sop[1], h1, sd);
        float q0 = (l & 1) ? gq[1] : gq[0], q1 = (l & 1) ? gq[3] : gq[2];
        PIN(q0); PIN(q1);
        const v4u pv = {f2s(sd[0]), f2s((l & 2) ? q1 : q0), f2s(sd[1]), swc};
        ST16(pv, vG5, sG5);
        asm volatile("s_nop 1" :: "v"(pv));
    }
    __builtin_amdgcn_s_setprio(0);
    STAMPW2(10, 0, 4);
    unsigned wp2[4][8];
    auto perm2 = [&]() {
#pragma unroll
        for (int i = 16; i < 24; i++) PIN(wreg[i]);
#pragma unroll
        for (int ip = 0; ip < 4; ip++)
#pragma unroll
            for (int c = 0; c < 4; c++) {
                const unsigned a = wreg[16 + 2 * ip][c], b = wreg[16 + 2 * ip + 1][c];
                wp2[ip][2 * c] = __builtin_amdgcn_perm(b, a, 0x05040100);
                wp2[ip][2 * c + 1] = __builtin_amdgcn_perm(b, a, 0x07060302);
            }
#pragma unroll
        for (int ip = 0; ip < 4; ip++)
#pragma unroll
            for (int c = 0; c < 8; c++) PIN(wp2[ip][c]);
    };
    const unsigned mt = rt2 * 8 + lw * 2 + half;
    v4u outv[2];
    unsigned vP = t16 == rt2 ? OOB : t16 * 128, pk;
#define POL 0
    unsigned sRep;
    asm volatile("s_getreg_b32 %0, hwreg(HW_REG_XCC_ID, 0, 3)" : "=s"(sRep));
    sRep = WS_GK + (POL == 2 ? bid & 15 : (sRep ^ (POL * 4) ^ (((unsigned)(size_t)ws >> 11) & 4)) + (idx & 1) * 8) * 2048;
    {
        v4u pv[4];
#define NW 4
#define NRP 16
        unsigned vK = g < NW ? ((ct + (NRP / NW) * g) & (NRP - 1)) * 2048 : OOB;
        PIN(vK);
        unsigned sK = WS_GK + rt2 * 128, rk = 2047u - R2, aKy = ldsb + L_WO + 576, aKw = aKy + w * 4;
        asm volatile("" : "+s"(sK));
        PIN(rk); PIN(aKy); PIN(aKw); PIN(vP);
        SLEEP(S4);
        __builtin_amdgcn_s_setprio(1);
        sweepz<4, 4>([&](int k) { return LD16(l * 16, WS_G5 + mt * 4096 + k * 1024); }, pv);
        STAMPW2(21, 0, 4);
        const f32x4 ps = (__builtin_bit_cast(f32x4, pv[0]) + __builtin_bit_cast(f32x4, pv[1])) + (__builtin_bit_cast(f32x4, pv[2]) + __builtin_bit_cast(f32x4, pv[3]));
        float T[4];
        xrow_gather(xrow_scatter<false>(ps[0], ps[1], ps[2], ps[3]), T);
        const float mean = T[0] * (1.0f / 1024.0f), var = fmaxf(T[1] * (1.0f / 1024.0f) - mean * mean, 0.f);
        const unsigned sg = pk2(mean, __builtin_amdgcn_sqrtf(rbf(rbf(var) + 1e-5f)));
        const float mb2 = bflo(sg), isd2 = __builtin_amdgcn_rcpf(bfhi(sg));
        const float rsc = (T[2] - mb2 * T[3]) * isd2;
        const unsigned rb = pk2(rsc, rsc) & 0xffffu;
        const unsigned ord = (rb & 0x8000u) ? (~rb & 0xffffu) : (rb | 0x8000u);
        const unsigned key = (ord << 11) | rk;
        unsigned kb = umx(key, dppu<0x128>(key));
        kb = umx(kb, dppu<0x124>(kb)); kb = umx(kb, dppu<0x122>(kb)); kb = umx(kb, dppu<0x121>(kb));
        stsu(aKw, kb);
        WAITL0();
        BAR();
        SCHED();
        pk = umx(wmax(*(const LAS v4u*)aKy), wmax(*(const LAS v4u*)(aKy + 16)));
        const bool cand = key == pk;
        ST4(pk, cand ? vK : OOB, sK);
        SCHED();
        __builtin_amdgcn_s_setprio(0);
#define KS 6
        unsigned k1;
        if (w == 1) {
            if (KS) SLEEP(KS);
            asm volatile("buffer_load_dword %0, %1, %2, %3 offen sc1" : "=v"(k1) : "v"(vP), "s"(rsWS), "s"(sRep) : "memory");
        }
        SCHED();
#define DP 1
        if (DP == 0 && half) { dloads(DN1, 8); SCHED(); }
        v4u e[2];
#pragma unroll
        for (int i = 0; i < 8; i++) {
            const float h0 = bflo(hpk[i >> 2][i & 3]), h1 = bfhi(hpk[i >> 2][i & 3]);
            const unsigned d = pk2(h0 - mb2, h1 - mb2);
            e[i >> 2][i & 3] = nsent(pk2(bflo(d) * isd2, bfhi(d) * isd2));
        }
        ST16(e[0], cand ? g * 16 : OOB, WS_C + (rt2 * 16 + ct) * 128);
        ST16(e[1], cand ? g * 16 + 64 : OOB, WS_C + (rt2 * 16 + ct) * 128);
        SCHED();
        if (DP == 1 && half) { dloads(DN1, 8); SCHED(); }
        STAMPW2(22, 0, 4);
#pragma unroll
        for (int i = 0; i < 8; i++)
            outv[i >> 2][i & 3] = pk2(bflo(hpk[i >> 2][i & 3]) + bflo(e[i >> 2][i & 3]), bfhi(hpk[i >> 2][i & 3]) + bfhi(e[i >> 2][i & 3]));
        if (cand) {
#pragma unroll
            for (int i = 0; i < 2; i++) { *(LAS v4u*)(ldsb + L_STASH + i * 64 + g * 16) = hpk[i]; *(LAS v4u*)(ldsb + L_STASH + 128 + i * 64 + g * 16) = e[i]; }
        }
        PIN(outv[0]); PIN(outv[1]);
        if (DP == 2 && half) { dloads(DN1, 8); SCHED(); }
        if (w == 1) {
            waitv<2>();
            PIN(k1);
            unsigned hb[1] = {k1};
#define KR 0
            if (!__all(k1 != SENT)) sweepz<1, KR>([&](int) { return LD4(vP, sRep); }, hb);
            STAMPR(23, 2);
            unsigned gk = umx(hb[0], pk);
            gk = umx(gk, dppu<0x128>(gk)); gk = umx(gk, dppu<0x124>(gk)); gk = umx(gk, dppu<0x122>(gk)); gk = umx(gk, dppu<0x121>(gk));
            stsu(ldsb + L_WO + 512, gk);
        }
    }
    const rsrc_t rsO = __builtin_amdgcn_make_buffer_rsrc((void*)OUT, 0, 2048 * 1024 * 2, 0x00020000);
#define PP 1
    if (PP == 0 && half) perm2();
    STAMPW(24, 7);
    WAITL0();
    if (half) __builtin_amdgcn_s_setprio(1);
    BAR();
    STAMPW2(11, 0, 4);
    const unsigned gkey = rfl(ldsu(ldsb + L_WO + 512));
    const unsigned srow = 2047u - (gkey & 2047u);
    auto store_out = [&]() {
        if (R2 != srow) {
#pragma unroll
            for (int i = 0; i < 2; i++)
                __builtin_amdgcn_raw_buffer_store_b128(outv[i], rsO, R2 * 2048 + g * 16, ct * 128 + i * 64, AUX_SC1);
        }
    };
    if (w < 4) {
        if (bid == 0 && tid == 0) __hip_atomic_store((gu32*)(ws + WS_EPOCH), tag, RLX_AGENT);
        store_out();
        STAMPW(12, 0);
        RDUMP();
        return;
    }
#pragma unroll
    for (int nt = 0; nt < 2; nt++) PIN(outv[nt]);

    auto tree16 = [&](unsigned a, unsigned stride) {
        float v[16];
#pragma unroll
        for (int i = 0; i < 16; i++) v[i] = ldsf(a + i * stride);
        SCHED();
#pragma unroll
        for (int s = 8; s > 0; s >>= 1)
#pragma unroll
            for (int i = 0; i < s; i++) v[i] = v[2 * i] + v[2 * i + 1];
        return v[0];
    };
    if (ffn) {
        v4u xq[1];
        sweepz<1>([&](int) { return LD16(kr * 16, WS_C + (srow >> 7) * 2048 + kq * 512 + lw * 128); }, xq);
        const v4u xp = xq[0];
        if (DP == 3) { SCHED(); dloads(DN1, 8); SCHED(); }
        STAMPW(13, 4);
#pragma unroll
        for (int m = 0; m < 2; m++) {
            float a1[8];
#pragma unroll
            for (int c = 0; c < 8; c++) {
                float s = 0.f;
#pragma unroll
                for (int ip = 0; ip < 4; ip++) s = dot2(wp1[m][ip][c], xp[ip], s);
                a1[c] = s + dppf<0x128>(s);
            }
            if (t16 < 8) {
                const unsigned o = ldsb + L_RED + (((lw * 4 + g) * 2 + m) * 64 + t16 * 8) * 4;
                stsf4(o, (f32x4){a1[0], a1[1], a1[2], a1[3]});
                stsf4(o + 16, (f32x4){a1[4], a1[5], a1[6], a1[7]});
            }
        }
    }
    if (DP == 3) WAITL0(); else __builtin_amdgcn_s_waitcnt(0x0070);
    BAR();
    if (ffn) {
        if (lw < 2) {
            ST4(f2s(tree16(ldsb + L_RED + (lw * 64 + l) * 4, 512)), l * 4, WS_G8 + ((kq * 2 + lw) * 3584 + (lw ? cg1u : cg1) * 64) * 4);
        }
        STAMPW(14, 4);
        unsigned pv[8];
#define S6 12
        if (PP == 1) { SCHED(); perm2(); SCHED(); }
        SLEEP(S6);
        sweepz<8>([&](int k) { return LD4(l * 4, WS_G8 + (kc * 256 + lw * 64 + k * 3584) * 4); }, pv);
        STAMPW(15, 4);
        const float gg = rbf(((__uint_as_float(pv[0]) + __uint_as_float(pv[2])) + __uint_as_float(pv[4])) + __uint_as_float(pv[6]));
        const float uu = rbf(((__uint_as_float(pv[1]) + __uint_as_float(pv[3])) + __uint_as_float(pv[5])) + __uint_as_float(pv[7]));
        const float act = rbf(gg * __builtin_amdgcn_rcpf(1.0f + __expf(-gg))) * uu;
        const unsigned apk = pk2(act, dppf<0xB1>(act));
        unsigned ap[4];
#pragma unroll
        for (int ip = 0; ip < 4; ip++) ap[ip] = __builtin_amdgcn_ds_bpermute((ip * 16 + kr * 2) * 4, apk);
        float a2[8];
#pragma unroll
        for (int c = 0; c < 8; c++) {
            float s = 0.f;
#pragma unroll
            for (int ip = 0; ip < 4; ip++) s = dot2(wp2[ip][c], ap[ip], s);
            a2[c] = s + dppf<0x128>(s);
        }
        if (t16 < 8) {
            const unsigned o = ldsb + L_RED + 8192 + ((lw * 4 + g) * 64 + t16 * 8) * 4;
            stsf4(o, (f32x4){a2[0], a2[1], a2[2], a2[3]});
            stsf4(o + 16, (f32x4){a2[4], a2[5], a2[6], a2[7]});
        }
    }
    WAITL0();
    BAR();
    if (lw != 0) { store_out(); RDUMP(); return; }
    if (ffn) {
        ST4(f2s(tree16(ldsb + L_RED + 8192 + l * 4, 256)), l * 4, WS_G9 + (cg * 14 + kc) * 256);
    }
    SCHED();
    store_out();
    STAMPW(16, 4);
    if ((srow >> 7) == rt2) {
        const unsigned ord = gkey >> 11;
        const unsigned rb = (ord & 0x8000u) ? (ord & 0x7fffu) : (~ord & 0xffffu);
        const float rs = bf2f(rb);
        float sig = rbf(1.0f / (1.0f + __expf(-rs)));
        const unsigned sb = ldsb + L_STASH + (t16 * 4 + g) * 2;
        float hc = lds_bf(sb), lc = lds_bf(sb + 128);
        PIN(sig); PIN(hc); PIN(lc);
        v4u yp[4];
        SLEEP(S7);
        sweepz<4>([&](int k) { return LD16((k < 3 || l < 32) ? l * 16 : OOB, WS_G9 + ct * 3584 + k * 1024); }, yp);
        const f32x4 ys = (__builtin_bit_cast(f32x4, yp[0]) + __builtin_bit_cast(f32x4, yp[1])) + (__builtin_bit_cast(f32x4, yp[2]) + __builtin_bit_cast(f32x4, yp[3]));
        const float y = rbf(rbf(xrow_scatter<false>(ys[0], ys[1], ys[2], ys[3])) * sig);
        OUT[(size_t)srow * 1024 + ct * 64 + t16 * 4 + g] = (u16)(pk2(hc + rbf(lc + y), 0.f) & 0xffffu);
        STAMPW(17, 4);
    }
    RDUMP();
}
__device__ __attribute__((used, noinline)) void fk_tail() { asm volatile(".Lfk_end:"); }

extern "C" void kernel_launch(void* const* d_in, const int* in_sizes, int n_in, void* d_out, int out_size, void* d_ws,
                              size_t ws_size, hipStream_t stream) {
    (void)in_sizes; (void)n_in; (void)out_size; (void)ws_size;
    (void)hipFuncSetAttribute((const void*)fused_kernel, hipFuncAttributeMaxDynamicSharedMemorySize, LDS_BYTES);
    fused_kernel<<<256, 512, LDS_BYTES, stream>>>((const u16*)d_in[0], (const u16*)d_in[1], (const u16*)d_in[2], (const u16*)d_in[3],
                                                  (const u16*)d_in[5], (const u16*)d_in[6], (const u16*)d_in[7], (const u16*)d_in[8],
                                                  (u16*)d_out, (char*)d_ws);
}
```

```cpp
#include <hip/hip_runtime.h>
#include <stdint.h>
#include <utility>

typedef unsigned short u16;
typedef unsigned long long u64;
typedef short bf16x8 __attribute__((ext_vector_type(8)));
typedef short s16x4 __attribute__((ext_vector_type(4)));
typedef float f32x4 __attribute__((ext_vector_type(4)));
typedef float f32x2 __attribute__((ext_vector_type(2)));
typedef __bf16 bf16x2 __attribute__((ext_vector_type(2)));
typedef unsigned v4u __attribute__((ext_vector_type(4)));
typedef unsigned v2u __attribute__((ext_vector_type(2)));
#define LAS __attribute__((address_space(3)))
#define GAS __attribute__((address_space(1)))
#define CAS __attribute__((address_space(4)))
typedef GAS u64 gu64;
typedef GAS unsigned gu32;
typedef __amdgpu_buffer_rsrc_t rsrc_t;
#define DI __device__ __forceinline__
#define RLX_AGENT __ATOMIC_RELAXED, __HIP_MEMORY_SCOPE_AGENT
#define BAR() __builtin_amdgcn_s_barrier()
#define SCHED() __builtin_amdgcn_sched_barrier(0)
#define SGB(m, n) __builtin_amdgcn_sched_group_barrier((m), (n), 0)
#define WAITL0() asm volatile("s_waitcnt lgkmcnt(0)" ::: "memory")
#define WL0() __builtin_amdgcn_s_waitcnt(0xc07f)
#define MFMA(a, b, c) __builtin_amdgcn_mfma_f32_16x16x32_bf16((a), (b), (c), 0, 0, 0)
#define ORDER_BEHIND_POLL() __builtin_amdgcn_fence(__ATOMIC_ACQUIRE, "wavefront")
#define AUX_SC1 16
template <int N> DI void waitv() { asm volatile("s_waitcnt vmcnt(%0)" ::"i"(N) : "memory"); }

#define WS_EPOCH 0
#define WS_G0 0x1000
#define WS_G1 0x8000
#define WS_F4 0x78000
#define WS_GC 0x70000
#define WS_PR 0x74000
#define WS_G3 0x80000
#define WS_GZ 0xC0000
#define SENT 0xFFFFFFFFu
#define WS_TL 0x100000
#define WS_G5 0x100000
#define WS_G8 0x180000
#define WS_GK 0x1B8000
#define WS_G9 0x1C0000
#define WS_C 0x1E0000
#define WS_P2 0x200000
#define P2_STRIDE 0x2180
#define WS_P4 0x600000
#define WS_USED 0xB00000
#define WS_STAMP 0x1000000

#define STAGE_B 40960
#define LDS_BYTES 163840
#define L_WO 32768
#define L_MB 161792
#define L_WPHI (L_MB - 8192)
#define L_SD (L_MB + 512)
#define L_CS (L_MB + 1024)
#define L_V L_WO
#define L_KH (L_WO + 65536)
#define L_XB 0
#define L_STASH (L_WO + 1024)
#define L_RED 4096

#define SPIN_CAP 20000u
#define DN0 3
#define DNP 1
#define DN1 8
#define S3 10
#define S3H 4
#define S4 20
#define S7 8
#define W_AUX 2
#define LOG2E 1.4426950408889634f

DI unsigned rfl(unsigned x) { return __builtin_amdgcn_readfirstlane(x); }
DI float bf2f(unsigned u) { return __uint_as_float(u << 16); }
DI float bflo(unsigned u) { return __uint_as_float(u << 16); }
DI float bfhi(unsigned u) { return __uint_as_float(u & 0xffff0000u); }
DI unsigned pk2(float lo, float hi) { f32x2 v = {lo, hi}; return __builtin_bit_cast(unsigned, __builtin_convertvector(v, bf16x2)); }
DI unsigned nsent(unsigned x) { return __builtin_elementwise_min(x, SENT - 1); }
DI unsigned umx(unsigned a, unsigned b) { return __builtin_elementwise_max(a, b); }
DI float rbf(float f) { return bflo(pk2(f, f) & 0xffffu); }
DI bf16x8 mk8(unsigned a, unsigned b, unsigned c, unsigned d) { v4u v = {a, b, c, d}; return __builtin_bit_cast(bf16x8, v); }
DI void dma16(unsigned ldsaddr, unsigned voff, rsrc_t rs, unsigned soff) {
    asm volatile("s_mov_b32 m0, %0\n\ts_nop 0\n\tbuffer_load_dwordx4 %1, %2, %3 offen lds" ::"s"(ldsaddr), "v"(voff), "s"(rs), "s"(soff) : "memory");
}
DI void dma16x4(unsigned ldsaddr, const unsigned* v, rsrc_t rs, unsigned soff) {
    asm volatile("s_mov_b32 m0, %0\n\ts_nop 0\n\tbuffer_load_dwordx4 %1, %5, %6 offen lds\n\tbuffer_load_dwordx4 %2, %5, %6 offen offset:1024 lds\n\t"
                 "buffer_load_dwordx4 %3, %5, %6 offen offset:2048 lds\n\tbuffer_load_dwordx4 %4, %5, %6 offen offset:3072 lds"
                 ::"s"(ldsaddr), "v"(v[0]), "v"(v[1]), "v"(v[2]), "v"(v[3]), "s"(rs), "s"(soff) : "memory");
}
DI void touch(unsigned voff, rsrc_t rs, unsigned soff) { asm volatile("buffer_load_dword v255, %0, %1, %2 offen" ::"v"(voff), "s"(rs), "s"(soff) : "memory", "v255"); }
DI v4u ld16_asm(unsigned voff, rsrc_t rs, unsigned soff) {
    v4u r; asm volatile("buffer_load_dwordx4 %0, %1, %2, %3 offen" : "=v"(r) : "v"(voff), "s"(rs), "s"(soff) : "memory"); return r;
}
#define PIN(x) asm volatile("" : "+v"(x))
DI s16x4 tr_read(unsigned a) { return __builtin_amdgcn_ds_read_tr16_b64_v4i16((LAS s16x4*)(a)); }
DI bf16x8 cat8(s16x4 a, s16x4 b) { return __builtin_shufflevector(a, b, 0, 1, 2, 3, 4, 5, 6, 7); }
DI bf16x8 lds128(unsigned a) { return *(const LAS bf16x8*)(a); }
DI f32x4 ldsf4(unsigned a) { return *(const LAS f32x4*)(a); }
DI float ldsf(unsigned a) { return *(const LAS float*)(a); }
DI unsigned ldsu(unsigned a) { return *(const LAS unsigned*)(a); }
DI void stsf4(unsigned a, f32x4 v) { *(LAS f32x4*)(a) = v; }
DI void stsf(unsigned a, float v) { *(LAS float*)(a) = v; }
DI void stsu(unsigned a, unsigned v) { *(LAS unsigned*)(a) = v; }
template <int C> DI float dppf(float x) { return __uint_as_float(__builtin_amdgcn_update_dpp(0, __float_as_uint(x), C, 0xf, 0xf, true)); }
template <int C> DI unsigned dppu(unsigned x) { return __builtin_amdgcn_update_dpp(0, x, C, 0xf, 0xf, true); }
DI float max2f(float a, float b) { float r; asm("v_max_f32_e32 %0, %1, %2" : "=v"(r) : "v"(a), "v"(b)); return r; }
DI float max3f(float a, float b, float c) { float r; asm("v_max3_f32 %0, %1, %2, %3" : "=v"(r) : "v"(a), "v"(b), "v"(c)); return r; }
DI float row_sum(float x) { x += dppf<0x128>(x); x += dppf<0x124>(x); x += dppf<0x122>(x); x += dppf<0x121>(x); return x; }
DI float xrow_sum(float x) {
    auto a = __builtin_amdgcn_permlane16_swap(__float_as_uint(x), __float_as_uint(x), false, false);
    x = __uint_as_float(a[0]) + __uint_as_float(a[1]);
    auto b = __builtin_amdgcn_permlane32_swap(__float_as_uint(x), __float_as_uint(x), false, false);
    return __uint_as_float(b[0]) + __uint_as_float(b[1]);
}
DI float wave_sum(float x) { return xrow_sum(row_sum(x)); }
DI unsigned wave_maxu(unsigned x) {
    x = umx(x, dppu<0x128>(x)); x = umx(x, dppu<0x124>(x)); x = umx(x, dppu<0x122>(x)); x = umx(x, dppu<0x121>(x));
    auto a = __builtin_amdgcn_permlane16_swap(x, x, false, false);
    x = umx(a[0], a[1]);
    auto b = __builtin_amdgcn_permlane32_swap(x, x, false, false);
    return umx(b[0], b[1]);
}
DI float nmax3f(float a, float b, float c) { float r; asm("v_max3_f32 %0, -%1, -%2, -%3" : "=v"(r) : "v"(a), "v"(b), "v"(c)); return r; }
DI float nmax2f(float a, float b) { float r; asm("v_max_f32_e64 %0, -%1, -%2" : "=v"(r) : "v"(a), "v"(b)); return r; }
DI float mx2(float a, float b) { return __builtin_amdgcn_fmed3f(a, b, __builtin_inff()); }
template <bool MAX> DI float xrow_scatter(float a, float b, float c, float d) {
    const auto s = __builtin_amdgcn_permlane16_swap(__float_as_uint(a), __float_as_uint(b), false, false);
    const auto t = __builtin_amdgcn_permlane16_swap(__float_as_uint(c), __float_as_uint(d), false, false);
    const float u = MAX ? mx2(__uint_as_float(s[0]), __uint_as_float(s[1])) : __uint_as_float(s[0]) + __uint_as_float(s[1]);
    const float v = MAX ? mx2(__uint_as_float(t[0]), __uint_as_float(t[1])) : __uint_as_float(t[0]) + __uint_as_float(t[1]);
    const auto r = __builtin_amdgcn_permlane32_swap(__float_as_uint(u), __float_as_uint(v), false, false);
    return MAX ? mx2(__uint_as_float(r[0]), __uint_as_float(r[1])) : __uint_as_float(r[0]) + __uint_as_float(r[1]);
}
DI void xrow_gather(float w, float (&o)[4]) {
    const auto r = __builtin_amdgcn_permlane32_swap(__float_as_uint(w), __float_as_uint(w), false, false);
    const auto s = __builtin_amdgcn_permlane16_swap(r[0], r[0], false, false);
    const auto t = __builtin_amdgcn_permlane16_swap(r[1], r[1], false, false);
    o[0] = __uint_as_float(s[0]); o[1] = __uint_as_float(s[1]); o[2] = __uint_as_float(t[0]); o[3] = __uint_as_float(t[1]);
}
struct NoHook { DI void operator()(int, float&) const {} DI void operator()(int, float&, float&) const {} };
template <bool MUL, class H = NoHook> DI void softmax2(const f32x4 (&p)[2][2], float rs0, float rs1, bool hi, bf16x8 (&out)[4], H hook = H()) {
    float m[4];
#pragma unroll
    for (int i = 0; i < 2; i++) {
        const f32x4 &p0 = p[i][0], &p1 = p[i][1];
        m[2 * i] = max3f(max3f(p0[0], p0[1], p0[2]), max3f(p0[3], p1[0], p1[1]), max2f(p1[2], p1[3]));
        m[2 * i + 1] = max3f(nmax3f(p0[0], p0[1], p0[2]), nmax3f(p0[3], p1[0], p1[1]), nmax2f(p1[2], p1[3]));
    }
    asm volatile("s_nop 1" : "+v"(m[0]), "+v"(m[1]), "+v"(m[2]), "+v"(m[3]));
    hook(1, m[3]);
    const float rsr = hi ? rs1 : rs0;
    float a[4];
    xrow_gather(xrow_scatter<true>(m[0], m[1], m[2], m[3]) * (rsr * -LOG2E), a);
    f32x2 e[4][4];
    float s[4];
#pragma unroll
    for (int k = 0; k < 4; k++) {
        if (k == 2) hook(2, a[2], s[1]);
        const float sc = ((k >> 1) ? rs1 : rs0) * ((k & 1) ? -LOG2E : LOG2E);
#pragma unroll
        for (int j = 0; j < 4; j++) {
            const f32x4& q = p[k >> 1][j >> 1];
            const f32x2 t = __builtin_elementwise_fma((f32x2){q[2 * (j & 1)], q[2 * (j & 1) + 1]}, (f32x2){sc, sc}, (f32x2){a[k], a[k]});
            e[k][j] = (f32x2){__builtin_amdgcn_exp2f(t[0]), __builtin_amdgcn_exp2f(t[1])};
        }
        const f32x2 h = (e[k][0] + e[k][1]) + (e[k][2] + e[k][3]);
        s[k] = h[0] + h[1];
        if (k == 3) hook(3, s[k]);
    }
    float iv = __builtin_amdgcn_rcpf(xrow_scatter<false>(s[0], s[1], s[2], s[3]));
    if (MUL) iv *= rsr;
    xrow_gather(iv, a);
#pragma unroll
    for (int k = 0; k < 4; k++) {
        if (k == 2) hook(4, a[2]);
        const f32x2 c = {a[k], a[k]};
        const f32x2 x0 = e[k][0] * c, x1 = e[k][1] * c, x2 = e[k][2] * c, x3 = e[k][3] * c;
        out[k] = mk8(pk2(x0[0], x0[1]), pk2(x1[0], x1[1]), pk2(x2[0], x2[1]), pk2(x3[0], x3[1]));
    }
}
DI float dot2(unsigned a, unsigned b, float c) {
    return __builtin_amdgcn_fdot2_f32_bf16(__builtin_bit_cast(bf16x2, a), __builtin_bit_cast(bf16x2, b), c, false);
}
DI void st_gran(char* ws, unsigned off, unsigned tag, unsigned val) {
    __hip_atomic_store((gu64*)(ws + off), ((u64)tag << 32) | val, RLX_AGENT);
}
DI v2u ldg(rsrc_t rs, unsigned voff, unsigned soff) { return __builtin_amdgcn_raw_buffer_load_b64(rs, voff, soff, AUX_SC1); }
#define RARE(c) __builtin_expect(!!(c), 0)
#define FRESH() asm volatile("" ::: "memory")
template <int N> DI void sweep(rsrc_t rs, unsigned voff, unsigned soff, unsigned S, unsigned tag, unsigned (&v)[N]) {
#pragma nounroll
    for (unsigned spins = 0;;) {
        unsigned bad = 0;
        FRESH();
#pragma unroll
        for (int k = 0; k < N; ++k) { const v2u x = ldg(rs, voff, soff + k * S); v[k] = x[0]; bad |= x[1] ^ tag; }
        if (__all(bad == 0)) break;
        if (++spins > SPIN_CAP) break;
        __builtin_amdgcn_s_sleep(1);
    }
}
DI unsigned wmax(unsigned x) { return x; }
DI unsigned wmax(v2u x) { return umx(x[0], x[1]); }
DI unsigned wmax(v4u x) { return umx(umx(x[0], x[1]), umx(x[2], x[3])); }
template <int N, int SL = 0, class T, class F> DI void sweepz(F ld, T (&v)[N]) {
#pragma nounroll
    for (unsigned spins = 0;;) {
        FRESH();
        unsigned m = 0;
#pragma unroll
        for (int k = 0; k < N; ++k) v[k] = ld(k);
#pragma unroll
        for (int k = 0; k < N; ++k) m = umx(m, wmax(v[k]));
        if (__all(m != SENT)) break;
        if (++spins > SPIN_CAP) break;
        if (SL) __builtin_amdgcn_s_sleep(SL);
    }
}
DI unsigned f2s(float x) { return nsent(__float_as_uint(x)); }
#define OOB 0x4000000u
#define LD16(v, s) __builtin_amdgcn_raw_buffer_load_b128(rsWS, (v), (s), AUX_SC1)
#define LD4(v, s) __builtin_amdgcn_raw_buffer_load_b32(rsWS, (v), (s), AUX_SC1)
#define ST16(x, v, s) __builtin_amdgcn_raw_buffer_store_b128((x), rsWS, (v), (s), AUX_SC1)
#define ST4(x, v, s) __builtin_amdgcn_raw_buffer_store_b32((x), rsWS, (v), (s), AUX_SC1)
#define SLEEP(n) __builtin_amdgcn_s_sleep(n)
DI void poll16(rsrc_t rs, unsigned off, unsigned tag, int l) {
#pragma nounroll
    for (unsigned spins = 0; spins < SPIN_CAP; spins++) {
        FRESH();
        const v2u x = ldg(rs, (l & 15) * 8, off);
        if (__all(x[1] == tag && x[0] == (tag ^ 0x5bd1e995u))) break;
    }
    ORDER_BEHIND_POLL();
}
#define STAMPR(i, role)
#define STAMP(i)
#define STAMPW(i, ww)

#define STAMPW2(i, a, b) STAMPW(i, a); STAMPW(i, b)
#define RINIT()
#define RDUMP()
#define ARR_DECL
#define KATTR
#define ARR_MARK(i)
#define ARR_END()
template <class F, int... Is> DI void static_for_impl(F&& f, std::integer_sequence<int, Is...>) { (f(std::integral_constant<int, Is>{}), ...); }
template <int N, class F> DI void static_for(F&& f) { static_for_impl(f, std::make_integer_sequence<int, N>{}); }

DI s16x4 bq(unsigned b0, unsigned b1, int j, int i) {
    const int ni = (j + 2) % 6;
    return tr_read(((ni & 1) ? b1 : b0) + (ni >> 1) * 8192 + (i >> 1) * 4096 + (i & 1) * 512);
}
template <class F> DI void group(f32x4 (&acc)[4][6], const bf16x8 (&A)[2][4], const bf16x8 (&B)[2], int j, F gap) {
    const int ni = (j + 2) % 6;
#pragma unroll
    for (int i = 0; i < 8; i++) {
        const int mi = i >> 1, ks = i & 1;
        if (ni < 4) asm volatile("v_mfma_f32_16x16x32_bf16 %0, %1, %2, %0" : "+v"(acc[mi][ni]) : "v"(B[ks]), "v"(A[ks][mi]));
        else asm volatile("v_mfma_f32_16x16x32_bf16 %0, %1, %2, %0" : "+v"(acc[mi][ni]) : "v"(A[ks][mi]), "v"(B[ks]));
        gap(i);
        SCHED();
    }
}
DI void img_write2(const unsigned (&ia)[4], const bf16x8 (&f)[4]) {
#pragma unroll
    for (int k = 0; k < 4; k++) {
        const v4u u = __builtin_bit_cast(v4u, f[k]);
        *(LAS v2u*)(ia[2 * (k & 1)] + (k >> 1) * 2048) = (v2u){u[0], u[1]};
        *(LAS v2u*)(ia[2 * (k & 1) + 1] + (k >> 1) * 2048) = (v2u){u[2], u[3]};
    }
}
DI float lds_bf(unsigned a) { return bf2f(*(const LAS u16*)(a)); }
#define MFMA_SETTLE2(p) asm volatile("s_nop 7\n\ts_nop 1" : "+v"(p[0][0]), "+v"(p[0][1]), "+v"(p[1][0]), "+v"(p[1][1]))

__global__ KATTR __launch_bounds__(512, 2) void fused_kernel(const u16* __restrict__ X, const u16* __restrict__ WQKV, const u16* __restrict__ WPHI,
                                                       const u16* __restrict__ WO, const u16* __restrict__ WR, const u16* __restrict__ WG,
                                                       const u16* __restrict__ WU, const u16* __restrict__ WD, u16* __restrict__ OUT,
                                                       char* __restrict__ ws) {
    extern __shared__ __attribute__((aligned(16))) char lds[];
    u64 pc0; unsigned clen;
    const int tid = threadIdx.x, l = tid & 63;
    asm volatile("s_getpc_b64 %0\n.Lfk_beg:\n\ts_lshr_b32 %1, %3, 3\n\tv_and_b32 v255, 0x1e0, %2\n\tv_add_lshl_u32 v255, v255, %1, 7\n\ts_mov_b32 %1, .Lfk_end-.Lfk_beg\n\t"
                 "v_min_u32 v255, %1, v255\n\tglobal_load_dword v255, v255, %0" : "=&s"(pc0), "=&s"(clen) : "v"(tid), "s"(blockIdx.x) : "v255", "scc");
    SCHED();
    const unsigned w = rfl(tid >> 6);
    const unsigned bid = blockIdx.x, xcd = (bid & 3) * 2 + ((bid >> 2) & 1), idx = bid >> 3;
    const unsigned rt = (xcd & 1) * 8 + (idx & 7), hp = (xcd >> 1) * 4 + (idx >> 3);
    const unsigned rt2 = (bid & 6) * 2 + (idx & 3), ct = (bid & 1) * 8 + (idx >> 2);
    const unsigned hq = (hp + 4) & 15;
    const unsigned rot = (rt >> 2) * 4;
    const unsigned rot2 = (2 * rt2 + 4 * ct) & 31;
    const unsigned ldsb = (unsigned)(size_t)lds;
    const int g = l >> 4, q4 = (l & 15) >> 2, p4 = l & 3, t16 = l & 15;
    const unsigned kr = l >> 3;
    asm volatile("" ::"s"(X), "s"(WQKV), "s"(WPHI), "s"(WO), "s"(WR), "s"(WG), "s"(WU), "s"(WD), "s"(OUT), "s"(ws));
    unsigned tag;
#define READ_TAG() do { SCHED(); tag = *(const volatile CAS unsigned*)(ws + WS_EPOCH) + 1u; asm volatile("" : "+s"(tag)); } while (0)
    const rsrc_t rsWS = __builtin_amdgcn_make_buffer_rsrc((void*)ws, 0, WS_USED, 0x00020000);
    const rsrc_t rsX = __builtin_amdgcn_make_buffer_rsrc((void*)X, 0, 2048 * 1024 * 2, 0x00020000);
    const rsrc_t rsW = __builtin_amdgcn_make_buffer_rsrc((void*)WQKV, 0, 1024 * 3072 * 2, 0x00020000);
    const unsigned vA = (kr * 1024 + (((l & 7) ^ kr) * 8)) * 2;
    const unsigned vB0 = (kr * 3072 + (((l & 7) ^ (((kr >> 1) & 1) << 1)) * 8)) * 2;
    const unsigned vB1 = (kr * 3072 + (((l & 7) ^ ((((kr >> 1) & 1) | 2) << 1)) * 8)) * 2;
    const unsigned srow1 = rt * 128 + hp * 8 + (w & 3) * 2;
    v4u xr[4];
    if (w < 4) {
#pragma unroll
        for (int i = 0; i < 4; i++) xr[i] = ld16_asm(l * 16, rsX, srow1 * 2048 + i * 1024);
    }
    {
        const unsigned vBw = (w & 1) ? vB1 : vB0;
#pragma unroll
        for (int i = 0; i < 2; i++) dma16(ldsb + (w * 2 + i) * 1024, vA, rsX, (rt * 128 + w * 16 + i * 8) * 2048 + rot * 128);
#pragma unroll
        for (int i = 0; i < 3; i++) dma16(ldsb + 16384 + i * 8192 + w * 1024, vBw, rsW, (rot * 64 + w * 8) * 6144 + (i ? hp : hq) * 128 + i * 2048);
    }
    const f32x4 zero4 = {0.f, 0.f, 0.f, 0.f};
    ARR_DECL;
    const bf16x8 ones = mk8(0x3F803F80u, 0x3F803F80u, 0x3F803F80u, 0x3F803F80u);
    const unsigned lw = w & 3;
    const bool ffn = (ct & 7) != 3;
    const unsigned fb = !ffn ? 255u : (bid & 7) * 28 + (idx < 12 ? idx : idx - 4);
    const unsigned cg = (fb + 5) & 15, kc = fb >> 4;
    const unsigned kq = fb / 56, cg1 = fb % 56, cg1u = (cg1 + 3) % 56;
    v4u wreg[24];
    RINIT();
    STAMPW2(0, 0, 4);

    const rsrc_t rsWO = __builtin_amdgcn_make_buffer_rsrc((void*)WO, 0, 1024 * 1024 * 2, 0x00020000);
    const unsigned vWO = kr * 2048 + (((l & 7) ^ (((kr >> 1) & 3) << 1)) * 16);
    unsigned vwo[4];
    auto wo_so = [&](unsigned b) { return (((2 * b & 30) + (b >> 4) + rot2) & 31) * 65536 + ct * 128; };
    const unsigned fw = (((t16 >> 1) & 3) << 2) | (((t16 >> 3) & 1) << 1) | (t16 & 1);
    unsigned ad;
    unsigned ia[4];
    bf16x8 sdv[4];
    auto pre_addr = [&]() {
#pragma unroll
        for (int k = 0; k < 4; k++) { ia[k] = ldsb + L_XB + lw * 8192 + (w >> 2) * 4096 + t16 * 128 + (((4 * k + g) ^ fw) * 8); PIN(ia[k]); }
        const unsigned rr = 4 * g + q4, fr = (((rr >> 1) & 3) << 2) | (((rr >> 3) & 1) << 1) | (rr & 1);
        ad = ldsb + L_XB + (w & 1) * 16384 + rr * 128 + ((((w >> 1) * 4 + p4) ^ fr) * 8);
        PIN(ad);
    };
    auto sd_reads = [&]() {
        unsigned sa = ldsb + L_SD + g * 8;
        PIN(sa);
#pragma unroll
        for (int s = 0; s < 4; s++) {
            const v2u a = *(const LAS v2u*)(sa + s * 64), b = *(const LAS v2u*)(sa + s * 64 + 32);
            sdv[s] = mk8(a[0], a[1], b[0], b[1]);
        }
    };
    bf16x8 v0[4], v1[4];
    auto v_reads = [&]() {
        unsigned vd = ldsb + L_V + (w & 1) * 8192 + l * 16;
        PIN(vd);
#pragma unroll
        for (int s = 0; s < 4; s++) { v0[s] = lds128(vd + s * 2048); v1[s] = lds128(vd + s * 2048 + 1024); }
    };
    auto kv_tile = [&](auto hook6) {
        const unsigned h2 = w & 1, ft = w >> 1;
        WAITL0();
        ARR_MARK(18);
        BAR();
        SCHED();
        bf16x8 pf[4];
#pragma unroll
        for (int s = 0; s < 4; s++) pf[s] = cat8(tr_read(ad + s * 4096), tr_read(ad + s * 4096 + 2048));
        SCHED();
        hook6();
        SCHED();
        f32x4 k0 = zero4, k1 = zero4, kz = zero4;
#pragma unroll
        for (int s = 0; s < 4; s++) { k0 = MFMA(pf[s], v0[s], k0); k1 = MFMA(pf[s], v1[s], k1); }
#pragma unroll
        for (int s = 0; s < 4; s++) kz = MFMA(pf[s], sdv[s], kz);
        const unsigned pbase = WS_P2 + (hp * 16 + rt) * P2_STRIDE;
        const v4u o0 = {nsent(pk2(k0[0], k0[1])), nsent(pk2(k0[2], k0[3])), nsent(pk2(k1[0], k1[1])), nsent(pk2(k1[2], k1[3]))};
        __builtin_amdgcn_raw_buffer_store_b128(o0, rsWS, l * 16, pbase + h2 * 4096 + ft * 1024, AUX_SC1);
        asm volatile("s_nop 1" :: "v"(o0));
        __builtin_amdgcn_raw_buffer_store_b64((v2u){nsent(pk2(kz[0], kz[1])), nsent(pk2(kz[2], kz[3]))}, rsWS, t16 == 0 ? g * 8 : OOB,
                                              pbase + 8192 + h2 * 128 + ft * 32, AUX_SC1);
        SCHED();
        ARR_MARK(19);
        BAR();
    };

    const bool zjob = lw == 0 && (rt & 7) == 0;
    v4u pp[4];
    auto pp_ld = [&](int k) {
        return LD16(kr * P2_STRIDE + (l & 7) * 16, WS_P2 + (hp * 16 + (k & 1) * 8) * P2_STRIDE +
                    (k < 2 ? (rt >> 2) * 2048 + (lw & 1) * 1024 + (rt & 3) * 256 + (lw >> 1) * 128 : 8192 + (rt >> 3) * 128 + (zjob ? 0u : OOB)));
    };
    auto kv_reduce = [&]() {
        SLEEP(2);
        sweepz<4, 2>(pp_ld, pp);
        STAMPW(25, 0);
        auto red = [&](const v4u* p) {
            float lo = xrow_scatter<false>(bflo(p[0][0]) + bflo(p[1][0]), bflo(p[0][1]) + bflo(p[1][1]), bflo(p[0][2]) + bflo(p[1][2]), bflo(p[0][3]) + bflo(p[1][3]));
            float hi = xrow_scatter<false>(bfhi(p[0][0]) + bfhi(p[1][0]), bfhi(p[0][1]) + bfhi(p[1][1]), bfhi(p[0][2]) + bfhi(p[1][2]), bfhi(p[0][3]) + bfhi(p[1][3]));
            lo += dppf<0x128>(lo); hi += dppf<0x128>(hi);
            return nsent(pk2(lo, hi));
        };
        __builtin_amdgcn_raw_buffer_store_b32(red(pp), rsWS, (l & 8) ? OOB : (l & 7) * 16 + g * 4,
                                              WS_G3 + (hp * 4 + (rt >> 2)) * 2048 + (lw & 1) * 1024 + (rt & 3) * 256 + (lw >> 1) * 128, AUX_SC1);
        if (RARE(zjob))
            __builtin_amdgcn_raw_buffer_store_b32(red(pp + 2), rsWS, (l & 8) ? OOB : ((l >> 2) & 1) * 64 + ((l & 1) * 2 + (g >> 1)) * 16 + ((l >> 1) & 1) * 8 + (g & 1) * 4,
                                                  WS_GZ + (hp * 2 + (rt >> 3)) * 128, AUX_SC1);
    };

    const unsigned half = w >> 2;
    const unsigned R2 = rt2 * 128 + lw * 32 + half * 16 + t16;

    auto dloads = [&](int i0, int i1) {
        const rsrc_t rsD = __builtin_amdgcn_make_buffer_rsrc((void*)WD, 0, 1024 * 3584 * 2, 0x00020000);
#pragma unroll
        for (int i = i0; i < i1; i++)
            wreg[16 + i] = __builtin_amdgcn_raw_buffer_load_b128(rsD, kr * 4096 + (l & 7) * 16, (kc * 256 + lw * 64 + (i >> 1) * 16 + (i & 1)) * 2048 + cg * 128, W_AUX);
        FRESH();
    };
    v4u xres[2], wrv[2];
    v2u pl;
    unsigned pv, nh;
    auto place = [&]() {
        if (RARE(rfl(pl[1]) != tag)) { unsigned c[1]; sweep<1>(rsWS, 0, WS_PR + 1024, 0, tag, c); pl[0] = c[0]; }
        pv = rfl(pl[0]);
        nh = ((bid ^ (pv >> 4)) & 4) * 2048;
        asm volatile("" : "+s"(pv), "+s"(nh));
    };
    auto res_loads = [&]() {
        pl = ldg(rsWS, 0, WS_PR + 1024);
#pragma unroll
        for (int i = 0; i < 2; i++) {
            xres[i] = *(const v4u*)(X + (size_t)R2 * 1024 + ct * 64 + i * 32 + 8 * g);
            wrv[i] = *(const v4u*)(WR + ct * 64 + i * 32 + 8 * g);
        }
        FRESH();
    };

    if (w < 4) {
        const unsigned wm = w & 1, hh = w >> 1;
        f32x4 acc[4][6];
        bf16x8 wf[2], wfq[2];
        float mbt[4], rst[4], csv[2];
        f32x4 mbv[4], csq[2], csk[2];
        unsigned vgc = WS_GC + (l < 32 ? hp : hq) * 256 + (l & 31) * 8;
        PIN(vgc);
        v2u gc;
#pragma unroll
        for (int i = 0; i < 4; i++)
#pragma unroll
            for (int j = 0; j < 6; j++) { acc[i][j] = zero4; PIN(acc[i][j]); }
        {
            const unsigned aoff0 = ldsb + (wm * 64 + t16) * 128 + (((0 + g) ^ (l & 7)) * 16);
            const unsigned aoff1 = ldsb + (wm * 64 + t16) * 128 + (((4 + g) ^ (l & 7)) * 16);
            const unsigned sk = ((q4 >> 1) & 1) | ((g & 1) << 1);
            const unsigned boffA = ldsb + 16384 + (8 * g + q4) * 128 + (((2 * hh) ^ sk) * 32) + p4 * 8;
            const unsigned boffB = ldsb + 16384 + (8 * g + q4) * 128 + (((2 * hh + 1) ^ sk) * 32) + p4 * 8;
            bf16x8 A[2][4], B[3][2];
            s16x4 lo;
            unsigned a0[3], a1[3], bA[3], bB[3];
#pragma unroll
            for (int i = 0; i < 3; i++) {
                a0[i] = aoff0 + i * STAGE_B; a1[i] = aoff1 + i * STAGE_B; bA[i] = boffA + i * STAGE_B; bB[i] = boffB + i * STAGE_B;
                PIN(a0[i]); PIN(a1[i]); PIN(bA[i]); PIN(bB[i]);
            }
            unsigned aT = ldsb + L_MB + (wm * 64 + t16) * 4, aS = ldsb + L_SD + (wm * 64 + t16) * 2, aM = ldsb + L_MB + wm * 256 + g * 16;
            unsigned aC = ldsb + L_CS + hh * 128 + g * 16, aV = ldsb + L_CS + (128 + hh * 32 + t16) * 4;
            unsigned aW = ldsb + L_WPHI + hh * 2048 + (4 * g + q4) * 64 + p4 * 8, aK = ldsb + L_KH + w * 2048 + l * 16;
            PIN(aT); PIN(aS); PIN(aM); PIN(aC); PIN(aV); PIN(aW); PIN(aK);
            pre_addr();
            READ_TAG();
            waitv<5>();
            PIN(xr[0]); PIN(xr[1]); PIN(xr[2]); PIN(xr[3]);
            {
                float s[2], q[2], x0[2];
#pragma unroll
                for (int rr = 0; rr < 2; rr++) {
                    x0[rr] = bflo(rfl(xr[2 * rr][0]));
                    s[rr] = 0.f; q[rr] = 0.f;
#pragma unroll
                    for (int i = 0; i < 4; i++) {
                        const float a = bflo(xr[2 * rr][i]) - x0[rr], b = bfhi(xr[2 * rr][i]) - x0[rr];
                        const float c = bflo(xr[2 * rr + 1][i]) - x0[rr], d = bfhi(xr[2 * rr + 1][i]) - x0[rr];
                        s[rr] += (a + b) + (c + d);
                        q[rr] += (a * a + b * b) + (c * c + d * d);
                    }
                }
                const float sq = row_sum(xrow_scatter<false>(s[0], s[1], q[0], q[1]));
#define RLF(x, n) __uint_as_float(__builtin_amdgcn_readlane(__float_as_uint(x), (n)))
                const float r0 = RLF(sq, 0), r1 = RLF(sq, 16), r2 = RLF(sq, 32), r3 = RLF(sq, 48);
                const float sm = (l ? r1 : r0) * (1.0f / 1024.0f), qm = (l ? r3 : r2) * (1.0f / 1024.0f);
                const float var = fmaxf(qm - sm * sm, 0.f);
                if (l < 2) st_gran(ws, WS_G0 + (srow1 + l) * 8, tag, pk2((l ? x0[1] : x0[0]) + sm, sqrtf(rbf(rbf(var) + 1e-5f))));
            }
            waitv<1>();
            WAITL0();
            ARR_MARK(0);
            BAR();
            SCHED();
            STAMPW(1, 0);
            auto rdB = [&](int u, int j, int i) {
                if (i & 1) B[j % 3][i >> 1] = cat8(lo, bq(bA[u], bB[u], j, i)); else lo = bq(bA[u], bB[u], j, i);
            };
            auto rdA = [&](int u, int i) { A[i & 1][i >> 1] = lds128(((i & 1) ? a1[u] : a0[u]) + (i >> 1) * 2048); };
            auto head = [&](int u) {
                group(acc, A, B[0], 0, [&](int i) { rdB(u, 1 + (i >> 2), i & 3); });
                group(acc, A, B[1], 1, [&](int i) { if (i < 4) rdB(u, 3, i); });
                group(acc, A, B[2], 2, [&](int i) { if (i < 4) rdB(u, 4, i); });
                group(acc, A, B[0], 3, [&](int i) { if (i < 4) rdB(u, 5, i); });
                WL0();
            };
#pragma unroll
            for (int i = 0; i < 4; i++) rdB(0, 0, i);
#pragma unroll
            for (int i = 0; i < 8; i++) rdA(0, i);
            SCHED();
#pragma nounroll
            for (unsigned t = 0; t < 5; t++) {
#pragma unroll
                for (int u = 0; u < 3; u++) {
                    head(u);
                    ARR_MARK(1 + t * 3 + u);
                    BAR();
                    SCHED();
                    group(acc, A, B[1], 4, [&](int i) { if (i < 4) rdB((u + 1) % 3, 0, i); });
                    group(acc, A, B[2], 5, [&](int i) { rdA((u + 1) % 3, i); });
                }
            }
            unsigned kw[4][4];
            f32x2 kx;
            auto kpiece = [&](int n) {
                if (n > 0) { kw[((n - 1 >> 2) + 2) & 3][(n - 1) & 3] = pk2(kx[0], kx[1]); PIN(kw[((n - 1 >> 2) + 2) & 3][(n - 1) & 3]); }
                if (n < 16) {
                    const int mi = ((n >> 2) + 2) & 3, j = n & 3;
                    const f32x4 &c = acc[mi][2 + (j >> 1)], &s_ = csk[j >> 1];
                    float x0 = __builtin_fmaf(mbt[mi], s_[2 * (j & 1)], c[2 * (j & 1)]); PIN(x0);
                    float x1 = __builtin_fmaf(mbt[mi], s_[2 * (j & 1) + 1], c[2 * (j & 1) + 1]); PIN(x1);
                    kx = (f32x2){x0, x1};
                }
                if (n == 5 || n == 9) *(LAS v4u*)(aK + (n == 9) * 1024) = (v4u){kw[2 + (n == 9)][0], kw[2 + (n == 9)][1], kw[2 + (n == 9)][2], kw[2 + (n == 9)][3]};
            };
            group(acc, A, B[0], 0, [&](int i) { rdB(0, 1 + (i >> 2), i & 3); });
            group(acc, A, B[1], 1, [&](int i) {
                if (i < 4) { rdB(0, 3, i); mbt[(i + 2) & 3] = ldsf(aT + ((i + 2) & 3) * 64); }
                else if (i < 6) csk[i - 4] = ldsf4(aC + 256 + (i - 4) * 64);
                else wf[i - 6] = cat8(tr_read(aW + (i - 6) * 32), tr_read(aW + (i - 6) * 32 + 1024));
            });
            group(acc, A, B[2], 2, [&](int i) { if (i < 4) rdB(0, 4, i); else mbv[i - 4] = ldsf4(aM + (i - 4) * 64); kpiece(i); });
            group(acc, A, B[0], 3, [&](int i) { if (i < 4) rdB(0, 5, i); if (i == 2 || i == 3) csv[i - 2] = ldsf(aV + (i - 2) * 64); kpiece(8 + i); });
            WL0();
            ARR_MARK(16);
            BAR();
            SCHED();
            kpiece(16);
            f32x4 p[2][2];
#pragma unroll
            for (int i = 0; i < 4; i++) p[i >> 1][i & 1] = MFMA(wf[i & 1], mk8(kw[i >> 1][0], kw[i >> 1][1], kw[i >> 1][2], kw[i >> 1][3]), zero4);
            SCHED();
            STAMPW(2, 0);
#pragma unroll
            for (int mi = 0; mi < 4; mi++) rst[mi] = lds_bf(aS + mi * 32);
            gc = ldg(rsWS, vgc, 0);
#pragma unroll
            for (int a = 0; a < 2; a++)
#pragma unroll
                for (int dt = 0; dt < 2; dt++) {
                    const f32x4 x0 = acc[2 * a][4 + dt] + mbv[2 * a] * csv[dt], x1 = acc[2 * a + 1][4 + dt] + mbv[2 * a + 1] * csv[dt];
                    *(LAS v4u*)(aK + (L_V - L_KH) + w * 2048 + (a * 2 + dt) * 1024) = (v4u){pk2(x0[0], x0[1]), pk2(x0[2], x0[3]), pk2(x1[0], x1[1]), pk2(x1[2], x1[3])};
                }
            SCHED();
            rst[0] = __builtin_amdgcn_rcpf(rst[0]); rst[1] = __builtin_amdgcn_rcpf(rst[1]);
            bf16x8 f[4];
            struct HookC {
                decltype(v_reads)& vr; decltype(sd_reads)& sr;
                DI void operator()(int k, float& x) const { if (k == 1) { PIN(x); WAITL0(); ARR_MARK(17); BAR(); PIN(x); } if (k == 3) { PIN(x); vr(); sr(); PIN(x); } }
                DI void operator()(int, float&, float&) const {}
            };
            softmax2<true>(p, rst[0], rst[1], g >= 2, f, HookC{v_reads, sd_reads});
            img_write2(ia, f);
#pragma unroll
            for (int ft = 0; ft < 2; ft++) wfq[ft] = cat8(tr_read(aW + ft * 32 + 4096), tr_read(aW + ft * 32 + 5120));
            csq[0] = ldsf4(aC); csq[1] = ldsf4(aC + 64);
            STAMPW(3, 0);
            kv_tile([]() {});
            STAMPW(4, 0);
            SCHED();
            group(acc, A, B[1], 4, [&](int i) { if (i == 2 || i == 3) { rst[i] = __builtin_amdgcn_rcpf(rst[i]); PIN(rst[i]); } });
            group(acc, A, B[2], 5, [&](int) {});
            asm volatile("s_nop 7\n\ts_nop 1");
            SCHED();
        }
        bf16x8 phq[2][4];
        v4u kg[6];
        auto kg_ld = [&](int k) { return k < 4 ? LD16(l * 16, WS_G3 + (hq * 2 + hh) * 4096 + k * 1024) : LD16(g * 16, WS_GZ + (hq * 2 + hh) * 128 + (k - 4) * 64); };
        {
            f32x4 pq[2][2][2];
#pragma unroll
            for (int mi = 0; mi < 4; mi++) {
                const f32x4 x0 = acc[mi][0] + mbt[mi] * csq[0], x1 = acc[mi][1] + mbt[mi] * csq[1];
                const bf16x8 qf = mk8(pk2(x0[0], x0[1]), pk2(x0[2], x0[3]), pk2(x1[0], x1[1]), pk2(x1[2], x1[3]));
                pq[mi >> 1][mi & 1][0] = MFMA(wfq[0], qf, zero4); pq[mi >> 1][mi & 1][1] = MFMA(wfq[1], qf, zero4);
            }
            MFMA_SETTLE2(pq[0]); MFMA_SETTLE2(pq[1]);
            if (RARE(!__all(gc[1] == tag))) { unsigned c[1]; sweep<1>(rsWS, vgc, 0, 0, tag, c); }
            ORDER_BEHIND_POLL();
            softmax2<false>(pq[0], rst[0], rst[1], g >= 2, phq[0]);
#pragma unroll
            for (int i = 0; i < 4; i++) PIN(phq[0][i]);
            SCHED();
            kv_reduce();
            SCHED();
            res_loads();
            if (DNP == 3) { stsu(ldsb + w * 4, 0); WAITL0(); }
            SCHED();
            BAR();
            SCHED();
            softmax2<false>(pq[1], rst[2], rst[3], g >= 2, phq[1]);
#pragma unroll
            for (int i = 0; i < 4; i++) { PIN(phq[0][i]); PIN(phq[1][i]); }
        }
        STAMPW(5, 0);
        STAMPW(26, 0);
        v2u fv = {tag ^ 0x5bd1e995u, tag};
        place();
        unsigned sF = WS_F4 + ((rt * 4 + w) * 16 + hq) * 8 + nh, vF = l == 0 ? 0 : OOB;
        {
            unsigned vO = l * 16, sO = WS_P4 + ((rt & 7) * 8 + wm * 4) * 65536 + (hq >> 2) * 16384 + ((hq & 3) * 2 + hh) * 1024 + (nh ^ (~rt & 4) * 2048);
            asm volatile("" : "+s"(sO), "+s"(sF));
            PIN(fv); PIN(vF);
#define S2N 8
            SLEEP(S2N);
            sweepz<6, 6>(kg_ld, kg);
            STAMPW(6, 0);
            bf16x8 kvA[2][2], zA[2];
            v4u ov[4];
#pragma unroll
            for (int s = 0; s < 2; s++) {
#pragma unroll
                for (int dt = 0; dt < 2; dt++) kvA[s][dt] = mk8(kg[2 * s][2 * dt], kg[2 * s][2 * dt + 1], kg[2 * s + 1][2 * dt], kg[2 * s + 1][2 * dt + 1]);
                zA[s] = __builtin_bit_cast(bf16x8, kg[4 + s]);
            }
#define PH(mi, s) phq[(mi) >> 1][2 * ((mi) & 1) + (s)]
            f32x4 dn[4], n0[4], n1[4];
            float id[4];
#pragma unroll
            for (int mi = 0; mi < 4; mi++) dn[mi] = MFMA(zA[0], PH(mi, 0), zero4);
#pragma unroll
            for (int mi = 0; mi < 4; mi++) dn[mi] = MFMA(zA[1], PH(mi, 1), dn[mi]);
            SCHED();
#pragma unroll
            for (int mi = 0; mi <= 4; mi++) {
                if (mi < 4) {
                    n0[mi] = MFMA(kvA[0][0], PH(mi, 0), zero4); n1[mi] = MFMA(kvA[0][1], PH(mi, 0), zero4);
                    n0[mi] = MFMA(kvA[1][0], PH(mi, 1), n0[mi]); n1[mi] = MFMA(kvA[1][1], PH(mi, 1), n1[mi]);
                    id[mi] = __builtin_amdgcn_rcpf(rbf(rbf(dn[mi][0]) + 1e-6f));
                }
                if (mi > 0) {
                    const f32x4 a = n0[mi - 1] * id[mi - 1], b = n1[mi - 1] * id[mi - 1];
                    ov[mi - 1] = (v4u){pk2(a[0], a[1]), pk2(a[2], a[3]), pk2(b[0], b[1]), pk2(b[2], b[3])};
                    ST16(ov[mi - 1], vO + (mi - 1) * 65536, sO);
                }
                if (mi < 4) { SGB(0x8, 1); SGB(0x2, 6); SGB(0x8, 1); SGB(0x2, 3); SGB(0x8, 1); SGB(0x2, 3); SGB(0x8, 1); SGB(0x2, 4); }
                SCHED();
            }
#pragma unroll
            for (int mi = 0; mi < 4; mi++) PIN(ov[mi]);
        }
        if (DNP == 3) { stsu(ldsb + w * 4, 1); SCHED(); }
        waitv<0>();
        __builtin_amdgcn_raw_buffer_store_b64(fv, rsWS, vF, sF, AUX_SC1);
        STAMPW(7, 0);
    } else {
        const unsigned pw = lw & 1;
        const rsrc_t rsG = __builtin_amdgcn_make_buffer_rsrc((void*)WG, 0, 1024 * 3584 * 2, 0x00020000);
        const rsrc_t rsU = __builtin_amdgcn_make_buffer_rsrc((void*)WU, 0, 1024 * 3584 * 2, 0x00020000);
        unsigned vwl = kr * 57344 + (l & 7) * 16;
        unsigned sg = (kq * 256 + lw * 64) * 7168 + cg1 * 128, su = (kq * 256 + lw * 64) * 7168 + cg1u * 128;
        auto wloads = [&](int n0, int n1) {
#pragma unroll
            for (int n = n0; n < n1; n++)
                asm volatile("buffer_load_dwordx4 %0, %1, %2, %3 offen nt" : "=v"(wreg[n]) : "v"(vwl), "s"(n < 8 ? rsG : rsU), "s"((n < 8 ? sg : su) + (n & 7) * 7168));
        };
        auto dl_asm = [&]() {
            const rsrc_t rsD = __builtin_amdgcn_make_buffer_rsrc((void*)WD, 0, 1024 * 3584 * 2, 0x00020000);
#pragma unroll
            for (int i = 0; i < DN0; i++)
                asm volatile("buffer_load_dwordx4 %0, %1, %2, %3 offen nt" : "=v"(wreg[16 + i]) : "v"(kr * 4096 + (l & 7) * 16), "s"(rsD),
                             "s"((kc * 256 + lw * 64 + (i >> 1) * 16 + (i & 1)) * 2048 + cg * 128));
        };
        auto repack = [&](unsigned gI) {
            const unsigned s2 = (2 * g + (q4 >> 1)) & 3;
            bf16x8 f[4];
#pragma unroll
            for (int nt = 0; nt < 4; nt++) {
                const unsigned a = ldsb + L_WO + gI * 4096 + (4 * g + q4) * 128 + ((((nt >> 1) * 2 + (p4 >> 1)) ^ s2) * 32) + (p4 & 1) * 16 + (nt & 1) * 8;
                f[nt] = cat8(tr_read(a), tr_read(a + 2048));
            }
            WAITL0();
#pragma unroll
            for (int nt = 0; nt < 4; nt++) *(LAS bf16x8*)(ldsb + L_WO + gI * 4096 + nt * 1024 + l * 16) = f[nt];
        };
        unsigned aK, aW, aS;
        auto k_pre = [&]() {
            pre_addr();
            aK = ldsb + L_KH + lw * 2048 + l * 16; aW = ldsb + L_WPHI + (lw >> 1) * 2048 + (4 * g + q4) * 64 + p4 * 8; aS = ldsb + L_SD + (pw * 64 + 32 + t16) * 2;
            PIN(aK); PIN(aW); PIN(aS);
        };
        auto kfeat = [&](auto hook) {
            bf16x8 wf[2];
            float rst[2];
#pragma unroll
            for (int ft = 0; ft < 2; ft++) {
                wf[ft] = cat8(tr_read(aW + ft * 32), tr_read(aW + ft * 32 + 1024));
                rst[ft] = __builtin_amdgcn_rcpf(lds_bf(aS + ft * 32));
            }
            sd_reads();
            PIN(wf[0]); PIN(wf[1]); PIN(rst[0]); PIN(rst[1]);
            SCHED();
            ARR_MARK(16);
            BAR();
            SCHED();
            const bf16x8 kf0 = lds128(aK), kf1 = lds128(aK + 1024);
            SCHED();
            hook(-1, rst[0]);
            hook(0, rst[0]);
            SCHED();
            f32x4 p[2][2];
            p[0][0] = MFMA(wf[0], kf0, zero4); p[0][1] = MFMA(wf[1], kf0, zero4);
            p[1][0] = MFMA(wf[0], kf1, zero4); p[1][1] = MFMA(wf[1], kf1, zero4);
            MFMA_SETTLE2(p);
            bf16x8 f[4];
            softmax2<true>(p, rst[0], rst[1], g >= 2, f, hook);
            img_write2(ia, f);
            SCHED();
            hook(5, rst[0]);
            SCHED();
            STAMPW2(3, 4, 7);
            kv_tile([&]() { hook(6, rst[0]); });
            STAMPW2(4, 4, 7);
        };
        auto wblk = [&](int k) { return k == 0 ? 22 + lw : k == 1 ? (lw < 3 ? 26 + lw : 4) : k < 6 ? 4 * k - 3 + lw : k == 6 ? lw : (lw < 3 ? 29 + lw : 21); };
        auto wo_init = [&]() {
#pragma unroll
            for (int i = 0; i < 4; i++) { vwo[i] = vWO + i * (16384 - 1024); PIN(vwo[i]); }
        };
        unsigned hs[8];
        auto wl_pre = [&]() { asm volatile("" : "+s"(sg), "+s"(su)); PIN(vwl); };
        auto wo_pre = [&]() {
            wo_init();
#pragma unroll
            for (int k = 0; k < 8; k++) { hs[k] = wo_so(wblk(k)); asm volatile("" : "+s"(hs[k])); }
        };
        auto wo_rng = [&](int k0, int k1) {
#pragma unroll
            for (int k = k0; k < k1; k++) dma16x4(ldsb + L_WO + wblk(k) * 4096, vwo, rsWO, hs[k]);
        };
        auto pf = [&](int i) {
            if (i == 0) { wloads(0, 6); SCHED(); if (lw < 2) wo_pre(); SCHED(); }
            if (i < 3 && !ffn) wo_rng(i, i + 1);
            if (i == 1 || i == 2) wloads(6 * i, i == 1 ? 12 : 16);
#define DHK 4
            if (DNP == 2 && i == DHK) dl_asm();
        };
        struct Hook {
            decltype(pf)& f; decltype(v_reads)& vr;
            DI void operator()(int k, float& x) const { PIN(x); if (k == 1) { ARR_MARK(17); BAR(); } if (k == 3) vr(); f(3 + k); PIN(x); }
            DI void operator()(int k, float& x, float& y) const { PIN(y); f(3 + k); PIN(x); }
        };
        auto tail16 = [&]() {
            kfeat(Hook{pf, v_reads});
            if (ffn) wo_rng(0, 3);
            wo_rng(3, 4);
            static_for<4>([&](auto kc_) {
                constexpr int k = decltype(kc_)::value;
                waitv<4 * (3 - k)>();
                repack(wblk(k));
            });
            BAR();
            res_loads();
            wo_rng(4, 8);
            static_for<4>([&](auto kc_) {
                constexpr int k = decltype(kc_)::value + 4;
                waitv<4 * (7 - k)>();
                repack(wblk(k));
            });
            waitv<0>();
#pragma unroll
            for (int n = 0; n < 16 + (DNP == 2 ? DN0 : 0); n++) PIN(wreg[n]);
            PIN(xres[0]); PIN(xres[1]); PIN(wrv[0]); PIN(wrv[1]); PIN(pl);
        };
        auto vT23 = [&]() {
            const unsigned n = (idx * 2 + pw) * 64 + l, r = n / 12, c = n % 12;
            return idx < 24 ? ((r & 127) + (r >> 7) * 256 + 128) * 6144 + (c >> 2) * 2048 + (((xcd >> 1) * 4 + (c >> 2 ? 0 : 4) + (c & 3)) & 15) * 128 : OOB;
        };
        if (lw < 2) {
            const unsigned sA = (rt * 128 + lw * 64) * 2048, sB = lw * 32 * 6144 + hp * 128, sBq = lw * 32 * 6144 + hq * 128;
            unsigned buf = 1;
            unsigned va[4], vb[4];
#pragma unroll
            for (int i = 0; i < 4; i++) { va[i] = vA + i * (16384 - 1024); vb[i] = ((i & 1) ? vB1 : vB0) + i * (49152 - 1024); PIN(va[i]); PIN(vb[i]); }
            auto stage = [&](unsigned s, unsigned bf, int i0, int i1) {
                const unsigned kt = (s + rot) & 15;
                const unsigned lb = ldsb + bf * STAGE_B;
                const unsigned sa = sA + kt * 128, sb = kt * 393216;
                if (i0 <= 0 && 0 < i1) dma16x4(lb + lw * 8192, va, rsX, sa);
                if (i0 <= 1 && 1 < i1) dma16x4(lb + lw * 8192 + 4096, va, rsX, sa + 65536);
                if (i0 <= 2 && 2 < i1) dma16x4(lb + 16384 + 8192 + lw * 4096, vb, rsW, sb + sB + 2048);
                if (i0 <= 3 && 3 < i1) dma16x4(lb + 16384 + 16384 + lw * 4096, vb, rsW, sb + sB + 4096);
                if (i0 <= 4 && 4 < i1) dma16x4(lb + 16384 + lw * 4096, vb, rsW, sb + sBq);
            };
            stage(1, 1, 0, 4);
            SCHED();
            unsigned vt = vT23();
            PIN(vt);
            wl_pre(); k_pre();
            waitv<16>(); ARR_MARK(0); BAR();
            stage(1, 1, 4, 5);
            touch(vt, rsW, (xcd & 1) * 512 * 6144);
            waitv<1>(); ARR_MARK(1); BAR();
            stage(2, 2, 0, 5); stage(3, 0, 0, 3);
            waitv<12>(); ARR_MARK(2); BAR();
            stage(3, 0, 3, 5); stage(4, 1, 0, 4);
            waitv<16>(); ARR_MARK(3); BAR();
            stage(4, 1, 4, 5);
            buf = 2;
#pragma nounroll
            for (unsigned s = 5; s < 16; s++) {
                if (s > 5) { waitv<20>(); ARR_MARK(s - 2); BAR(); }
                stage(s, buf, 0, 5);
                buf = buf == 2 ? 0 : buf + 1;
            }
            waitv<20>(); ARR_MARK(14); BAR();
            pf(0);
            if (ffn) waitv<6>(); else waitv<10>();
            ARR_MARK(15); BAR();
            pf(1);
            tail16();
            READ_TAG();
        } else {
            SCHED();
            unsigned v3;
            const unsigned h3 = (xcd >> 1) * 4 + 3, kv3 = !(xcd & 2), wv = idx * 2 + pw;
            auto line3 = [&](unsigned per, unsigned rows, auto rowf) {
                const unsigned n = wv * (kv3 ? per : (per + 2) / 3) + l, r3 = kv3 ? n / 3 : n, c3 = kv3 ? n % 3 : 0;
                return (idx < 24 && l < (kv3 ? per : (per + 2) / 3) && r3 < rows) ? rowf(r3) * 6144 + c3 * 2048 + ((c3 ? h3 : h3 + 4) & 15) * 128 : OOB;
            };
            v3 = line3(32, 512, [](unsigned r) { return r; });
            touch(line3(16, 256, [](unsigned r) { return (r & 127) + (r >> 7) * 256; }), rsW, (xcd & 1) * 512 * 6144);
            {
                const rsrc_t rsP = __builtin_amdgcn_make_buffer_rsrc((void*)WPHI, 0, 32 * 1024 * 2, 0x00020000);
#pragma unroll
                for (int i = 0; i < 4; i++) dma16(ldsb + L_WPHI + pw * 4096 + i * 1024, l * 16, rsP, (pw ? hq : hp) * 4096 + i * 1024);
            }
            v4u sv = {SENT, SENT, SENT, SENT};
            unsigned vc4 = l < 16 ? WS_G3 + (hp * 4 + (rt >> 2)) * 2048 + pw * 1024 + (rt & 3) * 256 + l * 16
                         : l < 24 ? WS_P2 + (hp * 16 + rt) * P2_STRIDE + 8192 + pw * 128 + (l - 16) * 16
                         : (l < 32 && pw == 0 && (rt & 7) == 0) ? WS_GZ + (hp * 2 + (rt >> 3)) * 128 + (l - 24) * 16 : OOB;
            auto clears = [&](int i0, int i1) {
#pragma unroll
                for (int i = i0; i < i1; i++) {
                    if (i < 4) ST16(sv, l * 16, WS_P2 + (hp * 16 + rt) * P2_STRIDE + pw * 4096 + i * 1024);
                    if (i == 4) ST16(sv, vc4, 0);
                    if (i > 4) ST16(sv, l * 16, WS_TL + (bid * 4 + pw * 2 + i - 5) * 1024);
                }
                FRESH();
            };
            PIN(vc4); PIN(sv);
            unsigned ca = ldsb + 16384 + pw * 8192 + (8 * g + q4) * 128 + (((rt & 3) ^ (((q4 >> 1) & 1) | ((g & 1) << 1))) * 32) + p4 * 8;
            PIN(ca);
            bf16x8 cf[4];
            f32x4 cd[2] = {zero4, zero4};
            auto cs_read = [&](int st) {
#pragma unroll
                for (int i = 0; i < 4; i++) cf[i] = cat8(tr_read(ca + (st % 3) * STAGE_B + i * 4096), tr_read(ca + (st % 3) * STAGE_B + i * 4096 + 512));
                SCHED();
            };
            READ_TAG();
            waitv<5>();
            ARR_MARK(0);
            BAR();
            SCHED();
            cs_read(0);
            FRESH();
            const rsrc_t rsC = __builtin_amdgcn_make_buffer_rsrc((void*)pc0, 0, clen, 0x00020000);
            auto code_pf = [&]() { touch(l < 6 ? l * 128 : OOB, rsC, (idx * 2 + pw) * 768); };
            v4u gl[4];
            float s0 = 0.f, s1 = 0.f;
            unsigned bad;
            v2u gs;
            unsigned lat[2] = {~0u, ~0u};
            unsigned vgl = (l < 48 ? l : 0) * 16;
            unsigned aw0 = ldsb + L_CS + pw * 384 + (vgl >> 1), aw1 = ldsb + L_MB + (pw * 64 + l) * 4, aw2 = ldsb + L_SD + (pw * 64 + l) * 2;
            static_for<15>([&](auto tc) {
                constexpr int t = decltype(tc)::value;
                WAITL0();
                ARR_MARK(t + 1);
                BAR();
                SCHED();
                if constexpr (t < 4) {
                    __builtin_amdgcn_s_setprio(2);
#pragma unroll
                    for (int i = 0; i < 4; i++) cd[i & 1] = MFMA(ones, cf[(i & 1) * 2 + (i >> 1)], cd[i & 1]);
                    SCHED();
                    __builtin_amdgcn_s_setprio(0);
                    if constexpr (t < 3) cs_read(t + 1);
                    else __builtin_amdgcn_raw_buffer_store_b64((v2u){__float_as_uint(g ? cd[1][0] : cd[0][0]), tag}, rsWS, (g == 1 || g + pw == 0) ? g * 512 + t16 * 8 : OOB,
                                                               WS_G1 + ((hp * 4 + (rt >> 2)) * 192 + pw * 64 + (rt & 3) * 16) * 8, AUX_SC1);
                }
                if constexpr (t == 0) touch(v3, rsW, (~xcd & 1) * 512 * 6144);
                if constexpr (t == 12) code_pf();
                if constexpr (t == 8) {
                    waitv<0>();
                    __builtin_amdgcn_raw_buffer_store_b64((v2u){0u, tag}, rsWS, l == 0 ? 0 : OOB, WS_GC + ((hp * 2 + pw) * 16 + rt) * 8, AUX_SC1);
                }
                if constexpr (t == 8 || t == 9) {
#pragma unroll
                    for (int r = (t - 8) * 2; r < (t - 8) * 2 + 2; r++)
                        asm volatile("buffer_load_dwordx4 %0, %1, %2, %3 offen sc1" : "=v"(gl[r]) : "v"(vgl), "s"(rsWS), "s"(WS_G1 + ((hp * 4 + r) * 192 + pw * 96) * 8) : "memory");
                }
                if constexpr (t == 6) {
                    asm volatile("buffer_load_dwordx2 %0, %1, %2, %3 offen sc1" : "=v"(gs) : "v"(l * 8), "s"(rsWS), "s"(WS_G0 + (rt * 128 + pw * 64) * 8) : "memory");
                }
                if constexpr (t == 11) {
                    waitv<0>();
                    PIN(gs);
                    bad = gs[1] ^ tag;
#pragma unroll
                    for (int r = 0; r < 4; r++) {
                        PIN(gl[r]);
                        bad |= (gl[r][1] ^ tag) | (gl[r][3] ^ tag);
                        s0 += __uint_as_float(gl[r][0]); s1 += __uint_as_float(gl[r][2]);
                    }
                    PIN(s0); PIN(s1); PIN(bad);
                }
                if constexpr (t == 12) {
                    if (RARE(!__all(bad == 0))) {
                        s0 = 0.f; s1 = 0.f;
#pragma nounroll
                        for (unsigned r = 0; r < 4; r++) {
                            unsigned a[2];
                            sweep<2>(rsWS, vgl, WS_G1 + ((hp * 4 + r) * 192 + pw * 96) * 8, 8, tag, a);
                            s0 += __uint_as_float(a[0]); s1 += __uint_as_float(a[1]);
                        }
                        unsigned c[1];
                        sweep<1>(rsWS, l * 8, WS_G0 + (rt * 128 + pw * 64) * 8, 0, tag, c);
                        gs[0] = c[0];
                    }
                    *(LAS f32x2*)(aw0) = (f32x2){-s0, -s1};
                    stsf(aw1, bflo(gs[0]));
                    *(LAS u16*)(aw2) = (u16)(gs[0] >> 16);
                    SCHED();
                }
#define PT0 10
                if constexpr (t == PT0 - 2) { if (RARE(bid + pw == 0)) ST16(sv, (l >> 5) * 8192 + (l & 31) * 16, WS_PR); }
                if constexpr (t >= PT0 && t < 12) {
                    if (RARE(bid + pw == 0)) {
                        u64 c0, c1; unsigned x;
                        asm volatile("s_memtime %0\n\ts_load_dword %2, %3, %4\n\ts_waitcnt lgkmcnt(0)\n\ts_memtime %1\n\ts_waitcnt lgkmcnt(0)"
                                     : "=&s"(c0), "=&s"(c1), "=&s"(x) : "s"(ws), "i"(WS_PR + (t & 1) * 8192 + ((t - PT0) >> 1) * 128) : "memory");
                        lat[t & 1] = __builtin_elementwise_min(lat[t & 1], (unsigned)c1 - (unsigned)c0);
                        if constexpr (t == 11) {
                            asm volatile("s_getreg_b32 %0, hwreg(HW_REG_XCC_ID, 0, 3)" : "=s"(x));
                            __builtin_amdgcn_raw_buffer_store_b64((v2u){(lat[1] < lat[0] ? 0x44 : 0) ^ (x & 4), tag}, rsWS, l == 0 ? 0 : OOB, WS_PR + 1024, AUX_SC1);
                        }
                    }
                }
                if constexpr (t == 5) k_pre();
                if constexpr (t == 6) { wl_pre(); wo_pre(); }
                if constexpr (t == 7) { PIN(vgl); PIN(aw0); PIN(aw1); PIN(aw2); }
                clears(t < 7 ? t : 7, t < 7 ? t + 1 : 7);
                if constexpr (t == 13) pf(0);
                if constexpr (t == 14) pf(1);
            });
            tail16();
        }
        place();
        WAITL0();
        STAMPW2(7, 4, 7);
    }
    const unsigned sFl = WS_F4 + (rt2 * 4 + (lw >> 1) + 2 * half) * 128 + nh;
    if (DNP == 0 && half) { SCHED(); dloads(0, DN0); SCHED(); }
    if (DNP == 3 && half) {
#pragma nounroll
        for (unsigned spins = 0; spins < 16 * SPIN_CAP; spins++) {
            const v4u f = *(const volatile LAS v4u*)ldsb;
            if (rfl(f[0] & f[1] & f[2] & f[3])) break;
            SLEEP(1);
        }
        SCHED(); dloads(0, DN0); SCHED();
    }
    ARR_MARK(20);
    BAR();
    ARR_END();
    if (DNP == 1 && half) { SCHED(); dloads(0, DN0); SCHED(); }
    const unsigned wb = ldsb + L_WO + half * 65536 + l * 16;
    bf16x8 bb[2][4];
#pragma unroll
    for (int nt = 0; nt < 4; nt++) { bb[0][nt] = lds128(wb + nt * 1024); PIN(bb[0][nt]); }
    unsigned so[16], va0 = l * 16 + half * 65536 + (nh ^ (~rt2 & 4) * 2048), va1 = va0 ^ 65536;
    unsigned aXw = ldsb + L_XB + w * 4096 + l * 16, aXr = ldsb + L_XB + (w ^ 4) * 4096 + l * 16, sG5, sG5r, sRep, nr;
    {
        asm volatile("s_getreg_b32 %0, hwreg(HW_REG_XCC_ID, 0, 3)" : "=s"(sRep));
        sRep ^= pv & 4;
        nr = (sRep & 4) * 2048;
        sRep = WS_GK + (sRep + (idx & 1) * 8) * 2048;
        sG5r = WS_G5 + (rt2 & 7) * 65536 + lw * 16384 + half * 4096 + nh;
        sG5 = sG5r + ct * 256;
        asm volatile("" : "+s"(sG5r), "+s"(sRep), "+s"(nr));
    }
    unsigned vG5 = g == q4 ? t16 * 16 : OOB;
#pragma unroll
    for (int st = 0; st < 16; st++) { const unsigned h = (2 * st + half + rot2) & 31; so[st] = WS_P4 + ((rt2 & 7) * 8 + lw * 2) * 65536 + (h + (h & 24)) * 1024; asm volatile("" : "+s"(so[st])); }
    PIN(va0); PIN(va1); PIN(aXw); PIN(aXr); PIN(vG5);
    asm volatile("" : "+s"(sG5));
    unsigned wp1[2][4][8];
    if (w >= 4) {
#pragma unroll
        for (int ip = 0; ip < 4; ip++)
#pragma unroll
            for (int c = 0; c < 4; c++) {
#pragma unroll
                for (int m = 0; m < 2; m++) {
                    const unsigned a = wreg[m * 8 + 2 * ip][c], b = wreg[m * 8 + 2 * ip + 1][c];
                    wp1[m][ip][2 * c] = __builtin_amdgcn_perm(b, a, 0x05040100);
                    wp1[m][ip][2 * c + 1] = __builtin_amdgcn_perm(b, a, 0x07060302);
                }
            }
#pragma unroll
        for (int ip = 0; ip < 4; ip++)
#pragma unroll
            for (int c = 0; c < 8; c++) { PIN(wp1[0][ip][c]); PIN(wp1[1][ip][c]); }
    }
    if (half) SLEEP(S3H); else SLEEP(S3);
    poll16(rsWS, sFl, tag, l);
    FRESH();
    SCHED();
    STAMPW2(8, 0, 4);
    unsigned swc;
    f32x4 acc2[2][4];
    bf16x8 sop[2];
    {
        auto aload = [&](int st, int m2) { return LD16(m2 ? va1 : va0, so[st]); };
        constexpr int PD = 2;
        v4u af[PD][2];
#pragma unroll
        for (int i = 0; i < PD; i++) {
#pragma unroll
            for (int m2 = 1; m2 >= 0; m2--) af[i][m2] = aload(i, m2);
            SCHED();
        }
#pragma unroll
        for (int nt = 0; nt < 4; nt++) {
            const unsigned x0 = xres[nt >> 1][2 * (nt & 1)], x1 = xres[nt >> 1][2 * (nt & 1) + 1];
            acc2[0][nt] = (f32x4){bflo(x0), bfhi(x0), bflo(x1), bfhi(x1)};
            acc2[1][nt] = zero4;
        }
        unsigned m0 = p4 == 0 ? 0x3F803F80u : 0u, m1 = p4 == 1 ? ~0u : 0u;
        PIN(m0); PIN(m1);
#pragma unroll
        for (int i = 0; i < 2; i++) {
            v4u u;
#pragma unroll
            for (int j = 0; j < 4; j++) u[j] = (wrv[i][j] & m1) | m0;
            sop[i] = __builtin_bit_cast(bf16x8, u);
            PIN(sop[i]);
        }
        {
            float sw = 0.f;
#pragma unroll
            for (int i = 0; i < 8; i++) sw = dot2(wrv[i >> 2][i & 3], 0x3F803F80u, sw);
            swc = f2s(xrow_sum(sw));
            PIN(swc);
        }
        SCHED();
#pragma unroll
        for (int st = 0; st < 16; st++) {
            if (st + 1 < 16) {
#pragma unroll
                for (int nt = 0; nt < 4; nt++)
                    bb[(st + 1) % (2)][nt] = lds128(wb + (st + 1) * 4096 + nt * 1024);
            }
#pragma unroll
            for (int mm = 0; mm < 2; mm++) {
                const int m2 = mm ^ 1;
                const bf16x8 a = __builtin_bit_cast(bf16x8, af[st % PD][m2]);
#pragma unroll
                for (int nt = 0; nt < 4; nt++) acc2[m2][nt] = MFMA(bb[st % (2)][nt], a, acc2[m2][nt]);
            }
            if (st + PD < 16) {
#pragma unroll
                for (int m2 = 1; m2 >= 0; m2--) af[st % PD][m2] = aload(PD + st, m2);
            }
#pragma unroll
            for (int i = 0; i < 4; i++) { SGB(0x8, 1); SGB(0x100, 1); }
            SCHED();
        }
    }
    STAMPW2(9, 0, 4);
#pragma unroll
    for (int nt = 0; nt < 4; nt++) stsf4(aXw + nt * 1024, acc2[1][nt]);
    SCHED();
    if (half) dloads(DN0, DN1);
    SCHED();
    WAITL0();
    if (half) __builtin_amdgcn_s_setprio(1);
    BAR();
    SCHED();
    v4u hpk[2];
    {
        f32x4 pa[4];
#pragma unroll
        for (int nt = 0; nt < 4; nt++) pa[nt] = ldsf4(aXr + nt * 1024);
        SCHED();
#pragma unroll
        for (int i = 0; i < 8; i++) {
            const f32x4 a = acc2[0][i >> 1] + pa[i >> 1];
            hpk[i >> 2][i & 3] = pk2(a[2 * (i & 1)], a[2 * (i & 1) + 1]);
        }
        const bf16x8 h0 = __builtin_bit_cast(bf16x8, hpk[0]), h1 = __builtin_bit_cast(bf16x8, hpk[1]);
        f32x4 gq = MFMA(h0, h0, zero4), sd = MFMA(sop[0], h0, zero4);
        gq = MFMA(h1, h1, gq); sd = MFMA(sop[1], h1, sd);
        float q0 = (l & 1) ? gq[1] : gq[0], q1 = (l & 1) ? gq[3] : gq[2];
        PIN(q0); PIN(q1);
        const v4u pv = {f2s(sd[0]), f2s((l & 2) ? q1 : q0), f2s(sd[1]), swc};
        ST16(pv, vG5, sG5);
        asm volatile("s_nop 1" :: "v"(pv));
    }
    __builtin_amdgcn_s_setprio(0);
    STAMPW2(10, 0, 4);
    unsigned wp2[4][8];
    auto perm2 = [&]() {
#pragma unroll
        for (int i = 16; i < 24; i++) PIN(wreg[i]);
#pragma unroll
        for (int ip = 0; ip < 4; ip++)
#pragma unroll
            for (int c = 0; c < 4; c++) {
                const unsigned a = wreg[16 + 2 * ip][c], b = wreg[16 + 2 * ip + 1][c];
                wp2[ip][2 * c] = __builtin_amdgcn_perm(b, a, 0x05040100);
                wp2[ip][2 * c + 1] = __builtin_amdgcn_perm(b, a, 0x07060302);
            }
#pragma unroll
        for (int ip = 0; ip < 4; ip++)
#pragma unroll
            for (int c = 0; c < 8; c++) PIN(wp2[ip][c]);
    };
    const unsigned mt = rt2 * 8 + lw * 2 + half;
    v4u outv[2];
    unsigned vP = t16 == rt2 ? OOB : t16 * 128, pk;
#define NC 3
    {
        v4u pv[4];
#define NW 4
#define NRP 16
        unsigned vK = g < NW ? ((ct + (NRP / NW) * g) & (NRP - 1)) * 2048 : OOB;
        PIN(vK);
        unsigned sK = WS_GK + rt2 * 128, rk = 2047u - R2, aKy = ldsb + L_WO + 576, aKw = aKy + w * 4;
        asm volatile("" : "+s"(sK));
        PIN(rk); PIN(aKy); PIN(aKw); PIN(vP);
        SLEEP(S4);
        __builtin_amdgcn_s_setprio(1);
        sweepz<4, 4>([&](int k) { return LD16(l * 16 + k * 1024, sG5r); }, pv);
        STAMPW2(21, 0, 4);
        const f32x4 ps = (__builtin_bit_cast(f32x4, pv[0]) + __builtin_bit_cast(f32x4, pv[1])) + (__builtin_bit_cast(f32x4, pv[2]) + __builtin_bit_cast(f32x4, pv[3]));
        float T[4];
        xrow_gather(xrow_scatter<false>(ps[0], ps[1], ps[2], ps[3]), T);
        const float mean = T[0] * (1.0f / 1024.0f), var = fmaxf(T[1] * (1.0f / 1024.0f) - mean * mean, 0.f);
        const unsigned sg = pk2(mean, __builtin_amdgcn_sqrtf(rbf(rbf(var) + 1e-5f)));
        const float mb2 = bflo(sg), isd2 = __builtin_amdgcn_rcpf(bfhi(sg));
        const float rsc = (T[2] - mb2 * T[3]) * isd2;
        const unsigned rb = pk2(rsc, rsc) & 0xffffu;
        const unsigned ord = (rb & 0x8000u) ? (~rb & 0xffffu) : (rb | 0x8000u);
        const unsigned key = (ord << 11) | rk;
        unsigned kb = umx(key, dppu<0x128>(key));
        kb = umx(kb, dppu<0x124>(kb)); kb = umx(kb, dppu<0x122>(kb)); kb = umx(kb, dppu<0x121>(kb));
        stsu(aKw, kb);
        WAITL0();
        BAR();
        SCHED();
        pk = umx(wmax(*(const LAS v4u*)aKy), wmax(*(const LAS v4u*)(aKy + 16)));
        const bool cand = key == pk;
        ST4(pk, cand ? vK : OOB, sK);
        SCHED();
        __builtin_amdgcn_s_setprio(0);
#define KS 6
        unsigned k1;
        if (w == 1) {
            if (KS) SLEEP(KS);
            asm volatile("buffer_load_dword %0, %1, %2, %3 offen sc1" : "=v"(k1) : "v"(vP), "s"(rsWS), "s"(sRep) : "memory");
        }
        SCHED();
#define DP 1
        if (DP == 0 && half) { dloads(DN1, 8); SCHED(); }
        v4u e[2];
#pragma unroll
        for (int i = 0; i < 8; i++) {
            const float h0 = bflo(hpk[i >> 2][i & 3]), h1 = bfhi(hpk[i >> 2][i & 3]);
            const unsigned d = pk2(h0 - mb2, h1 - mb2);
            e[i >> 2][i & 3] = nsent(pk2(bflo(d) * isd2, bfhi(d) * isd2));
        }
#pragma unroll
        for (int i = 0; i < ((NC & 1) ? 4 : 2); i++) ST16(e[i & 1], cand ? g * 16 + (i & 1) * 64 : OOB, WS_C + (rt2 >> 2) * 16384 + (rt2 & 3) * 2048 + ct * 128 + (i >> 1) * 8192);
        SCHED();
        if (DP == 1 && half) { dloads(DN1, 8); SCHED(); }
        STAMPW2(22, 0, 4);
#pragma unroll
        for (int i = 0; i < 8; i++)
            outv[i >> 2][i & 3] = pk2(bflo(hpk[i >> 2][i & 3]) + bflo(e[i >> 2][i & 3]), bfhi(hpk[i >> 2][i & 3]) + bfhi(e[i >> 2][i & 3]));
        if (cand) {
#pragma unroll
            for (int i = 0; i < 2; i++) { *(LAS v4u*)(ldsb + L_STASH + i * 64 + g * 16) = hpk[i]; *(LAS v4u*)(ldsb + L_STASH + 128 + i * 64 + g * 16) = e[i]; }
        }
        PIN(outv[0]); PIN(outv[1]);
        if (DP == 2 && half) { dloads(DN1, 8); SCHED(); }
        if (w == 1) {
            waitv<(NC & 1) ? 4 : 2>();
            PIN(k1);
            unsigned hb[1] = {k1};
#define KR 0
            if (!__all(k1 != SENT)) sweepz<1, KR>([&](int) { return LD4(vP, sRep); }, hb);
            STAMPR(23, 2);
            unsigned gk = umx(hb[0], pk);
            gk = umx(gk, dppu<0x128>(gk)); gk = umx(gk, dppu<0x124>(gk)); gk = umx(gk, dppu<0x122>(gk)); gk = umx(gk, dppu<0x121>(gk));
            stsu(ldsb + L_WO + 512, gk);
        }
    }
    const rsrc_t rsO = __builtin_amdgcn_make_buffer_rsrc((void*)OUT, 0, 2048 * 1024 * 2, 0x00020000);
#define PP 1
    if (PP == 0 && half) perm2();
    STAMPW(24, 7);
    WAITL0();
    if (half) __builtin_amdgcn_s_setprio(1);
    BAR();
    STAMPW2(11, 0, 4);
    const unsigned gkey = rfl(ldsu(ldsb + L_WO + 512));
    const unsigned srow = 2047u - (gkey & 2047u);
    auto store_out = [&]() {
        if (R2 != srow) {
#pragma unroll
            for (int i = 0; i < 2; i++)
                __builtin_amdgcn_raw_buffer_store_b128(outv[i], rsO, R2 * 2048 + g * 16, ct * 128 + i * 64, AUX_SC1);
        }
    };
    if (w < 4) {
        if (bid == 0 && tid == 0) __hip_atomic_store((gu32*)(ws + WS_EPOCH), tag, RLX_AGENT);
        store_out();
        STAMPW(12, 0);
        RDUMP();
        return;
    }
#pragma unroll
    for (int nt = 0; nt < 2; nt++) PIN(outv[nt]);

    auto tree16 = [&](unsigned a, unsigned stride) {
        float v[16];
#pragma unroll
        for (int i = 0; i < 16; i++) v[i] = ldsf(a + i * stride);
        SCHED();
#pragma unroll
        for (int s = 8; s > 0; s >>= 1)
#pragma unroll
            for (int i = 0; i < s; i++) v[i] = v[2 * i] + v[2 * i + 1];
        return v[0];
    };
    if (ffn) {
        v4u xq[1];
        sweepz<1>([&](int) { return LD16(kr * 16, WS_C + (srow >> 9) * 16384 + ((srow >> 7) & 3) * 2048 + kq * 512 + lw * 128 + ((NC & 1) ? nr : 0)); }, xq);
        const v4u xp = xq[0];
        if (DP == 3) { SCHED(); dloads(DN1, 8); SCHED(); }
        STAMPW(13, 4);
#pragma unroll
        for (int m = 0; m < 2; m++) {
            float a1[8];
#pragma unroll
            for (int c = 0; c < 8; c++) {
                float s = 0.f;
#pragma unroll
                for (int ip = 0; ip < 4; ip++) s = dot2(wp1[m][ip][c], xp[ip], s);
                a1[c] = s + dppf<0x128>(s);
            }
            if (t16 < 8) {
                const unsigned o = ldsb + L_RED + (((lw * 4 + g) * 2 + m) * 64 + t16 * 8) * 4;
                stsf4(o, (f32x4){a1[0], a1[1], a1[2], a1[3]});
                stsf4(o + 16, (f32x4){a1[4], a1[5], a1[6], a1[7]});
            }
        }
    }
    if (DP == 3) WAITL0(); else __builtin_amdgcn_s_waitcnt(0x0070);
    BAR();
    if (ffn) {
        if ((NC & 2) || lw < 2) {
            const unsigned cx = (lw & 1) ? cg1u : cg1;
            ST4(f2s(tree16(ldsb + L_RED + ((lw & 1) * 64 + l) * 4, 512)), l * 4, WS_G8 + (cx >> 2) * 16384 + (lw >> 1) * 8192 + (kq * 2 + (lw & 1)) * 1024 + (cx & 3) * 256);
        }
        STAMPW(14, 4);
        unsigned pv[8];
#define S6 7
        if (PP == 1) { SCHED(); perm2(); SCHED(); }
        SLEEP(S6);
        sweepz<8>([&](int k) { return LD4(l * 4 + k * 1024, WS_G8 + kc * 16384 + lw * 256 + ((NC & 2) ? nr : 0)); }, pv);
        STAMPW(15, 4);
        const float gg = rbf(((__uint_as_float(pv[0]) + __uint_as_float(pv[2])) + __uint_as_float(pv[4])) + __uint_as_float(pv[6]));
        const float uu = rbf(((__uint_as_float(pv[1]) + __uint_as_float(pv[3])) + __uint_as_float(pv[5])) + __uint_as_float(pv[7]));
        const float act = rbf(gg * __builtin_amdgcn_rcpf(1.0f + __expf(-gg))) * uu;
        const unsigned apk = pk2(act, dppf<0xB1>(act));
        unsigned ap[4];
#pragma unroll
        for (int ip = 0; ip < 4; ip++) ap[ip] = __builtin_amdgcn_ds_bpermute((ip * 16 + kr * 2) * 4, apk);
        float a2[8];
#pragma unroll
        for (int c = 0; c < 8; c++) {
            float s = 0.f;
#pragma unroll
            for (int ip = 0; ip < 4; ip++) s = dot2(wp2[ip][c], ap[ip], s);
            a2[c] = s + dppf<0x128>(s);
        }
        if (t16 < 8) {
            const unsigned o = ldsb + L_RED + 8192 + ((lw * 4 + g) * 64 + t16 * 8) * 4;
            stsf4(o, (f32x4){a2[0], a2[1], a2[2], a2[3]});
            stsf4(o + 16, (f32x4){a2[4], a2[5], a2[6], a2[7]});
        }
    }
    WAITL0();
    BAR();
    if (ffn && lw < ((NC & 4) ? 2 : 1)) {
        ST4(f2s(tree16(ldsb + L_RED + 8192 + l * 4, 256)), l * 4, WS_G9 + (cg >> 1) * 16384 + lw * 8192 + (cg & 1) * 4096 + kc * 256);
    }
    SCHED();
    store_out();
    if (lw != 0) { RDUMP(); return; }
    STAMPW(16, 4);
    if ((srow >> 7) == rt2) {
        const unsigned ord = gkey >> 11;
        const unsigned rb = (ord & 0x8000u) ? (ord & 0x7fffu) : (~ord & 0xffffu);
        const float rs = bf2f(rb);
        float sig = rbf(1.0f / (1.0f + __expf(-rs)));
        const unsigned sb = ldsb + L_STASH + (t16 * 4 + g) * 2;
        float hc = lds_bf(sb), lc = lds_bf(sb + 128);
        PIN(sig); PIN(hc); PIN(lc);
        v4u yp[4];
        SLEEP(S7);
        sweepz<4>([&](int k) { return LD16((k < 3 || l < 32) ? l * 16 : OOB, WS_G9 + (ct >> 1) * 16384 + (ct & 1) * 4096 + k * 1024 + ((NC & 4) ? nr : 0)); }, yp);
        const f32x4 ys = (__builtin_bit_cast(f32x4, yp[0]) + __builtin_bit_cast(f32x4, yp[1])) + (__builtin_bit_cast(f32x4, yp[2]) + __builtin_bit_cast(f32x4, yp[3]));
        const float y = rbf(rbf(xrow_scatter<false>(ys[0], ys[1], ys[2], ys[3])) * sig);
        OUT[(size_t)srow * 1024 + ct * 64 + t16 * 4 + g] = (u16)(pk2(hc + rbf(lc + y), 0.f) & 0xffffu);
        STAMPW(17, 4);
    }
    RDUMP();
}
__device__ __attribute__((used, noinline)) void fk_tail() { asm volatile(".Lfk_end:"); }

extern "C" void kernel_launch(void* const* d_in, const int* in_sizes, int n_in, void* d_out, int out_size, void* d_ws,
                              size_t ws_size, hipStream_t stream) {
    (void)in_sizes; (void)n_in; (void)out_size; (void)ws_size;
    (void)hipFuncSetAttribute((const void*)fused_kernel, hipFuncAttributeMaxDynamicSharedMemorySize, LDS_BYTES);
    fused_kernel<<<256, 512, LDS_BYTES, stream>>>((const u16*)d_in[0], (const u16*)d_in[1], (const u16*)d_in[2], (const u16*)d_in[3],
                                                  (const u16*)d_in[5], (const u16*)d_in[6], (const u16*)d_in[7], (const u16*)d_in[8],
                                                  (u16*)d_out, (char*)d_ws);
}
```

```cpp
#include <hip/hip_runtime.h>
#include <stdint.h>
#include <utility>

typedef unsigned short u16;
typedef unsigned long long u64;
typedef short bf16x8 __attribute__((ext_vector_type(8)));
typedef short s16x4 __attribute__((ext_vector_type(4)));
typedef float f32x4 __attribute__((ext_vector_type(4)));
typedef float f32x2 __attribute__((ext_vector_type(2)));
typedef __bf16 bf16x2 __attribute__((ext_vector_type(2)));
typedef unsigned v4u __attribute__((ext_vector_type(4)));
typedef unsigned v2u __attribute__((ext_vector_type(2)));
#define LAS __attribute__((address_space(3)))
#define GAS __attribute__((address_space(1)))
#define CAS __attribute__((address_space(4)))
typedef GAS u64 gu64;
typedef GAS unsigned gu32;
typedef __amdgpu_buffer_rsrc_t rsrc_t;
#define DI __device__ __forceinline__
#define RLX_AGENT __ATOMIC_RELAXED, __HIP_MEMORY_SCOPE_AGENT
#define BAR() __builtin_amdgcn_s_barrier()
#define SCHED() __builtin_amdgcn_sched_barrier(0)
#define SGB(m, n) __builtin_amdgcn_sched_group_barrier((m), (n), 0)
#define WAITL0() asm volatile("s_waitcnt lgkmcnt(0)" ::: "memory")
#define WL0() __builtin_amdgcn_s_waitcnt(0xc07f)
#define MFMA(a, b, c) __builtin_amdgcn_mfma_f32_16x16x32_bf16((a), (b), (c), 0, 0, 0)
#define ORDER_BEHIND_POLL() __builtin_amdgcn_fence(__ATOMIC_ACQUIRE, "wavefront")
#define AUX_SC1 16
template <int N> DI void waitv() { asm volatile("s_waitcnt vmcnt(%0)" ::"i"(N) : "memory"); }

#define WS_EPOCH 0
#define WS_G0 0x1000
#define WS_G1 0x8000
#define WS_F4 0x60000
#define WS_GC 0x70000
#define WS_PR 0x74000
#define WS_G3 0x80000
#define WS_GZ 0xC0000
#define SENT 0xFFFFFFFFu
#define WS_TL 0x100000
#define WS_G5 0x100000
#define WS_G8 0x180000
#define WS_GK 0x1B8000
#define WS_G9 0x1C0000
#define WS_C 0x1E0000
#define WS_P2 0x200000
#define P2_STRIDE 0x2180
#define WS_P4 0x600000
#define WS_USED 0xB00000
#define WS_STAMP 0x1000000

#define STAGE_B 40960
#define LDS_BYTES 163840
#define L_WO 32768
#define L_MB 161792
#define L_WPHI (L_MB - 8192)
#define L_SD (L_MB + 512)
#define L_CS (L_MB + 1024)
#define L_V L_WO
#define L_KH (L_WO + 65536)
#define L_XB 0
#define L_STASH (L_WO + 1024)
#define L_RED 4096

#define SPIN_CAP 20000u
#define G9X 0
#define DN0 3
#define S3 2
#define S4 17
#define S7 8
#define W_AUX 2
#define LOG2E 1.4426950408889634f

DI unsigned rfl(unsigned x) { return __builtin_amdgcn_readfirstlane(x); }
DI float bf2f(unsigned u) { return __uint_as_float(u << 16); }
DI float bflo(unsigned u) { return __uint_as_float(u << 16); }
DI float bfhi(unsigned u) { return __uint_as_float(u & 0xffff0000u); }
DI unsigned pk2(float lo, float hi) { f32x2 v = {lo, hi}; return __builtin_bit_cast(unsigned, __builtin_convertvector(v, bf16x2)); }
DI unsigned nsent(unsigned x) { return __builtin_elementwise_min(x, SENT - 1); }
DI unsigned umx(unsigned a, unsigned b) { return __builtin_elementwise_max(a, b); }
DI float rbf(float f) { return bflo(pk2(f, f) & 0xffffu); }
DI bf16x8 mk8(unsigned a, unsigned b, unsigned c, unsigned d) { v4u v = {a, b, c, d}; return __builtin_bit_cast(bf16x8, v); }
DI void dma16(unsigned ldsaddr, unsigned voff, rsrc_t rs, unsigned soff) {
    asm volatile("s_mov_b32 m0, %0\n\ts_nop 0\n\tbuffer_load_dwordx4 %1, %2, %3 offen lds" ::"s"(ldsaddr), "v"(voff), "s"(rs), "s"(soff) : "memory");
}
DI void dma16x4(unsigned ldsaddr, const unsigned* v, rsrc_t rs, unsigned soff) {
    asm volatile("s_mov_b32 m0, %0\n\ts_nop 0\n\tbuffer_load_dwordx4 %1, %5, %6 offen lds\n\tbuffer_load_dwordx4 %2, %5, %6 offen offset:1024 lds\n\t"
                 "buffer_load_dwordx4 %3, %5, %6 offen offset:2048 lds\n\tbuffer_load_dwordx4 %4, %5, %6 offen offset:3072 lds"
                 ::"s"(ldsaddr), "v"(v[0]), "v"(v[1]), "v"(v[2]), "v"(v[3]), "s"(rs), "s"(soff) : "memory");
}
DI void touch(unsigned voff, rsrc_t rs, unsigned soff) { asm volatile("buffer_load_dword v255, %0, %1, %2 offen" ::"v"(voff), "s"(rs), "s"(soff) : "memory", "v255"); }
DI v4u ld16_asm(unsigned voff, rsrc_t rs, unsigned soff) {
    v4u r; asm volatile("buffer_load_dwordx4 %0, %1, %2, %3 offen" : "=v"(r) : "v"(voff), "s"(rs), "s"(soff) : "memory"); return r;
}
#define PIN(x) asm volatile("" : "+v"(x))
DI s16x4 tr_read(unsigned a) { return __builtin_amdgcn_ds_read_tr16_b64_v4i16((LAS s16x4*)(a)); }
DI bf16x8 cat8(s16x4 a, s16x4 b) { return __builtin_shufflevector(a, b, 0, 1, 2, 3, 4, 5, 6, 7); }
DI bf16x8 lds128(unsigned a) { return *(const LAS bf16x8*)(a); }
DI f32x4 ldsf4(unsigned a) { return *(const LAS f32x4*)(a); }
DI float ldsf(unsigned a) { return *(const LAS float*)(a); }
DI unsigned ldsu(unsigned a) { return *(const LAS unsigned*)(a); }
DI void stsf4(unsigned a, f32x4 v) { *(LAS f32x4*)(a) = v; }
DI void stsf(unsigned a, float v) { *(LAS float*)(a) = v; }
DI void stsu(unsigned a, unsigned v) { *(LAS unsigned*)(a) = v; }
template <int C> DI float dppf(float x) { return __uint_as_float(__builtin_amdgcn_update_dpp(0, __float_as_uint(x), C, 0xf, 0xf, true)); }
template <int C> DI unsigned dppu(unsigned x) { return __builtin_amdgcn_update_dpp(0, x, C, 0xf, 0xf, true); }
DI float max2f(float a, float b) { float r; asm("v_max_f32_e32 %0, %1, %2" : "=v"(r) : "v"(a), "v"(b)); return r; }
DI float max3f(float a, float b, float c) { float r; asm("v_max3_f32 %0, %1, %2, %3" : "=v"(r) : "v"(a), "v"(b), "v"(c)); return r; }
DI float row_sum(float x) { x += dppf<0x128>(x); x += dppf<0x124>(x); x += dppf<0x122>(x); x += dppf<0x121>(x); return x; }
DI float xrow_sum(float x) {
    auto a = __builtin_amdgcn_permlane16_swap(__float_as_uint(x), __float_as_uint(x), false, false);
    x = __uint_as_float(a[0]) + __uint_as_float(a[1]);
    auto b = __builtin_amdgcn_permlane32_swap(__float_as_uint(x), __float_as_uint(x), false, false);
    return __uint_as_float(b[0]) + __uint_as_float(b[1]);
}
DI float wave_sum(float x) { return xrow_sum(row_sum(x)); }
DI unsigned wave_maxu(unsigned x) {
    x = umx(x, dppu<0x128>(x)); x = umx(x, dppu<0x124>(x)); x = umx(x, dppu<0x122>(x)); x = umx(x, dppu<0x121>(x));
    auto a = __builtin_amdgcn_permlane16_swap(x, x, false, false);
    x = umx(a[0], a[1]);
    auto b = __builtin_amdgcn_permlane32_swap(x, x, false, false);
    return umx(b[0], b[1]);
}
DI float nmax3f(float a, float b, float c) { float r; asm("v_max3_f32 %0, -%1, -%2, -%3" : "=v"(r) : "v"(a), "v"(b), "v"(c)); return r; }
DI float nmax2f(float a, float b) { float r; asm("v_max_f32_e64 %0, -%1, -%2" : "=v"(r) : "v"(a), "v"(b)); return r; }
DI float mx2(float a, float b) { return __builtin_amdgcn_fmed3f(a, b, __builtin_inff()); }
template <bool MAX> DI float xrow_scatter(float a, float b, float c, float d) {
    const auto s = __builtin_amdgcn_permlane16_swap(__float_as_uint(a), __float_as_uint(b), false, false);
    const auto t = __builtin_amdgcn_permlane16_swap(__float_as_uint(c), __float_as_uint(d), false, false);
    const float u = MAX ? mx2(__uint_as_float(s[0]), __uint_as_float(s[1])) : __uint_as_float(s[0]) + __uint_as_float(s[1]);
    const float v = MAX ? mx2(__uint_as_float(t[0]), __uint_as_float(t[1])) : __uint_as_float(t[0]) + __uint_as_float(t[1]);
    const auto r = __builtin_amdgcn_permlane32_swap(__float_as_uint(u), __float_as_uint(v), false, false);
    return MAX ? mx2(__uint_as_float(r[0]), __uint_as_float(r[1])) : __uint_as_float(r[0]) + __uint_as_float(r[1]);
}
DI void xrow_gather(float w, float (&o)[4]) {
    const auto r = __builtin_amdgcn_permlane32_swap(__float_as_uint(w), __float_as_uint(w), false, false);
    const auto s = __builtin_amdgcn_permlane16_swap(r[0], r[0], false, false);
    const auto t = __builtin_amdgcn_permlane16_swap(r[1], r[1], false, false);
    o[0] = __uint_as_float(s[0]); o[1] = __uint_as_float(s[1]); o[2] = __uint_as_float(t[0]); o[3] = __uint_as_float(t[1]);
}
struct NoHook { DI void operator()(int, float&) const {} DI void operator()(int, float&, float&) const {} };
template <bool MUL, class H = NoHook> DI void softmax2(const f32x4 (&p)[2][2], float rs0, float rs1, bool hi, bf16x8 (&out)[4], H hook = H()) {
    float m[4];
#pragma unroll
    for (int i = 0; i < 2; i++) {
        const f32x4 &p0 = p[i][0], &p1 = p[i][1];
        m[2 * i] = max3f(max3f(p0[0], p0[1], p0[2]), max3f(p0[3], p1[0], p1[1]), max2f(p1[2], p1[3]));
        m[2 * i + 1] = max3f(nmax3f(p0[0], p0[1], p0[2]), nmax3f(p0[3], p1[0], p1[1]), nmax2f(p1[2], p1[3]));
    }
    asm volatile("s_nop 1" : "+v"(m[0]), "+v"(m[1]), "+v"(m[2]), "+v"(m[3]));
    hook(1, m[3]);
    const float rsr = hi ? rs1 : rs0;
    float a[4];
    xrow_gather(xrow_scatter<true>(m[0], m[1], m[2], m[3]) * (rsr * -LOG2E), a);
    f32x2 e[4][4];
    float s[4];
#pragma unroll
    for (int k = 0; k < 4; k++) {
        if (k == 2) hook(2, a[2], s[1]);
        const float sc = ((k >> 1) ? rs1 : rs0) * ((k & 1) ? -LOG2E : LOG2E);
#pragma unroll
        for (int j = 0; j < 4; j++) {
            const f32x4& q = p[k >> 1][j >> 1];
            const f32x2 t = __builtin_elementwise_fma((f32x2){q[2 * (j & 1)], q[2 * (j & 1) + 1]}, (f32x2){sc, sc}, (f32x2){a[k], a[k]});
            e[k][j] = (f32x2){__builtin_amdgcn_exp2f(t[0]), __builtin_amdgcn_exp2f(t[1])};
        }
        const f32x2 h = (e[k][0] + e[k][1]) + (e[k][2] + e[k][3]);
        s[k] = h[0] + h[1];
        if (k == 3) hook(3, s[k]);
    }
    float iv = __builtin_amdgcn_rcpf(xrow_scatter<false>(s[0], s[1], s[2], s[3]));
    if (MUL) iv *= rsr;
    xrow_gather(iv, a);
#pragma unroll
    for (int k = 0; k < 4; k++) {
        if (k == 2) hook(4, a[2]);
        const f32x2 c = {a[k], a[k]};
        const f32x2 x0 = e[k][0] * c, x1 = e[k][1] * c, x2 = e[k][2] * c, x3 = e[k][3] * c;
        out[k] = mk8(pk2(x0[0], x0[1]), pk2(x1[0], x1[1]), pk2(x2[0], x2[1]), pk2(x3[0], x3[1]));
    }
}
DI void swap16(v4u& x) {
    const auto a = __builtin_amdgcn_permlane16_swap(x[0], x[2], false, false), b = __builtin_amdgcn_permlane16_swap(x[1], x[3], false, false);
    x = (v4u){a[0], b[0], a[1], b[1]};
}
DI float dot2(unsigned a, unsigned b, float c) {
    return __builtin_amdgcn_fdot2_f32_bf16(__builtin_bit_cast(bf16x2, a), __builtin_bit_cast(bf16x2, b), c, false);
}
DI void st_gran(char* ws, unsigned off, unsigned tag, unsigned val) {
    __hip_atomic_store((gu64*)(ws + off), ((u64)tag << 32) | val, RLX_AGENT);
}
DI v2u ldg(rsrc_t rs, unsigned voff, unsigned soff) { return __builtin_amdgcn_raw_buffer_load_b64(rs, voff, soff, AUX_SC1); }
#define RARE(c) __builtin_expect(!!(c), 0)
#define FRESH() asm volatile("" ::: "memory")
template <int N> DI void sweep(rsrc_t rs, unsigned voff, unsigned soff, unsigned S, unsigned tag, unsigned (&v)[N]) {
#pragma nounroll
    for (unsigned spins = 0;;) {
        unsigned bad = 0;
        FRESH();
#pragma unroll
        for (int k = 0; k < N; ++k) { const v2u x = ldg(rs, voff, soff + k * S); v[k] = x[0]; bad |= x[1] ^ tag; }
        if (__all(bad == 0)) break;
        if (++spins > SPIN_CAP) break;
        __builtin_amdgcn_s_sleep(1);
    }
}
DI unsigned wmax(unsigned x) { return x; }
DI unsigned wmax(v2u x) { return umx(x[0], x[1]); }
DI unsigned wmax(v4u x) { return umx(umx(x[0], x[1]), umx(x[2], x[3])); }
template <int N, int SL = 0, class T, class F> DI void sweepz(F ld, T (&v)[N]) {
#pragma nounroll
    for (unsigned spins = 0;;) {
        FRESH();
        unsigned m = 0;
#pragma unroll
        for (int k = 0; k < N; ++k) v[k] = ld(k);
#pragma unroll
        for (int k = 0; k < N; ++k) m = umx(m, wmax(v[k]));
        if (__all(m != SENT)) break;
        if (++spins > SPIN_CAP) break;
        if (SL) __builtin_amdgcn_s_sleep(SL);
    }
}
DI unsigned f2s(float x) { return nsent(__float_as_uint(x)); }
#define OOB 0x4000000u
#define LD16(v, s) __builtin_amdgcn_raw_buffer_load_b128(rsWS, (v), (s), AUX_SC1)
#define LD4(v, s) __builtin_amdgcn_raw_buffer_load_b32(rsWS, (v), (s), AUX_SC1)
#define ST16(x, v, s) __builtin_amdgcn_raw_buffer_store_b128((x), rsWS, (v), (s), AUX_SC1)
#define ST4(x, v, s) __builtin_amdgcn_raw_buffer_store_b32((x), rsWS, (v), (s), AUX_SC1)
#define SLEEP(n) __builtin_amdgcn_s_sleep(n)
DI void poll16(rsrc_t rs, unsigned off, unsigned tag, unsigned v) {
#pragma nounroll
    for (unsigned spins = 0; spins < SPIN_CAP; spins++) {
        FRESH();
        const v2u x = ldg(rs, v, off);
        if (__all(x[1] == tag && x[0] == (tag ^ 0x5bd1e995u))) break;
    }
    ORDER_BEHIND_POLL();
}
#define STAMPR(i, role)
#define STAMP(i)
#define STAMPW(i, ww)

#define STAMPW2(i, a, b) STAMPW(i, a); STAMPW(i, b)
#define RINIT()
#define RDUMP()
#define ARR_DECL
#define KATTR
#define ARR_MARK(i)
#define ARR_END()
#define HST(i)
template <class F, int... Is> DI void static_for_impl(F&& f, std::integer_sequence<int, Is...>) { (f(std::integral_constant<int, Is>{}), ...); }
template <int N, class F> DI void static_for(F&& f) { static_for_impl(f, std::make_integer_sequence<int, N>{}); }

DI s16x4 bq(unsigned b0, unsigned b1, int j, int i) {
    const int ni = (j + 2) % 6;
    return tr_read(((ni & 1) ? b1 : b0) + (ni >> 1) * 8192 + (i >> 1) * 4096 + (i & 1) * 512);
}
template <class F> DI void group(f32x4 (&acc)[4][6], const bf16x8 (&A)[2][4], const bf16x8 (&B)[2], int j, F gap) {
    const int ni = (j + 2) % 6;
#pragma unroll
    for (int i = 0; i < 8; i++) {
        const int mi = i >> 1, ks = i & 1;
        if (ni < 4) asm volatile("v_mfma_f32_16x16x32_bf16 %0, %1, %2, %0" : "+v"(acc[mi][ni]) : "v"(B[ks]), "v"(A[ks][mi]));
        else asm volatile("v_mfma_f32_16x16x32_bf16 %0, %1, %2, %0" : "+v"(acc[mi][ni]) : "v"(A[ks][mi]), "v"(B[ks]));
        gap(i);
        SCHED();
    }
}
DI void img_write2(const unsigned (&ia)[4], const bf16x8 (&f)[4]) {
#pragma unroll
    for (int k = 0; k < 4; k++) {
        const v4u u = __builtin_bit_cast(v4u, f[k]);
        *(LAS v2u*)(ia[2 * (k & 1)] + (k >> 1) * 2048) = (v2u){u[0], u[1]};
        *(LAS v2u*)(ia[2 * (k & 1) + 1] + (k >> 1) * 2048) = (v2u){u[2], u[3]};
    }
}
DI float lds_bf(unsigned a) { return bf2f(*(const LAS u16*)(a)); }
#define MFMA_SETTLE2(p) asm volatile("s_nop 7\n\ts_nop 1" : "+v"(p[0][0]), "+v"(p[0][1]), "+v"(p[1][0]), "+v"(p[1][1]))

__global__ KATTR __launch_bounds__(512, 2) void fused_kernel(const u16* __restrict__ X, const u16* __restrict__ WQKV, const u16* __restrict__ WPHI,
                                                       const u16* __restrict__ WO, const u16* __restrict__ WR, const u16* __restrict__ WG,
                                                       const u16* __restrict__ WU, const u16* __restrict__ WD, u16* __restrict__ OUT,
                                                       char* __restrict__ ws) {
    extern __shared__ __attribute__((aligned(16))) char lds[];
    u64 pc0; unsigned clen;
    const int tid = threadIdx.x, l = tid & 63;
    asm volatile("s_getpc_b64 %0\n.Lfk_beg:\n\ts_lshr_b32 %1, %3, 3\n\tv_and_b32 v255, 0x1e0, %2\n\tv_add_lshl_u32 v255, v255, %1, 7\n\ts_mov_b32 %1, .Lfk_end-.Lfk_beg\n\t"
                 "v_min_u32 v255, %1, v255\n\tglobal_load_dword v255, v255, %0" : "=&s"(pc0), "=&s"(clen) : "v"(tid), "s"(blockIdx.x) : "v255", "scc");
    SCHED();
    const unsigned w = rfl(tid >> 6);
    const unsigned bid = blockIdx.x, xcd = (bid & 3) * 2 + ((bid >> 2) & 1), idx = bid >> 3;
    const unsigned rt = (xcd & 1) * 8 + (idx & 7), hp = (xcd >> 1) * 4 + (idx >> 3);
    const unsigned rt2 = (bid & 6) * 2 + (idx & 3), ct = (bid & 1) * 8 + (idx >> 2);
    const unsigned hq = (hp + 4) & 15;
    const unsigned rot = (rt >> 2) * 4;
    const unsigned rot2 = (2 * rt2 + 4 * ct) & 31;
    const unsigned ldsb = (unsigned)(size_t)lds;
    const int g = l >> 4, q4 = (l & 15) >> 2, p4 = l & 3, t16 = l & 15;
    const unsigned kr = l >> 3, gx = (g & 1) * 2 + (g >> 1);
    asm volatile("" ::"s"(X), "s"(WQKV), "s"(WPHI), "s"(WO), "s"(WR), "s"(WG), "s"(WU), "s"(WD), "s"(OUT), "s"(ws));
    unsigned tag;
#define READ_TAG() do { SCHED(); tag = *(const volatile CAS unsigned*)(ws + WS_EPOCH) + 1u; asm volatile("" : "+s"(tag)); } while (0)
    const rsrc_t rsWS = __builtin_amdgcn_make_buffer_rsrc((void*)ws, 0, WS_USED, 0x00020000);
    const rsrc_t rsX = __builtin_amdgcn_make_buffer_rsrc((void*)X, 0, 2048 * 1024 * 2, 0x00020000);
    const rsrc_t rsW = __builtin_amdgcn_make_buffer_rsrc((void*)WQKV, 0, 1024 * 3072 * 2, 0x00020000);
    const unsigned vA = (kr * 1024 + (((l & 7) ^ kr) * 8)) * 2;
    const unsigned vB0 = (kr * 3072 + (((l & 7) ^ (((kr >> 1) & 1) << 1)) * 8)) * 2;
    const unsigned vB1 = (kr * 3072 + (((l & 7) ^ ((((kr >> 1) & 1) | 2) << 1)) * 8)) * 2;
    const unsigned srow1 = rt * 128 + hp * 8 + (w & 3) * 2;
    v4u xr[4];
    if (w < 4) {
#pragma unroll
        for (int i = 0; i < 4; i++) xr[i] = ld16_asm(l * 16, rsX, srow1 * 2048 + i * 1024);
    }
    {
        const unsigned vBw = (w & 1) ? vB1 : vB0;
#pragma unroll
        for (int i = 0; i < 2; i++) dma16(ldsb + (w * 2 + i) * 1024, vA, rsX, (rt * 128 + w * 16 + i * 8) * 2048 + rot * 128);
#pragma unroll
        for (int i = 0; i < 3; i++) dma16(ldsb + 16384 + i * 8192 + w * 1024, vBw, rsW, (rot * 64 + w * 8) * 6144 + (i ? hp : hq) * 128 + i * 2048);
    }
    const f32x4 zero4 = {0.f, 0.f, 0.f, 0.f};
    ARR_DECL;
    const bf16x8 ones = mk8(0x3F803F80u, 0x3F803F80u, 0x3F803F80u, 0x3F803F80u);
    const unsigned lw = w & 3;
    const bool ffn = (ct & 7) != 3;
    const unsigned fb = !ffn ? 255u : (bid & 7) * 28 + (idx < 12 ? idx : idx - 4);
    const unsigned cg = (fb + 5) & 15, kc = fb >> 4;
    const unsigned kq = fb / 56, cg1 = fb % 56, cg1u = (cg1 + 3) % 56;
    v4u wreg[24];
    RINIT();
    STAMPW2(0, 0, 4);

    const rsrc_t rsWO = __builtin_amdgcn_make_buffer_rsrc((void*)WO, 0, 1024 * 1024 * 2, 0x00020000);
    const unsigned vWO = kr * 2048 + (((l & 7) ^ (((kr >> 1) & 3) << 1)) * 16);
    unsigned vwo[4];
    auto wo_so = [&](unsigned b) { return (((2 * b & 30) + (b >> 4) + rot2) & 31) * 65536 + ct * 128; };
    const unsigned fw = (((t16 >> 1) & 3) << 2) | (((t16 >> 3) & 1) << 1) | (t16 & 1);
    unsigned ad;
    unsigned ia[4];
    bf16x8 sdv[4];
    auto pre_addr = [&]() {
#pragma unroll
        for (int k = 0; k < 4; k++) { ia[k] = ldsb + L_XB + lw * 8192 + (w >> 2) * 4096 + t16 * 128 + (((4 * k + g) ^ fw) * 8); PIN(ia[k]); }
        const unsigned rr = 4 * g + q4, fr = (((rr >> 1) & 3) << 2) | (((rr >> 3) & 1) << 1) | (rr & 1);
        ad = ldsb + L_XB + (w & 1) * 16384 + rr * 128 + ((((w >> 1) * 4 + p4) ^ fr) * 8);
        PIN(ad);
    };
    auto sd_reads = [&]() {
        unsigned sa = ldsb + L_SD + g * 8;
        PIN(sa);
#pragma unroll
        for (int s = 0; s < 4; s++) {
            const v2u a = *(const LAS v2u*)(sa + s * 64), b = *(const LAS v2u*)(sa + s * 64 + 32);
            sdv[s] = mk8(a[0], a[1], b[0], b[1]);
        }
    };
    bf16x8 v0[4], v1[4];
    auto v_reads = [&]() {
        unsigned vd = ldsb + L_V + (w & 1) * 8192 + l * 16;
        PIN(vd);
#pragma unroll
        for (int s = 0; s < 4; s++) { v0[s] = lds128(vd + s * 2048); v1[s] = lds128(vd + s * 2048 + 1024); }
    };
    auto kv_tile = [&](auto hook6) {
        const unsigned h2 = w & 1, ft = w >> 1;
        WAITL0();
        ARR_MARK(18);
        BAR();
        SCHED();
        bf16x8 pf[4];
#pragma unroll
        for (int s = 0; s < 4; s++) pf[s] = cat8(tr_read(ad + s * 4096), tr_read(ad + s * 4096 + 2048));
        SCHED();
        hook6();
        SCHED();
        f32x4 k0 = zero4, k1 = zero4, kz = zero4;
#pragma unroll
        for (int s = 0; s < 4; s++) { k0 = MFMA(pf[s], v0[s], k0); k1 = MFMA(pf[s], v1[s], k1); }
#pragma unroll
        for (int s = 0; s < 4; s++) kz = MFMA(pf[s], sdv[s], kz);
        const unsigned pbase = WS_P2 + (hp * 16 + rt) * P2_STRIDE;
        const v4u o0 = {nsent(pk2(k0[0], k0[1])), nsent(pk2(k0[2], k0[3])), nsent(pk2(k1[0], k1[1])), nsent(pk2(k1[2], k1[3]))};
        __builtin_amdgcn_raw_buffer_store_b128(o0, rsWS, l * 16, pbase + h2 * 4096 + ft * 1024, AUX_SC1);
        asm volatile("s_nop 1" :: "v"(o0));
        __builtin_amdgcn_raw_buffer_store_b64((v2u){nsent(pk2(kz[0], kz[1])), nsent(pk2(kz[2], kz[3]))}, rsWS, t16 == 0 ? g * 8 : OOB,
                                              pbase + 8192 + h2 * 128 + ft * 32, AUX_SC1);
        SCHED();
        ARR_MARK(19);
        BAR();
    };

    v4u pp[2];
    auto red = [&]() {
        const v4u* p = pp;
        float lo = xrow_scatter<false>(bflo(p[0][0]) + bflo(p[1][0]), bflo(p[0][1]) + bflo(p[1][1]), bflo(p[0][2]) + bflo(p[1][2]), bflo(p[0][3]) + bflo(p[1][3]));
        float hi = xrow_scatter<false>(bfhi(p[0][0]) + bfhi(p[1][0]), bfhi(p[0][1]) + bfhi(p[1][1]), bfhi(p[0][2]) + bfhi(p[1][2]), bfhi(p[0][3]) + bfhi(p[1][3]));
        lo += dppf<0x128>(lo); hi += dppf<0x128>(hi);
        return nsent(pk2(lo, hi));
    };
    auto pp_so = [&](int k) { return WS_P2 + (hp * 16 + k * 8) * P2_STRIDE + (rt >> 2) * 2048 + (lw & 1) * 1024 + (rt & 3) * 256 + (lw >> 1) * 128; };
#define SR1 1
#define SR2 4
    auto red_issue = [&]() {
        const unsigned vpp = kr * P2_STRIDE + (l & 7) * 16;
#pragma unroll
        for (int k = 0; k < 2; k++) asm volatile("buffer_load_dwordx4 %0, %1, %2, %3 offen sc1" : "=v"(pp[k]) : "v"(vpp), "s"(rsWS), "s"(pp_so(k)) : "memory");
        SCHED();
    };
    auto kv_reduce = [&]() {
        waitv<5>();
        PIN(pp[0]); PIN(pp[1]);
        if (!__all(umx(wmax(pp[0]), wmax(pp[1])) != SENT)) sweepz<2, SR1>([&](int k) { return LD16(kr * P2_STRIDE + (l & 7) * 16, pp_so(k)); }, pp);
        STAMPW(25, 0);
        ST4(red(), (l & 8) ? OOB : (l & 7) * 16 + g * 4, WS_G3 + (hp * 4 + (rt >> 2)) * 2048 + (lw & 1) * 1024 + (rt & 3) * 256 + (lw >> 1) * 128);
    };

    const unsigned half = w >> 2;
    const unsigned R2 = rt2 * 128 + lw * 32 + half * 16 + t16;

    auto dloads = [&](int i0, int i1) {
        const rsrc_t rsD = __builtin_amdgcn_make_buffer_rsrc((void*)WD, 0, 1024 * 3584 * 2, 0x00020000);
#pragma unroll
        for (int i = i0; i < i1; i++)
            wreg[16 + i] = __builtin_amdgcn_raw_buffer_load_b128(rsD, kr * 4096 + (l & 7) * 16, (kc * 256 + lw * 64 + (i >> 1) * 16 + (i & 1)) * 2048 + cg * 128, W_AUX);
        FRESH();
    };
    unsigned wp1[2][4][8];
    v4u xres[2], wrv[2];
    v2u pl;
    unsigned nh;
    unsigned so, va0, va1, wb[4], vFl, sFl, swc, sG5, sG5r, sRep, nr, aXw, aXr, vG5;
    f32x4 acc2[2][4];
    bf16x8 sop[2];
    unsigned pv;
    auto place = [&]() {
        SCHED();
        PIN(pl);
        if (RARE(rfl(pl[1]) != tag)) { unsigned c[1]; sweep<1>(rsWS, 0, WS_PR + 1024, 0, tag, c); pl[0] = c[0]; }
        pv = rfl(pl[0]);
        nh = ((bid ^ (pv >> 4)) & 4) * 2048;
        asm volatile("" : "+s"(pv), "+s"(nh));
    };
    auto prep2 = [&]() {
        SCHED();
        vFl = t16 * 128 + g * 8;
        sFl = WS_F4 + (rt2 & 12) * 4096 + (rt2 & 3) * 2048 + nh;
        va0 = WS_P4 + ((rt2 & 7) * 8 + lw * 2 + half) * 65536 + (nh ^ (~rt2 & 4) * 2048) + l * 16; va1 = va0 ^ 65536;
        so = (half + rot2 + (rot2 & 24)) * 1024;
#pragma unroll
        for (int nt = 0; nt < 4; nt++) { wb[nt] = ldsb + L_WO + half * 65536 + (4 * g + q4) * 128 + ((nt ^ ((2 * g + (q4 >> 1)) & 3)) * 32) + p4 * 8; PIN(wb[nt]); }
        swap16(xres[0]); swap16(xres[1]); swap16(wrv[0]); swap16(wrv[1]);
        asm volatile("s_getreg_b32 %0, hwreg(HW_REG_XCC_ID, 0, 3)" : "=s"(sRep));
        sRep ^= pv & 4;
        nr = (sRep & 4) * 2048;
        sRep = WS_GK + (sRep + (idx & 1) * 8) * 2048;
        sG5r = WS_G5 + (rt2 & 7) * 65536 + lw * 16384 + half * 4096 + nh;
        sG5 = sG5r + ct * 256;
        aXw = ldsb + L_XB + w * 4096 + l * 16; aXr = aXw ^ 16384; vG5 = g == q4 ? t16 * 16 : OOB;
        asm volatile("" : "+s"(sG5), "+s"(sG5r), "+s"(sRep), "+s"(nr), "+s"(sFl), "+s"(nh), "+s"(so));
        PIN(aXw); PIN(aXr); PIN(vG5); PIN(va0); PIN(va1); PIN(vFl);
#pragma unroll
        for (int nt = 0; nt < 4; nt++) {
            const unsigned x0 = xres[nt >> 1][2 * (nt & 1)], x1 = xres[nt >> 1][2 * (nt & 1) + 1];
            acc2[0][nt] = (f32x4){bflo(x0), bfhi(x0), bflo(x1), bfhi(x1)};
            PIN(acc2[0][nt]);
        }
        unsigned m0 = p4 == 0 ? 0x3F803F80u : 0u, m1 = p4 == 1 ? ~0u : 0u;
        PIN(m0); PIN(m1);
#pragma unroll
        for (int i = 0; i < 2; i++) {
            v4u u;
#pragma unroll
            for (int j = 0; j < 4; j++) u[j] = (wrv[i][j] & m1) | m0;
            sop[i] = __builtin_bit_cast(bf16x8, u);
            PIN(sop[i]);
        }
        float sw = 0.f;
#pragma unroll
        for (int i = 0; i < 8; i++) sw = dot2(wrv[i >> 2][i & 3], 0x3F803F80u, sw);
        swc = f2s(xrow_sum(sw));
        PIN(swc);
        SCHED();
    };
    auto res_loads = [&]() {
        pl = ldg(rsWS, 0, WS_PR + 1024);
#pragma unroll
        for (int i = 0; i < 2; i++) {
            xres[i] = *(const v4u*)(X + (size_t)R2 * 1024 + ct * 64 + i * 32 + 8 * gx);
            wrv[i] = *(const v4u*)(WR + ct * 64 + i * 32 + 8 * gx);
        }
        FRESH();
    };

    if (w < 4) {
        const unsigned wm = w & 1, hh = w >> 1;
        f32x4 acc[4][6];
        bf16x8 wf[2], wfq[2];
        float mbt[4], rst[4], csv[2];
        f32x4 mbv[4], csq[2], csk[2];
#pragma unroll
        for (int i = 0; i < 4; i++)
#pragma unroll
            for (int j = 0; j < 6; j++) { acc[i][j] = zero4; PIN(acc[i][j]); }
        {
            const unsigned aoff0 = ldsb + (wm * 64 + t16) * 128 + (((0 + g) ^ (l & 7)) * 16);
            const unsigned aoff1 = ldsb + (wm * 64 + t16) * 128 + (((4 + g) ^ (l & 7)) * 16);
            const unsigned sk = ((q4 >> 1) & 1) | ((g & 1) << 1);
            const unsigned boffA = ldsb + 16384 + (8 * g + q4) * 128 + (((2 * hh) ^ sk) * 32) + p4 * 8;
            const unsigned boffB = ldsb + 16384 + (8 * g + q4) * 128 + (((2 * hh + 1) ^ sk) * 32) + p4 * 8;
            bf16x8 A[2][4], B[3][2];
            s16x4 lo;
            unsigned a0[3], a1[3], bA[3], bB[3];
#pragma unroll
            for (int i = 0; i < 3; i++) {
                a0[i] = aoff0 + i * STAGE_B; a1[i] = aoff1 + i * STAGE_B; bA[i] = boffA + i * STAGE_B; bB[i] = boffB + i * STAGE_B;
                PIN(a0[i]); PIN(a1[i]); PIN(bA[i]); PIN(bB[i]);
            }
            unsigned aT = ldsb + L_MB + (wm * 64 + t16) * 4, aS = ldsb + L_SD + (wm * 64 + t16) * 2, aM = ldsb + L_MB + wm * 256 + g * 16;
            unsigned aC = ldsb + L_CS + hh * 128 + g * 16, aV = ldsb + L_CS + (128 + hh * 32 + t16) * 4;
            unsigned aW = ldsb + L_WPHI + hh * 2048 + (4 * g + q4) * 64 + p4 * 8, aK = ldsb + L_KH + w * 2048 + l * 16;
            PIN(aT); PIN(aS); PIN(aM); PIN(aC); PIN(aV); PIN(aW); PIN(aK);
            pre_addr();
            READ_TAG();
            waitv<5>();
            PIN(xr[0]); PIN(xr[1]); PIN(xr[2]); PIN(xr[3]);
            {
                float s[2], q[2], x0[2];
#pragma unroll
                for (int rr = 0; rr < 2; rr++) {
                    x0[rr] = bflo(rfl(xr[2 * rr][0]));
                    s[rr] = 0.f; q[rr] = 0.f;
#pragma unroll
                    for (int i = 0; i < 4; i++) {
                        const float a = bflo(xr[2 * rr][i]) - x0[rr], b = bfhi(xr[2 * rr][i]) - x0[rr];
                        const float c = bflo(xr[2 * rr + 1][i]) - x0[rr], d = bfhi(xr[2 * rr + 1][i]) - x0[rr];
                        s[rr] += (a + b) + (c + d);
                        q[rr] += (a * a + b * b) + (c * c + d * d);
                    }
                }
                const float sq = row_sum(xrow_scatter<false>(s[0], s[1], q[0], q[1]));
#define RLF(x, n) __uint_as_float(__builtin_amdgcn_readlane(__float_as_uint(x), (n)))
                const float r0 = RLF(sq, 0), r1 = RLF(sq, 16), r2 = RLF(sq, 32), r3 = RLF(sq, 48);
                const float sm = (l ? r1 : r0) * (1.0f / 1024.0f), qm = (l ? r3 : r2) * (1.0f / 1024.0f);
                const float var = fmaxf(qm - sm * sm, 0.f);
                if (l < 2) st_gran(ws, WS_G0 + (srow1 + l) * 8, tag, pk2((l ? x0[1] : x0[0]) + sm, sqrtf(rbf(rbf(var) + 1e-5f))));
            }
            waitv<1>();
            WAITL0();
            ARR_MARK(0);
            BAR();
            SCHED();
            STAMPW(1, 0);
            auto rdB = [&](int u, int j, int i) {
                if (i & 1) B[j % 3][i >> 1] = cat8(lo, bq(bA[u], bB[u], j, i)); else lo = bq(bA[u], bB[u], j, i);
            };
            auto rdA = [&](int u, int i) { A[i & 1][i >> 1] = lds128(((i & 1) ? a1[u] : a0[u]) + (i >> 1) * 2048); };
            auto head = [&](int u) {
                group(acc, A, B[0], 0, [&](int i) { rdB(u, 1 + (i >> 2), i & 3); });
                group(acc, A, B[1], 1, [&](int i) { if (i < 4) rdB(u, 3, i); });
                group(acc, A, B[2], 2, [&](int i) { if (i < 4) rdB(u, 4, i); });
                group(acc, A, B[0], 3, [&](int i) { if (i < 4) rdB(u, 5, i); });
                WL0();
            };
#pragma unroll
            for (int i = 0; i < 4; i++) rdB(0, 0, i);
#pragma unroll
            for (int i = 0; i < 8; i++) rdA(0, i);
            SCHED();
#pragma nounroll
            for (unsigned t = 0; t < 5; t++) {
#pragma unroll
                for (int u = 0; u < 3; u++) {
                    head(u);
                    ARR_MARK(1 + t * 3 + u);
                    BAR();
                    SCHED();
                    group(acc, A, B[1], 4, [&](int i) { if (i < 4) rdB((u + 1) % 3, 0, i); });
                    group(acc, A, B[2], 5, [&](int i) { rdA((u + 1) % 3, i); });
                }
            }
            unsigned kw[4][4];
            f32x2 kx;
            auto kpiece = [&](int n) {
                if (n > 0) { kw[((n - 1 >> 2) + 2) & 3][(n - 1) & 3] = pk2(kx[0], kx[1]); PIN(kw[((n - 1 >> 2) + 2) & 3][(n - 1) & 3]); }
                if (n < 16) {
                    const int mi = ((n >> 2) + 2) & 3, j = n & 3;
                    const f32x4 &c = acc[mi][2 + (j >> 1)], &s_ = csk[j >> 1];
                    float x0 = __builtin_fmaf(mbt[mi], s_[2 * (j & 1)], c[2 * (j & 1)]); PIN(x0);
                    float x1 = __builtin_fmaf(mbt[mi], s_[2 * (j & 1) + 1], c[2 * (j & 1) + 1]); PIN(x1);
                    kx = (f32x2){x0, x1};
                }
                if (n == 5 || n == 9) *(LAS v4u*)(aK + (n == 9) * 1024) = (v4u){kw[2 + (n == 9)][0], kw[2 + (n == 9)][1], kw[2 + (n == 9)][2], kw[2 + (n == 9)][3]};
            };
            group(acc, A, B[0], 0, [&](int i) { rdB(0, 1 + (i >> 2), i & 3); });
            group(acc, A, B[1], 1, [&](int i) {
                if (i < 4) { rdB(0, 3, i); mbt[(i + 2) & 3] = ldsf(aT + ((i + 2) & 3) * 64); }
                else if (i < 6) csk[i - 4] = ldsf4(aC + 256 + (i - 4) * 64);
                else wf[i - 6] = cat8(tr_read(aW + (i - 6) * 32), tr_read(aW + (i - 6) * 32 + 1024));
            });
            group(acc, A, B[2], 2, [&](int i) { if (i < 4) rdB(0, 4, i); else mbv[i - 4] = ldsf4(aM + (i - 4) * 64); kpiece(i); });
            group(acc, A, B[0], 3, [&](int i) { if (i < 4) rdB(0, 5, i); if (i == 2 || i == 3) csv[i - 2] = ldsf(aV + (i - 2) * 64); kpiece(8 + i); });
            WL0();
            ARR_MARK(16);
            BAR();
            SCHED();
            kpiece(16);
            f32x4 p[2][2];
#pragma unroll
            for (int i = 0; i < 4; i++) p[i >> 1][i & 1] = MFMA(wf[i & 1], mk8(kw[i >> 1][0], kw[i >> 1][1], kw[i >> 1][2], kw[i >> 1][3]), zero4);
            SCHED();
            STAMPW(2, 0);
#pragma unroll
            for (int mi = 0; mi < 4; mi++) rst[mi] = lds_bf(aS + mi * 32);
#pragma unroll
            for (int a = 0; a < 2; a++)
#pragma unroll
                for (int dt = 0; dt < 2; dt++) {
                    const f32x4 x0 = acc[2 * a][4 + dt] + mbv[2 * a] * csv[dt], x1 = acc[2 * a + 1][4 + dt] + mbv[2 * a + 1] * csv[dt];
                    *(LAS v4u*)(aK + (L_V - L_KH) + w * 2048 + (a * 2 + dt) * 1024) = (v4u){pk2(x0[0], x0[1]), pk2(x0[2], x0[3]), pk2(x1[0], x1[1]), pk2(x1[2], x1[3])};
                }
            SCHED();
            rst[0] = __builtin_amdgcn_rcpf(rst[0]); rst[1] = __builtin_amdgcn_rcpf(rst[1]);
            bf16x8 f[4];
            struct HookC {
                decltype(v_reads)& vr; decltype(sd_reads)& sr;
                DI void operator()(int k, float& x) const { if (k == 1) { PIN(x); WAITL0(); ARR_MARK(17); BAR(); PIN(x); } if (k == 3) { PIN(x); vr(); sr(); PIN(x); } }
                DI void operator()(int, float&, float&) const {}
            };
            softmax2<true>(p, rst[0], rst[1], g >= 2, f, HookC{v_reads, sd_reads});
            img_write2(ia, f);
#pragma unroll
            for (int ft = 0; ft < 2; ft++) wfq[ft] = cat8(tr_read(aW + ft * 32 + 4096), tr_read(aW + ft * 32 + 5120));
            csq[0] = ldsf4(aC); csq[1] = ldsf4(aC + 64);
            STAMPW(3, 0);
            kv_tile([]() {});
            STAMPW(4, 0);
            SCHED();
            group(acc, A, B[1], 4, [&](int i) { if (i == 2 || i == 3) { rst[i] = __builtin_amdgcn_rcpf(rst[i]); PIN(rst[i]); } });
            group(acc, A, B[2], 5, [&](int) {});
            asm volatile("s_nop 7\n\ts_nop 1");
            SCHED();
        }
        bf16x8 phq[2][4];
        v4u kg[6];
        auto kg_ld = [&](int k) { return k < 4 ? LD16(l * 16, WS_G3 + (hq * 2 + hh) * 4096 + k * 1024) : LD16(g * 16, WS_GZ + (hq * 2 + hh) * 128 + (k - 4) * 64); };
        {
            f32x4 pq[2][2][2];
#pragma unroll
            for (int mi = 0; mi < 4; mi++) {
                const f32x4 x0 = acc[mi][0] + mbt[mi] * csq[0], x1 = acc[mi][1] + mbt[mi] * csq[1];
                const bf16x8 qf = mk8(pk2(x0[0], x0[1]), pk2(x0[2], x0[3]), pk2(x1[0], x1[1]), pk2(x1[2], x1[3]));
                pq[mi >> 1][mi & 1][0] = MFMA(wfq[0], qf, zero4); pq[mi >> 1][mi & 1][1] = MFMA(wfq[1], qf, zero4);
            }
            MFMA_SETTLE2(pq[0]); MFMA_SETTLE2(pq[1]);
            softmax2<false>(pq[0], rst[0], rst[1], g >= 2, phq[0]);
#pragma unroll
            for (int i = 0; i < 4; i++) PIN(phq[0][i]);
            SCHED();
            red_issue();
            res_loads();
            SCHED();
            kv_reduce();
            SCHED();
            BAR();
            SCHED();
            softmax2<false>(pq[1], rst[2], rst[3], g >= 2, phq[1]);
#pragma unroll
            for (int i = 0; i < 4; i++) { PIN(phq[0][i]); PIN(phq[1][i]); }
            SCHED();
        }
        STAMPW(5, 0);
        STAMPW(26, 0);
        v2u fv = {tag ^ 0x5bd1e995u, tag};
        place();
        PIN(xres[0]); PIN(xres[1]); PIN(wrv[0]); PIN(wrv[1]);
        unsigned sF = WS_F4 + (rt & 12) * 4096 + (rt & 3) * 2048 + hq * 128 + w * 8 + nh, vF = l == 0 ? 0 : OOB;
        {
            unsigned vO = l * 16, sO = WS_P4 + ((rt & 7) * 8 + wm * 4) * 65536 + (hq >> 2) * 16384 + ((hq & 3) * 2 + hh) * 1024 + (nh ^ (~rt & 4) * 2048);
            asm volatile("" : "+s"(sO), "+s"(sF));
            PIN(fv); PIN(vF);
#define S2N 1
            SLEEP(S2N);
            sweepz<6, SR2>(kg_ld, kg);
            STAMPW(6, 0);
            bf16x8 kvA[2][2], zA[2];
            v4u ov[4];
#pragma unroll
            for (int s = 0; s < 2; s++) {
#pragma unroll
                for (int dt = 0; dt < 2; dt++) kvA[s][dt] = mk8(kg[2 * s][2 * dt], kg[2 * s][2 * dt + 1], kg[2 * s + 1][2 * dt], kg[2 * s + 1][2 * dt + 1]);
                zA[s] = __builtin_bit_cast(bf16x8, kg[4 + s]);
            }
#define PH(mi, s) phq[(mi) >> 1][2 * ((mi) & 1) + (s)]
            f32x4 dn[4], n0[4], n1[4];
            float id[4];
#pragma unroll
            for (int mi = 0; mi < 4; mi++) dn[mi] = MFMA(zA[0], PH(mi, 0), zero4);
#pragma unroll
            for (int mi = 0; mi < 4; mi++) dn[mi] = MFMA(zA[1], PH(mi, 1), dn[mi]);
            SCHED();
#pragma unroll
            for (int mi = 0; mi <= 4; mi++) {
                if (mi < 4) {
                    n0[mi] = MFMA(kvA[0][0], PH(mi, 0), zero4); n1[mi] = MFMA(kvA[0][1], PH(mi, 0), zero4);
                    n0[mi] = MFMA(kvA[1][0], PH(mi, 1), n0[mi]); n1[mi] = MFMA(kvA[1][1], PH(mi, 1), n1[mi]);
                    id[mi] = __builtin_amdgcn_rcpf(rbf(rbf(dn[mi][0]) + 1e-6f));
                }
                if (mi > 0) {
                    const f32x4 a = n0[mi - 1] * id[mi - 1], b = n1[mi - 1] * id[mi - 1];
                    ov[mi - 1] = (v4u){pk2(a[0], a[1]), pk2(a[2], a[3]), pk2(b[0], b[1]), pk2(b[2], b[3])};
                    ST16(ov[mi - 1], vO + (mi - 1) * 65536, sO);
                }
                if (mi < 4) { SGB(0x8, 1); SGB(0x2, 6); SGB(0x8, 1); SGB(0x2, 3); SGB(0x8, 1); SGB(0x2, 3); SGB(0x8, 1); SGB(0x2, 4); }
                SCHED();
            }
#pragma unroll
            for (int mi = 0; mi < 4; mi++) PIN(ov[mi]);
        }
        waitv<0>();
        __builtin_amdgcn_raw_buffer_store_b64(fv, rsWS, vF, sF, AUX_SC1);
        STAMPW(7, 0);
    } else {
        const unsigned pw = lw & 1;
        const rsrc_t rsG = __builtin_amdgcn_make_buffer_rsrc((void*)WG, 0, 1024 * 3584 * 2, 0x00020000);
        const rsrc_t rsU = __builtin_amdgcn_make_buffer_rsrc((void*)WU, 0, 1024 * 3584 * 2, 0x00020000);
        unsigned vwl = kr * 57344 + (l & 7) * 16;
        unsigned sg = (kq * 256 + lw * 64) * 7168 + cg1 * 128, su = (kq * 256 + lw * 64) * 7168 + cg1u * 128;
        auto wloads = [&](int n0, int n1) {
#pragma unroll
            for (int n = n0; n < n1; n++)
                asm volatile("buffer_load_dwordx4 %0, %1, %2, %3 offen nt" : "=v"(wreg[n]) : "v"(vwl), "s"(n < 8 ? rsG : rsU), "s"((n < 8 ? sg : su) + (n & 7) * 7168));
        };
        auto dl_asm = [&]() {
            const rsrc_t rsD = __builtin_amdgcn_make_buffer_rsrc((void*)WD, 0, 1024 * 3584 * 2, 0x00020000);
#pragma unroll
            for (int i = 0; i < DN0; i++)
                asm volatile("buffer_load_dwordx4 %0, %1, %2, %3 offen nt" : "=v"(wreg[16 + i]) : "v"(kr * 4096 + (l & 7) * 16), "s"(rsD),
                             "s"((kc * 256 + lw * 64 + (i >> 1) * 16 + (i & 1)) * 2048 + cg * 128));
        };
        unsigned aK, aW, aS;
        auto k_pre = [&]() {
            pre_addr();
            aK = ldsb + L_KH + lw * 2048 + l * 16; aW = ldsb + L_WPHI + (lw >> 1) * 2048 + (4 * g + q4) * 64 + p4 * 8; aS = ldsb + L_SD + (pw * 64 + 32 + t16) * 2;
            PIN(aK); PIN(aW); PIN(aS);
        };
        auto kfeat = [&](auto hook) {
            bf16x8 wf[2];
            float rst[2];
#pragma unroll
            for (int ft = 0; ft < 2; ft++) {
                wf[ft] = cat8(tr_read(aW + ft * 32), tr_read(aW + ft * 32 + 1024));
                rst[ft] = __builtin_amdgcn_rcpf(lds_bf(aS + ft * 32));
            }
            sd_reads();
            PIN(wf[0]); PIN(wf[1]); PIN(rst[0]); PIN(rst[1]);
            SCHED();
            ARR_MARK(16);
            BAR();
            SCHED();
            const bf16x8 kf0 = lds128(aK), kf1 = lds128(aK + 1024);
            SCHED();
            hook(-1, rst[0]);
            hook(0, rst[0]);
            SCHED();
            f32x4 p[2][2];
            p[0][0] = MFMA(wf[0], kf0, zero4); p[0][1] = MFMA(wf[1], kf0, zero4);
            p[1][0] = MFMA(wf[0], kf1, zero4); p[1][1] = MFMA(wf[1], kf1, zero4);
            MFMA_SETTLE2(p);
            bf16x8 f[4];
            softmax2<true>(p, rst[0], rst[1], g >= 2, f, hook);
            img_write2(ia, f);
            SCHED();
            hook(5, rst[0]);
            SCHED();
            STAMPW2(3, 4, 7);
            kv_tile([&]() { hook(6, rst[0]); });
            STAMPW2(4, 4, 7);
        };
        auto wblk = [&](int k) { return k == 0 ? 22 + lw : k == 1 ? (lw < 3 ? 26 + lw : 4) : k < 6 ? 4 * k - 3 + lw : k == 6 ? lw : (lw < 3 ? 29 + lw : 21); };
        auto wo_init = [&]() {
#pragma unroll
            for (int i = 0; i < 4; i++) { vwo[i] = vWO + i * (16384 - 1024); PIN(vwo[i]); }
        };
        unsigned hs[8];
        auto wl_pre = [&]() { asm volatile("" : "+s"(sg), "+s"(su)); PIN(vwl); };
        auto wo_pre = [&]() {
            wo_init();
#pragma unroll
            for (int k = 0; k < 8; k++) { hs[k] = wo_so(wblk(k)); asm volatile("" : "+s"(hs[k])); }
        };
        auto wo_rng = [&](int k0, int k1) {
#pragma unroll
            for (int k = k0; k < k1; k++) dma16x4(ldsb + L_WO + wblk(k) * 4096, vwo, rsWO, hs[k]);
        };
        auto pf = [&](int i) {
            if (i == 0) { wloads(0, 6); SCHED(); if (lw < 2) wo_pre(); SCHED(); }
            if (i < 3 && !ffn) wo_rng(i, i + 1);
            if (i == 1 || i == 2) wloads(6 * i, i == 1 ? 12 : 16);
#define DHK 6
            if (i == DHK) dl_asm();
        };
        struct Hook {
            decltype(pf)& f; decltype(v_reads)& vr;
            DI void operator()(int k, float& x) const { PIN(x); if (k == 1) { ARR_MARK(17); BAR(); } if (k == 3) vr(); f(3 + k); PIN(x); }
            DI void operator()(int k, float& x, float& y) const { PIN(y); f(3 + k); PIN(x); }
        };
        auto tail16 = [&]() {
            kfeat(Hook{pf, v_reads});
            if (ffn) wo_rng(0, 3);
            wo_rng(3, 4);
            HST(27);
            if (ffn) waitv<18>(); else waitv<6>();
#pragma unroll
            for (int n = 0; n < 16 + DN0; n++) PIN(wreg[n]);
        };
        auto vT23 = [&]() {
            const unsigned n = (idx * 2 + pw) * 64 + l, r = n / 12, c = n % 12;
            return idx < 24 ? ((r & 127) + (r >> 7) * 256 + 128) * 6144 + (c >> 2) * 2048 + (((xcd >> 1) * 4 + (c >> 2 ? 0 : 4) + (c & 3)) & 15) * 128 : OOB;
        };
        if (lw < 2) {
            const unsigned sA = (rt * 128 + lw * 64) * 2048, sB = lw * 32 * 6144 + hp * 128, sBq = lw * 32 * 6144 + hq * 128;
            unsigned buf = 1;
            unsigned va[4], vb[4];
#pragma unroll
            for (int i = 0; i < 4; i++) { va[i] = vA + i * (16384 - 1024); vb[i] = ((i & 1) ? vB1 : vB0) + i * (49152 - 1024); PIN(va[i]); PIN(vb[i]); }
            auto stage = [&](unsigned s, unsigned bf, int i0, int i1) {
                const unsigned kt = (s + rot) & 15;
                const unsigned lb = ldsb + bf * STAGE_B;
                const unsigned sa = sA + kt * 128, sb = kt * 393216;
                if (i0 <= 0 && 0 < i1) dma16x4(lb + lw * 8192, va, rsX, sa);
                if (i0 <= 1 && 1 < i1) dma16x4(lb + lw * 8192 + 4096, va, rsX, sa + 65536);
                if (i0 <= 2 && 2 < i1) dma16x4(lb + 16384 + 8192 + lw * 4096, vb, rsW, sb + sB + 2048);
                if (i0 <= 3 && 3 < i1) dma16x4(lb + 16384 + 16384 + lw * 4096, vb, rsW, sb + sB + 4096);
                if (i0 <= 4 && 4 < i1) dma16x4(lb + 16384 + lw * 4096, vb, rsW, sb + sBq);
            };
            stage(1, 1, 0, 4);
            SCHED();
            unsigned vt = vT23();
            PIN(vt);
            wl_pre(); k_pre();
            waitv<16>(); ARR_MARK(0); BAR();
            stage(1, 1, 4, 5);
            touch(vt, rsW, (xcd & 1) * 512 * 6144);
            waitv<1>(); ARR_MARK(1); BAR();
            stage(2, 2, 0, 5); stage(3, 0, 0, 3);
            waitv<12>(); ARR_MARK(2); BAR();
            stage(3, 0, 3, 5); stage(4, 1, 0, 4);
            waitv<16>(); ARR_MARK(3); BAR();
            stage(4, 1, 4, 5);
            buf = 2;
#pragma nounroll
            for (unsigned s = 5; s < 16; s++) {
                if (s > 5) { waitv<20>(); ARR_MARK(s - 2); BAR(); }
                stage(s, buf, 0, 5);
                buf = buf == 2 ? 0 : buf + 1;
            }
            waitv<20>(); ARR_MARK(14); BAR();
            pf(0);
            READ_TAG();
            if (ffn) waitv<6>(); else waitv<10>();
            ARR_MARK(15); BAR();
            pf(1);
            tail16();
        } else {
            SCHED();
            unsigned v3;
            const unsigned h3 = (xcd >> 1) * 4 + 3, kv3 = !(xcd & 2), wv = idx * 2 + pw;
            auto line3 = [&](unsigned per, unsigned rows, auto rowf) {
                const unsigned n = wv * (kv3 ? per : (per + 2) / 3) + l, r3 = kv3 ? n / 3 : n, c3 = kv3 ? n % 3 : 0;
                return (idx < 24 && l < (kv3 ? per : (per + 2) / 3) && r3 < rows) ? rowf(r3) * 6144 + c3 * 2048 + ((c3 ? h3 : h3 + 4) & 15) * 128 : OOB;
            };
            v3 = line3(32, 512, [](unsigned r) { return r; });
            touch(line3(16, 256, [](unsigned r) { return (r & 127) + (r >> 7) * 256; }), rsW, (xcd & 1) * 512 * 6144);
            {
                const rsrc_t rsP = __builtin_amdgcn_make_buffer_rsrc((void*)WPHI, 0, 32 * 1024 * 2, 0x00020000);
#pragma unroll
                for (int i = 0; i < 4; i++) dma16(ldsb + L_WPHI + pw * 4096 + i * 1024, l * 16, rsP, (pw ? hq : hp) * 4096 + i * 1024);
            }
            v4u sv = {SENT, SENT, SENT, SENT};
            unsigned vc4 = l < 16 ? WS_G3 + (hp * 4 + (rt >> 2)) * 2048 + pw * 1024 + (rt & 3) * 256 + l * 16
                         : l < 24 ? WS_P2 + (hp * 16 + rt) * P2_STRIDE + 8192 + pw * 128 + (l - 16) * 16
                         : (l < 32 && pw == 0 && (rt & 7) == 0) ? WS_GZ + (hp * 2 + (rt >> 3)) * 128 + (l - 24) * 16
                         : l >= 56 ? WS_F4 + (rt & 12) * 4096 + (rt & 3) * 2048 + hq * 128 + pw * 8192 + (l & 7) * 16 : OOB;
            auto clears = [&](int i0, int i1) {
#pragma unroll
                for (int i = i0; i < i1; i++) {
                    if (i < 4) ST16(sv, l * 16, WS_P2 + (hp * 16 + rt) * P2_STRIDE + pw * 4096 + i * 1024);
                    if (i == 4) ST16(sv, vc4, 0);
                    if (i > 4) ST16(sv, l * 16, WS_TL + (bid * 4 + pw * 2 + i - 5) * 1024);
                }
                FRESH();
            };
            PIN(vc4); PIN(sv);
            unsigned ca = ldsb + 16384 + pw * 8192 + (8 * g + q4) * 128 + (((rt & 3) ^ (((q4 >> 1) & 1) | ((g & 1) << 1))) * 32) + p4 * 8;
            PIN(ca);
            bf16x8 cf[4];
            f32x4 cd[2] = {zero4, zero4};
            auto cs_read = [&](int st) {
#pragma unroll
                for (int i = 0; i < 4; i++) cf[i] = cat8(tr_read(ca + (st % 3) * STAGE_B + i * 4096), tr_read(ca + (st % 3) * STAGE_B + i * 4096 + 512));
                SCHED();
            };
            READ_TAG();
            waitv<5>();
            ARR_MARK(0);
            BAR();
            SCHED();
            cs_read(0);
            FRESH();
            const rsrc_t rsC = __builtin_amdgcn_make_buffer_rsrc((void*)pc0, 0, clen, 0x00020000);
            auto code_pf = [&]() { touch(l < 6 ? l * 128 : OOB, rsC, (idx * 2 + pw) * 768); };
            v4u gl[4];
            float s0 = 0.f, s1 = 0.f;
            unsigned bad;
            v2u gs, gc;
            unsigned vgc = WS_GC + (pw ? hq : hp) * 256 + (l & 31) * 8;
            unsigned lat[2] = {~0u, ~0u};
            unsigned vgl = (l < 48 ? l : 0) * 16;
            unsigned aw0 = ldsb + L_CS + pw * 384 + (vgl >> 1), aw1 = ldsb + L_MB + (pw * 64 + l) * 4, aw2 = ldsb + L_SD + (pw * 64 + l) * 2;
            static_for<15>([&](auto tc) {
                constexpr int t = decltype(tc)::value;
                WAITL0();
                ARR_MARK(t + 1);
                BAR();
                SCHED();
                if constexpr (t < 4) {
                    __builtin_amdgcn_s_setprio(2);
#pragma unroll
                    for (int i = 0; i < 4; i++) cd[i & 1] = MFMA(ones, cf[(i & 1) * 2 + (i >> 1)], cd[i & 1]);
                    SCHED();
                    __builtin_amdgcn_s_setprio(0);
                    if constexpr (t < 3) cs_read(t + 1);
                    else __builtin_amdgcn_raw_buffer_store_b64((v2u){__float_as_uint(g ? cd[1][0] : cd[0][0]), tag}, rsWS, (g == 1 || g + pw == 0) ? g * 512 + t16 * 8 : OOB,
                                                               WS_G1 + ((hp * 4 + (rt >> 2)) * 192 + pw * 64 + (rt & 3) * 16) * 8, AUX_SC1);
                }
                if constexpr (t == 0) touch(v3, rsW, (~xcd & 1) * 512 * 6144);
                if constexpr (t == 12) code_pf();
                if constexpr (t == 8) {
                    waitv<0>();
                    __builtin_amdgcn_raw_buffer_store_b64((v2u){0u, tag}, rsWS, l == 0 ? 0 : OOB, WS_GC + ((hp * 2 + pw) * 16 + rt) * 8, AUX_SC1);
                }
                if constexpr (t == 8 || t == 9) {
#pragma unroll
                    for (int r = (t - 8) * 2; r < (t - 8) * 2 + 2; r++)
                        asm volatile("buffer_load_dwordx4 %0, %1, %2, %3 offen sc1" : "=v"(gl[r]) : "v"(vgl), "s"(rsWS), "s"(WS_G1 + ((hp * 4 + r) * 192 + pw * 96) * 8) : "memory");
                }
                if constexpr (t == 6) {
                    asm volatile("buffer_load_dwordx2 %0, %1, %2, %3 offen sc1" : "=v"(gs) : "v"(l * 8), "s"(rsWS), "s"(WS_G0 + (rt * 128 + pw * 64) * 8) : "memory");
                }
                if constexpr (t == 11) {
                    waitv<0>();
                    PIN(gs);
                    bad = gs[1] ^ tag;
#pragma unroll
                    for (int r = 0; r < 4; r++) {
                        PIN(gl[r]);
                        bad |= (gl[r][1] ^ tag) | (gl[r][3] ^ tag);
                        s0 += __uint_as_float(gl[r][0]); s1 += __uint_as_float(gl[r][2]);
                    }
                    PIN(s0); PIN(s1); PIN(bad);
                }
                if constexpr (t == 12) {
                    if (RARE(!__all(bad == 0))) {
                        s0 = 0.f; s1 = 0.f;
#pragma nounroll
                        for (unsigned r = 0; r < 4; r++) {
                            unsigned a[2];
                            sweep<2>(rsWS, vgl, WS_G1 + ((hp * 4 + r) * 192 + pw * 96) * 8, 8, tag, a);
                            s0 += __uint_as_float(a[0]); s1 += __uint_as_float(a[1]);
                        }
                        unsigned c[1];
                        sweep<1>(rsWS, l * 8, WS_G0 + (rt * 128 + pw * 64) * 8, 0, tag, c);
                        gs[0] = c[0];
                    }
                    *(LAS f32x2*)(aw0) = (f32x2){-s0, -s1};
                    stsf(aw1, bflo(gs[0]));
                    *(LAS u16*)(aw2) = (u16)(gs[0] >> 16);
                    asm volatile("buffer_load_dwordx2 %0, %1, %2, 0 offen sc1" : "=v"(gc) : "v"(vgc), "s"(rsWS) : "memory");
                    SCHED();
                }
#define PT0 10
                if constexpr (t == PT0 - 2) { if (RARE(bid + pw == 0)) ST16(sv, (l >> 5) * 8192 + (l & 31) * 16, WS_PR); }
                if constexpr (t >= PT0 && t < 12) {
                    if (RARE(bid + pw == 0)) {
                        u64 c0, c1; unsigned x;
                        asm volatile("s_memtime %0\n\ts_load_dword %2, %3, %4\n\ts_waitcnt lgkmcnt(0)\n\ts_memtime %1\n\ts_waitcnt lgkmcnt(0)"
                                     : "=&s"(c0), "=&s"(c1), "=&s"(x) : "s"(ws), "i"(WS_PR + (t & 1) * 8192 + ((t - PT0) >> 1) * 128) : "memory");
                        lat[t & 1] = __builtin_elementwise_min(lat[t & 1], (unsigned)c1 - (unsigned)c0);
                        if constexpr (t == 11) {
                            asm volatile("s_getreg_b32 %0, hwreg(HW_REG_XCC_ID, 0, 3)" : "=s"(x));
                            __builtin_amdgcn_raw_buffer_store_b64((v2u){(lat[1] < lat[0] ? 0x44 : 0) ^ (x & 4), tag}, rsWS, l == 0 ? 0 : OOB, WS_PR + 1024, AUX_SC1);
                        }
                    }
                }
                if constexpr (t == 5) k_pre();
                if constexpr (t == 6) { wl_pre(); wo_pre(); }
                if constexpr (t == 7) { PIN(vgc); PIN(vgl); PIN(aw0); PIN(aw1); PIN(aw2); }
                clears(t < 7 ? t : 7, t < 7 ? t + 1 : 7);
                if constexpr (t == 13) {
                    waitv<0>();
                    PIN(gc);
                    if (RARE(!__all(gc[1] == tag))) { unsigned c[1]; sweep<1>(rsWS, vgc, 0, 0, tag, c); }
                    SCHED();
                    pf(0);
                }
                if constexpr (t == 14) pf(1);
            });
            tail16();
        }
        HST(28);
        auto perm1 = [&]() {
#pragma unroll
            for (int ip = 0; ip < 4; ip++)
#pragma unroll
                for (int c = 0; c < 4; c++) {
#pragma unroll
                    for (int m = 0; m < 2; m++) {
                        const unsigned a = wreg[m * 8 + 2 * ip][c], b = wreg[m * 8 + 2 * ip + 1][c];
                        wp1[m][ip][2 * c] = __builtin_amdgcn_perm(b, a, 0x05040100);
                        wp1[m][ip][2 * c + 1] = __builtin_amdgcn_perm(b, a, 0x07060302);
                    }
                }
#pragma unroll
            for (int ip = 0; ip < 4; ip++)
#pragma unroll
                for (int c = 0; c < 8; c++) { PIN(wp1[0][ip][c]); PIN(wp1[1][ip][c]); }
            SCHED();
            HST(29);
        };
#define ZP 0
        if (ZP) perm1();
        if (RARE(lw == 0 && (rt & 7) == 0)) {
            __builtin_amdgcn_s_setprio(1);
            sweepz<2, SR1>([&](int k) { return LD16(kr * P2_STRIDE + (l & 7) * 16, WS_P2 + (hp * 16 + k * 8) * P2_STRIDE + 8192 + (rt >> 3) * 128); }, pp);
            ST4(red(), (l & 8) ? OOB : ((l >> 2) & 1) * 64 + ((l & 1) * 2 + (g >> 1)) * 16 + ((l >> 1) & 1) * 8 + (g & 1) * 4, WS_GZ + (hp * 2 + (rt >> 3)) * 128);
            __builtin_amdgcn_s_setprio(0);
        }
        SCHED();
        if (!ZP) perm1();
        BAR();
        HST(30);
        res_loads();
        wo_rng(4, 8);
        HST(31);
        waitv<0>();
        PIN(xres[0]); PIN(xres[1]); PIN(wrv[0]); PIN(wrv[1]);
        place();
        STAMPW2(7, 4, 7);
    }
    prep2();
    if (w == 0) { if (S3) SLEEP(S3); poll16(rsWS, sFl, tag, vFl); }
    ARR_MARK(20);
    BAR();
    ARR_END();
    FRESH();
    SCHED();
    STAMPW2(8, 0, 4);
    bf16x8 bb[2][4];
    {
        auto aload = [&](int st, int m2) {
            const v4u r = LD16(m2 ? va1 : va0, so);
            if (m2 == 0) so = ((so | 8192) + 2048) & 0xDC00;
            return r;
        };
        constexpr int PD = 2;
        v4u af[PD][2];
#pragma unroll
        for (int i = 0; i < PD; i++) {
#pragma unroll
            for (int m2 = 1; m2 >= 0; m2--) af[i][m2] = aload(i, m2);
            SCHED();
        }
#pragma unroll
        for (int nt = 0; nt < 4; nt++) bb[0][nt] = cat8(tr_read(wb[nt]), tr_read(wb[nt] + 2048));
#pragma unroll
        for (int nt = 0; nt < 4; nt++) acc2[1][nt] = zero4;
        SCHED();
#pragma unroll
        for (int st = 0; st < 16; st++) {
            if (st + 1 < 16) {
#pragma unroll
                for (int nt = 0; nt < 4; nt++)
                    bb[(st + 1) % (2)][nt] = cat8(tr_read(wb[nt] + (st + 1) * 4096), tr_read(wb[nt] + (st + 1) * 4096 + 2048));
            }
#pragma unroll
            for (int mm = 0; mm < 2; mm++) {
                const int m2 = mm ^ 1;
                const bf16x8 a = __builtin_bit_cast(bf16x8, af[st % PD][m2]);
#pragma unroll
                for (int nt = 0; nt < 4; nt++) acc2[m2][nt] = MFMA(bb[st % (2)][nt], a, acc2[m2][nt]);
            }
            if (st + PD < 16) {
#pragma unroll
                for (int m2 = 1; m2 >= 0; m2--) af[st % PD][m2] = aload(PD + st, m2);
            }
#pragma unroll
            for (int i = 0; i < 4; i++) { SGB(0x8, 1); SGB(0x100, 2); }
            SCHED();
        }
    }
    STAMPW2(9, 0, 4);
#pragma unroll
    for (int nt = 0; nt < 4; nt++) stsf4(aXw + nt * 1024, acc2[1][nt]);
    SCHED();
    if (half) dloads(DN0, 8);
    SCHED();
    WAITL0();
    if (half) __builtin_amdgcn_s_setprio(1);
    BAR();
    SCHED();
    v4u hpk[2];
    {
        f32x4 pa[4];
#pragma unroll
        for (int nt = 0; nt < 4; nt++) pa[nt] = ldsf4(aXr + nt * 1024);
        SCHED();
#pragma unroll
        for (int i = 0; i < 8; i++) {
            const f32x4 a = acc2[0][i >> 1] + pa[i >> 1];
            hpk[i >> 2][i & 3] = pk2(a[2 * (i & 1)], a[2 * (i & 1) + 1]);
        }
        const bf16x8 h0 = __builtin_bit_cast(bf16x8, hpk[0]), h1 = __builtin_bit_cast(bf16x8, hpk[1]);
        f32x4 gq = MFMA(h0, h0, zero4), sd = MFMA(sop[0], h0, zero4);
        gq = MFMA(h1, h1, gq); sd = MFMA(sop[1], h1, sd);
        float q0 = (l & 1) ? gq[1] : gq[0], q1 = (l & 1) ? gq[3] : gq[2];
        PIN(q0); PIN(q1);
        const v4u pv = {f2s(sd[0]), f2s((l & 2) ? q1 : q0), f2s(sd[1]), swc};
        ST16(pv, vG5, sG5);
        asm volatile("s_nop 1" :: "v"(pv));
    }
    __builtin_amdgcn_s_setprio(0);
    SCHED();
    swap16(hpk[0]); swap16(hpk[1]);
    PIN(hpk[0]); PIN(hpk[1]);
    STAMPW2(10, 0, 4);
    unsigned wp2[4][8];
    auto perm2 = [&]() {
#pragma unroll
        for (int i = 16; i < 24; i++) PIN(wreg[i]);
#pragma unroll
        for (int ip = 0; ip < 4; ip++)
#pragma unroll
            for (int c = 0; c < 4; c++) {
                const unsigned a = wreg[16 + 2 * ip][c], b = wreg[16 + 2 * ip + 1][c];
                wp2[ip][2 * c] = __builtin_amdgcn_perm(b, a, 0x05040100);
                wp2[ip][2 * c + 1] = __builtin_amdgcn_perm(b, a, 0x07060302);
            }
#pragma unroll
        for (int ip = 0; ip < 4; ip++)
#pragma unroll
            for (int c = 0; c < 8; c++) PIN(wp2[ip][c]);
    };
    const unsigned mt = rt2 * 8 + lw * 2 + half;
    v4u outv[2];
    unsigned vP = t16 == rt2 ? OOB : t16 * 128, pk;
    {
        v4u pv[4];
        unsigned vK = ((ct + 4 * g) & 15) * 2048;
        PIN(vK);
        unsigned sK = WS_GK + rt2 * 128, rk = 2047u - R2, aKy = ldsb + L_WO + 576, aKw = aKy + w * 4;
        asm volatile("" : "+s"(sK));
        PIN(rk); PIN(aKy); PIN(aKw); PIN(vP);
        SLEEP(S4);
        __builtin_amdgcn_s_setprio(1);
        sweepz<4, 4>([&](int k) { return LD16(l * 16 + k * 1024, sG5r); }, pv);
        STAMPW2(21, 0, 4);
        const f32x4 ps = (__builtin_bit_cast(f32x4, pv[0]) + __builtin_bit_cast(f32x4, pv[1])) + (__builtin_bit_cast(f32x4, pv[2]) + __builtin_bit_cast(f32x4, pv[3]));
        float T[4];
        xrow_gather(xrow_scatter<false>(ps[0], ps[1], ps[2], ps[3]), T);
        const float mean = T[0] * (1.0f / 1024.0f), var = fmaxf(T[1] * (1.0f / 1024.0f) - mean * mean, 0.f);
        const unsigned sg = pk2(mean, __builtin_amdgcn_sqrtf(rbf(rbf(var) + 1e-5f)));
        const float mb2 = bflo(sg), isd2 = __builtin_amdgcn_rcpf(bfhi(sg));
        const float rsc = (T[2] - mb2 * T[3]) * isd2;
        const unsigned rb = pk2(rsc, rsc) & 0xffffu;
        const unsigned ord = (rb & 0x8000u) ? (~rb & 0xffffu) : (rb | 0x8000u);
        const unsigned key = (ord << 11) | rk;
        unsigned kb = umx(key, dppu<0x128>(key));
        kb = umx(kb, dppu<0x124>(kb)); kb = umx(kb, dppu<0x122>(kb)); kb = umx(kb, dppu<0x121>(kb));
        stsu(aKw, kb);
        WAITL0();
        BAR();
        SCHED();
        pk = umx(wmax(*(const LAS v4u*)aKy), wmax(*(const LAS v4u*)(aKy + 16)));
        const bool cand = key == pk;
        ST4(pk, cand ? vK : OOB, sK);
        SCHED();
        __builtin_amdgcn_s_setprio(0);
        v4u e[2];
#pragma unroll
        for (int i = 0; i < 8; i++) {
            const float h0 = bflo(hpk[i >> 2][i & 3]), h1 = bfhi(hpk[i >> 2][i & 3]);
            const unsigned d = pk2(h0 - mb2, h1 - mb2);
            e[i >> 2][i & 3] = nsent(pk2(bflo(d) * isd2, bfhi(d) * isd2));
        }
#pragma unroll
        for (int i = 0; i < 4; i++) ST16(e[i & 1], cand ? gx * 16 + (i & 1) * 64 : OOB, WS_C + (rt2 >> 2) * 16384 + (rt2 & 3) * 2048 + ct * 128 + (i >> 1) * 8192);
        SCHED();
#define KS 0
        unsigned k1;
        if (w == 1) {
            if (KS) SLEEP(KS);
            asm volatile("buffer_load_dword %0, %1, %2, %3 offen sc1" : "=v"(k1) : "v"(vP), "s"(rsWS), "s"(sRep) : "memory");
        }
        SCHED();
        STAMPW2(22, 0, 4);
#pragma unroll
        for (int i = 0; i < 8; i++)
            outv[i >> 2][i & 3] = pk2(bflo(hpk[i >> 2][i & 3]) + bflo(e[i >> 2][i & 3]), bfhi(hpk[i >> 2][i & 3]) + bfhi(e[i >> 2][i & 3]));
        if (cand) {
#pragma unroll
            for (int i = 0; i < 2; i++) { *(LAS v4u*)(ldsb + L_STASH + i * 64 + gx * 16) = hpk[i]; *(LAS v4u*)(ldsb + L_STASH + 128 + i * 64 + gx * 16) = e[i]; }
        }
        PIN(outv[0]); PIN(outv[1]);
        if (w == 1) {
            waitv<0>();
            PIN(k1);
            unsigned hb[1] = {k1};
            if (!__all(k1 != SENT)) sweepz<1>([&](int) { return LD4(vP, sRep); }, hb);
            STAMPR(23, 2);
            unsigned gk = umx(hb[0], pk);
            gk = umx(gk, dppu<0x128>(gk)); gk = umx(gk, dppu<0x124>(gk)); gk = umx(gk, dppu<0x122>(gk)); gk = umx(gk, dppu<0x121>(gk));
            stsu(ldsb + L_WO + 512, gk);
        }
    }
    const rsrc_t rsO = __builtin_amdgcn_make_buffer_rsrc((void*)OUT, 0, 2048 * 1024 * 2, 0x00020000);
    STAMPW(24, 7);
    WAITL0();
    if (half) __builtin_amdgcn_s_setprio(1);
    BAR();
    STAMPW2(11, 0, 4);
    const unsigned gkey = rfl(ldsu(ldsb + L_WO + 512));
    const unsigned srow = 2047u - (gkey & 2047u);
    auto store_out = [&]() {
        if (R2 != srow) {
#pragma unroll
            for (int i = 0; i < 2; i++)
                __builtin_amdgcn_raw_buffer_store_b128(outv[i], rsO, R2 * 2048 + gx * 16, ct * 128 + i * 64, AUX_SC1);
        }
    };
    if (w < 4) {
        if (bid == 0 && tid == 0) __hip_atomic_store((gu32*)(ws + WS_EPOCH), tag, RLX_AGENT);
        store_out();
        STAMPW(12, 0);
        RDUMP();
        return;
    }
#pragma unroll
    for (int nt = 0; nt < 2; nt++) PIN(outv[nt]);

    auto tree16 = [&](unsigned a, unsigned stride) {
        float v[16];
#pragma unroll
        for (int i = 0; i < 16; i++) v[i] = ldsf(a + i * stride);
        SCHED();
#pragma unroll
        for (int s = 8; s > 0; s >>= 1)
#pragma unroll
            for (int i = 0; i < s; i++) v[i] = v[2 * i] + v[2 * i + 1];
        return v[0];
    };
    if (ffn) {
        v4u xq[1];
        sweepz<1>([&](int) { return LD16(kr * 16, WS_C + (srow >> 9) * 16384 + ((srow >> 7) & 3) * 2048 + kq * 512 + lw * 128 + nr); }, xq);
        const v4u xp = xq[0];
        STAMPW(13, 4);
#pragma unroll
        for (int m = 0; m < 2; m++) {
            float a1[8];
#pragma unroll
            for (int c = 0; c < 8; c++) {
                float s = 0.f;
#pragma unroll
                for (int ip = 0; ip < 4; ip++) s = dot2(wp1[m][ip][c], xp[ip], s);
                a1[c] = s + dppf<0x128>(s);
            }
            if (t16 < 8) {
                const unsigned o = ldsb + L_RED + (((lw * 4 + g) * 2 + m) * 64 + t16 * 8) * 4;
                stsf4(o, (f32x4){a1[0], a1[1], a1[2], a1[3]});
                stsf4(o + 16, (f32x4){a1[4], a1[5], a1[6], a1[7]});
            }
        }
    }
    __builtin_amdgcn_s_waitcnt(0x0070);
    BAR();
    if (ffn) {
        {
            const unsigned cx = (lw & 1) ? cg1u : cg1;
            ST4(f2s(tree16(ldsb + L_RED + ((lw & 1) * 64 + l) * 4, 512)), l * 4, WS_G8 + (cx >> 2) * 16384 + (lw >> 1) * 8192 + (kq * 2 + (lw & 1)) * 1024 + (cx & 3) * 256);
        }
        STAMPW(14, 4);
        unsigned pv[8];
#define S6 7
        SCHED(); perm2(); SCHED();
        SLEEP(S6);
        sweepz<8>([&](int k) { return LD4(l * 4 + k * 1024, WS_G8 + kc * 16384 + lw * 256 + nr); }, pv);
        STAMPW(15, 4);
        const float gg = rbf(((__uint_as_float(pv[0]) + __uint_as_float(pv[2])) + __uint_as_float(pv[4])) + __uint_as_float(pv[6]));
        const float uu = rbf(((__uint_as_float(pv[1]) + __uint_as_float(pv[3])) + __uint_as_float(pv[5])) + __uint_as_float(pv[7]));
        const float act = rbf(gg * __builtin_amdgcn_rcpf(1.0f + __expf(-gg))) * uu;
        const unsigned apk = pk2(act, dppf<0xB1>(act));
        unsigned ap[4];
#pragma unroll
        for (int ip = 0; ip < 4; ip++) ap[ip] = __builtin_amdgcn_ds_bpermute((ip * 16 + kr * 2) * 4, apk);
        float a2[8];
#pragma unroll
        for (int c = 0; c < 8; c++) {
            float s = 0.f;
#pragma unroll
            for (int ip = 0; ip < 4; ip++) s = dot2(wp2[ip][c], ap[ip], s);
            a2[c] = s + dppf<0x128>(s);
        }
        if (t16 < 8) {
            const unsigned o = ldsb + L_RED + 8192 + ((lw * 4 + g) * 64 + t16 * 8) * 4;
            stsf4(o, (f32x4){a2[0], a2[1], a2[2], a2[3]});
            stsf4(o + 16, (f32x4){a2[4], a2[5], a2[6], a2[7]});
        }
    }
    WAITL0();
    BAR();
    if (ffn && lw == 0) {
        ST4(f2s(tree16(ldsb + L_RED + 8192 + l * 4, 256)), l * 4, WS_G9 + (cg >> 1) * 16384 + (cg & 1) * 4096 + kc * 256 + (nh ^ G9X ^ (((srow >> 8) ^ bid) & 4) * 2048));
    }
    SCHED();
    store_out();
    if (lw != 0) { RDUMP(); return; }
    STAMPW(16, 4);
    if ((srow >> 7) == rt2) {
        const unsigned ord = gkey >> 11;
        const unsigned rb = (ord & 0x8000u) ? (ord & 0x7fffu) : (~ord & 0xffffu);
        const float rs = bf2f(rb);
        float sig = rbf(1.0f / (1.0f + __expf(-rs)));
        const unsigned sb = ldsb + L_STASH + (t16 * 4 + g) * 2;
        float hc = lds_bf(sb), lc = lds_bf(sb + 128);
        PIN(sig); PIN(hc); PIN(lc);
        v4u yp[4];
        SLEEP(S7);
        sweepz<4>([&](int k) { return LD16((k < 3 || l < 32) ? l * 16 : OOB, WS_G9 + (ct >> 1) * 16384 + (ct & 1) * 4096 + k * 1024 + (nh ^ G9X)); }, yp);
        const f32x4 ys = (__builtin_bit_cast(f32x4, yp[0]) + __builtin_bit_cast(f32x4, yp[1])) + (__builtin_bit_cast(f32x4, yp[2]) + __builtin_bit_cast(f32x4, yp[3]));
        const float y = rbf(rbf(xrow_scatter<false>(ys[0], ys[1], ys[2], ys[3])) * sig);
        OUT[(size_t)srow * 1024 + ct * 64 + t16 * 4 + g] = (u16)(pk2(hc + rbf(lc + y), 0.f) & 0xffffu);
        STAMPW(17, 4);
    }
    RDUMP();
}
__device__ __attribute__((used, noinline)) void fk_tail() { asm volatile(".Lfk_end:"); }

extern "C" void kernel_launch(void* const* d_in, const int* in_sizes, int n_in, void* d_out, int out_size, void* d_ws,
                              size_t ws_size, hipStream_t stream) {
    (void)in_sizes; (void)n_in; (void)out_size; (void)ws_size;
    (void)hipFuncSetAttribute((const void*)fused_kernel, hipFuncAttributeMaxDynamicSharedMemorySize, LDS_BYTES);
    fused_kernel<<<256, 512, LDS_BYTES, stream>>>((const u16*)d_in[0], (const u16*)d_in[1], (const u16*)d_in[2], (const u16*)d_in[3],
                                                  (const u16*)d_in[5], (const u16*)d_in[6], (const u16*)d_in[7], (const u16*)d_in[8],
                                                  (u16*)d_out, (char*)d_ws);
}
```

```cpp
#include <hip/hip_runtime.h>
#include <stdint.h>
#include <utility>

typedef unsigned short u16;
typedef unsigned long long u64;
typedef short bf16x8 __attribute__((ext_vector_type(8)));
typedef short s16x4 __attribute__((ext_vector_type(4)));
typedef float f32x4 __attribute__((ext_vector_type(4)));
typedef float f32x2 __attribute__((ext_vector_type(2)));
typedef __bf16 bf16x2 __attribute__((ext_vector_type(2)));
typedef unsigned v4u __attribute__((ext_vector_type(4)));
typedef unsigned v2u __attribute__((ext_vector_type(2)));
#define LAS __attribute__((address_space(3)))
#define GAS __attribute__((address_space(1)))
#define CAS __attribute__((address_space(4)))
typedef GAS u64 gu64;
typedef GAS unsigned gu32;
typedef __amdgpu_buffer_rsrc_t rsrc_t;
#define DI __device__ __forceinline__
#define RLX_AGENT __ATOMIC_RELAXED, __HIP_MEMORY_SCOPE_AGENT
#define BAR() __builtin_amdgcn_s_barrier()
#define SCHED() __builtin_amdgcn_sched_barrier(0)
#define SGB(m, n) __builtin_amdgcn_sched_group_barrier((m), (n), 0)
#define WAITL0() asm volatile("s_waitcnt lgkmcnt(0)" ::: "memory")
#define WL0() __builtin_amdgcn_s_waitcnt(0xc07f)
#define MFMA(a, b, c) __builtin_amdgcn_mfma_f32_16x16x32_bf16((a), (b), (c), 0, 0, 0)
#define ORDER_BEHIND_POLL() __builtin_amdgcn_fence(__ATOMIC_ACQUIRE, "wavefront")
#define AUX_SC1 16
template <int N> DI void waitv() { asm volatile("s_waitcnt vmcnt(%0)" ::"i"(N) : "memory"); }

#define WS_EPOCH 0
#define WS_G0 0x1000
#define WS_G1 0x8000
#define WS_F4 0x60000
#define WS_GC 0x70000
#define WS_PR 0x74000
#define WS_G3 0x80000
#define WS_GZ 0xC0000
#define SENT 0xFFFFFFFFu
#define WS_TL 0x100000
#define WS_G5 0x100000
#define WS_G8 0x180000
#define WS_GK 0x1B8000
#define WS_G9 0x1C0000
#define WS_C 0x1E0000
#define WS_P2 0x200000
#define P2_STRIDE 0x2180
#define WS_P4 0x600000
#define WS_USED 0xB00000
#define WS_STAMP 0x1000000

#define STAGE_B 40960
#define LDS_BYTES 163840
#define L_WO 32768
#define L_MB 161792
#define L_WPHI (L_MB - 8192)
#define L_SD (L_MB + 512)
#define L_CS (L_MB + 1024)
#define L_V L_WO
#define L_KH (L_WO + 65536)
#define L_XB 0
#define L_STASH (L_WO + 1024)
#define L_RED 4096

#define SPIN_CAP 20000u
#define OH 6
#define DN0 3
#define S3 4
#define S4 13
#define S7 8
#define W_AUX 2
#define LOG2E 1.4426950408889634f

DI unsigned rfl(unsigned x) { return __builtin_amdgcn_readfirstlane(x); }
DI float bf2f(unsigned u) { return __uint_as_float(u << 16); }
DI float bflo(unsigned u) { return __uint_as_float(u << 16); }
DI float bfhi(unsigned u) { return __uint_as_float(u & 0xffff0000u); }
DI unsigned pk2(float lo, float hi) { f32x2 v = {lo, hi}; return __builtin_bit_cast(unsigned, __builtin_convertvector(v, bf16x2)); }
DI unsigned nsent(unsigned x) { return __builtin_elementwise_min(x, SENT - 1); }
DI unsigned umx(unsigned a, unsigned b) { return __builtin_elementwise_max(a, b); }
DI float rbf(float f) { return bflo(pk2(f, f) & 0xffffu); }
DI bf16x8 mk8(unsigned a, unsigned b, unsigned c, unsigned d) { v4u v = {a, b, c, d}; return __builtin_bit_cast(bf16x8, v); }
DI void dma16(unsigned ldsaddr, unsigned voff, rsrc_t rs, unsigned soff) {
    asm volatile("s_mov_b32 m0, %0\n\ts_nop 0\n\tbuffer_load_dwordx4 %1, %2, %3 offen lds" ::"s"(ldsaddr), "v"(voff), "s"(rs), "s"(soff) : "memory");
}
DI void dma16x4(unsigned ldsaddr, const unsigned* v, rsrc_t rs, unsigned soff) {
    asm volatile("s_mov_b32 m0, %0\n\ts_nop 0\n\tbuffer_load_dwordx4 %1, %5, %6 offen lds\n\tbuffer_load_dwordx4 %2, %5, %6 offen offset:1024 lds\n\t"
                 "buffer_load_dwordx4 %3, %5, %6 offen offset:2048 lds\n\tbuffer_load_dwordx4 %4, %5, %6 offen offset:3072 lds"
                 ::"s"(ldsaddr), "v"(v[0]), "v"(v[1]), "v"(v[2]), "v"(v[3]), "s"(rs), "s"(soff) : "memory");
}
DI void touch(unsigned voff, rsrc_t rs, unsigned soff) { asm volatile("buffer_load_dword v255, %0, %1, %2 offen" ::"v"(voff), "s"(rs), "s"(soff) : "memory", "v255"); }
DI v4u ld16_asm(unsigned voff, rsrc_t rs, unsigned soff) {
    v4u r; asm volatile("buffer_load_dwordx4 %0, %1, %2, %3 offen" : "=v"(r) : "v"(voff), "s"(rs), "s"(soff) : "memory"); return r;
}
#define PIN(x) asm volatile("" : "+v"(x))
DI s16x4 tr_read(unsigned a) { return __builtin_amdgcn_ds_read_tr16_b64_v4i16((LAS s16x4*)(a)); }
DI bf16x8 cat8(s16x4 a, s16x4 b) { return __builtin_shufflevector(a, b, 0, 1, 2, 3, 4, 5, 6, 7); }
DI bf16x8 lds128(unsigned a) { return *(const LAS bf16x8*)(a); }
DI f32x4 ldsf4(unsigned a) { return *(const LAS f32x4*)(a); }
DI float ldsf(unsigned a) { return *(const LAS float*)(a); }
DI unsigned ldsu(unsigned a) { return *(const LAS unsigned*)(a); }
DI void stsf4(unsigned a, f32x4 v) { *(LAS f32x4*)(a) = v; }
DI void stsf(unsigned a, float v) { *(LAS float*)(a) = v; }
DI void stsu(unsigned a, unsigned v) { *(LAS unsigned*)(a) = v; }
template <int C> DI float dppf(float x) { return __uint_as_float(__builtin_amdgcn_update_dpp(0, __float_as_uint(x), C, 0xf, 0xf, true)); }
template <int C> DI unsigned dppu(unsigned x) { return __builtin_amdgcn_update_dpp(0, x, C, 0xf, 0xf, true); }
DI float max2f(float a, float b) { float r; asm("v_max_f32_e32 %0, %1, %2" : "=v"(r) : "v"(a), "v"(b)); return r; }
DI float max3f(float a, float b, float c) { float r; asm("v_max3_f32 %0, %1, %2, %3" : "=v"(r) : "v"(a), "v"(b), "v"(c)); return r; }
DI float row_sum(float x) { x += dppf<0x128>(x); x += dppf<0x124>(x); x += dppf<0x122>(x); x += dppf<0x121>(x); return x; }
DI float xrow_sum(float x) {
    auto a = __builtin_amdgcn_permlane16_swap(__float_as_uint(x), __float_as_uint(x), false, false);
    x = __uint_as_float(a[0]) + __uint_as_float(a[1]);
    auto b = __builtin_amdgcn_permlane32_swap(__float_as_uint(x), __float_as_uint(x), false, false);
    return __uint_as_float(b[0]) + __uint_as_float(b[1]);
}
DI float wave_sum(float x) { return xrow_sum(row_sum(x)); }
DI unsigned wave_maxu(unsigned x) {
    x = umx(x, dppu<0x128>(x)); x = umx(x, dppu<0x124>(x)); x = umx(x, dppu<0x122>(x)); x = umx(x, dppu<0x121>(x));
    auto a = __builtin_amdgcn_permlane16_swap(x, x, false, false);
    x = umx(a[0], a[1]);
    auto b = __builtin_amdgcn_permlane32_swap(x, x, false, false);
    return umx(b[0], b[1]);
}
DI float nmax3f(float a, float b, float c) { float r; asm("v_max3_f32 %0, -%1, -%2, -%3" : "=v"(r) : "v"(a), "v"(b), "v"(c)); return r; }
DI float nmax2f(float a, float b) { float r; asm("v_max_f32_e64 %0, -%1, -%2" : "=v"(r) : "v"(a), "v"(b)); return r; }
DI float mx2(float a, float b) { return __builtin_amdgcn_fmed3f(a, b, __builtin_inff()); }
template <bool MAX> DI float xrow_scatter(float a, float b, float c, float d) {
    const auto s = __builtin_amdgcn_permlane16_swap(__float_as_uint(a), __float_as_uint(b), false, false);
    const auto t = __builtin_amdgcn_permlane16_swap(__float_as_uint(c), __float_as_uint(d), false, false);
    const float u = MAX ? mx2(__uint_as_float(s[0]), __uint_as_float(s[1])) : __uint_as_float(s[0]) + __uint_as_float(s[1]);
    const float v = MAX ? mx2(__uint_as_float(t[0]), __uint_as_float(t[1])) : __uint_as_float(t[0]) + __uint_as_float(t[1]);
    const auto r = __builtin_amdgcn_permlane32_swap(__float_as_uint(u), __float_as_uint(v), false, false);
    return MAX ? mx2(__uint_as_float(r[0]), __uint_as_float(r[1])) : __uint_as_float(r[0]) + __uint_as_float(r[1]);
}
DI void xrow_gather(float w, float (&o)[4]) {
    const auto r = __builtin_amdgcn_permlane32_swap(__float_as_uint(w), __float_as_uint(w), false, false);
    const auto s = __builtin_amdgcn_permlane16_swap(r[0], r[0], false, false);
    const auto t = __builtin_amdgcn_permlane16_swap(r[1], r[1], false, false);
    o[0] = __uint_as_float(s[0]); o[1] = __uint_as_float(s[1]); o[2] = __uint_as_float(t[0]); o[3] = __uint_as_float(t[1]);
}
struct NoHook { DI void operator()(int, float&) const {} DI void operator()(int, float&, float&) const {} };
template <bool MUL, class H = NoHook> DI void softmax2(const f32x4 (&p)[2][2], float rs0, float rs1, bool hi, bf16x8 (&out)[4], H hook = H()) {
    float m[4];
#pragma unroll
    for (int i = 0; i < 2; i++) {
        const f32x4 &p0 = p[i][0], &p1 = p[i][1];
        m[2 * i] = max3f(max3f(p0[0], p0[1], p0[2]), max3f(p0[3], p1[0], p1[1]), max2f(p1[2], p1[3]));
        m[2 * i + 1] = max3f(nmax3f(p0[0], p0[1], p0[2]), nmax3f(p0[3], p1[0], p1[1]), nmax2f(p1[2], p1[3]));
    }
    asm volatile("s_nop 1" : "+v"(m[0]), "+v"(m[1]), "+v"(m[2]), "+v"(m[3]));
    hook(1, m[3]);
    const float rsr = hi ? rs1 : rs0;
    float a[4];
    xrow_gather(xrow_scatter<true>(m[0], m[1], m[2], m[3]) * (rsr * -LOG2E), a);
    f32x2 e[4][4];
    float s[4];
#pragma unroll
    for (int k = 0; k < 4; k++) {
        if (k == 2) hook(2, a[2], s[1]);
        const float sc = ((k >> 1) ? rs1 : rs0) * ((k & 1) ? -LOG2E : LOG2E);
#pragma unroll
        for (int j = 0; j < 4; j++) {
            const f32x4& q = p[k >> 1][j >> 1];
            const f32x2 t = __builtin_elementwise_fma((f32x2){q[2 * (j & 1)], q[2 * (j & 1) + 1]}, (f32x2){sc, sc}, (f32x2){a[k], a[k]});
            e[k][j] = (f32x2){__builtin_amdgcn_exp2f(t[0]), __builtin_amdgcn_exp2f(t[1])};
        }
        const f32x2 h = (e[k][0] + e[k][1]) + (e[k][2] + e[k][3]);
        s[k] = h[0] + h[1];
        if (k == 3) hook(3, s[k]);
    }
    float iv = __builtin_amdgcn_rcpf(xrow_scatter<false>(s[0], s[1], s[2], s[3]));
    if (MUL) iv *= rsr;
    xrow_gather(iv, a);
#pragma unroll
    for (int k = 0; k < 4; k++) {
        if (k == 2) hook(4, a[2]);
        const f32x2 c = {a[k], a[k]};
        const f32x2 x0 = e[k][0] * c, x1 = e[k][1] * c, x2 = e[k][2] * c, x3 = e[k][3] * c;
        out[k] = mk8(pk2(x0[0], x0[1]), pk2(x1[0], x1[1]), pk2(x2[0], x2[1]), pk2(x3[0], x3[1]));
    }
}
DI void swap16(v4u& x) {
    const auto a = __builtin_amdgcn_permlane16_swap(x[0], x[2], false, false), b = __builtin_amdgcn_permlane16_swap(x[1], x[3], false, false);
    x = (v4u){a[0], b[0], a[1], b[1]};
}
DI float dot2(unsigned a, unsigned b, float c) {
    return __builtin_amdgcn_fdot2_f32_bf16(__builtin_bit_cast(bf16x2, a), __builtin_bit_cast(bf16x2, b), c, false);
}
DI void st_gran(char* ws, unsigned off, unsigned tag, unsigned val) {
    __hip_atomic_store((gu64*)(ws + off), ((u64)tag << 32) | val, RLX_AGENT);
}
DI v2u ldg(rsrc_t rs, unsigned voff, unsigned soff) { return __builtin_amdgcn_raw_buffer_load_b64(rs, voff, soff, AUX_SC1); }
#define RARE(c) __builtin_expect(!!(c), 0)
#define FRESH() asm volatile("" ::: "memory")
template <int N> DI void sweep(rsrc_t rs, unsigned voff, unsigned soff, unsigned S, unsigned tag, unsigned (&v)[N]) {
#pragma nounroll
    for (unsigned spins = 0;;) {
        unsigned bad = 0;
        FRESH();
#pragma unroll
        for (int k = 0; k < N; ++k) { const v2u x = ldg(rs, voff, soff + k * S); v[k] = x[0]; bad |= x[1] ^ tag; }
        if (__all(bad == 0)) break;
        if (++spins > SPIN_CAP) break;
        __builtin_amdgcn_s_sleep(1);
    }
}
DI unsigned wmax(unsigned x) { return x; }
DI unsigned wmax(v2u x) { return umx(x[0], x[1]); }
DI unsigned wmax(v4u x) { return umx(umx(x[0], x[1]), umx(x[2], x[3])); }
template <int N, int SL = 0, class T, class F> DI void sweepz(F ld, T (&v)[N]) {
#pragma nounroll
    for (unsigned spins = 0;;) {
        FRESH();
        unsigned m = 0;
#pragma unroll
        for (int k = 0; k < N; ++k) v[k] = ld(k);
#pragma unroll
        for (int k = 0; k < N; ++k) m = umx(m, wmax(v[k]));
        if (__all(m != SENT)) break;
        if (++spins > SPIN_CAP) break;
        if (SL) __builtin_amdgcn_s_sleep(SL);
    }
}
DI f32x4 f4(v4u x) { return __builtin_bit_cast(f32x4, x); }
DI bool anynan(float x) { return __builtin_amdgcn_ballot_w64(x != x) != 0; }
DI void dot2c(float& c, unsigned a, unsigned b) { asm volatile("v_dot2c_f32_bf16 %0, %1, %2" : "+v"(c) : "v"(a), "v"(b)); }
DI void fold8(float& a, float b) {
    asm volatile("v_add_f32_dpp %0, %0, %0 row_ror:8 row_mask:0xf bank_mask:0x3\n\tv_add_f32_dpp %0, %1, %1 row_ror:8 row_mask:0xf bank_mask:0xc" : "+v"(a) : "v"(b));
}
template <int N, int SL, bool FAST, class T, class F, class S> DI void sweepn(F ld, T (&v)[N], S sum) {
    if (FAST) {
        FRESH();
#pragma unroll
        for (int k = 0; k < N; ++k) v[k] = ld(k);
        if (RARE(anynan(sum()))) { sweepz<N, SL>(ld, v); sum(); }
    } else { sweepz<N, SL>(ld, v); sum(); }
}
#define FM 7
DI unsigned f2s(float x) { return nsent(__float_as_uint(x)); }
#define OOB 0x4000000u
#define LD16(v, s) __builtin_amdgcn_raw_buffer_load_b128(rsWS, (v), (s), AUX_SC1)
#define LD4(v, s) __builtin_amdgcn_raw_buffer_load_b32(rsWS, (v), (s), AUX_SC1)
#define ST16(x, v, s) __builtin_amdgcn_raw_buffer_store_b128((x), rsWS, (v), (s), AUX_SC1)
#define ST4(x, v, s) __builtin_amdgcn_raw_buffer_store_b32((x), rsWS, (v), (s), AUX_SC1)
#define SLEEP(n) __builtin_amdgcn_s_sleep(n)
DI void poll_flags(rsrc_t rs, unsigned v, unsigned off, v2u want) {
    v2u x; unsigned n = SPIN_CAP;
    asm volatile("1:\n\tbuffer_load_dwordx2 %0, %2, %3, %4 offen sc1\n\ts_sub_u32 %1, %1, 1\n\ts_cbranch_scc1 2f\n\ts_waitcnt vmcnt(0)\n\t"
                 "v_cmp_ne_u64 vcc, %0, %5\n\ts_cbranch_vccnz 1b\n2:\n\ts_waitcnt vmcnt(0)" : "=&v"(x), "+s"(n) : "v"(v), "s"(rs), "s"(off), "v"(want) : "vcc", "scc", "memory");
}
DI void poll_words(unsigned& x, rsrc_t rs, unsigned v, unsigned off) {
    unsigned n = SPIN_CAP;
    asm volatile("s_branch 3f\n1:\n\tbuffer_load_dword %0, %2, %3, %4 offen sc1\n\ts_sub_u32 %1, %1, 1\n\ts_cbranch_scc1 2f\n3:\n\ts_waitcnt vmcnt(0)\n\t"
                 "v_cmp_eq_u32 vcc, -1, %0\n\ts_cbranch_vccnz 1b\n2:\n\ts_waitcnt vmcnt(0)" : "+v"(x), "+s"(n) : "v"(v), "s"(rs), "s"(off) : "vcc", "scc", "memory");
}
#define STAMPR(i, role)
#define STAMP(i)
#define STAMPW(i, ww)

#define STAMPW2(i, a, b) STAMPW(i, a); STAMPW(i, b)
#define RINIT()
#define RDUMP()
#define ARR_DECL
#define KATTR
#define ARR_MARK(i)
#define ARR_END()
#define HST(i)
template <class F, int... Is> DI void static_for_impl(F&& f, std::integer_sequence<int, Is...>) { (f(std::integral_constant<int, Is>{}), ...); }
template <int N, class F> DI void static_for(F&& f) { static_for_impl(f, std::make_integer_sequence<int, N>{}); }

DI s16x4 bq(unsigned b0, unsigned b1, int j, int i) {
    const int ni = (j + 2) % 6;
    return tr_read(((ni & 1) ? b1 : b0) + (ni >> 1) * 8192 + (i >> 1) * 4096 + (i & 1) * 512);
}
template <class F> DI void group(f32x4 (&acc)[4][6], const bf16x8 (&A)[2][4], const bf16x8 (&B)[2], int j, F gap) {
    const int ni = (j + 2) % 6;
#pragma unroll
    for (int i = 0; i < 8; i++) {
        const int mi = i >> 1, ks = i & 1;
        if (ni < 4) asm volatile("v_mfma_f32_16x16x32_bf16 %0, %1, %2, %0" : "+v"(acc[mi][ni]) : "v"(B[ks]), "v"(A[ks][mi]));
        else asm volatile("v_mfma_f32_16x16x32_bf16 %0, %1, %2, %0" : "+v"(acc[mi][ni]) : "v"(A[ks][mi]), "v"(B[ks]));
        gap(i);
        SCHED();
    }
}
DI void img_write2(const unsigned (&ia)[4], const bf16x8 (&f)[4]) {
#pragma unroll
    for (int k = 0; k < 4; k++) {
        const v4u u = __builtin_bit_cast(v4u, f[k]);
        *(LAS v2u*)(ia[2 * (k & 1)] + (k >> 1) * 2048) = (v2u){u[0], u[1]};
        *(LAS v2u*)(ia[2 * (k & 1) + 1] + (k >> 1) * 2048) = (v2u){u[2], u[3]};
    }
}
DI float lds_bf(unsigned a) { return bf2f(*(const LAS u16*)(a)); }
#define MFMA_SETTLE2(p) asm volatile("s_nop 7\n\ts_nop 1" : "+v"(p[0][0]), "+v"(p[0][1]), "+v"(p[1][0]), "+v"(p[1][1]))

__global__ KATTR __launch_bounds__(512, 2) void fused_kernel(const u16* __restrict__ X, const u16* __restrict__ WQKV, const u16* __restrict__ WPHI,
                                                       const u16* __restrict__ WO, const u16* __restrict__ WR, const u16* __restrict__ WG,
                                                       const u16* __restrict__ WU, const u16* __restrict__ WD, u16* __restrict__ OUT,
                                                       char* __restrict__ ws) {
    extern __shared__ __attribute__((aligned(16))) char lds[];
    u64 pc0; unsigned clen;
    const int tid = threadIdx.x, l = tid & 63;
    asm volatile("s_getpc_b64 %0\n.Lfk_beg:\n\ts_lshr_b32 %1, %3, 3\n\tv_and_b32 v255, 0x1e0, %2\n\tv_add_lshl_u32 v255, v255, %1, 7\n\ts_mov_b32 %1, .Lfk_end-.Lfk_beg\n\t"
                 "v_min_u32 v255, %1, v255\n\tglobal_load_dword v255, v255, %0" : "=&s"(pc0), "=&s"(clen) : "v"(tid), "s"(blockIdx.x) : "v255", "scc");
    SCHED();
    const unsigned w = rfl(tid >> 6);
    const unsigned bid = blockIdx.x, xcd = (bid & 3) * 2 + ((bid >> 2) & 1), idx = bid >> 3;
    const unsigned rt = (xcd & 1) * 8 + (idx & 7), hp = (xcd >> 1) * 4 + (idx >> 3);
    const unsigned rt2 = (bid & 6) * 2 + (idx & 3), ct = (bid & 1) * 8 + (idx >> 2);
    const unsigned hq = (hp + 4) & 15;
    const unsigned rot = (rt >> 2) * 4;
    const unsigned rot2 = (2 * rt2 + 4 * ct) & 31;
    const unsigned ldsb = (unsigned)(size_t)lds;
    const int g = l >> 4, q4 = (l & 15) >> 2, p4 = l & 3, t16 = l & 15;
    const unsigned kr = l >> 3, gx = (g & 1) * 2 + (g >> 1);
    asm volatile("" ::"s"(X), "s"(WQKV), "s"(WPHI), "s"(WO), "s"(WR), "s"(WG), "s"(WU), "s"(WD), "s"(OUT), "s"(ws));
    unsigned tag;
#define READ_TAG() do { SCHED(); tag = *(const volatile CAS unsigned*)(ws + WS_EPOCH) + 1u; asm volatile("" : "+s"(tag)); } while (0)
    const rsrc_t rsWS = __builtin_amdgcn_make_buffer_rsrc((void*)ws, 0, WS_USED, 0x00020000);
    const rsrc_t rsX = __builtin_amdgcn_make_buffer_rsrc((void*)X, 0, 2048 * 1024 * 2, 0x00020000);
    const rsrc_t rsW = __builtin_amdgcn_make_buffer_rsrc((void*)WQKV, 0, 1024 * 3072 * 2, 0x00020000);
    const unsigned vA = (kr * 1024 + (((l & 7) ^ kr) * 8)) * 2;
    const unsigned vB0 = (kr * 3072 + (((l & 7) ^ (((kr >> 1) & 1) << 1)) * 8)) * 2;
    const unsigned vB1 = (kr * 3072 + (((l & 7) ^ ((((kr >> 1) & 1) | 2) << 1)) * 8)) * 2;
    const unsigned srow1 = rt * 128 + hp * 8 + (w & 3) * 2;
    v4u xr[4];
    if (w < 4) {
#pragma unroll
        for (int i = 0; i < 4; i++) xr[i] = ld16_asm(l * 16, rsX, srow1 * 2048 + i * 1024);
    }
    {
        const unsigned vBw = (w & 1) ? vB1 : vB0;
#pragma unroll
        for (int i = 0; i < 2; i++) dma16(ldsb + (w * 2 + i) * 1024, vA, rsX, (rt * 128 + w * 16 + i * 8) * 2048 + rot * 128);
#pragma unroll
        for (int i = 0; i < 3; i++) dma16(ldsb + 16384 + i * 8192 + w * 1024, vBw, rsW, (rot * 64 + w * 8) * 6144 + (i ? hp : hq) * 128 + i * 2048);
    }
    const f32x4 zero4 = {0.f, 0.f, 0.f, 0.f};
    ARR_DECL;
    const bf16x8 ones = mk8(0x3F803F80u, 0x3F803F80u, 0x3F803F80u, 0x3F803F80u);
    const unsigned lw = w & 3;
    const bool ffn = (ct & 7) != 3;
    const unsigned fb = !ffn ? 255u : (bid & 7) * 28 + (idx < 12 ? idx : idx - 4);
    const unsigned cg = (fb + 5) & 15, kc = fb >> 4;
    const unsigned kq = fb / 56, cg1 = fb % 56, cg1u = (cg1 + 3) % 56;
    v4u wreg[24];
    RINIT();
    STAMPW2(0, 0, 4);

    const rsrc_t rsWO = __builtin_amdgcn_make_buffer_rsrc((void*)WO, 0, 1024 * 1024 * 2, 0x00020000);
    const unsigned vWO = kr * 2048 + (((l & 7) ^ (((kr >> 1) & 3) << 1)) * 16);
    unsigned vwo[4];
    auto wo_so = [&](unsigned b) { return (((2 * b & 30) + (b >> 4) + rot2) & 31) * 65536 + ct * 128; };
    const unsigned fw = (((t16 >> 1) & 3) << 2) | (((t16 >> 3) & 1) << 1) | (t16 & 1);
    unsigned ad;
    unsigned ia[4];
    bf16x8 sdv[4];
    auto pre_addr = [&]() {
#pragma unroll
        for (int k = 0; k < 4; k++) { ia[k] = ldsb + L_XB + lw * 8192 + (w >> 2) * 4096 + t16 * 128 + (((4 * k + g) ^ fw) * 8); PIN(ia[k]); }
        const unsigned rr = 4 * g + q4, fr = (((rr >> 1) & 3) << 2) | (((rr >> 3) & 1) << 1) | (rr & 1);
        ad = ldsb + L_XB + (w & 1) * 16384 + rr * 128 + ((((w >> 1) * 4 + p4) ^ fr) * 8);
        PIN(ad);
    };
    auto sd_reads = [&]() {
        unsigned sa = ldsb + L_SD + g * 8;
        PIN(sa);
#pragma unroll
        for (int s = 0; s < 4; s++) {
            const v2u a = *(const LAS v2u*)(sa + s * 64), b = *(const LAS v2u*)(sa + s * 64 + 32);
            sdv[s] = mk8(a[0], a[1], b[0], b[1]);
        }
    };
    bf16x8 v0[4], v1[4];
    auto v_reads = [&]() {
        unsigned vd = ldsb + L_V + (w & 1) * 8192 + l * 16;
        PIN(vd);
#pragma unroll
        for (int s = 0; s < 4; s++) { v0[s] = lds128(vd + s * 2048); v1[s] = lds128(vd + s * 2048 + 1024); }
    };
    auto kv_tile = [&](auto hook6) {
        const unsigned h2 = w & 1, ft = w >> 1;
        WAITL0();
        ARR_MARK(18);
        BAR();
        SCHED();
        bf16x8 pf[4];
#pragma unroll
        for (int s = 0; s < 4; s++) pf[s] = cat8(tr_read(ad + s * 4096), tr_read(ad + s * 4096 + 2048));
        SCHED();
        hook6();
        SCHED();
        f32x4 k0 = zero4, k1 = zero4, kz = zero4;
#pragma unroll
        for (int s = 0; s < 4; s++) { k0 = MFMA(pf[s], v0[s], k0); k1 = MFMA(pf[s], v1[s], k1); }
#pragma unroll
        for (int s = 0; s < 4; s++) kz = MFMA(pf[s], sdv[s], kz);
        const unsigned pbase = WS_P2 + (hp * 16 + rt) * P2_STRIDE;
        const v4u o0 = {nsent(pk2(k0[0], k0[1])), nsent(pk2(k0[2], k0[3])), nsent(pk2(k1[0], k1[1])), nsent(pk2(k1[2], k1[3]))};
        __builtin_amdgcn_raw_buffer_store_b128(o0, rsWS, l * 16, pbase + h2 * 4096 + ft * 1024, AUX_SC1);
        asm volatile("s_nop 1" :: "v"(o0));
        __builtin_amdgcn_raw_buffer_store_b64((v2u){nsent(pk2(kz[0], kz[1])), nsent(pk2(kz[2], kz[3]))}, rsWS, t16 == 0 ? g * 8 : OOB,
                                              pbase + 8192 + h2 * 128 + ft * 32, AUX_SC1);
        SCHED();
        ARR_MARK(19);
        BAR();
    };

    v4u pp[2];
    auto red = [&]() {
        const v4u* p = pp;
        float lo = xrow_scatter<false>(bflo(p[0][0]) + bflo(p[1][0]), bflo(p[0][1]) + bflo(p[1][1]), bflo(p[0][2]) + bflo(p[1][2]), bflo(p[0][3]) + bflo(p[1][3]));
        float hi = xrow_scatter<false>(bfhi(p[0][0]) + bfhi(p[1][0]), bfhi(p[0][1]) + bfhi(p[1][1]), bfhi(p[0][2]) + bfhi(p[1][2]), bfhi(p[0][3]) + bfhi(p[1][3]));
        lo += dppf<0x128>(lo); hi += dppf<0x128>(hi);
        return nsent(pk2(lo, hi));
    };
    auto pp_so = [&](int k) { return WS_P2 + (hp * 16 + k * 8) * P2_STRIDE + (rt >> 2) * 2048 + (lw & 1) * 1024 + (rt & 3) * 256 + (lw >> 1) * 128; };
#define SR1 1
#define SR2 4
    auto red_issue = [&]() {
        const unsigned vpp = kr * P2_STRIDE + (l & 7) * 16;
#pragma unroll
        for (int k = 0; k < 2; k++) asm volatile("buffer_load_dwordx4 %0, %1, %2, %3 offen sc1" : "=v"(pp[k]) : "v"(vpp), "s"(rsWS), "s"(pp_so(k)) : "memory");
        SCHED();
    };
    auto kv_reduce = [&]() {
        waitv<5>();
        PIN(pp[0]); PIN(pp[1]);
        if (!__all(umx(wmax(pp[0]), wmax(pp[1])) != SENT)) sweepz<2, SR1>([&](int k) { return LD16(kr * P2_STRIDE + (l & 7) * 16, pp_so(k)); }, pp);
        STAMPW(25, 0);
        ST4(red(), (l & 8) ? OOB : (l & 7) * 16 + g * 4, WS_G3 + (hp * 4 + (rt >> 2)) * 2048 + (lw & 1) * 1024 + (rt & 3) * 256 + (lw >> 1) * 128);
    };

    const unsigned half = w >> 2;
    const unsigned R2 = rt2 * 128 + lw * 32 + half * 16 + t16;

    auto dloads = [&](int i0, int i1) {
        const rsrc_t rsD = __builtin_amdgcn_make_buffer_rsrc((void*)WD, 0, 1024 * 3584 * 2, 0x00020000);
#pragma unroll
        for (int i = i0; i < i1; i++)
            wreg[16 + i] = __builtin_amdgcn_raw_buffer_load_b128(rsD, kr * 4096 + (l & 7) * 16, (kc * 256 + lw * 64 + (i >> 1) * 16 + (i & 1)) * 2048 + cg * 128, W_AUX);
        FRESH();
    };
    unsigned wp1[2][4][8];
    v4u xres[2], wrv[2];
    v2u pl;
    unsigned nh;
    unsigned so, va0, va1, wb[4], vFl, sFl, swc, sG5, sG5r, sRep, nr, aXw, aXr, vG5;
    f32x4 acc2[2][4];
    bf16x8 sop[2];
    unsigned pv;
    auto place = [&]() {
        SCHED();
        PIN(pl);
        if (RARE(rfl(pl[1]) != tag)) { unsigned c[1]; sweep<1>(rsWS, 0, WS_PR + 1024, 0, tag, c); pl[0] = c[0]; }
        pv = rfl(pl[0]);
        nh = ((bid ^ (pv >> 4)) & 4) * 2048;
        asm volatile("" : "+s"(pv), "+s"(nh));
    };
    auto prep2 = [&]() {
        SCHED();
        vFl = t16 * 128 + g * 8;
        sFl = WS_F4 + (rt2 & 12) * 4096 + (rt2 & 3) * 2048 + nh;
        va0 = WS_P4 + ((rt2 & 7) * 8 + lw * 2 + half) * 65536 + (nh ^ (~rt2 & 4) * 2048) + l * 16; va1 = va0 ^ 65536;
        so = (half + rot2 + (rot2 & 24)) * 1024;
#pragma unroll
        for (int nt = 0; nt < 4; nt++) { wb[nt] = ldsb + L_WO + half * 65536 + (4 * g + q4) * 128 + ((nt ^ ((2 * g + (q4 >> 1)) & 3)) * 32) + p4 * 8; PIN(wb[nt]); }
        swap16(xres[0]); swap16(xres[1]); swap16(wrv[0]); swap16(wrv[1]);
        asm volatile("s_getreg_b32 %0, hwreg(HW_REG_XCC_ID, 0, 3)" : "=s"(sRep));
        sRep ^= pv & 4;
        nr = (sRep & 4) * 2048;
        sRep = WS_GK + (sRep + (idx & 1) * 8) * 2048;
        sG5r = WS_G5 + (rt2 & 7) * 65536 + lw * 16384 + half * 4096 + nh;
        sG5 = sG5r + ct * 256;
        aXw = ldsb + L_XB + w * 4096 + l * 16; aXr = aXw ^ 16384; vG5 = g == q4 ? t16 * 16 : OOB;
        asm volatile("" : "+s"(sG5), "+s"(sG5r), "+s"(sRep), "+s"(nr), "+s"(sFl), "+s"(nh), "+s"(so));
        PIN(aXw); PIN(aXr); PIN(vG5); PIN(va0); PIN(va1); PIN(vFl);
#pragma unroll
        for (int nt = 0; nt < 4; nt++) {
            const unsigned x0 = xres[nt >> 1][2 * (nt & 1)], x1 = xres[nt >> 1][2 * (nt & 1) + 1];
            acc2[0][nt] = (f32x4){bflo(x0), bfhi(x0), bflo(x1), bfhi(x1)};
            PIN(acc2[0][nt]);
        }
        unsigned m0 = p4 == 0 ? 0x3F803F80u : 0u, m1 = p4 == 1 ? ~0u : 0u;
        PIN(m0); PIN(m1);
#pragma unroll
        for (int i = 0; i < 2; i++) {
            v4u u;
#pragma unroll
            for (int j = 0; j < 4; j++) u[j] = (wrv[i][j] & m1) | m0;
            sop[i] = __builtin_bit_cast(bf16x8, u);
            PIN(sop[i]);
        }
        float sw = 0.f;
#pragma unroll
        for (int i = 0; i < 8; i++) sw = dot2(wrv[i >> 2][i & 3], 0x3F803F80u, sw);
        swc = f2s(xrow_sum(sw));
        PIN(swc);
        SCHED();
    };
    auto res_loads = [&]() {
        pl = ldg(rsWS, 0, WS_PR + 1024);
#pragma unroll
        for (int i = 0; i < 2; i++) {
            xres[i] = *(const v4u*)(X + (size_t)R2 * 1024 + ct * 64 + i * 32 + 8 * gx);
            wrv[i] = *(const v4u*)(WR + ct * 64 + i * 32 + 8 * gx);
        }
        FRESH();
    };

    if (w < 4) {
        const unsigned wm = w & 1, hh = w >> 1;
        f32x4 acc[4][6];
        bf16x8 wf[2], wfq[2];
        float mbt[4], rst[4], csv[2];
        f32x4 mbv[4], csq[2], csk[2];
#pragma unroll
        for (int i = 0; i < 4; i++)
#pragma unroll
            for (int j = 0; j < 6; j++) { acc[i][j] = zero4; PIN(acc[i][j]); }
        {
            const unsigned aoff0 = ldsb + (wm * 64 + t16) * 128 + (((0 + g) ^ (l & 7)) * 16);
            const unsigned aoff1 = ldsb + (wm * 64 + t16) * 128 + (((4 + g) ^ (l & 7)) * 16);
            const unsigned sk = ((q4 >> 1) & 1) | ((g & 1) << 1);
            const unsigned boffA = ldsb + 16384 + (8 * g + q4) * 128 + (((2 * hh) ^ sk) * 32) + p4 * 8;
            const unsigned boffB = ldsb + 16384 + (8 * g + q4) * 128 + (((2 * hh + 1) ^ sk) * 32) + p4 * 8;
            bf16x8 A[2][4], B[3][2];
            s16x4 lo;
            unsigned a0[3], a1[3], bA[3], bB[3];
#pragma unroll
            for (int i = 0; i < 3; i++) {
                a0[i] = aoff0 + i * STAGE_B; a1[i] = aoff1 + i * STAGE_B; bA[i] = boffA + i * STAGE_B; bB[i] = boffB + i * STAGE_B;
                PIN(a0[i]); PIN(a1[i]); PIN(bA[i]); PIN(bB[i]);
            }
            unsigned aT = ldsb + L_MB + (wm * 64 + t16) * 4, aS = ldsb + L_SD + (wm * 64 + t16) * 2, aM = ldsb + L_MB + wm * 256 + g * 16;
            unsigned aC = ldsb + L_CS + hh * 128 + g * 16, aV = ldsb + L_CS + (128 + hh * 32 + t16) * 4;
            unsigned aW = ldsb + L_WPHI + hh * 2048 + (4 * g + q4) * 64 + p4 * 8, aK = ldsb + L_KH + w * 2048 + l * 16;
            PIN(aT); PIN(aS); PIN(aM); PIN(aC); PIN(aV); PIN(aW); PIN(aK);
            pre_addr();
            READ_TAG();
            waitv<5>();
            PIN(xr[0]); PIN(xr[1]); PIN(xr[2]); PIN(xr[3]);
            {
                float s[2], q[2], x0[2];
#pragma unroll
                for (int rr = 0; rr < 2; rr++) {
                    x0[rr] = bflo(rfl(xr[2 * rr][0]));
                    s[rr] = 0.f; q[rr] = 0.f;
#pragma unroll
                    for (int i = 0; i < 4; i++) {
                        const float a = bflo(xr[2 * rr][i]) - x0[rr], b = bfhi(xr[2 * rr][i]) - x0[rr];
                        const float c = bflo(xr[2 * rr + 1][i]) - x0[rr], d = bfhi(xr[2 * rr + 1][i]) - x0[rr];
                        s[rr] += (a + b) + (c + d);
                        q[rr] += (a * a + b * b) + (c * c + d * d);
                    }
                }
                const float sq = row_sum(xrow_scatter<false>(s[0], s[1], q[0], q[1]));
#define RLF(x, n) __uint_as_float(__builtin_amdgcn_readlane(__float_as_uint(x), (n)))
                const float r0 = RLF(sq, 0), r1 = RLF(sq, 16), r2 = RLF(sq, 32), r3 = RLF(sq, 48);
                const float sm = (l ? r1 : r0) * (1.0f / 1024.0f), qm = (l ? r3 : r2) * (1.0f / 1024.0f);
                const float var = fmaxf(qm - sm * sm, 0.f);
                if (l < 2) st_gran(ws, WS_G0 + (srow1 + l) * 8, tag, pk2((l ? x0[1] : x0[0]) + sm, sqrtf(rbf(rbf(var) + 1e-5f))));
            }
            waitv<1>();
            WAITL0();
            ARR_MARK(0);
            BAR();
            SCHED();
            STAMPW(1, 0);
            auto rdB = [&](int u, int j, int i) {
                if (i & 1) B[j % 3][i >> 1] = cat8(lo, bq(bA[u], bB[u], j, i)); else lo = bq(bA[u], bB[u], j, i);
            };
            auto rdA = [&](int u, int i) { A[i & 1][i >> 1] = lds128(((i & 1) ? a1[u] : a0[u]) + (i >> 1) * 2048); };
            auto head = [&](int u) {
                group(acc, A, B[0], 0, [&](int i) { rdB(u, 1 + (i >> 2), i & 3); });
                group(acc, A, B[1], 1, [&](int i) { if (i < 4) rdB(u, 3, i); });
                group(acc, A, B[2], 2, [&](int i) { if (i < 4) rdB(u, 4, i); });
                group(acc, A, B[0], 3, [&](int i) { if (i < 4) rdB(u, 5, i); });
                WL0();
            };
#pragma unroll
            for (int i = 0; i < 4; i++) rdB(0, 0, i);
#pragma unroll
            for (int i = 0; i < 8; i++) rdA(0, i);
            SCHED();
#pragma nounroll
            for (unsigned t = 0; t < 5; t++) {
#pragma unroll
                for (int u = 0; u < 3; u++) {
                    head(u);
                    ARR_MARK(1 + t * 3 + u);
                    BAR();
                    SCHED();
                    group(acc, A, B[1], 4, [&](int i) { if (i < 4) rdB((u + 1) % 3, 0, i); });
                    group(acc, A, B[2], 5, [&](int i) { rdA((u + 1) % 3, i); });
                }
            }
            unsigned kw[4][4];
            f32x2 kx;
            auto kpiece = [&](int n) {
                if (n > 0) { kw[((n - 1 >> 2) + 2) & 3][(n - 1) & 3] = pk2(kx[0], kx[1]); PIN(kw[((n - 1 >> 2) + 2) & 3][(n - 1) & 3]); }
                if (n < 16) {
                    const int mi = ((n >> 2) + 2) & 3, j = n & 3;
                    const f32x4 &c = acc[mi][2 + (j >> 1)], &s_ = csk[j >> 1];
                    float x0 = __builtin_fmaf(mbt[mi], s_[2 * (j & 1)], c[2 * (j & 1)]); PIN(x0);
                    float x1 = __builtin_fmaf(mbt[mi], s_[2 * (j & 1) + 1], c[2 * (j & 1) + 1]); PIN(x1);
                    kx = (f32x2){x0, x1};
                }
                if (n == 5 || n == 9) *(LAS v4u*)(aK + (n == 9) * 1024) = (v4u){kw[2 + (n == 9)][0], kw[2 + (n == 9)][1], kw[2 + (n == 9)][2], kw[2 + (n == 9)][3]};
            };
            group(acc, A, B[0], 0, [&](int i) { rdB(0, 1 + (i >> 2), i & 3); });
            group(acc, A, B[1], 1, [&](int i) {
                if (i < 4) { rdB(0, 3, i); mbt[(i + 2) & 3] = ldsf(aT + ((i + 2) & 3) * 64); }
                else if (i < 6) csk[i - 4] = ldsf4(aC + 256 + (i - 4) * 64);
                else wf[i - 6] = cat8(tr_read(aW + (i - 6) * 32), tr_read(aW + (i - 6) * 32 + 1024));
            });
            group(acc, A, B[2], 2, [&](int i) { if (i < 4) rdB(0, 4, i); else mbv[i - 4] = ldsf4(aM + (i - 4) * 64); kpiece(i); });
            group(acc, A, B[0], 3, [&](int i) { if (i < 4) rdB(0, 5, i); if (i == 2 || i == 3) csv[i - 2] = ldsf(aV + (i - 2) * 64); kpiece(8 + i); });
            WL0();
            ARR_MARK(16);
            BAR();
            SCHED();
            kpiece(16);
            f32x4 p[2][2];
#pragma unroll
            for (int i = 0; i < 4; i++) p[i >> 1][i & 1] = MFMA(wf[i & 1], mk8(kw[i >> 1][0], kw[i >> 1][1], kw[i >> 1][2], kw[i >> 1][3]), zero4);
            SCHED();
            STAMPW(2, 0);
#pragma unroll
            for (int mi = 0; mi < 4; mi++) rst[mi] = lds_bf(aS + mi * 32);
#pragma unroll
            for (int a = 0; a < 2; a++)
#pragma unroll
                for (int dt = 0; dt < 2; dt++) {
                    const f32x4 x0 = acc[2 * a][4 + dt] + mbv[2 * a] * csv[dt], x1 = acc[2 * a + 1][4 + dt] + mbv[2 * a + 1] * csv[dt];
                    *(LAS v4u*)(aK + (L_V - L_KH) + w * 2048 + (a * 2 + dt) * 1024) = (v4u){pk2(x0[0], x0[1]), pk2(x0[2], x0[3]), pk2(x1[0], x1[1]), pk2(x1[2], x1[3])};
                }
            SCHED();
            rst[0] = __builtin_amdgcn_rcpf(rst[0]); rst[1] = __builtin_amdgcn_rcpf(rst[1]);
            bf16x8 f[4];
            struct HookC {
                decltype(v_reads)& vr; decltype(sd_reads)& sr;
                DI void operator()(int k, float& x) const { if (k == 1) { PIN(x); WAITL0(); ARR_MARK(17); BAR(); PIN(x); } if (k == 3) { PIN(x); vr(); sr(); PIN(x); } }
                DI void operator()(int, float&, float&) const {}
            };
            softmax2<true>(p, rst[0], rst[1], g >= 2, f, HookC{v_reads, sd_reads});
            img_write2(ia, f);
#pragma unroll
            for (int ft = 0; ft < 2; ft++) wfq[ft] = cat8(tr_read(aW + ft * 32 + 4096), tr_read(aW + ft * 32 + 5120));
            csq[0] = ldsf4(aC); csq[1] = ldsf4(aC + 64);
            STAMPW(3, 0);
            kv_tile([]() {});
            STAMPW(4, 0);
            SCHED();
            group(acc, A, B[1], 4, [&](int i) { if (i == 2 || i == 3) { rst[i] = __builtin_amdgcn_rcpf(rst[i]); PIN(rst[i]); } });
            group(acc, A, B[2], 5, [&](int) {});
            asm volatile("s_nop 7\n\ts_nop 1");
            SCHED();
        }
        bf16x8 phq[2][4];
        v4u kg[6];
        auto kg_ld = [&](int k) { return k < 4 ? LD16(l * 16, WS_G3 + (hq * 2 + hh) * 4096 + k * 1024) : LD16(g * 16, WS_GZ + (hq * 2 + hh) * 128 + (k - 4) * 64); };
        {
            f32x4 pq[2][2][2];
#pragma unroll
            for (int mi = 0; mi < 4; mi++) {
                const f32x4 x0 = acc[mi][0] + mbt[mi] * csq[0], x1 = acc[mi][1] + mbt[mi] * csq[1];
                const bf16x8 qf = mk8(pk2(x0[0], x0[1]), pk2(x0[2], x0[3]), pk2(x1[0], x1[1]), pk2(x1[2], x1[3]));
                pq[mi >> 1][mi & 1][0] = MFMA(wfq[0], qf, zero4); pq[mi >> 1][mi & 1][1] = MFMA(wfq[1], qf, zero4);
            }
            MFMA_SETTLE2(pq[0]); MFMA_SETTLE2(pq[1]);
            softmax2<false>(pq[0], rst[0], rst[1], g >= 2, phq[0]);
#pragma unroll
            for (int i = 0; i < 4; i++) PIN(phq[0][i]);
            SCHED();
            red_issue();
            res_loads();
            SCHED();
            kv_reduce();
            SCHED();
            BAR();
            SCHED();
            softmax2<false>(pq[1], rst[2], rst[3], g >= 2, phq[1]);
#pragma unroll
            for (int i = 0; i < 4; i++) { PIN(phq[0][i]); PIN(phq[1][i]); }
            SCHED();
        }
        STAMPW(5, 0);
        STAMPW(26, 0);
        v2u fv = {tag ^ 0x5bd1e995u, tag};
        place();
        PIN(xres[0]); PIN(xres[1]); PIN(wrv[0]); PIN(wrv[1]);
        unsigned sF = WS_F4 + (rt & 12) * 4096 + (rt & 3) * 2048 + hq * 128 + w * 8 + nh, vF = l == 0 ? 0 : OOB;
        {
            unsigned vO = l * 16, sO = WS_P4 + ((rt & 7) * 8 + wm * 4) * 65536 + (hq >> 2) * 16384 + ((hq & 3) * 2 + hh) * 1024 + (nh ^ (~rt & 4) * 2048);
            asm volatile("" : "+s"(sO), "+s"(sF));
            PIN(fv); PIN(vF);
#define S2N 1
            SLEEP(S2N);
            sweepz<6, SR2>(kg_ld, kg);
            STAMPW(6, 0);
            bf16x8 kvA[2][2], zA[2];
            v4u ov[4];
#pragma unroll
            for (int s = 0; s < 2; s++) {
#pragma unroll
                for (int dt = 0; dt < 2; dt++) kvA[s][dt] = mk8(kg[2 * s][2 * dt], kg[2 * s][2 * dt + 1], kg[2 * s + 1][2 * dt], kg[2 * s + 1][2 * dt + 1]);
                zA[s] = __builtin_bit_cast(bf16x8, kg[4 + s]);
            }
#define PH(mi, s) phq[(mi) >> 1][2 * ((mi) & 1) + (s)]
            f32x4 dn[4], n0[4], n1[4];
            float id[4];
#pragma unroll
            for (int mi = 0; mi < 4; mi++) dn[mi] = MFMA(zA[0], PH(mi, 0), zero4);
#pragma unroll
            for (int mi = 0; mi < 4; mi++) dn[mi] = MFMA(zA[1], PH(mi, 1), dn[mi]);
            SCHED();
#pragma unroll
            for (int mi = 0; mi <= 4; mi++) {
                if (mi < 4) {
                    n0[mi] = MFMA(kvA[0][0], PH(mi, 0), zero4); n1[mi] = MFMA(kvA[0][1], PH(mi, 0), zero4);
                    n0[mi] = MFMA(kvA[1][0], PH(mi, 1), n0[mi]); n1[mi] = MFMA(kvA[1][1], PH(mi, 1), n1[mi]);
                    id[mi] = __builtin_amdgcn_rcpf(rbf(rbf(dn[mi][0]) + 1e-6f));
                }
                if (mi > 0) {
                    const f32x4 a = n0[mi - 1] * id[mi - 1], b = n1[mi - 1] * id[mi - 1];
                    ov[mi - 1] = (v4u){pk2(a[0], a[1]), pk2(a[2], a[3]), pk2(b[0], b[1]), pk2(b[2], b[3])};
                    ST16(ov[mi - 1], vO + (mi - 1) * 65536, sO);
                }
                if (mi < 4) { SGB(0x8, 1); SGB(0x2, 6); SGB(0x8, 1); SGB(0x2, 3); SGB(0x8, 1); SGB(0x2, 3); SGB(0x8, 1); SGB(0x2, 4); }
                SCHED();
            }
#pragma unroll
            for (int mi = 0; mi < 4; mi++) PIN(ov[mi]);
        }
        waitv<0>();
        __builtin_amdgcn_raw_buffer_store_b64(fv, rsWS, vF, sF, AUX_SC1);
        STAMPW(7, 0);
    } else {
        const unsigned pw = lw & 1;
        const rsrc_t rsG = __builtin_amdgcn_make_buffer_rsrc((void*)WG, 0, 1024 * 3584 * 2, 0x00020000);
        const rsrc_t rsU = __builtin_amdgcn_make_buffer_rsrc((void*)WU, 0, 1024 * 3584 * 2, 0x00020000);
        unsigned vwl = kr * 57344 + (l & 7) * 16;
        unsigned sg = (kq * 256 + lw * 64) * 7168 + cg1 * 128, su = (kq * 256 + lw * 64) * 7168 + cg1u * 128;
        auto wloads = [&](int n0, int n1) {
#pragma unroll
            for (int n = n0; n < n1; n++)
                asm volatile("buffer_load_dwordx4 %0, %1, %2, %3 offen nt" : "=v"(wreg[n]) : "v"(vwl), "s"(n < 8 ? rsG : rsU), "s"((n < 8 ? sg : su) + (n & 7) * 7168));
        };
        auto dl_asm = [&]() {
            const rsrc_t rsD = __builtin_amdgcn_make_buffer_rsrc((void*)WD, 0, 1024 * 3584 * 2, 0x00020000);
#pragma unroll
            for (int i = 0; i < DN0; i++)
                asm volatile("buffer_load_dwordx4 %0, %1, %2, %3 offen nt" : "=v"(wreg[16 + i]) : "v"(kr * 4096 + (l & 7) * 16), "s"(rsD),
                             "s"((kc * 256 + lw * 64 + (i >> 1) * 16 + (i & 1)) * 2048 + cg * 128));
        };
        unsigned aK, aW, aS;
        auto k_pre = [&]() {
            pre_addr();
            aK = ldsb + L_KH + lw * 2048 + l * 16; aW = ldsb + L_WPHI + (lw >> 1) * 2048 + (4 * g + q4) * 64 + p4 * 8; aS = ldsb + L_SD + (pw * 64 + 32 + t16) * 2;
            PIN(aK); PIN(aW); PIN(aS);
        };
        auto kfeat = [&](auto hook) {
            bf16x8 wf[2];
            float rst[2];
#pragma unroll
            for (int ft = 0; ft < 2; ft++) {
                wf[ft] = cat8(tr_read(aW + ft * 32), tr_read(aW + ft * 32 + 1024));
                rst[ft] = __builtin_amdgcn_rcpf(lds_bf(aS + ft * 32));
            }
            sd_reads();
            PIN(wf[0]); PIN(wf[1]); PIN(rst[0]); PIN(rst[1]);
            SCHED();
            ARR_MARK(16);
            BAR();
            SCHED();
            const bf16x8 kf0 = lds128(aK), kf1 = lds128(aK + 1024);
            SCHED();
            hook(-1, rst[0]);
            hook(0, rst[0]);
            SCHED();
            f32x4 p[2][2];
            p[0][0] = MFMA(wf[0], kf0, zero4); p[0][1] = MFMA(wf[1], kf0, zero4);
            p[1][0] = MFMA(wf[0], kf1, zero4); p[1][1] = MFMA(wf[1], kf1, zero4);
            MFMA_SETTLE2(p);
            bf16x8 f[4];
            softmax2<true>(p, rst[0], rst[1], g >= 2, f, hook);
            img_write2(ia, f);
            SCHED();
            hook(5, rst[0]);
            SCHED();
            STAMPW2(3, 4, 7);
            kv_tile([&]() { hook(6, rst[0]); });
            STAMPW2(4, 4, 7);
        };
        auto wblk = [&](int k) { return k == 0 ? 22 + lw : k == 1 ? (lw < 3 ? 26 + lw : 4) : k < 6 ? 4 * k - 3 + lw : k == 6 ? lw : (lw < 3 ? 29 + lw : 21); };
        auto wo_init = [&]() {
#pragma unroll
            for (int i = 0; i < 4; i++) { vwo[i] = vWO + i * (16384 - 1024); PIN(vwo[i]); }
        };
        unsigned hs[8];
        auto wl_pre = [&]() { asm volatile("" : "+s"(sg), "+s"(su)); PIN(vwl); };
        auto wo_pre = [&]() {
            wo_init();
#pragma unroll
            for (int k = 0; k < 8; k++) { hs[k] = wo_so(wblk(k)); asm volatile("" : "+s"(hs[k])); }
        };
        auto wo_rng = [&](int k0, int k1) {
#pragma unroll
            for (int k = k0; k < k1; k++) dma16x4(ldsb + L_WO + wblk(k) * 4096, vwo, rsWO, hs[k]);
        };
        auto pf = [&](int i) {
            if (i == 0) { wloads(0, 6); SCHED(); if (lw < 2) wo_pre(); SCHED(); }
            if (i < 3 && !ffn) wo_rng(i, i + 1);
            if (i == 1 || i == 2) wloads(6 * i, i == 1 ? 12 : 16);
#define DHK 6
            if (i == DHK) dl_asm();
        };
        struct Hook {
            decltype(pf)& f; decltype(v_reads)& vr;
            DI void operator()(int k, float& x) const { PIN(x); if (k == 1) { ARR_MARK(17); BAR(); } if (k == 3) vr(); f(3 + k); PIN(x); }
            DI void operator()(int k, float& x, float& y) const { PIN(y); f(3 + k); PIN(x); }
        };
        auto tail16 = [&]() {
            kfeat(Hook{pf, v_reads});
            if (ffn) wo_rng(0, 3);
            wo_rng(3, 4);
            HST(27);
            if (ffn) waitv<18>(); else waitv<6>();
#pragma unroll
            for (int n = 0; n < 16 + DN0; n++) PIN(wreg[n]);
        };
        auto vT23 = [&]() {
            const unsigned n = (idx * 2 + pw) * 64 + l, r = n / 12, c = n % 12;
            return idx < 24 ? ((r & 127) + (r >> 7) * 256 + 128) * 6144 + (c >> 2) * 2048 + (((xcd >> 1) * 4 + (c >> 2 ? 0 : 4) + (c & 3)) & 15) * 128 : OOB;
        };
        if (lw < 2) {
            const unsigned sA = (rt * 128 + lw * 64) * 2048, sB = lw * 32 * 6144 + hp * 128, sBq = lw * 32 * 6144 + hq * 128;
            unsigned buf = 1;
            unsigned va[4], vb[4];
#pragma unroll
            for (int i = 0; i < 4; i++) { va[i] = vA + i * (16384 - 1024); vb[i] = ((i & 1) ? vB1 : vB0) + i * (49152 - 1024); PIN(va[i]); PIN(vb[i]); }
            auto stage = [&](unsigned s, unsigned bf, int i0, int i1) {
                const unsigned kt = (s + rot) & 15;
                const unsigned lb = ldsb + bf * STAGE_B;
                const unsigned sa = sA + kt * 128, sb = kt * 393216;
                if (i0 <= 0 && 0 < i1) dma16x4(lb + lw * 8192, va, rsX, sa);
                if (i0 <= 1 && 1 < i1) dma16x4(lb + lw * 8192 + 4096, va, rsX, sa + 65536);
                if (i0 <= 2 && 2 < i1) dma16x4(lb + 16384 + 8192 + lw * 4096, vb, rsW, sb + sB + 2048);
                if (i0 <= 3 && 3 < i1) dma16x4(lb + 16384 + 16384 + lw * 4096, vb, rsW, sb + sB + 4096);
                if (i0 <= 4 && 4 < i1) dma16x4(lb + 16384 + lw * 4096, vb, rsW, sb + sBq);
            };
            stage(1, 1, 0, 4);
            SCHED();
            unsigned vt = vT23();
            PIN(vt);
            wl_pre(); k_pre();
            waitv<16>(); ARR_MARK(0); BAR();
            stage(1, 1, 4, 5);
            touch(vt, rsW, (xcd & 1) * 512 * 6144);
            waitv<1>(); ARR_MARK(1); BAR();
            stage(2, 2, 0, 5); stage(3, 0, 0, 3);
            waitv<12>(); ARR_MARK(2); BAR();
            stage(3, 0, 3, 5); stage(4, 1, 0, 4);
            waitv<16>(); ARR_MARK(3); BAR();
            stage(4, 1, 4, 5);
            buf = 2;
#pragma nounroll
            for (unsigned s = 5; s < 16; s++) {
                if (s > 5) { waitv<20>(); ARR_MARK(s - 2); BAR(); }
                stage(s, buf, 0, 5);
                buf = buf == 2 ? 0 : buf + 1;
            }
            waitv<20>(); ARR_MARK(14); BAR();
            pf(0);
            READ_TAG();
            if (ffn) waitv<6>(); else waitv<10>();
            ARR_MARK(15); BAR();
            pf(1);
            tail16();
        } else {
            SCHED();
            unsigned v3;
            const unsigned h3 = (xcd >> 1) * 4 + 3, kv3 = !(xcd & 2), wv = idx * 2 + pw;
            auto line3 = [&](unsigned per, unsigned rows, auto rowf) {
                const unsigned n = wv * (kv3 ? per : (per + 2) / 3) + l, r3 = kv3 ? n / 3 : n, c3 = kv3 ? n % 3 : 0;
                return (idx < 24 && l < (kv3 ? per : (per + 2) / 3) && r3 < rows) ? rowf(r3) * 6144 + c3 * 2048 + ((c3 ? h3 : h3 + 4) & 15) * 128 : OOB;
            };
            v3 = line3(32, 512, [](unsigned r) { return r; });
            touch(line3(16, 256, [](unsigned r) { return (r & 127) + (r >> 7) * 256; }), rsW, (xcd & 1) * 512 * 6144);
            {
                const rsrc_t rsP = __builtin_amdgcn_make_buffer_rsrc((void*)WPHI, 0, 32 * 1024 * 2, 0x00020000);
#pragma unroll
                for (int i = 0; i < 4; i++) dma16(ldsb + L_WPHI + pw * 4096 + i * 1024, l * 16, rsP, (pw ? hq : hp) * 4096 + i * 1024);
            }
            v4u sv = {SENT, SENT, SENT, SENT};
            unsigned vc4 = l < 16 ? WS_G3 + (hp * 4 + (rt >> 2)) * 2048 + pw * 1024 + (rt & 3) * 256 + l * 16
                         : l < 24 ? WS_P2 + (hp * 16 + rt) * P2_STRIDE + 8192 + pw * 128 + (l - 16) * 16
                         : (l < 32 && pw == 0 && (rt & 7) == 0) ? WS_GZ + (hp * 2 + (rt >> 3)) * 128 + (l - 24) * 16
                         : l >= 56 ? WS_F4 + (rt & 12) * 4096 + (rt & 3) * 2048 + hq * 128 + pw * 8192 + (l & 7) * 16 : OOB;
            auto clears = [&](int i0, int i1) {
#pragma unroll
                for (int i = i0; i < i1; i++) {
                    if (i < 4) ST16(sv, l * 16, WS_P2 + (hp * 16 + rt) * P2_STRIDE + pw * 4096 + i * 1024);
                    if (i == 4) ST16(sv, vc4, 0);
                    if (i > 4) ST16(sv, l * 16, WS_TL + (bid * 4 + pw * 2 + i - 5) * 1024);
                }
                FRESH();
            };
            PIN(vc4); PIN(sv);
            unsigned ca = ldsb + 16384 + pw * 8192 + (8 * g + q4) * 128 + (((rt & 3) ^ (((q4 >> 1) & 1) | ((g & 1) << 1))) * 32) + p4 * 8;
            PIN(ca);
            bf16x8 cf[4];
            f32x4 cd[2] = {zero4, zero4};
            auto cs_read = [&](int st) {
#pragma unroll
                for (int i = 0; i < 4; i++) cf[i] = cat8(tr_read(ca + (st % 3) * STAGE_B + i * 4096), tr_read(ca + (st % 3) * STAGE_B + i * 4096 + 512));
                SCHED();
            };
            READ_TAG();
            waitv<5>();
            ARR_MARK(0);
            BAR();
            SCHED();
            cs_read(0);
            FRESH();
            const rsrc_t rsC = __builtin_amdgcn_make_buffer_rsrc((void*)pc0, 0, clen, 0x00020000);
            auto code_pf = [&]() { touch(l < 6 ? l * 128 : OOB, rsC, (idx * 2 + pw) * 768); };
            v4u gl[4];
            float s0 = 0.f, s1 = 0.f;
            unsigned bad;
            v2u gs, gc;
            unsigned vgc = WS_GC + (pw ? hq : hp) * 256 + (l & 31) * 8;
            unsigned lat[2] = {~0u, ~0u};
            unsigned vgl = (l < 48 ? l : 0) * 16;
            unsigned aw0 = ldsb + L_CS + pw * 384 + (vgl >> 1), aw1 = ldsb + L_MB + (pw * 64 + l) * 4, aw2 = ldsb + L_SD + (pw * 64 + l) * 2;
            static_for<15>([&](auto tc) {
                constexpr int t = decltype(tc)::value;
                WAITL0();
                ARR_MARK(t + 1);
                BAR();
                SCHED();
                if constexpr (t < 4) {
                    __builtin_amdgcn_s_setprio(2);
#pragma unroll
                    for (int i = 0; i < 4; i++) cd[i & 1] = MFMA(ones, cf[(i & 1) * 2 + (i >> 1)], cd[i & 1]);
                    SCHED();
                    __builtin_amdgcn_s_setprio(0);
                    if constexpr (t < 3) cs_read(t + 1);
                    else __builtin_amdgcn_raw_buffer_store_b64((v2u){__float_as_uint(g ? cd[1][0] : cd[0][0]), tag}, rsWS, (g == 1 || g + pw == 0) ? g * 512 + t16 * 8 : OOB,
                                                               WS_G1 + ((hp * 4 + (rt >> 2)) * 192 + pw * 64 + (rt & 3) * 16) * 8, AUX_SC1);
                }
                if constexpr (t == 0) touch(v3, rsW, (~xcd & 1) * 512 * 6144);
                if constexpr (t == 12) code_pf();
                if constexpr (t == 8) {
                    waitv<0>();
                    __builtin_amdgcn_raw_buffer_store_b64((v2u){0u, tag}, rsWS, l == 0 ? 0 : OOB, WS_GC + ((hp * 2 + pw) * 16 + rt) * 8, AUX_SC1);
                }
                if constexpr (t == 8 || t == 9) {
#pragma unroll
                    for (int r = (t - 8) * 2; r < (t - 8) * 2 + 2; r++)
                        asm volatile("buffer_load_dwordx4 %0, %1, %2, %3 offen sc1" : "=v"(gl[r]) : "v"(vgl), "s"(rsWS), "s"(WS_G1 + ((hp * 4 + r) * 192 + pw * 96) * 8) : "memory");
                }
                if constexpr (t == 6) {
                    asm volatile("buffer_load_dwordx2 %0, %1, %2, %3 offen sc1" : "=v"(gs) : "v"(l * 8), "s"(rsWS), "s"(WS_G0 + (rt * 128 + pw * 64) * 8) : "memory");
                }
                if constexpr (t == 11) {
                    waitv<0>();
                    PIN(gs);
                    bad = gs[1] ^ tag;
#pragma unroll
                    for (int r = 0; r < 4; r++) {
                        PIN(gl[r]);
                        bad |= (gl[r][1] ^ tag) | (gl[r][3] ^ tag);
                        s0 += __uint_as_float(gl[r][0]); s1 += __uint_as_float(gl[r][2]);
                    }
                    PIN(s0); PIN(s1); PIN(bad);
                }
                if constexpr (t == 12) {
                    if (RARE(!__all(bad == 0))) {
                        s0 = 0.f; s1 = 0.f;
#pragma nounroll
                        for (unsigned r = 0; r < 4; r++) {
                            unsigned a[2];
                            sweep<2>(rsWS, vgl, WS_G1 + ((hp * 4 + r) * 192 + pw * 96) * 8, 8, tag, a);
                            s0 += __uint_as_float(a[0]); s1 += __uint_as_float(a[1]);
                        }
                        unsigned c[1];
                        sweep<1>(rsWS, l * 8, WS_G0 + (rt * 128 + pw * 64) * 8, 0, tag, c);
                        gs[0] = c[0];
                    }
                    *(LAS f32x2*)(aw0) = (f32x2){-s0, -s1};
                    stsf(aw1, bflo(gs[0]));
                    *(LAS u16*)(aw2) = (u16)(gs[0] >> 16);
                    asm volatile("buffer_load_dwordx2 %0, %1, %2, 0 offen sc1" : "=v"(gc) : "v"(vgc), "s"(rsWS) : "memory");
                    SCHED();
                }
#define PT0 10
                if constexpr (t == PT0 - 2) { if (RARE(bid + pw == 0)) ST16(sv, (l >> 5) * 8192 + (l & 31) * 16, WS_PR); }
                if constexpr (t >= PT0 && t < 12) {
                    if (RARE(bid + pw == 0)) {
                        u64 c0, c1; unsigned x;
                        asm volatile("s_memtime %0\n\ts_load_dword %2, %3, %4\n\ts_waitcnt lgkmcnt(0)\n\ts_memtime %1\n\ts_waitcnt lgkmcnt(0)"
                                     : "=&s"(c0), "=&s"(c1), "=&s"(x) : "s"(ws), "i"(WS_PR + (t & 1) * 8192 + ((t - PT0) >> 1) * 128) : "memory");
                        lat[t & 1] = __builtin_elementwise_min(lat[t & 1], (unsigned)c1 - (unsigned)c0);
                        if constexpr (t == 11) {
                            asm volatile("s_getreg_b32 %0, hwreg(HW_REG_XCC_ID, 0, 3)" : "=s"(x));
                            __builtin_amdgcn_raw_buffer_store_b64((v2u){(lat[1] < lat[0] ? 0x44 : 0) ^ (x & 4), tag}, rsWS, l == 0 ? 0 : OOB, WS_PR + 1024, AUX_SC1);
                        }
                    }
                }
                if constexpr (t == 5) k_pre();
                if constexpr (t == 6) { wl_pre(); wo_pre(); }
                if constexpr (t == 7) { PIN(vgc); PIN(vgl); PIN(aw0); PIN(aw1); PIN(aw2); }
                clears(t < 7 ? t : 7, t < 7 ? t + 1 : 7);
                if constexpr (t == 13) {
                    waitv<0>();
                    PIN(gc);
                    if (RARE(!__all(gc[1] == tag))) { unsigned c[1]; sweep<1>(rsWS, vgc, 0, 0, tag, c); }
                    SCHED();
                    pf(0);
                }
                if constexpr (t == 14) pf(1);
            });
            tail16();
        }
        HST(28);
        auto perm1 = [&]() {
#pragma unroll
            for (int ip = 0; ip < 4; ip++)
#pragma unroll
                for (int c = 0; c < 4; c++) {
#pragma unroll
                    for (int m = 0; m < 2; m++) {
                        const unsigned a = wreg[m * 8 + 2 * ip][c], b = wreg[m * 8 + 2 * ip + 1][c];
                        wp1[m][ip][2 * c] = __builtin_amdgcn_perm(b, a, 0x05040100);
                        wp1[m][ip][2 * c + 1] = __builtin_amdgcn_perm(b, a, 0x07060302);
                    }
                }
#pragma unroll
            for (int ip = 0; ip < 4; ip++)
#pragma unroll
                for (int c = 0; c < 8; c++) { PIN(wp1[0][ip][c]); PIN(wp1[1][ip][c]); }
            SCHED();
            HST(29);
        };
#define ZP 0
        if (ZP) perm1();
        if (RARE(lw == 0 && (rt & 7) == 0)) {
            __builtin_amdgcn_s_setprio(1);
            sweepz<2, SR1>([&](int k) { return LD16(kr * P2_STRIDE + (l & 7) * 16, WS_P2 + (hp * 16 + k * 8) * P2_STRIDE + 8192 + (rt >> 3) * 128); }, pp);
            ST4(red(), (l & 8) ? OOB : ((l >> 2) & 1) * 64 + ((l & 1) * 2 + (g >> 1)) * 16 + ((l >> 1) & 1) * 8 + (g & 1) * 4, WS_GZ + (hp * 2 + (rt >> 3)) * 128);
            __builtin_amdgcn_s_setprio(0);
        }
        SCHED();
        if (!ZP) perm1();
        BAR();
        HST(30);
        res_loads();
        wo_rng(4, 8);
        HST(31);
        waitv<0>();
        PIN(xres[0]); PIN(xres[1]); PIN(wrv[0]); PIN(wrv[1]);
        place();
        STAMPW2(7, 4, 7);
    }
    prep2();
    if (w == 0) { if (S3) SLEEP(S3); poll_flags(rsWS, vFl, sFl, (v2u){tag ^ 0x5bd1e995u, tag}); }
    ARR_MARK(20);
    BAR();
    ARR_END();
    FRESH();
    SCHED();
    STAMPW2(8, 0, 4);
    bf16x8 bb[2][4];
    {
        auto aload = [&](int st, int m2) {
            const v4u r = LD16(m2 ? va1 : va0, so);
            if (m2 == 0) so = ((so | 8192) + 2048) & 0xDC00;
            return r;
        };
        constexpr int PD = 2;
        v4u af[PD][2];
#pragma unroll
        for (int i = 0; i < PD; i++) {
#pragma unroll
            for (int m2 = 1; m2 >= 0; m2--) af[i][m2] = aload(i, m2);
            SCHED();
        }
#pragma unroll
        for (int nt = 0; nt < 4; nt++) bb[0][nt] = cat8(tr_read(wb[nt]), tr_read(wb[nt] + 2048));
#pragma unroll
        for (int nt = 0; nt < 4; nt++) acc2[1][nt] = zero4;
        SCHED();
#pragma unroll
        for (int st = 0; st < 16; st++) {
            if (st + 1 < 16) {
#pragma unroll
                for (int nt = 0; nt < 4; nt++)
                    bb[(st + 1) % (2)][nt] = cat8(tr_read(wb[nt] + (st + 1) * 4096), tr_read(wb[nt] + (st + 1) * 4096 + 2048));
            }
#pragma unroll
            for (int mm = 0; mm < 2; mm++) {
                const int m2 = mm ^ 1;
                const bf16x8 a = __builtin_bit_cast(bf16x8, af[st % PD][m2]);
#pragma unroll
                for (int nt = 0; nt < 4; nt++) acc2[m2][nt] = MFMA(bb[st % (2)][nt], a, acc2[m2][nt]);
            }
            if (st + PD < 16) {
#pragma unroll
                for (int m2 = 1; m2 >= 0; m2--) af[st % PD][m2] = aload(PD + st, m2);
            }
#pragma unroll
            for (int i = 0; i < 4; i++) { SGB(0x8, 1); SGB(0x100, 2); }
            SCHED();
        }
    }
    STAMPW2(9, 0, 4);
#pragma unroll
    for (int nt = 0; nt < 4; nt++) stsf4(aXw + nt * 1024, acc2[1][nt]);
    SCHED();
    if (half) dloads(DN0, 8);
    SCHED();
    WAITL0();
    if (half) __builtin_amdgcn_s_setprio(1);
    BAR();
    SCHED();
    v4u hpk[2];
    {
        f32x4 pa[4];
#pragma unroll
        for (int nt = 0; nt < 4; nt++) pa[nt] = ldsf4(aXr + nt * 1024);
        SCHED();
#pragma unroll
        for (int i = 0; i < 8; i++) {
            const f32x4 a = acc2[0][i >> 1] + pa[i >> 1];
            hpk[i >> 2][i & 3] = pk2(a[2 * (i & 1)], a[2 * (i & 1) + 1]);
        }
        const bf16x8 h0 = __builtin_bit_cast(bf16x8, hpk[0]), h1 = __builtin_bit_cast(bf16x8, hpk[1]);
        f32x4 gq = MFMA(h0, h0, zero4), sd = MFMA(sop[0], h0, zero4);
        gq = MFMA(h1, h1, gq); sd = MFMA(sop[1], h1, sd);
        float q0 = (l & 1) ? gq[1] : gq[0], q1 = (l & 1) ? gq[3] : gq[2];
        PIN(q0); PIN(q1);
        const v4u pv = {f2s(sd[0]), f2s((l & 2) ? q1 : q0), f2s(sd[1]), swc};
        ST16(pv, vG5, sG5);
        asm volatile("s_nop 1" :: "v"(pv));
    }
    __builtin_amdgcn_s_setprio(0);
    SCHED();
    swap16(hpk[0]); swap16(hpk[1]);
    PIN(hpk[0]); PIN(hpk[1]);
    STAMPW2(10, 0, 4);
    unsigned wp2[4][8];
    auto perm2 = [&]() {
#pragma unroll
        for (int i = 16; i < 24; i++) PIN(wreg[i]);
#pragma unroll
        for (int ip = 0; ip < 4; ip++)
#pragma unroll
            for (int c = 0; c < 4; c++) {
                const unsigned a = wreg[16 + 2 * ip][c], b = wreg[16 + 2 * ip + 1][c];
                wp2[ip][2 * c] = __builtin_amdgcn_perm(b, a, 0x05040100);
                wp2[ip][2 * c + 1] = __builtin_amdgcn_perm(b, a, 0x07060302);
            }
#pragma unroll
        for (int ip = 0; ip < 4; ip++)
#pragma unroll
            for (int c = 0; c < 8; c++) PIN(wp2[ip][c]);
    };
    const unsigned mt = rt2 * 8 + lw * 2 + half;
    v4u outv[2];
    const rsrc_t rsO = __builtin_amdgcn_make_buffer_rsrc((void*)OUT, 0, 2048 * 1024 * 2, 0x00020000);
    unsigned vOut = (R2 & ~8u) * 2048 + (t16 >> 3) * 64 + gx * 16;
    PIN(vOut);
    unsigned aG = ldsb + L_WO + 512, vX = kr, v4 = l * 4, sX = ffn ? WS_C + kq * 512 + lw * 128 + nr : OOB;
    unsigned aR1 = ldsb + L_RED + (lw * 4 + g) * 512 + t16 * 32, aT1 = ldsb + L_RED + (lw & 1) * 256 + l * 4;
    unsigned aR2 = ldsb + L_RED + 8192 + (lw * 4 + g) * 256 + (t16 & 7) * 32 + (t16 >> 3) * 16, aT2 = ldsb + L_RED + 8192 + l * 4, aB = kr * 8;
    const unsigned cx = (lw & 1) ? cg1u : cg1;
    unsigned s8w = ffn ? WS_G8 + (cx >> 2) * 16384 + (lw >> 1) * 8192 + (kq * 2 + (lw & 1)) * 1024 + (cx & 3) * 256 : OOB;
    unsigned s8r = ffn ? WS_G8 + kc * 16384 + lw * 256 + nr : OOB, s8s = s8r + 4096;
    float a1[2][8];
    if (half) {
#pragma unroll
        for (int c = 0; c < 16; c++) { a1[c >> 3][c & 7] = 0.f; PIN(a1[c >> 3][c & 7]); }
        PIN(vX); PIN(v4); PIN(aR1); PIN(aT1); PIN(aR2); PIN(aT2); PIN(aB);
    }
    PIN(aG);
    asm volatile("" : "+s"(sX), "+s"(s8w), "+s"(s8r), "+s"(s8s));
    unsigned vP = t16 == rt2 ? OOB : t16 * 128, pk;
    {
        v4u pv[4];
        unsigned vK = tid < 4 ? ((ct + 4 * tid) & 15) * 2048 : OOB, vS = l * 16;
        PIN(vK); PIN(vS);
        unsigned sK = WS_GK + rt2 * 128, rk = 2047u - R2, aKy = ldsb + L_WO + 576, aKw = aKy + w * 4;
        unsigned vC = gx * 16, sC = WS_C + (~rt2 & 12) * 4096 + (~rt2 & 3) * 2048 + ct * 128;
        PIN(vC);
        asm volatile("" : "+s"(sK), "+s"(sC));
        PIN(rk); PIN(aKy); PIN(aKw); PIN(vP);
        auto st_ld = [&](int k) { return LD16(vS + k * 1024, sG5r); };
        auto st_sum = [&]() { const f32x4 ps = (f4(pv[0]) + f4(pv[1])) + (f4(pv[2]) + f4(pv[3])); return xrow_scatter<false>(ps[0], ps[1], ps[2], ps[3]); };
        SCHED();
        SLEEP(S4);
        __builtin_amdgcn_s_setprio(1);
        float ts;
        sweepn<4, 4, (FM & 1) != 0>(st_ld, pv, [&]() { return ts = st_sum(); });
        STAMPW2(21, 0, 4);
        float T[4];
        xrow_gather(ts, T);
        const float mean = T[0] * (1.0f / 1024.0f), var = fmaxf(T[1] * (1.0f / 1024.0f) - mean * mean, 0.f);
        const unsigned sg = pk2(mean, __builtin_amdgcn_sqrtf(rbf(rbf(var) + 1e-5f)));
        const float mb2 = bflo(sg), isd2 = __builtin_amdgcn_rcpf(bfhi(sg));
        const float rsc = (T[2] - mb2 * T[3]) * isd2;
        const unsigned rb = pk2(rsc, rsc);
        const unsigned key = (((rb ^ (((int)rb >> 31) | 0x80000000u)) >> 16) << 11) | rk;
        unsigned kb = umx(key, dppu<0x128>(key));
        kb = umx(kb, dppu<0x124>(kb)); kb = umx(kb, dppu<0x122>(kb)); kb = umx(kb, dppu<0x121>(kb));
        stsu(aKw, kb);
        WAITL0();
        BAR();
        SCHED();
        pk = umx(wmax(*(const LAS v4u*)aKy), wmax(*(const LAS v4u*)(aKy + 16)));
        if (w == 0) ST4(pk, vK, sK);
        SCHED();
        const bool cand = key == pk;
        __builtin_amdgcn_s_setprio(0);
        v4u e[2];
#pragma unroll
        for (int i = 0; i < 8; i++) {
            const float h0 = bflo(hpk[i >> 2][i & 3]), h1 = bfhi(hpk[i >> 2][i & 3]);
            const unsigned d = pk2(h0 - mb2, h1 - mb2);
            e[i >> 2][i & 3] = nsent(pk2(bflo(d) * isd2, bfhi(d) * isd2));
        }
        if (cand) {
#pragma unroll
            for (int i = 0; i < 4; i++) ST16(e[i & 1], vC + (i & 1) * 64, sC + (i >> 1) * 8192);
#pragma unroll
            for (int i = 0; i < 2; i++) { *(LAS v4u*)(ldsb + L_STASH + i * 64 + vC) = hpk[i]; *(LAS v4u*)(ldsb + L_STASH + 128 + i * 64 + vC) = e[i]; }
        }
        SCHED();
#define KS 3
        unsigned k1;
        if (w == 1) {
            if (KS) SLEEP(KS);
            asm volatile("buffer_load_dword %0, %1, %2, %3 offen sc1" : "=v"(k1) : "v"(vP), "s"(rsWS), "s"(sRep) : "memory");
        }
        SCHED();
        STAMPW2(22, 0, 4);
#pragma unroll
        for (int i = 0; i < 8; i++)
            outv[i >> 2][i & 3] = pk2(bflo(hpk[i >> 2][i & 3]) + bflo(e[i >> 2][i & 3]), bfhi(hpk[i >> 2][i & 3]) + bfhi(e[i >> 2][i & 3]));
        {
#pragma unroll
            for (int j = 0; j < 4; j++) {
                const unsigned a = outv[0][j], b = outv[1][j];
                outv[0][j] = __builtin_amdgcn_update_dpp(a, b, 0x128, 0xf, 0xc, false);
                outv[1][j] = __builtin_amdgcn_update_dpp(b, a, 0x128, 0xf, 0x3, false);
            }
        }
        PIN(outv[0]); PIN(outv[1]);
        if (w == 1) {
            poll_words(k1, rsWS, vP, sRep);
            STAMPR(23, 2);
            unsigned gk = umx(k1, pk);
            gk = umx(gk, dppu<0x128>(gk)); gk = umx(gk, dppu<0x124>(gk)); gk = umx(gk, dppu<0x122>(gk)); gk = umx(gk, dppu<0x121>(gk));
            stsu(aG, gk);
        }
    }
    if (half) __builtin_amdgcn_s_setprio(1);
    STAMPW(24, 7);
    WL0();
    SCHED();
    BAR();
    SCHED();
    STAMPW2(11, 0, 4);
    const unsigned gv = ldsu(aG);
    auto store_out = [&](unsigned srow) {
#pragma unroll
        for (int i = 0; i < 2; i++) __builtin_amdgcn_raw_buffer_store_b128(outv[i], rsO, (R2 & ~8u) + 8 * i != srow ? vOut : OOB, ct * 128 + i * 16384, AUX_SC1);
    };
    if (w < 4) {
        const unsigned srow = ~rfl(gv) & 2047u;
        BAR(); BAR();
        SLEEP(OH);
        if (bid == 0 && tid == 0) __hip_atomic_store((gu32*)(ws + WS_EPOCH), tag, RLX_AGENT);
        store_out(srow);
        STAMPW(12, 0);
        RDUMP();
        return;
    }

    auto tree16 = [&](unsigned a, unsigned stride) {
        f32x2 v[8];
#pragma unroll
        for (int i = 0; i < 8; i++) v[i] = (f32x2){ldsf(a + 2 * i * stride), ldsf(a + (2 * i + 1) * stride)};
        SCHED();
        const f32x2 t = ((v[0] + v[1]) + (v[2] + v[3])) + ((v[4] + v[5]) + (v[6] + v[7]));
        return t[0] + t[1];
    };
    vX = (((gv & 0x780) | vX) + (gv & 0x600)) << 4;
    v4u xq[1] = {LD16(vX, sX)};
    SCHED();
    const unsigned gkey = rfl(gv), srow = ~gkey & 2047u;
    unsigned s9w = ffn && lw == 0 ? WS_G9 + (cg >> 1) * 16384 + (cg & 1) * 4096 + kc * 256 + (nh ^ (((srow >> 8) ^ bid) & 4) * 2048) : OOB;
    asm volatile("" : "+s"(s9w));
    SCHED();
    if (RARE(!__all(wmax(xq[0]) != SENT))) sweepz<1>([&](int) { return LD16(vX, sX); }, xq);
    STAMPW(13, 4);
#pragma unroll
    for (int ip = 0; ip < 4; ip++)
#pragma unroll
        for (int c = 0; c < 16; c++) dot2c(a1[c & 1][c >> 1], wp1[c & 1][ip][c >> 1], xq[0][ip]);
#pragma unroll
    for (int c = 0; c < 8; c++) fold8(a1[0][c], a1[1][c]);
    stsf4(aR1, (f32x4){a1[0][0], a1[0][1], a1[0][2], a1[0][3]});
    stsf4(aR1 + 16, (f32x4){a1[0][4], a1[0][5], a1[0][6], a1[0][7]});
    WL0();
    BAR();
    SCHED();
    ST4(f2s(tree16(aT1, 512)), v4, s8w);
    SCHED();
    STAMPW(14, 4);
#define S6 7
    perm2();
    float a2[8];
#pragma unroll
    for (int c = 0; c < 8; c++) { a2[c] = 0.f; PIN(a2[c]); }
    SCHED();
    SLEEP(S6);
    {
        unsigned pv[8];
        auto a_ld = [&](int k) { return LD4(v4 + (k & 3) * 1024, k < 4 ? s8r : s8s); };
        auto a_sum = [&](int m) { return (__uint_as_float(pv[m]) + __uint_as_float(pv[2 + m])) + (__uint_as_float(pv[4 + m]) + __uint_as_float(pv[6 + m])); };
        float gs, us;
        sweepn<8, 0, (FM & 2) != 0>(a_ld, pv, [&]() { gs = a_sum(0); us = a_sum(1); return gs + us; });
        STAMPW(15, 4);
        const unsigned gu = pk2(gs, us);
        const float gg = bflo(gu);
        const float act = rbf(gg * __builtin_amdgcn_rcpf(1.0f + __expf(-gg))) * bfhi(gu);
        const unsigned apk = pk2(act, dppf<0xB1>(act));
        unsigned ap[4];
#pragma unroll
        for (int ip = 0; ip < 4; ip++) ap[ip] = __builtin_amdgcn_ds_bpermute(aB + ip * 64, apk);
        SCHED();
#pragma unroll
        for (int ip = 0; ip < 4; ip++)
#pragma unroll
            for (int c = 0; c < 8; c++) dot2c(a2[(c & 1) * 4 + (c >> 1)], wp2[ip][(c & 1) * 4 + (c >> 1)], ap[ip]);
#pragma unroll
        for (int c = 0; c < 4; c++) fold8(a2[c], a2[4 + c]);
        stsf4(aR2, (f32x4){a2[0], a2[1], a2[2], a2[3]});
    }
    WL0();
    BAR();
    SCHED();
    const bool mine = (srow >> 7) == rt2;
    if (lw != 0) {
        SLEEP(OH);
        store_out(srow);
        RDUMP();
        return;
    }
    ST4(f2s(tree16(aT2, 256)), v4, s9w);
    SCHED();
    store_out(srow);
    STAMPW(16, 4);
    if (mine) {
        const unsigned ord = gkey >> 11;
        const unsigned rb = (ord & 0x8000u) ? (ord & 0x7fffu) : (~ord & 0xffffu);
        const float rs = bf2f(rb);
        float sig = rbf(1.0f / (1.0f + __expf(-rs)));
        const unsigned sb = ldsb + L_STASH + (t16 * 4 + g) * 2;
        float hc = lds_bf(sb), lc = lds_bf(sb + 128);
        unsigned vY = l * 16, vZ = l < 32 ? l * 16 : OOB, vO = (t16 * 4 + g) * 2, sY = WS_G9 + (ct >> 1) * 16384 + (ct & 1) * 4096 + nh, sO = srow * 2048 + ct * 128;
        PIN(sig); PIN(hc); PIN(lc); PIN(vY); PIN(vZ); PIN(vO);
        asm volatile("" : "+s"(sY), "+s"(sO));
        v4u yp[4];
        auto y_ld = [&](int k) { return LD16((k < 3 ? vY : vZ) + k * 1024, sY); };
        auto y_sum = [&]() { const f32x4 ys = (f4(yp[0]) + f4(yp[1])) + (f4(yp[2]) + f4(yp[3])); return xrow_scatter<false>(ys[0], ys[1], ys[2], ys[3]); };
        SCHED();
        SLEEP(S7);
        float yt;
        sweepn<4, 0, (FM & 4) != 0>(y_ld, yp, [&]() { return yt = y_sum(); });
        const float y = rbf(rbf(yt) * sig);
        __builtin_amdgcn_raw_buffer_store_b16((u16)pk2(hc + rbf(lc + y), 0.f), rsO, vO, sO, 0);
        STAMPW(17, 4);
    }
    RDUMP();
}
__device__ __attribute__((used, noinline)) void fk_tail() { asm volatile(".Lfk_end:"); }

extern "C" void kernel_launch(void* const* d_in, const int* in_sizes, int n_in, void* d_out, int out_size, void* d_ws,
                              size_t ws_size, hipStream_t stream) {
    (void)in_sizes; (void)n_in; (void)out_size; (void)ws_size;
    (void)hipFuncSetAttribute((const void*)fused_kernel, hipFuncAttributeMaxDynamicSharedMemorySize, LDS_BYTES);
    fused_kernel<<<256, 512, LDS_BYTES, stream>>>((const u16*)d_in[0], (const u16*)d_in[1], (const u16*)d_in[2], (const u16*)d_in[3],
                                                  (const u16*)d_in[5], (const u16*)d_in[6], (const u16*)d_in[7], (const u16*)d_in[8],
                                                  (u16*)d_out, (char*)d_ws);
}
```

```cpp
#include <hip/hip_runtime.h>
#include <stdint.h>
#include <utility>

typedef unsigned short u16;
typedef unsigned long long u64;
typedef short bf16x8 __attribute__((ext_vector_type(8)));
typedef short s16x4 __attribute__((ext_vector_type(4)));
typedef float f32x4 __attribute__((ext_vector_type(4)));
typedef float f32x2 __attribute__((ext_vector_type(2)));
typedef __bf16 bf16x2 __attribute__((ext_vector_type(2)));
typedef unsigned v4u __attribute__((ext_vector_type(4)));
typedef unsigned v2u __attribute__((ext_vector_type(2)));
#define LAS __attribute__((address_space(3)))
#define GAS __attribute__((address_space(1)))
#define CAS __attribute__((address_space(4)))
typedef GAS u64 gu64;
typedef GAS unsigned gu32;
typedef __amdgpu_buffer_rsrc_t rsrc_t;
#define DI __device__ __forceinline__
#define RLX_AGENT __ATOMIC_RELAXED, __HIP_MEMORY_SCOPE_AGENT
#define BAR() __builtin_amdgcn_s_barrier()
#define SCHED() __builtin_amdgcn_sched_barrier(0)
#define SGB(m, n) __builtin_amdgcn_sched_group_barrier((m), (n), 0)
#define WAITL0() asm volatile("s_waitcnt lgkmcnt(0)" ::: "memory")
#define WL0() __builtin_amdgcn_s_waitcnt(0xc07f)
#define MFMA(a, b, c) __builtin_amdgcn_mfma_f32_16x16x32_bf16((a), (b), (c), 0, 0, 0)
#define ORDER_BEHIND_POLL() __builtin_amdgcn_fence(__ATOMIC_ACQUIRE, "wavefront")
#define AUX_SC1 16
template <int N> DI void waitv() { asm volatile("s_waitcnt vmcnt(%0)" ::"i"(N) : "memory"); }

#define WS_EPOCH 0
#define WS_G0 0x1000
#define WS_G1 0x8000
#define WS_F4 0x60000
#define WS_GC 0x70000
#define WS_PR 0x74000
#define WS_G3 0x80000
#define WS_GZ 0xC0000
#define SENT 0xFFFFFFFFu
#define WS_TL 0x100000
#define WS_G5 0x100000
#define WS_G8 0x180000
#define WS_GK 0x1B8000
#define WS_G9 0x1C0000
#define WS_C 0x1E0000
#define WS_H 0x1F0000
#define WS_P2 0x200000
#define P2_STRIDE 0x2180
#define WS_P4 0x600000
#define WS_USED 0xB00000
#define WS_STAMP 0x1000000

#define STAGE_B 40960
#define LDS_BYTES 163840
#define L_WO 32768
#define L_MB 161792
#define L_WPHI (L_MB - 8192)
#define L_SD (L_MB + 512)
#define L_CS (L_MB + 1024)
#define L_V L_WO
#define L_KH (L_WO + 65536)
#define L_XB 0
#define L_STASH (L_WO + 1024)
#define L_RED 4096

#define SPIN_CAP 20000u
#define OH 6
#define DN0 3
#define S3 4
#define S4 13
#define S7 24
#define W_AUX 2
#define LOG2E 1.4426950408889634f

DI unsigned rfl(unsigned x) { return __builtin_amdgcn_readfirstlane(x); }
DI float bf2f(unsigned u) { return __uint_as_float(u << 16); }
DI float bflo(unsigned u) { return __uint_as_float(u << 16); }
DI float bfhi(unsigned u) { return __uint_as_float(u & 0xffff0000u); }
DI unsigned pk2(float lo, float hi) { f32x2 v = {lo, hi}; return __builtin_bit_cast(unsigned, __builtin_convertvector(v, bf16x2)); }
DI unsigned nsent(unsigned x) { return __builtin_elementwise_min(x, SENT - 1); }
DI unsigned umx(unsigned a, unsigned b) { return __builtin_elementwise_max(a, b); }
DI float rbf(float f) { return bflo(pk2(f, f) & 0xffffu); }
DI bf16x8 mk8(unsigned a, unsigned b, unsigned c, unsigned d) { v4u v = {a, b, c, d}; return __builtin_bit_cast(bf16x8, v); }
DI void dma16(unsigned ldsaddr, unsigned voff, rsrc_t rs, unsigned soff) {
    asm volatile("s_mov_b32 m0, %0\n\ts_nop 0\n\tbuffer_load_dwordx4 %1, %2, %3 offen lds" ::"s"(ldsaddr), "v"(voff), "s"(rs), "s"(soff) : "memory");
}
DI void dma16x4(unsigned ldsaddr, const unsigned* v, rsrc_t rs, unsigned soff) {
    asm volatile("s_mov_b32 m0, %0\n\ts_nop 0\n\tbuffer_load_dwordx4 %1, %5, %6 offen lds\n\tbuffer_load_dwordx4 %2, %5, %6 offen offset:1024 lds\n\t"
                 "buffer_load_dwordx4 %3, %5, %6 offen offset:2048 lds\n\tbuffer_load_dwordx4 %4, %5, %6 offen offset:3072 lds"
                 ::"s"(ldsaddr), "v"(v[0]), "v"(v[1]), "v"(v[2]), "v"(v[3]), "s"(rs), "s"(soff) : "memory");
}
DI void touch(unsigned voff, rsrc_t rs, unsigned soff) { asm volatile("buffer_load_dword v255, %0, %1, %2 offen" ::"v"(voff), "s"(rs), "s"(soff) : "memory", "v255"); }
DI v4u ld16_asm(unsigned voff, rsrc_t rs, unsigned soff) {
    v4u r; asm volatile("buffer_load_dwordx4 %0, %1, %2, %3 offen" : "=v"(r) : "v"(voff), "s"(rs), "s"(soff) : "memory"); return r;
}
#define PIN(x) asm volatile("" : "+v"(x))
DI s16x4 tr_read(unsigned a) { return __builtin_amdgcn_ds_read_tr16_b64_v4i16((LAS s16x4*)(a)); }
DI bf16x8 cat8(s16x4 a, s16x4 b) { return __builtin_shufflevector(a, b, 0, 1, 2, 3, 4, 5, 6, 7); }
DI bf16x8 lds128(unsigned a) { return *(const LAS bf16x8*)(a); }
DI f32x4 ldsf4(unsigned a) { return *(const LAS f32x4*)(a); }
DI float ldsf(unsigned a) { return *(const LAS float*)(a); }
DI unsigned ldsu(unsigned a) { return *(const LAS unsigned*)(a); }
DI void stsf4(unsigned a, f32x4 v) { *(LAS f32x4*)(a) = v; }
DI void stsf(unsigned a, float v) { *(LAS float*)(a) = v; }
DI void stsu(unsigned a, unsigned v) { *(LAS unsigned*)(a) = v; }
template <int C> DI float dppf(float x) { return __uint_as_float(__builtin_amdgcn_update_dpp(0, __float_as_uint(x), C, 0xf, 0xf, true)); }
template <int C> DI unsigned dppu(unsigned x) { return __builtin_amdgcn_update_dpp(0, x, C, 0xf, 0xf, true); }
DI float max2f(float a, float b) { float r; asm("v_max_f32_e32 %0, %1, %2" : "=v"(r) : "v"(a), "v"(b)); return r; }
DI float max3f(float a, float b, float c) { float r; asm("v_max3_f32 %0, %1, %2, %3" : "=v"(r) : "v"(a), "v"(b), "v"(c)); return r; }
DI float row_sum(float x) { x += dppf<0x128>(x); x += dppf<0x124>(x); x += dppf<0x122>(x); x += dppf<0x121>(x); return x; }
DI float xrow_sum(float x) {
    auto a = __builtin_amdgcn_permlane16_swap(__float_as_uint(x), __float_as_uint(x), false, false);
    x = __uint_as_float(a[0]) + __uint_as_float(a[1]);
    auto b = __builtin_amdgcn_permlane32_swap(__float_as_uint(x), __float_as_uint(x), false, false);
    return __uint_as_float(b[0]) + __uint_as_float(b[1]);
}
DI float wave_sum(float x) { return xrow_sum(row_sum(x)); }
DI unsigned wave_maxu(unsigned x) {
    x = umx(x, dppu<0x128>(x)); x = umx(x, dppu<0x124>(x)); x = umx(x, dppu<0x122>(x)); x = umx(x, dppu<0x121>(x));
    auto a = __builtin_amdgcn_permlane16_swap(x, x, false, false);
    x = umx(a[0], a[1]);
    auto b = __builtin_amdgcn_permlane32_swap(x, x, false, false);
    return umx(b[0], b[1]);
}
DI float nmax3f(float a, float b, float c) { float r; asm("v_max3_f32 %0, -%1, -%2, -%3" : "=v"(r) : "v"(a), "v"(b), "v"(c)); return r; }
DI float nmax2f(float a, float b) { float r; asm("v_max_f32_e64 %0, -%1, -%2" : "=v"(r) : "v"(a), "v"(b)); return r; }
DI float mx2(float a, float b) { return __builtin_amdgcn_fmed3f(a, b, __builtin_inff()); }
template <bool MAX> DI float xrow_scatter(float a, float b, float c, float d) {
    const auto s = __builtin_amdgcn_permlane16_swap(__float_as_uint(a), __float_as_uint(b), false, false);
    const auto t = __builtin_amdgcn_permlane16_swap(__float_as_uint(c), __float_as_uint(d), false, false);
    const float u = MAX ? mx2(__uint_as_float(s[0]), __uint_as_float(s[1])) : __uint_as_float(s[0]) + __uint_as_float(s[1]);
    const float v = MAX ? mx2(__uint_as_float(t[0]), __uint_as_float(t[1])) : __uint_as_float(t[0]) + __uint_as_float(t[1]);
    const auto r = __builtin_amdgcn_permlane32_swap(__float_as_uint(u), __float_as_uint(v), false, false);
    return MAX ? mx2(__uint_as_float(r[0]), __uint_as_float(r[1])) : __uint_as_float(r[0]) + __uint_as_float(r[1]);
}
DI void xrow_gather(float w, float (&o)[4]) {
    const auto r = __builtin_amdgcn_permlane32_swap(__float_as_uint(w), __float_as_uint(w), false, false);
    const auto s = __builtin_amdgcn_permlane16_swap(r[0], r[0], false, false);
    const auto t = __builtin_amdgcn_permlane16_swap(r[1], r[1], false, false);
    o[0] = __uint_as_float(s[0]); o[1] = __uint_as_float(s[1]); o[2] = __uint_as_float(t[0]); o[3] = __uint_as_float(t[1]);
}
struct NoHook { DI void operator()(int, float&) const {} DI void operator()(int, float&, float&) const {} };
template <bool MUL, class H = NoHook> DI void softmax2(const f32x4 (&p)[2][2], float rs0, float rs1, bool hi, bf16x8 (&out)[4], H hook = H()) {
    float m[4];
#pragma unroll
    for (int i = 0; i < 2; i++) {
        const f32x4 &p0 = p[i][0], &p1 = p[i][1];
        m[2 * i] = max3f(max3f(p0[0], p0[1], p0[2]), max3f(p0[3], p1[0], p1[1]), max2f(p1[2], p1[3]));
        m[2 * i + 1] = max3f(nmax3f(p0[0], p0[1], p0[2]), nmax3f(p0[3], p1[0], p1[1]), nmax2f(p1[2], p1[3]));
    }
    asm volatile("s_nop 1" : "+v"(m[0]), "+v"(m[1]), "+v"(m[2]), "+v"(m[3]));
    hook(1, m[3]);
    const float rsr = hi ? rs1 : rs0;
    float a[4];
    xrow_gather(xrow_scatter<true>(m[0], m[1], m[2], m[3]) * (rsr * -LOG2E), a);
    f32x2 e[4][4];
    float s[4];
#pragma unroll
    for (int k = 0; k < 4; k++) {
        if (k == 2) hook(2, a[2], s[1]);
        const float sc = ((k >> 1) ? rs1 : rs0) * ((k & 1) ? -LOG2E : LOG2E);
#pragma unroll
        for (int j = 0; j < 4; j++) {
            const f32x4& q = p[k >> 1][j >> 1];
            const f32x2 t = __builtin_elementwise_fma((f32x2){q[2 * (j & 1)], q[2 * (j & 1) + 1]}, (f32x2){sc, sc}, (f32x2){a[k], a[k]});
            e[k][j] = (f32x2){__builtin_amdgcn_exp2f(t[0]), __builtin_amdgcn_exp2f(t[1])};
        }
        const f32x2 h = (e[k][0] + e[k][1]) + (e[k][2] + e[k][3]);
        s[k] = h[0] + h[1];
        if (k == 3) hook(3, s[k]);
    }
    float iv = __builtin_amdgcn_rcpf(xrow_scatter<false>(s[0], s[1], s[2], s[3]));
    if (MUL) iv *= rsr;
    xrow_gather(iv, a);
#pragma unroll
    for (int k = 0; k < 4; k++) {
        if (k == 2) hook(4, a[2]);
        const f32x2 c = {a[k], a[k]};
        const f32x2 x0 = e[k][0] * c, x1 = e[k][1] * c, x2 = e[k][2] * c, x3 = e[k][3] * c;
        out[k] = mk8(pk2(x0[0], x0[1]), pk2(x1[0], x1[1]), pk2(x2[0], x2[1]), pk2(x3[0], x3[1]));
    }
}
DI void swap16(v4u& x) {
    const auto a = __builtin_amdgcn_permlane16_swap(x[0], x[2], false, false), b = __builtin_amdgcn_permlane16_swap(x[1], x[3], false, false);
    x = (v4u){a[0], b[0], a[1], b[1]};
}
DI float dot2(unsigned a, unsigned b, float c) {
    return __builtin_amdgcn_fdot2_f32_bf16(__builtin_bit_cast(bf16x2, a), __builtin_bit_cast(bf16x2, b), c, false);
}
DI void st_gran(char* ws, unsigned off, unsigned tag, unsigned val) {
    __hip_atomic_store((gu64*)(ws + off), ((u64)tag << 32) | val, RLX_AGENT);
}
DI v2u ldg(rsrc_t rs, unsigned voff, unsigned soff) { return __builtin_amdgcn_raw_buffer_load_b64(rs, voff, soff, AUX_SC1); }
#define RARE(c) __builtin_expect(!!(c), 0)
#define FRESH() asm volatile("" ::: "memory")
template <int N> DI void sweep(rsrc_t rs, unsigned voff, unsigned soff, unsigned S, unsigned tag, unsigned (&v)[N]) {
#pragma nounroll
    for (unsigned spins = 0;;) {
        unsigned bad = 0;
        FRESH();
#pragma unroll
        for (int k = 0; k < N; ++k) { const v2u x = ldg(rs, voff, soff + k * S); v[k] = x[0]; bad |= x[1] ^ tag; }
        if (__all(bad == 0)) break;
        if (++spins > SPIN_CAP) break;
        __builtin_amdgcn_s_sleep(1);
    }
}
DI unsigned wmax(unsigned x) { return x; }
DI unsigned wmax(v2u x) { return umx(x[0], x[1]); }
DI unsigned wmax(v4u x) { return umx(umx(x[0], x[1]), umx(x[2], x[3])); }
template <int N, int SL = 0, class T, class F> DI void sweepz(F ld, T (&v)[N]) {
#pragma nounroll
    for (unsigned spins = 0;;) {
        FRESH();
        unsigned m = 0;
#pragma unroll
        for (int k = 0; k < N; ++k) v[k] = ld(k);
#pragma unroll
        for (int k = 0; k < N; ++k) m = umx(m, wmax(v[k]));
        if (__all(m != SENT)) break;
        if (++spins > SPIN_CAP) break;
        if (SL) __builtin_amdgcn_s_sleep(SL);
    }
}
DI f32x4 f4(v4u x) { return __builtin_bit_cast(f32x4, x); }
DI bool anynan(float x) { return __builtin_amdgcn_ballot_w64(x != x) != 0; }
DI void dot2c(float& c, unsigned a, unsigned b) { asm volatile("v_dot2c_f32_bf16 %0, %1, %2" : "+v"(c) : "v"(a), "v"(b)); }
DI void fold8(float& a, float b) {
    asm volatile("v_add_f32_dpp %0, %0, %0 row_ror:8 row_mask:0xf bank_mask:0x3\n\tv_add_f32_dpp %0, %1, %1 row_ror:8 row_mask:0xf bank_mask:0xc" : "+v"(a) : "v"(b));
}
template <int N, int SL, bool FAST, class T, class F, class S> DI void sweepn(F ld, T (&v)[N], S sum) {
    if (FAST) {
        FRESH();
#pragma unroll
        for (int k = 0; k < N; ++k) v[k] = ld(k);
        if (RARE(anynan(sum()))) { sweepz<N, SL>(ld, v); sum(); }
    } else { sweepz<N, SL>(ld, v); sum(); }
}
#define FM 7
DI unsigned f2s(float x) { return nsent(__float_as_uint(x)); }
#define OOB 0x4000000u
#define LD16(v, s) __builtin_amdgcn_raw_buffer_load_b128(rsWS, (v), (s), AUX_SC1)
#define LD4(v, s) __builtin_amdgcn_raw_buffer_load_b32(rsWS, (v), (s), AUX_SC1)
#define ST16(x, v, s) __builtin_amdgcn_raw_buffer_store_b128((x), rsWS, (v), (s), AUX_SC1)
#define ST4(x, v, s) __builtin_amdgcn_raw_buffer_store_b32((x), rsWS, (v), (s), AUX_SC1)
#define SLEEP(n) __builtin_amdgcn_s_sleep(n)
DI void poll_flags(rsrc_t rs, unsigned v, unsigned off, v2u want) {
    v2u x; unsigned n = SPIN_CAP;
    asm volatile("1:\n\tbuffer_load_dwordx2 %0, %2, %3, %4 offen sc1\n\ts_sub_u32 %1, %1, 1\n\ts_cbranch_scc1 2f\n\ts_waitcnt vmcnt(0)\n\t"
                 "v_cmp_ne_u64 vcc, %0, %5\n\ts_cbranch_vccnz 1b\n2:\n\ts_waitcnt vmcnt(0)" : "=&v"(x), "+s"(n) : "v"(v), "s"(rs), "s"(off), "v"(want) : "vcc", "scc", "memory");
}
DI void poll_words(unsigned& x, rsrc_t rs, unsigned v, unsigned off) {
    unsigned n = SPIN_CAP;
    asm volatile("s_branch 3f\n1:\n\tbuffer_load_dword %0, %2, %3, %4 offen sc1\n\ts_sub_u32 %1, %1, 1\n\ts_cbranch_scc1 2f\n3:\n\ts_waitcnt vmcnt(0)\n\t"
                 "v_cmp_eq_u32 vcc, -1, %0\n\ts_cbranch_vccnz 1b\n2:\n\ts_waitcnt vmcnt(0)" : "+v"(x), "+s"(n) : "v"(v), "s"(rs), "s"(off) : "vcc", "scc", "memory");
}
#define STAMPR(i, role)
#define STAMP(i)
#define STAMPW(i, ww)

#define STAMPW2(i, a, b) STAMPW(i, a); STAMPW(i, b)
#define RINIT()
#define RDUMP()
#define ARR_DECL
#define KATTR
#define ARR_MARK(i)
#define ARR_END()
#define HST(i)
template <class F, int... Is> DI void static_for_impl(F&& f, std::integer_sequence<int, Is...>) { (f(std::integral_constant<int, Is>{}), ...); }
template <int N, class F> DI void static_for(F&& f) { static_for_impl(f, std::make_integer_sequence<int, N>{}); }

DI s16x4 bq(unsigned b0, unsigned b1, int j, int i) {
    const int ni = (j + 2) % 6;
    return tr_read(((ni & 1) ? b1 : b0) + (ni >> 1) * 8192 + (i >> 1) * 4096 + (i & 1) * 512);
}
template <class F> DI void group(f32x4 (&acc)[4][6], const bf16x8 (&A)[2][4], const bf16x8 (&B)[2], int j, F gap) {
    const int ni = (j + 2) % 6;
#pragma unroll
    for (int i = 0; i < 8; i++) {
        const int mi = i >> 1, ks = i & 1;
        if (ni < 4) asm volatile("v_mfma_f32_16x16x32_bf16 %0, %1, %2, %0" : "+v"(acc[mi][ni]) : "v"(B[ks]), "v"(A[ks][mi]));
        else asm volatile("v_mfma_f32_16x16x32_bf16 %0, %1, %2, %0" : "+v"(acc[mi][ni]) : "v"(A[ks][mi]), "v"(B[ks]));
        gap(i);
        SCHED();
    }
}
DI void img_write2(const unsigned (&ia)[4], const bf16x8 (&f)[4]) {
#pragma unroll
    for (int k = 0; k < 4; k++) {
        const v4u u = __builtin_bit_cast(v4u, f[k]);
        *(LAS v2u*)(ia[2 * (k & 1)] + (k >> 1) * 2048) = (v2u){u[0], u[1]};
        *(LAS v2u*)(ia[2 * (k & 1) + 1] + (k >> 1) * 2048) = (v2u){u[2], u[3]};
    }
}
DI float lds_bf(unsigned a) { return bf2f(*(const LAS u16*)(a)); }
#define MFMA_SETTLE2(p) asm volatile("s_nop 7\n\ts_nop 1" : "+v"(p[0][0]), "+v"(p[0][1]), "+v"(p[1][0]), "+v"(p[1][1]))

__global__ KATTR __launch_bounds__(512, 2) void fused_kernel(const u16* __restrict__ X, const u16* __restrict__ WQKV, const u16* __restrict__ WPHI,
                                                       const u16* __restrict__ WO, const u16* __restrict__ WR, const u16* __restrict__ WG,
                                                       const u16* __restrict__ WU, const u16* __restrict__ WD, u16* __restrict__ OUT,
                                                       char* __restrict__ ws) {
    extern __shared__ __attribute__((aligned(16))) char lds[];
    u64 pc0; unsigned clen;
    const int tid = threadIdx.x, l = tid & 63;
    asm volatile("s_getpc_b64 %0\n.Lfk_beg:\n\ts_lshr_b32 %1, %3, 3\n\tv_and_b32 v255, 0x1e0, %2\n\tv_add_lshl_u32 v255, v255, %1, 7\n\ts_mov_b32 %1, .Lfk_end-.Lfk_beg\n\t"
                 "v_min_u32 v255, %1, v255\n\tglobal_load_dword v255, v255, %0" : "=&s"(pc0), "=&s"(clen) : "v"(tid), "s"(blockIdx.x) : "v255", "scc");
    SCHED();
    const unsigned w = rfl(tid >> 6);
    const unsigned bid = blockIdx.x, xcd = (bid & 3) * 2 + ((bid >> 2) & 1), idx = bid >> 3;
    const unsigned rt = (xcd & 1) * 8 + (idx & 7), hp = (xcd >> 1) * 4 + (idx >> 3);
    const unsigned rt2 = (bid & 6) * 2 + (idx & 3), ct = (bid & 1) * 8 + (idx >> 2);
    const unsigned hq = (hp + 4) & 15;
    const unsigned rot = (rt >> 2) * 4;
#define RC 16
#define RR 2
    const unsigned rot2 = (RR * rt2 + RC * ct) & 31;
    const unsigned ldsb = (unsigned)(size_t)lds;
    const int g = l >> 4, q4 = (l & 15) >> 2, p4 = l & 3, t16 = l & 15;
    const unsigned kr = l >> 3, gx = (g & 1) * 2 + (g >> 1);
    asm volatile("" ::"s"(X), "s"(WQKV), "s"(WPHI), "s"(WO), "s"(WR), "s"(WG), "s"(WU), "s"(WD), "s"(OUT), "s"(ws));
    unsigned tag;
#define READ_TAG() do { SCHED(); tag = *(const volatile CAS unsigned*)(ws + WS_EPOCH) + 1u; asm volatile("" : "+s"(tag)); } while (0)
    const rsrc_t rsWS = __builtin_amdgcn_make_buffer_rsrc((void*)ws, 0, WS_USED, 0x00020000);
    const rsrc_t rsX = __builtin_amdgcn_make_buffer_rsrc((void*)X, 0, 2048 * 1024 * 2, 0x00020000);
    const rsrc_t rsW = __builtin_amdgcn_make_buffer_rsrc((void*)WQKV, 0, 1024 * 3072 * 2, 0x00020000);
    const unsigned vA = (kr * 1024 + (((l & 7) ^ kr) * 8)) * 2;
    const unsigned vB0 = (kr * 3072 + (((l & 7) ^ (((kr >> 1) & 1) << 1)) * 8)) * 2;
    const unsigned vB1 = (kr * 3072 + (((l & 7) ^ ((((kr >> 1) & 1) | 2) << 1)) * 8)) * 2;
    const unsigned srow1 = rt * 128 + hp * 8 + (w & 3) * 2;
    v4u xr[4];
    if (w < 4) {
#pragma unroll
        for (int i = 0; i < 4; i++) xr[i] = ld16_asm(l * 16, rsX, srow1 * 2048 + i * 1024);
    }
    {
        const unsigned vBw = (w & 1) ? vB1 : vB0;
#pragma unroll
        for (int i = 0; i < 2; i++) dma16(ldsb + (w * 2 + i) * 1024, vA, rsX, (rt * 128 + w * 16 + i * 8) * 2048 + rot * 128);
#pragma unroll
        for (int i = 0; i < 3; i++) dma16(ldsb + 16384 + i * 8192 + w * 1024, vBw, rsW, (rot * 64 + w * 8) * 6144 + (i ? hp : hq) * 128 + i * 2048);
    }
    const f32x4 zero4 = {0.f, 0.f, 0.f, 0.f};
    ARR_DECL;
    const bf16x8 ones = mk8(0x3F803F80u, 0x3F803F80u, 0x3F803F80u, 0x3F803F80u);
    const unsigned lw = w & 3;
    const bool ffn = (ct & 7) != 3;
    const unsigned fb = !ffn ? 255u : (bid & 7) * 28 + (idx < 12 ? idx : idx - 4);
    const unsigned cg = (fb + 5) & 15, kc = fb >> 4;
    const unsigned kq = fb / 56, cg1 = fb % 56, cg1u = (cg1 + 3) % 56;
    v4u wreg[24];
    RINIT();
    STAMPW2(0, 0, 4);

    const rsrc_t rsWO = __builtin_amdgcn_make_buffer_rsrc((void*)WO, 0, 1024 * 1024 * 2, 0x00020000);
    const unsigned vWO = kr * 2048 + (((l & 7) ^ (((kr >> 1) & 3) << 1)) * 16);
    unsigned vwo[4];
    auto wo_so = [&](unsigned b) { return (((2 * b & 30) + (b >> 4) + rot2) & 31) * 65536 + ct * 128; };
    const unsigned fw = (((t16 >> 1) & 3) << 2) | (((t16 >> 3) & 1) << 1) | (t16 & 1);
    unsigned ad;
    unsigned ia[4];
    bf16x8 sdv[4];
    auto pre_addr = [&]() {
#pragma unroll
        for (int k = 0; k < 4; k++) { ia[k] = ldsb + L_XB + lw * 8192 + (w >> 2) * 4096 + t16 * 128 + (((4 * k + g) ^ fw) * 8); PIN(ia[k]); }
        const unsigned rr = 4 * g + q4, fr = (((rr >> 1) & 3) << 2) | (((rr >> 3) & 1) << 1) | (rr & 1);
        ad = ldsb + L_XB + (w & 1) * 16384 + rr * 128 + ((((w >> 1) * 4 + p4) ^ fr) * 8);
        PIN(ad);
    };
    auto sd_reads = [&]() {
        unsigned sa = ldsb + L_SD + g * 8;
        PIN(sa);
#pragma unroll
        for (int s = 0; s < 4; s++) {
            const v2u a = *(const LAS v2u*)(sa + s * 64), b = *(const LAS v2u*)(sa + s * 64 + 32);
            sdv[s] = mk8(a[0], a[1], b[0], b[1]);
        }
    };
    bf16x8 v0[4], v1[4];
    auto v_reads = [&]() {
        unsigned vd = ldsb + L_V + (w & 1) * 8192 + l * 16;
        PIN(vd);
#pragma unroll
        for (int s = 0; s < 4; s++) { v0[s] = lds128(vd + s * 2048); v1[s] = lds128(vd + s * 2048 + 1024); }
    };
    auto kv_tile = [&](auto hook6) {
        const unsigned h2 = w & 1, ft = w >> 1;
        WAITL0();
        ARR_MARK(18);
        BAR();
        SCHED();
        bf16x8 pf[4];
#pragma unroll
        for (int s = 0; s < 4; s++) pf[s] = cat8(tr_read(ad + s * 4096), tr_read(ad + s * 4096 + 2048));
        SCHED();
        hook6();
        SCHED();
        f32x4 k0 = zero4, k1 = zero4, kz = zero4;
#pragma unroll
        for (int s = 0; s < 4; s++) { k0 = MFMA(pf[s], v0[s], k0); k1 = MFMA(pf[s], v1[s], k1); }
#pragma unroll
        for (int s = 0; s < 4; s++) kz = MFMA(pf[s], sdv[s], kz);
        const unsigned pbase = WS_P2 + (hp * 16 + rt) * P2_STRIDE;
        const v4u o0 = {nsent(pk2(k0[0], k0[1])), nsent(pk2(k0[2], k0[3])), nsent(pk2(k1[0], k1[1])), nsent(pk2(k1[2], k1[3]))};
        __builtin_amdgcn_raw_buffer_store_b128(o0, rsWS, l * 16, pbase + h2 * 4096 + ft * 1024, AUX_SC1);
        asm volatile("s_nop 1" :: "v"(o0));
        __builtin_amdgcn_raw_buffer_store_b64((v2u){nsent(pk2(kz[0], kz[1])), nsent(pk2(kz[2], kz[3]))}, rsWS, t16 == 0 ? g * 8 : OOB,
                                              pbase + 8192 + h2 * 128 + ft * 32, AUX_SC1);
        SCHED();
        ARR_MARK(19);
        BAR();
    };

    v4u pp[2];
    auto red = [&]() {
        const v4u* p = pp;
        float lo = xrow_scatter<false>(bflo(p[0][0]) + bflo(p[1][0]), bflo(p[0][1]) + bflo(p[1][1]), bflo(p[0][2]) + bflo(p[1][2]), bflo(p[0][3]) + bflo(p[1][3]));
        float hi = xrow_scatter<false>(bfhi(p[0][0]) + bfhi(p[1][0]), bfhi(p[0][1]) + bfhi(p[1][1]), bfhi(p[0][2]) + bfhi(p[1][2]), bfhi(p[0][3]) + bfhi(p[1][3]));
        lo += dppf<0x128>(lo); hi += dppf<0x128>(hi);
        return nsent(pk2(lo, hi));
    };
    auto pp_so = [&](int k) { return WS_P2 + (hp * 16 + k * 8) * P2_STRIDE + (rt >> 2) * 2048 + (lw & 1) * 1024 + (rt & 3) * 256 + (lw >> 1) * 128; };
#define SR1 1
#define SR2 4
    auto red_issue = [&]() {
        const unsigned vpp = kr * P2_STRIDE + (l & 7) * 16;
#pragma unroll
        for (int k = 0; k < 2; k++) asm volatile("buffer_load_dwordx4 %0, %1, %2, %3 offen sc1" : "=v"(pp[k]) : "v"(vpp), "s"(rsWS), "s"(pp_so(k)) : "memory");
        SCHED();
    };
    auto kv_reduce = [&]() {
        waitv<5>();
        PIN(pp[0]); PIN(pp[1]);
        if (!__all(umx(wmax(pp[0]), wmax(pp[1])) != SENT)) sweepz<2, SR1>([&](int k) { return LD16(kr * P2_STRIDE + (l & 7) * 16, pp_so(k)); }, pp);
        STAMPW(25, 0);
        ST4(red(), (l & 8) ? OOB : (l & 7) * 16 + g * 4, WS_G3 + (hp * 4 + (rt >> 2)) * 2048 + (lw & 1) * 1024 + (rt & 3) * 256 + (lw >> 1) * 128);
    };

    const unsigned half = w >> 2;
    const unsigned R2 = rt2 * 128 + lw * 32 + half * 16 + t16;

    auto dloads = [&](int i0, int i1) {
        const rsrc_t rsD = __builtin_amdgcn_make_buffer_rsrc((void*)WD, 0, 1024 * 3584 * 2, 0x00020000);
#pragma unroll
        for (int i = i0; i < i1; i++)
            wreg[16 + i] = __builtin_amdgcn_raw_buffer_load_b128(rsD, kr * 4096 + (l & 7) * 16, (kc * 256 + lw * 64 + (i >> 1) * 16 + (i & 1)) * 2048 + cg * 128, W_AUX);
        FRESH();
    };
    unsigned wp1[2][4][8];
    v4u xres[2], wrv[2];
    v2u pl;
    unsigned nh;
    unsigned so, va0, va1, wb[4], vFl, sFl, swc, sG5, sG5r, sRep, nr, aXw, aXr, vG5;
    f32x4 acc2[2][4];
    bf16x8 sop[2];
    unsigned pv;
    auto place = [&]() {
        SCHED();
        PIN(pl);
        if (RARE(rfl(pl[1]) != tag)) { unsigned c[1]; sweep<1>(rsWS, 0, WS_PR + 1024, 0, tag, c); pl[0] = c[0]; }
        pv = rfl(pl[0]);
        nh = ((bid ^ (pv >> 4)) & 4) * 2048;
        asm volatile("" : "+s"(pv), "+s"(nh));
    };
    auto prep2 = [&]() {
        SCHED();
        vFl = t16 * 128 + g * 8;
        sFl = WS_F4 + (rt2 & 12) * 4096 + (rt2 & 3) * 2048 + nh;
        va0 = WS_P4 + ((rt2 & 7) * 8 + lw * 2 + half) * 65536 + (nh ^ (~rt2 & 4) * 2048) + l * 16; va1 = va0 ^ 65536;
        so = (half + rot2 + (rot2 & 24)) * 1024;
#pragma unroll
        for (int nt = 0; nt < 4; nt++) { wb[nt] = ldsb + L_WO + half * 65536 + (4 * g + q4) * 128 + ((nt ^ ((2 * g + (q4 >> 1)) & 3)) * 32) + p4 * 8; PIN(wb[nt]); }
        swap16(xres[0]); swap16(xres[1]); swap16(wrv[0]); swap16(wrv[1]);
        asm volatile("s_getreg_b32 %0, hwreg(HW_REG_XCC_ID, 0, 3)" : "=s"(sRep));
        sRep ^= pv & 4;
        nr = (sRep & 4) * 2048;
        sRep = WS_GK + (sRep + (idx & 1) * 8) * 2048;
        sG5r = WS_G5 + (rt2 & 7) * 65536 + lw * 16384 + half * 4096 + nh;
        sG5 = sG5r + ct * 256;
        aXw = ldsb + L_XB + w * 4096 + l * 16; aXr = aXw ^ 16384; vG5 = g == q4 ? t16 * 16 : OOB;
        asm volatile("" : "+s"(sG5), "+s"(sG5r), "+s"(sRep), "+s"(nr), "+s"(sFl), "+s"(nh), "+s"(so));
        PIN(aXw); PIN(aXr); PIN(vG5); PIN(va0); PIN(va1); PIN(vFl);
#pragma unroll
        for (int nt = 0; nt < 4; nt++) {
            const unsigned x0 = xres[nt >> 1][2 * (nt & 1)], x1 = xres[nt >> 1][2 * (nt & 1) + 1];
            acc2[0][nt] = (f32x4){bflo(x0), bfhi(x0), bflo(x1), bfhi(x1)};
            PIN(acc2[0][nt]);
        }
        unsigned m0 = p4 == 0 ? 0x3F803F80u : 0u, m1 = p4 == 1 ? ~0u : 0u;
        PIN(m0); PIN(m1);
#pragma unroll
        for (int i = 0; i < 2; i++) {
            v4u u;
#pragma unroll
            for (int j = 0; j < 4; j++) u[j] = (wrv[i][j] & m1) | m0;
            sop[i] = __builtin_bit_cast(bf16x8, u);
            PIN(sop[i]);
        }
        float sw = 0.f;
#pragma unroll
        for (int i = 0; i < 8; i++) sw = dot2(wrv[i >> 2][i & 3], 0x3F803F80u, sw);
        swc = f2s(xrow_sum(sw));
        PIN(swc);
        SCHED();
    };
    auto res_loads = [&]() {
        pl = ldg(rsWS, 0, WS_PR + 1024);
#pragma unroll
        for (int i = 0; i < 2; i++) {
            xres[i] = *(const v4u*)(X + (size_t)R2 * 1024 + ct * 64 + i * 32 + 8 * gx);
            wrv[i] = *(const v4u*)(WR + ct * 64 + i * 32 + 8 * gx);
        }
        FRESH();
    };

    if (w < 4) {
        const unsigned wm = w & 1, hh = w >> 1;
        f32x4 acc[4][6];
        bf16x8 wf[2], wfq[2];
        float mbt[4], rst[4], csv[2];
        f32x4 mbv[4], csq[2], csk[2];
#pragma unroll
        for (int i = 0; i < 4; i++)
#pragma unroll
            for (int j = 0; j < 6; j++) { acc[i][j] = zero4; PIN(acc[i][j]); }
        {
            const unsigned aoff0 = ldsb + (wm * 64 + t16) * 128 + (((0 + g) ^ (l & 7)) * 16);
            const unsigned aoff1 = ldsb + (wm * 64 + t16) * 128 + (((4 + g) ^ (l & 7)) * 16);
            const unsigned sk = ((q4 >> 1) & 1) | ((g & 1) << 1);
            const unsigned boffA = ldsb + 16384 + (8 * g + q4) * 128 + (((2 * hh) ^ sk) * 32) + p4 * 8;
            const unsigned boffB = ldsb + 16384 + (8 * g + q4) * 128 + (((2 * hh + 1) ^ sk) * 32) + p4 * 8;
            bf16x8 A[2][4], B[3][2];
            s16x4 lo;
            unsigned a0[3], a1[3], bA[3], bB[3];
#pragma unroll
            for (int i = 0; i < 3; i++) {
                a0[i] = aoff0 + i * STAGE_B; a1[i] = aoff1 + i * STAGE_B; bA[i] = boffA + i * STAGE_B; bB[i] = boffB + i * STAGE_B;
                PIN(a0[i]); PIN(a1[i]); PIN(bA[i]); PIN(bB[i]);
            }
            unsigned aT = ldsb + L_MB + (wm * 64 + t16) * 4, aS = ldsb + L_SD + (wm * 64 + t16) * 2, aM = ldsb + L_MB + wm * 256 + g * 16;
            unsigned aC = ldsb + L_CS + hh * 128 + g * 16, aV = ldsb + L_CS + (128 + hh * 32 + t16) * 4;
            unsigned aW = ldsb + L_WPHI + hh * 2048 + (4 * g + q4) * 64 + p4 * 8, aK = ldsb + L_KH + w * 2048 + l * 16;
            PIN(aT); PIN(aS); PIN(aM); PIN(aC); PIN(aV); PIN(aW); PIN(aK);
            pre_addr();
            READ_TAG();
            waitv<5>();
            PIN(xr[0]); PIN(xr[1]); PIN(xr[2]); PIN(xr[3]);
            {
                float s[2], q[2], x0[2];
#pragma unroll
                for (int rr = 0; rr < 2; rr++) {
                    x0[rr] = bflo(rfl(xr[2 * rr][0]));
                    s[rr] = 0.f; q[rr] = 0.f;
#pragma unroll
                    for (int i = 0; i < 4; i++) {
                        const float a = bflo(xr[2 * rr][i]) - x0[rr], b = bfhi(xr[2 * rr][i]) - x0[rr];
                        const float c = bflo(xr[2 * rr + 1][i]) - x0[rr], d = bfhi(xr[2 * rr + 1][i]) - x0[rr];
                        s[rr] += (a + b) + (c + d);
                        q[rr] += (a * a + b * b) + (c * c + d * d);
                    }
                }
                const float sq = row_sum(xrow_scatter<false>(s[0], s[1], q[0], q[1]));
#define RLF(x, n) __uint_as_float(__builtin_amdgcn_readlane(__float_as_uint(x), (n)))
                const float r0 = RLF(sq, 0), r1 = RLF(sq, 16), r2 = RLF(sq, 32), r3 = RLF(sq, 48);
                const float sm = (l ? r1 : r0) * (1.0f / 1024.0f), qm = (l ? r3 : r2) * (1.0f / 1024.0f);
                const float var = fmaxf(qm - sm * sm, 0.f);
                if (l < 2) st_gran(ws, WS_G0 + (srow1 + l) * 8, tag, pk2((l ? x0[1] : x0[0]) + sm, sqrtf(rbf(rbf(var) + 1e-5f))));
            }
            waitv<1>();
            WAITL0();
            ARR_MARK(0);
            BAR();
            SCHED();
            STAMPW(1, 0);
            auto rdB = [&](int u, int j, int i) {
                if (i & 1) B[j % 3][i >> 1] = cat8(lo, bq(bA[u], bB[u], j, i)); else lo = bq(bA[u], bB[u], j, i);
            };
            auto rdA = [&](int u, int i) { A[i & 1][i >> 1] = lds128(((i & 1) ? a1[u] : a0[u]) + (i >> 1) * 2048); };
            auto head = [&](int u) {
                group(acc, A, B[0], 0, [&](int i) { rdB(u, 1 + (i >> 2), i & 3); });
                group(acc, A, B[1], 1, [&](int i) { if (i < 4) rdB(u, 3, i); });
                group(acc, A, B[2], 2, [&](int i) { if (i < 4) rdB(u, 4, i); });
                group(acc, A, B[0], 3, [&](int i) { if (i < 4) rdB(u, 5, i); });
                WL0();
            };
#pragma unroll
            for (int i = 0; i < 4; i++) rdB(0, 0, i);
#pragma unroll
            for (int i = 0; i < 8; i++) rdA(0, i);
            SCHED();
#pragma nounroll
            for (unsigned t = 0; t < 5; t++) {
#pragma unroll
                for (int u = 0; u < 3; u++) {
                    head(u);
                    ARR_MARK(1 + t * 3 + u);
                    BAR();
                    SCHED();
                    group(acc, A, B[1], 4, [&](int i) { if (i < 4) rdB((u + 1) % 3, 0, i); });
                    group(acc, A, B[2], 5, [&](int i) { rdA((u + 1) % 3, i); });
                }
            }
            unsigned kw[4][4];
            f32x2 kx;
            auto kpiece = [&](int n) {
                if (n > 0) { kw[((n - 1 >> 2) + 2) & 3][(n - 1) & 3] = pk2(kx[0], kx[1]); PIN(kw[((n - 1 >> 2) + 2) & 3][(n - 1) & 3]); }
                if (n < 16) {
                    const int mi = ((n >> 2) + 2) & 3, j = n & 3;
                    const f32x4 &c = acc[mi][2 + (j >> 1)], &s_ = csk[j >> 1];
                    float x0 = __builtin_fmaf(mbt[mi], s_[2 * (j & 1)], c[2 * (j & 1)]); PIN(x0);
                    float x1 = __builtin_fmaf(mbt[mi], s_[2 * (j & 1) + 1], c[2 * (j & 1) + 1]); PIN(x1);
                    kx = (f32x2){x0, x1};
                }
                if (n == 5 || n == 9) *(LAS v4u*)(aK + (n == 9) * 1024) = (v4u){kw[2 + (n == 9)][0], kw[2 + (n == 9)][1], kw[2 + (n == 9)][2], kw[2 + (n == 9)][3]};
            };
            group(acc, A, B[0], 0, [&](int i) { rdB(0, 1 + (i >> 2), i & 3); });
            group(acc, A, B[1], 1, [&](int i) {
                if (i < 4) { rdB(0, 3, i); mbt[(i + 2) & 3] = ldsf(aT + ((i + 2) & 3) * 64); }
                else if (i < 6) csk[i - 4] = ldsf4(aC + 256 + (i - 4) * 64);
                else wf[i - 6] = cat8(tr_read(aW + (i - 6) * 32), tr_read(aW + (i - 6) * 32 + 1024));
            });
            group(acc, A, B[2], 2, [&](int i) { if (i < 4) rdB(0, 4, i); else mbv[i - 4] = ldsf4(aM + (i - 4) * 64); kpiece(i); });
            group(acc, A, B[0], 3, [&](int i) { if (i < 4) rdB(0, 5, i); if (i == 2 || i == 3) csv[i - 2] = ldsf(aV + (i - 2) * 64); kpiece(8 + i); });
            WL0();
            ARR_MARK(16);
            BAR();
            SCHED();
            kpiece(16);
            f32x4 p[2][2];
#pragma unroll
            for (int i = 0; i < 4; i++) p[i >> 1][i & 1] = MFMA(wf[i & 1], mk8(kw[i >> 1][0], kw[i >> 1][1], kw[i >> 1][2], kw[i >> 1][3]), zero4);
            SCHED();
            STAMPW(2, 0);
#pragma unroll
            for (int mi = 0; mi < 4; mi++) rst[mi] = lds_bf(aS + mi * 32);
#pragma unroll
            for (int a = 0; a < 2; a++)
#pragma unroll
                for (int dt = 0; dt < 2; dt++) {
                    const f32x4 x0 = acc[2 * a][4 + dt] + mbv[2 * a] * csv[dt], x1 = acc[2 * a + 1][4 + dt] + mbv[2 * a + 1] * csv[dt];
                    *(LAS v4u*)(aK + (L_V - L_KH) + w * 2048 + (a * 2 + dt) * 1024) = (v4u){pk2(x0[0], x0[1]), pk2(x0[2], x0[3]), pk2(x1[0], x1[1]), pk2(x1[2], x1[3])};
                }
            SCHED();
            rst[0] = __builtin_amdgcn_rcpf(rst[0]); rst[1] = __builtin_amdgcn_rcpf(rst[1]);
            bf16x8 f[4];
            struct HookC {
                decltype(v_reads)& vr; decltype(sd_reads)& sr;
                DI void operator()(int k, float& x) const { if (k == 1) { PIN(x); WAITL0(); ARR_MARK(17); BAR(); PIN(x); } if (k == 3) { PIN(x); vr(); sr(); PIN(x); } }
                DI void operator()(int, float&, float&) const {}
            };
            softmax2<true>(p, rst[0], rst[1], g >= 2, f, HookC{v_reads, sd_reads});
            img_write2(ia, f);
#pragma unroll
            for (int ft = 0; ft < 2; ft++) wfq[ft] = cat8(tr_read(aW + ft * 32 + 4096), tr_read(aW + ft * 32 + 5120));
            csq[0] = ldsf4(aC); csq[1] = ldsf4(aC + 64);
            STAMPW(3, 0);
            kv_tile([]() {});
            STAMPW(4, 0);
            SCHED();
            group(acc, A, B[1], 4, [&](int i) { if (i == 2 || i == 3) { rst[i] = __builtin_amdgcn_rcpf(rst[i]); PIN(rst[i]); } });
            group(acc, A, B[2], 5, [&](int) {});
            asm volatile("s_nop 7\n\ts_nop 1");
            SCHED();
        }
        bf16x8 phq[2][4];
        v4u kg[6];
        auto kg_ld = [&](int k) { return k < 4 ? LD16(l * 16, WS_G3 + (hq * 2 + hh) * 4096 + k * 1024) : LD16(g * 16, WS_GZ + (hq * 2 + hh) * 128 + (k - 4) * 64); };
        {
            f32x4 pq[2][2][2];
#pragma unroll
            for (int mi = 0; mi < 4; mi++) {
                const f32x4 x0 = acc[mi][0] + mbt[mi] * csq[0], x1 = acc[mi][1] + mbt[mi] * csq[1];
                const bf16x8 qf = mk8(pk2(x0[0], x0[1]), pk2(x0[2], x0[3]), pk2(x1[0], x1[1]), pk2(x1[2], x1[3]));
                pq[mi >> 1][mi & 1][0] = MFMA(wfq[0], qf, zero4); pq[mi >> 1][mi & 1][1] = MFMA(wfq[1], qf, zero4);
            }
            MFMA_SETTLE2(pq[0]); MFMA_SETTLE2(pq[1]);
            softmax2<false>(pq[0], rst[0], rst[1], g >= 2, phq[0]);
#pragma unroll
            for (int i = 0; i < 4; i++) PIN(phq[0][i]);
            SCHED();
            red_issue();
            res_loads();
            SCHED();
            kv_reduce();
            SCHED();
            BAR();
            SCHED();
            softmax2<false>(pq[1], rst[2], rst[3], g >= 2, phq[1]);
#pragma unroll
            for (int i = 0; i < 4; i++) { PIN(phq[0][i]); PIN(phq[1][i]); }
            SCHED();
        }
        STAMPW(5, 0);
        STAMPW(26, 0);
        v2u fv = {tag ^ 0x5bd1e995u, tag};
        place();
        PIN(xres[0]); PIN(xres[1]); PIN(wrv[0]); PIN(wrv[1]);
        unsigned sF = WS_F4 + (rt & 12) * 4096 + (rt & 3) * 2048 + hq * 128 + w * 8 + nh, vF = l == 0 ? 0 : OOB;
        {
            unsigned vO = l * 16, sO = WS_P4 + ((rt & 7) * 8 + wm * 4) * 65536 + (hq >> 2) * 16384 + ((hq & 3) * 2 + hh) * 1024 + (nh ^ (~rt & 4) * 2048);
            asm volatile("" : "+s"(sO), "+s"(sF));
            PIN(fv); PIN(vF);
#define S2N 1
            SLEEP(S2N);
            sweepz<6, SR2>(kg_ld, kg);
            STAMPW(6, 0);
            bf16x8 kvA[2][2], zA[2];
            v4u ov[4];
#pragma unroll
            for (int s = 0; s < 2; s++) {
#pragma unroll
                for (int dt = 0; dt < 2; dt++) kvA[s][dt] = mk8(kg[2 * s][2 * dt], kg[2 * s][2 * dt + 1], kg[2 * s + 1][2 * dt], kg[2 * s + 1][2 * dt + 1]);
                zA[s] = __builtin_bit_cast(bf16x8, kg[4 + s]);
            }
#define PH(mi, s) phq[(mi) >> 1][2 * ((mi) & 1) + (s)]
            f32x4 dn[4], n0[4], n1[4];
            float id[4];
#pragma unroll
            for (int mi = 0; mi < 4; mi++) dn[mi] = MFMA(zA[0], PH(mi, 0), zero4);
#pragma unroll
            for (int mi = 0; mi < 4; mi++) dn[mi] = MFMA(zA[1], PH(mi, 1), dn[mi]);
            SCHED();
#pragma unroll
            for (int mi = 0; mi <= 4; mi++) {
                if (mi < 4) {
                    n0[mi] = MFMA(kvA[0][0], PH(mi, 0), zero4); n1[mi] = MFMA(kvA[0][1], PH(mi, 0), zero4);
                    n0[mi] = MFMA(kvA[1][0], PH(mi, 1), n0[mi]); n1[mi] = MFMA(kvA[1][1], PH(mi, 1), n1[mi]);
                    id[mi] = __builtin_amdgcn_rcpf(rbf(rbf(dn[mi][0]) + 1e-6f));
                }
                if (mi > 0) {
                    const f32x4 a = n0[mi - 1] * id[mi - 1], b = n1[mi - 1] * id[mi - 1];
                    ov[mi - 1] = (v4u){pk2(a[0], a[1]), pk2(a[2], a[3]), pk2(b[0], b[1]), pk2(b[2], b[3])};
                    ST16(ov[mi - 1], vO + (mi - 1) * 65536, sO);
                }
                if (mi < 4) { SGB(0x8, 1); SGB(0x2, 6); SGB(0x8, 1); SGB(0x2, 3); SGB(0x8, 1); SGB(0x2, 3); SGB(0x8, 1); SGB(0x2, 4); }
                SCHED();
            }
#pragma unroll
            for (int mi = 0; mi < 4; mi++) PIN(ov[mi]);
        }
        waitv<0>();
        __builtin_amdgcn_raw_buffer_store_b64(fv, rsWS, vF, sF, AUX_SC1);
        STAMPW(7, 0);
    } else {
        const unsigned pw = lw & 1;
        const rsrc_t rsG = __builtin_amdgcn_make_buffer_rsrc((void*)WG, 0, 1024 * 3584 * 2, 0x00020000);
        const rsrc_t rsU = __builtin_amdgcn_make_buffer_rsrc((void*)WU, 0, 1024 * 3584 * 2, 0x00020000);
        unsigned vwl = kr * 57344 + (l & 7) * 16;
        unsigned sg = (kq * 256 + lw * 64) * 7168 + cg1 * 128, su = (kq * 256 + lw * 64) * 7168 + cg1u * 128;
        auto wloads = [&](int n0, int n1) {
#pragma unroll
            for (int n = n0; n < n1; n++)
                asm volatile("buffer_load_dwordx4 %0, %1, %2, %3 offen nt" : "=v"(wreg[n]) : "v"(vwl), "s"(n < 8 ? rsG : rsU), "s"((n < 8 ? sg : su) + (n & 7) * 7168));
        };
        auto dl_asm = [&]() {
            const rsrc_t rsD = __builtin_amdgcn_make_buffer_rsrc((void*)WD, 0, 1024 * 3584 * 2, 0x00020000);
#pragma unroll
            for (int i = 0; i < DN0; i++)
                asm volatile("buffer_load_dwordx4 %0, %1, %2, %3 offen nt" : "=v"(wreg[16 + i]) : "v"(kr * 4096 + (l & 7) * 16), "s"(rsD),
                             "s"((kc * 256 + lw * 64 + (i >> 1) * 16 + (i & 1)) * 2048 + cg * 128));
        };
        unsigned aK, aW, aS;
        auto k_pre = [&]() {
            pre_addr();
            aK = ldsb + L_KH + lw * 2048 + l * 16; aW = ldsb + L_WPHI + (lw >> 1) * 2048 + (4 * g + q4) * 64 + p4 * 8; aS = ldsb + L_SD + (pw * 64 + 32 + t16) * 2;
            PIN(aK); PIN(aW); PIN(aS);
        };
        auto kfeat = [&](auto hook) {
            bf16x8 wf[2];
            float rst[2];
#pragma unroll
            for (int ft = 0; ft < 2; ft++) {
                wf[ft] = cat8(tr_read(aW + ft * 32), tr_read(aW + ft * 32 + 1024));
                rst[ft] = __builtin_amdgcn_rcpf(lds_bf(aS + ft * 32));
            }
            sd_reads();
            PIN(wf[0]); PIN(wf[1]); PIN(rst[0]); PIN(rst[1]);
            SCHED();
            ARR_MARK(16);
            BAR();
            SCHED();
            const bf16x8 kf0 = lds128(aK), kf1 = lds128(aK + 1024);
            SCHED();
            hook(-1, rst[0]);
            hook(0, rst[0]);
            SCHED();
            f32x4 p[2][2];
            p[0][0] = MFMA(wf[0], kf0, zero4); p[0][1] = MFMA(wf[1], kf0, zero4);
            p[1][0] = MFMA(wf[0], kf1, zero4); p[1][1] = MFMA(wf[1], kf1, zero4);
            MFMA_SETTLE2(p);
            bf16x8 f[4];
            softmax2<true>(p, rst[0], rst[1], g >= 2, f, hook);
            img_write2(ia, f);
            SCHED();
            hook(5, rst[0]);
            SCHED();
            STAMPW2(3, 4, 7);
            kv_tile([&]() { hook(6, rst[0]); });
            STAMPW2(4, 4, 7);
        };
        auto wblk = [&](int k) { return k == 0 ? 22 + lw : k == 1 ? (lw < 3 ? 26 + lw : 4) : k < 6 ? 4 * k - 3 + lw : k == 6 ? lw : (lw < 3 ? 29 + lw : 21); };
        auto wo_init = [&]() {
#pragma unroll
            for (int i = 0; i < 4; i++) { vwo[i] = vWO + i * (16384 - 1024); PIN(vwo[i]); }
        };
        unsigned hs[8];
        auto wl_pre = [&]() { asm volatile("" : "+s"(sg), "+s"(su)); PIN(vwl); };
        auto wo_pre = [&]() {
            wo_init();
#pragma unroll
            for (int k = 0; k < 8; k++) { hs[k] = wo_so(wblk(k)); asm volatile("" : "+s"(hs[k])); }
        };
        auto wo_rng = [&](int k0, int k1) {
#pragma unroll
            for (int k = k0; k < k1; k++) dma16x4(ldsb + L_WO + wblk(k) * 4096, vwo, rsWO, hs[k]);
        };
        auto pf = [&](int i) {
            if (i == 0) { wloads(0, 6); SCHED(); if (lw < 2) wo_pre(); SCHED(); }
            if (i < 3 && !ffn) wo_rng(i, i + 1);
            if (i == 1 || i == 2) wloads(6 * i, i == 1 ? 12 : 16);
#define DHK 6
            if (i == DHK) dl_asm();
        };
        struct Hook {
            decltype(pf)& f; decltype(v_reads)& vr;
            DI void operator()(int k, float& x) const { PIN(x); if (k == 1) { ARR_MARK(17); BAR(); } if (k == 3) vr(); f(3 + k); PIN(x); }
            DI void operator()(int k, float& x, float& y) const { PIN(y); f(3 + k); PIN(x); }
        };
        auto tail16 = [&]() {
            kfeat(Hook{pf, v_reads});
            if (ffn) wo_rng(0, 3);
            wo_rng(3, 4);
            HST(27);
            if (ffn) waitv<18>(); else waitv<6>();
#pragma unroll
            for (int n = 0; n < 16 + DN0; n++) PIN(wreg[n]);
        };
        auto vT23 = [&]() {
            const unsigned n = (idx * 2 + pw) * 64 + l, r = n / 12, c = n % 12;
            return idx < 24 ? ((r & 127) + (r >> 7) * 256 + 128) * 6144 + (c >> 2) * 2048 + (((xcd >> 1) * 4 + (c >> 2 ? 0 : 4) + (c & 3)) & 15) * 128 : OOB;
        };
        if (lw < 2) {
            const unsigned sA = (rt * 128 + lw * 64) * 2048, sB = lw * 32 * 6144 + hp * 128, sBq = lw * 32 * 6144 + hq * 128;
            unsigned buf = 1;
            unsigned va[4], vb[4];
#pragma unroll
            for (int i = 0; i < 4; i++) { va[i] = vA + i * (16384 - 1024); vb[i] = ((i & 1) ? vB1 : vB0) + i * (49152 - 1024); PIN(va[i]); PIN(vb[i]); }
            auto stage = [&](unsigned s, unsigned bf, int i0, int i1) {
                const unsigned kt = (s + rot) & 15;
                const unsigned lb = ldsb + bf * STAGE_B;
                const unsigned sa = sA + kt * 128, sb = kt * 393216;
                if (i0 <= 0 && 0 < i1) dma16x4(lb + lw * 8192, va, rsX, sa);
                if (i0 <= 1 && 1 < i1) dma16x4(lb + lw * 8192 + 4096, va, rsX, sa + 65536);
                if (i0 <= 2 && 2 < i1) dma16x4(lb + 16384 + 8192 + lw * 4096, vb, rsW, sb + sB + 2048);
                if (i0 <= 3 && 3 < i1) dma16x4(lb + 16384 + 16384 + lw * 4096, vb, rsW, sb + sB + 4096);
                if (i0 <= 4 && 4 < i1) dma16x4(lb + 16384 + lw * 4096, vb, rsW, sb + sBq);
            };
            stage(1, 1, 0, 4);
            SCHED();
            unsigned vt = vT23();
            PIN(vt);
            wl_pre(); k_pre();
            waitv<16>(); ARR_MARK(0); BAR();
            stage(1, 1, 4, 5);
            touch(vt, rsW, (xcd & 1) * 512 * 6144);
            waitv<1>(); ARR_MARK(1); BAR();
            stage(2, 2, 0, 5); stage(3, 0, 0, 3);
            waitv<12>(); ARR_MARK(2); BAR();
            stage(3, 0, 3, 5); stage(4, 1, 0, 4);
            waitv<16>(); ARR_MARK(3); BAR();
            stage(4, 1, 4, 5);
            buf = 2;
#pragma nounroll
            for (unsigned s = 5; s < 16; s++) {
                if (s > 5) { waitv<20>(); ARR_MARK(s - 2); BAR(); }
                stage(s, buf, 0, 5);
                buf = buf == 2 ? 0 : buf + 1;
            }
            waitv<20>(); ARR_MARK(14); BAR();
            pf(0);
            READ_TAG();
            if (ffn) waitv<6>(); else waitv<10>();
            ARR_MARK(15); BAR();
            pf(1);
            tail16();
        } else {
            SCHED();
            unsigned v3;
            const unsigned h3 = (xcd >> 1) * 4 + 3, kv3 = !(xcd & 2), wv = idx * 2 + pw;
            auto line3 = [&](unsigned per, unsigned rows, auto rowf) {
                const unsigned n = wv * (kv3 ? per : (per + 2) / 3) + l, r3 = kv3 ? n / 3 : n, c3 = kv3 ? n % 3 : 0;
                return (idx < 24 && l < (kv3 ? per : (per + 2) / 3) && r3 < rows) ? rowf(r3) * 6144 + c3 * 2048 + ((c3 ? h3 : h3 + 4) & 15) * 128 : OOB;
            };
            v3 = line3(32, 512, [](unsigned r) { return r; });
            touch(line3(16, 256, [](unsigned r) { return (r & 127) + (r >> 7) * 256; }), rsW, (xcd & 1) * 512 * 6144);
            {
                const rsrc_t rsP = __builtin_amdgcn_make_buffer_rsrc((void*)WPHI, 0, 32 * 1024 * 2, 0x00020000);
#pragma unroll
                for (int i = 0; i < 4; i++) dma16(ldsb + L_WPHI + pw * 4096 + i * 1024, l * 16, rsP, (pw ? hq : hp) * 4096 + i * 1024);
            }
            v4u sv = {SENT, SENT, SENT, SENT};
            unsigned vc4 = l < 16 ? WS_G3 + (hp * 4 + (rt >> 2)) * 2048 + pw * 1024 + (rt & 3) * 256 + l * 16
                         : l < 24 ? WS_P2 + (hp * 16 + rt) * P2_STRIDE + 8192 + pw * 128 + (l - 16) * 16
                         : (l < 32 && pw == 0 && (rt & 7) == 0) ? WS_GZ + (hp * 2 + (rt >> 3)) * 128 + (l - 24) * 16
                         : l >= 56 ? WS_F4 + (rt & 12) * 4096 + (rt & 3) * 2048 + hq * 128 + pw * 8192 + (l & 7) * 16 : OOB;
            auto clears = [&](int i0, int i1) {
#pragma unroll
                for (int i = i0; i < i1; i++) {
                    if (i < 4) ST16(sv, l * 16, WS_P2 + (hp * 16 + rt) * P2_STRIDE + pw * 4096 + i * 1024);
                    if (i == 4) ST16(sv, vc4, 0);
                    if (i > 4) ST16(sv, l * 16, WS_TL + (bid * 4 + pw * 2 + i - 5) * 1024);
                }
                FRESH();
            };
            PIN(vc4); PIN(sv);
            unsigned ca = ldsb + 16384 + pw * 8192 + (8 * g + q4) * 128 + (((rt & 3) ^ (((q4 >> 1) & 1) | ((g & 1) << 1))) * 32) + p4 * 8;
            PIN(ca);
            bf16x8 cf[4];
            f32x4 cd[2] = {zero4, zero4};
            auto cs_read = [&](int st) {
#pragma unroll
                for (int i = 0; i < 4; i++) cf[i] = cat8(tr_read(ca + (st % 3) * STAGE_B + i * 4096), tr_read(ca + (st % 3) * STAGE_B + i * 4096 + 512));
                SCHED();
            };
            READ_TAG();
            waitv<5>();
            ARR_MARK(0);
            BAR();
            SCHED();
            cs_read(0);
            FRESH();
            const rsrc_t rsC = __builtin_amdgcn_make_buffer_rsrc((void*)pc0, 0, clen, 0x00020000);
            auto code_pf = [&]() { touch(l < 6 ? l * 128 : OOB, rsC, (idx * 2 + pw) * 768); };
            v4u gl[4];
            float s0 = 0.f, s1 = 0.f;
            unsigned bad;
            v2u gs, gc;
            unsigned vgc = WS_GC + (pw ? hq : hp) * 256 + (l & 31) * 8;
            unsigned lat[2] = {~0u, ~0u};
            unsigned vgl = (l < 48 ? l : 0) * 16;
            unsigned aw0 = ldsb + L_CS + pw * 384 + (vgl >> 1), aw1 = ldsb + L_MB + (pw * 64 + l) * 4, aw2 = ldsb + L_SD + (pw * 64 + l) * 2;
            static_for<15>([&](auto tc) {
                constexpr int t = decltype(tc)::value;
                WAITL0();
                ARR_MARK(t + 1);
                BAR();
                SCHED();
                if constexpr (t < 4) {
                    __builtin_amdgcn_s_setprio(2);
#pragma unroll
                    for (int i = 0; i < 4; i++) cd[i & 1] = MFMA(ones, cf[(i & 1) * 2 + (i >> 1)], cd[i & 1]);
                    SCHED();
                    __builtin_amdgcn_s_setprio(0);
                    if constexpr (t < 3) cs_read(t + 1);
                    else __builtin_amdgcn_raw_buffer_store_b64((v2u){__float_as_uint(g ? cd[1][0] : cd[0][0]), tag}, rsWS, (g == 1 || g + pw == 0) ? g * 512 + t16 * 8 : OOB,
                                                               WS_G1 + ((hp * 4 + (rt >> 2)) * 192 + pw * 64 + (rt & 3) * 16) * 8, AUX_SC1);
                }
                if constexpr (t == 0) touch(v3, rsW, (~xcd & 1) * 512 * 6144);
                if constexpr (t == 12) code_pf();
                if constexpr (t == 8) {
                    waitv<0>();
                    __builtin_amdgcn_raw_buffer_store_b64((v2u){0u, tag}, rsWS, l == 0 ? 0 : OOB, WS_GC + ((hp * 2 + pw) * 16 + rt) * 8, AUX_SC1);
                }
                if constexpr (t == 8 || t == 9) {
#pragma unroll
                    for (int r = (t - 8) * 2; r < (t - 8) * 2 + 2; r++)
                        asm volatile("buffer_load_dwordx4 %0, %1, %2, %3 offen sc1" : "=v"(gl[r]) : "v"(vgl), "s"(rsWS), "s"(WS_G1 + ((hp * 4 + r) * 192 + pw * 96) * 8) : "memory");
                }
                if constexpr (t == 6) {
                    asm volatile("buffer_load_dwordx2 %0, %1, %2, %3 offen sc1" : "=v"(gs) : "v"(l * 8), "s"(rsWS), "s"(WS_G0 + (rt * 128 + pw * 64) * 8) : "memory");
                }
                if constexpr (t == 11) {
                    waitv<0>();
                    PIN(gs);
                    bad = gs[1] ^ tag;
#pragma unroll
                    for (int r = 0; r < 4; r++) {
                        PIN(gl[r]);
                        bad |= (gl[r][1] ^ tag) | (gl[r][3] ^ tag);
                        s0 += __uint_as_float(gl[r][0]); s1 += __uint_as_float(gl[r][2]);
                    }
                    PIN(s0); PIN(s1); PIN(bad);
                }
                if constexpr (t == 12) {
                    if (RARE(!__all(bad == 0))) {
                        s0 = 0.f; s1 = 0.f;
#pragma nounroll
                        for (unsigned r = 0; r < 4; r++) {
                            unsigned a[2];
                            sweep<2>(rsWS, vgl, WS_G1 + ((hp * 4 + r) * 192 + pw * 96) * 8, 8, tag, a);
                            s0 += __uint_as_float(a[0]); s1 += __uint_as_float(a[1]);
                        }
                        unsigned c[1];
                        sweep<1>(rsWS, l * 8, WS_G0 + (rt * 128 + pw * 64) * 8, 0, tag, c);
                        gs[0] = c[0];
                    }
                    *(LAS f32x2*)(aw0) = (f32x2){-s0, -s1};
                    stsf(aw1, bflo(gs[0]));
                    *(LAS u16*)(aw2) = (u16)(gs[0] >> 16);
                    asm volatile("buffer_load_dwordx2 %0, %1, %2, 0 offen sc1" : "=v"(gc) : "v"(vgc), "s"(rsWS) : "memory");
                    SCHED();
                }
#define PT0 10
                if constexpr (t == PT0 - 2) { if (RARE(bid + pw == 0)) ST16(sv, (l >> 5) * 8192 + (l & 31) * 16, WS_PR); }
                if constexpr (t >= PT0 && t < 12) {
                    if (RARE(bid + pw == 0)) {
                        u64 c0, c1; unsigned x;
                        asm volatile("s_memtime %0\n\ts_load_dword %2, %3, %4\n\ts_waitcnt lgkmcnt(0)\n\ts_memtime %1\n\ts_waitcnt lgkmcnt(0)"
                                     : "=&s"(c0), "=&s"(c1), "=&s"(x) : "s"(ws), "i"(WS_PR + (t & 1) * 8192 + ((t - PT0) >> 1) * 128) : "memory");
                        lat[t & 1] = __builtin_elementwise_min(lat[t & 1], (unsigned)c1 - (unsigned)c0);
                        if constexpr (t == 11) {
                            asm volatile("s_getreg_b32 %0, hwreg(HW_REG_XCC_ID, 0, 3)" : "=s"(x));
                            __builtin_amdgcn_raw_buffer_store_b64((v2u){(lat[1] < lat[0] ? 0x44 : 0) ^ (x & 4), tag}, rsWS, l == 0 ? 0 : OOB, WS_PR + 1024, AUX_SC1);
                        }
                    }
                }
                if constexpr (t == 5) k_pre();
                if constexpr (t == 6) { wl_pre(); wo_pre(); }
                if constexpr (t == 7) { PIN(vgc); PIN(vgl); PIN(aw0); PIN(aw1); PIN(aw2); }
                clears(t < 7 ? t : 7, t < 7 ? t + 1 : 7);
                if constexpr (t == 13) {
                    waitv<0>();
                    PIN(gc);
                    if (RARE(!__all(gc[1] == tag))) { unsigned c[1]; sweep<1>(rsWS, vgc, 0, 0, tag, c); }
                    SCHED();
                    pf(0);
                }
                if constexpr (t == 14) pf(1);
            });
            tail16();
        }
        HST(28);
        auto perm1 = [&]() {
#pragma unroll
            for (int ip = 0; ip < 4; ip++)
#pragma unroll
                for (int c = 0; c < 4; c++) {
#pragma unroll
                    for (int m = 0; m < 2; m++) {
                        const unsigned a = wreg[m * 8 + 2 * ip][c], b = wreg[m * 8 + 2 * ip + 1][c];
                        wp1[m][ip][2 * c] = __builtin_amdgcn_perm(b, a, 0x05040100);
                        wp1[m][ip][2 * c + 1] = __builtin_amdgcn_perm(b, a, 0x07060302);
                    }
                }
#pragma unroll
            for (int ip = 0; ip < 4; ip++)
#pragma unroll
                for (int c = 0; c < 8; c++) { PIN(wp1[0][ip][c]); PIN(wp1[1][ip][c]); }
            SCHED();
            HST(29);
        };
#define ZP 0
        if (ZP) perm1();
        if (RARE(lw == 0 && (rt & 7) == 0)) {
            __builtin_amdgcn_s_setprio(1);
            sweepz<2, SR1>([&](int k) { return LD16(kr * P2_STRIDE + (l & 7) * 16, WS_P2 + (hp * 16 + k * 8) * P2_STRIDE + 8192 + (rt >> 3) * 128); }, pp);
            ST4(red(), (l & 8) ? OOB : ((l >> 2) & 1) * 64 + ((l & 1) * 2 + (g >> 1)) * 16 + ((l >> 1) & 1) * 8 + (g & 1) * 4, WS_GZ + (hp * 2 + (rt >> 3)) * 128);
            __builtin_amdgcn_s_setprio(0);
        }
        SCHED();
        if (!ZP) perm1();
        BAR();
        HST(30);
        res_loads();
        wo_rng(4, 8);
        HST(31);
        waitv<0>();
        PIN(xres[0]); PIN(xres[1]); PIN(wrv[0]); PIN(wrv[1]);
        place();
        STAMPW2(7, 4, 7);
    }
    prep2();
    if (w == 0) { if (S3) SLEEP(S3); poll_flags(rsWS, vFl, sFl, (v2u){tag ^ 0x5bd1e995u, tag}); }
    ARR_MARK(20);
    BAR();
    ARR_END();
    FRESH();
    SCHED();
    STAMPW2(8, 0, 4);
    bf16x8 bb[2][4];
    {
        auto aload = [&](int st, int m2) {
            const v4u r = LD16(m2 ? va1 : va0, so);
            if (m2 == 0) so = ((so | 8192) + 2048) & 0xDC00;
            return r;
        };
#define PDN 2
        constexpr int PD = PDN;
        v4u af[PD][2];
#pragma unroll
        for (int i = 0; i < PD; i++) {
#pragma unroll
            for (int m2 = 1; m2 >= 0; m2--) af[i][m2] = aload(i, m2);
            SCHED();
        }
#pragma unroll
        for (int nt = 0; nt < 4; nt++) bb[0][nt] = cat8(tr_read(wb[nt]), tr_read(wb[nt] + 2048));
#pragma unroll
        for (int nt = 0; nt < 4; nt++) acc2[1][nt] = zero4;
        SCHED();
#pragma unroll
        for (int st = 0; st < 16; st++) {
            if (st + 1 < 16) {
#pragma unroll
                for (int nt = 0; nt < 4; nt++)
                    bb[(st + 1) % (2)][nt] = cat8(tr_read(wb[nt] + (st + 1) * 4096), tr_read(wb[nt] + (st + 1) * 4096 + 2048));
            }
#pragma unroll
            for (int mm = 0; mm < 2; mm++) {
                const int m2 = mm ^ 1;
                const bf16x8 a = __builtin_bit_cast(bf16x8, af[st % PD][m2]);
#pragma unroll
                for (int nt = 0; nt < 4; nt++) acc2[m2][nt] = MFMA(bb[st % (2)][nt], a, acc2[m2][nt]);
            }
            if (st + PD < 16) {
#pragma unroll
                for (int m2 = 1; m2 >= 0; m2--) af[st % PD][m2] = aload(PD + st, m2);
            }
#pragma unroll
            for (int i = 0; i < 4; i++) { SGB(0x8, 1); SGB(0x100, 2); }
            SCHED();
        }
    }
    STAMPW2(9, 0, 4);
#pragma unroll
    for (int nt = 0; nt < 4; nt++) stsf4(aXw + nt * 1024, acc2[1][nt]);
    SCHED();
    if (half) dloads(DN0, 8);
    SCHED();
    WAITL0();
    if (half) __builtin_amdgcn_s_setprio(1);
    BAR();
    SCHED();
    v4u hpk[2];
    {
        f32x4 pa[4];
#pragma unroll
        for (int nt = 0; nt < 4; nt++) pa[nt] = ldsf4(aXr + nt * 1024);
        SCHED();
#pragma unroll
        for (int i = 0; i < 8; i++) {
            const f32x4 a = acc2[0][i >> 1] + pa[i >> 1];
            hpk[i >> 2][i & 3] = pk2(a[2 * (i & 1)], a[2 * (i & 1) + 1]);
        }
        const bf16x8 h0 = __builtin_bit_cast(bf16x8, hpk[0]), h1 = __builtin_bit_cast(bf16x8, hpk[1]);
        f32x4 gq = MFMA(h0, h0, zero4), sd = MFMA(sop[0], h0, zero4);
        gq = MFMA(h1, h1, gq); sd = MFMA(sop[1], h1, sd);
        float q0 = (l & 1) ? gq[1] : gq[0], q1 = (l & 1) ? gq[3] : gq[2];
        PIN(q0); PIN(q1);
        const v4u pv = {f2s(sd[0]), f2s((l & 2) ? q1 : q0), f2s(sd[1]), swc};
        ST16(pv, vG5, sG5);
        asm volatile("s_nop 1" :: "v"(pv));
    }
    __builtin_amdgcn_s_setprio(0);
    SCHED();
    swap16(hpk[0]); swap16(hpk[1]);
    PIN(hpk[0]); PIN(hpk[1]);
    STAMPW2(10, 0, 4);
    unsigned wp2[4][8];
    auto perm2 = [&]() {
#pragma unroll
        for (int i = 16; i < 24; i++) PIN(wreg[i]);
#pragma unroll
        for (int ip = 0; ip < 4; ip++)
#pragma unroll
            for (int c = 0; c < 4; c++) {
                const unsigned a = wreg[16 + 2 * ip][c], b = wreg[16 + 2 * ip + 1][c];
                wp2[ip][2 * c] = __builtin_amdgcn_perm(b, a, 0x05040100);
                wp2[ip][2 * c + 1] = __builtin_amdgcn_perm(b, a, 0x07060302);
            }
#pragma unroll
        for (int ip = 0; ip < 4; ip++)
#pragma unroll
            for (int c = 0; c < 8; c++) PIN(wp2[ip][c]);
    };
    const unsigned mt = rt2 * 8 + lw * 2 + half;
    v4u outv[2];
    const rsrc_t rsO = __builtin_amdgcn_make_buffer_rsrc((void*)OUT, 0, 2048 * 1024 * 2, 0x00020000);
    unsigned vOut = (R2 & ~8u) * 2048 + (t16 >> 3) * 64 + gx * 16;
    PIN(vOut);
    unsigned aG = ldsb + L_WO + 512, vX = kr, v4 = l * 4, sX = WS_C + kq * 512 + lw * 128 + nr;
    unsigned aR1 = ldsb + L_RED + (lw * 4 + g) * 512 + t16 * 32, aT1 = ldsb + L_RED + (lw & 1) * 256 + l * 4;
    unsigned aR2 = ldsb + L_RED + 8192 + (lw * 4 + g) * 256 + (t16 & 7) * 32 + (t16 >> 3) * 16, aT2 = ldsb + L_RED + 8192 + l * 4, aB = kr * 8;
    const unsigned cx = (lw & 1) ? cg1u : cg1;
    unsigned s8w = WS_G8 + (cx >> 2) * 16384 + (lw >> 1) * 8192 + (kq * 2 + (lw & 1)) * 1024 + (cx & 3) * 256;
    unsigned s8r = WS_G8 + kc * 16384 + lw * 256 + nr, s8s = s8r + 4096;
    unsigned s9w = WS_G9 + (cg >> 2) * 16384 + (cg & 3) * 2048 + kc * 128, v9 = (l & 31) * 4 + (nh ^ (((l >> 3) ^ bid) & 4) * 2048);
    float a1[2][8];
    if (half) {
#pragma unroll
        for (int c = 0; c < 16; c++) { a1[c >> 3][c & 7] = 0.f; PIN(a1[c >> 3][c & 7]); }
        PIN(vX); PIN(v4); PIN(aR1); PIN(aT1); PIN(aR2); PIN(aT2); PIN(aB); PIN(v9);
    }
    PIN(aG);
    asm volatile("" : "+s"(sX), "+s"(s8w), "+s"(s8r), "+s"(s8s), "+s"(s9w));
    unsigned vP = t16 == rt2 ? OOB : t16 * 128, pk;
    {
        v4u pv[4];
        unsigned vK = tid < 4 ? ((ct + 4 * tid) & 15) * 2048 : OOB, vS = l * 16;
        PIN(vK); PIN(vS);
        unsigned sK = WS_GK + rt2 * 128, rk = 2047u - R2, aKy = ldsb + L_WO + 576, aKw = aKy + w * 4;
        unsigned vC = gx * 16, sC = WS_C + (~rt2 & 12) * 4096 + (~rt2 & 3) * 2048 + ct * 128;
        PIN(vC);
        asm volatile("" : "+s"(sK), "+s"(sC));
        PIN(rk); PIN(aKy); PIN(aKw); PIN(vP);
        auto st_ld = [&](int k) { return LD16(vS + k * 1024, sG5r); };
        auto st_sum = [&]() { const f32x4 ps = (f4(pv[0]) + f4(pv[1])) + (f4(pv[2]) + f4(pv[3])); return xrow_scatter<false>(ps[0], ps[1], ps[2], ps[3]); };
        SCHED();
        SLEEP(S4);
        __builtin_amdgcn_s_setprio(1);
        float ts;
        sweepn<4, 4, (FM & 1) != 0>(st_ld, pv, [&]() { return ts = st_sum(); });
        STAMPW2(21, 0, 4);
        float T[4];
        xrow_gather(ts, T);
        const float mean = T[0] * (1.0f / 1024.0f), var = fmaxf(T[1] * (1.0f / 1024.0f) - mean * mean, 0.f);
        const unsigned sg = pk2(mean, __builtin_amdgcn_sqrtf(rbf(rbf(var) + 1e-5f)));
        const float mb2 = bflo(sg), isd2 = __builtin_amdgcn_rcpf(bfhi(sg));
        const float rsc = (T[2] - mb2 * T[3]) * isd2;
        const unsigned rb = pk2(rsc, rsc);
        const unsigned key = (((rb ^ (((int)rb >> 31) | 0x80000000u)) >> 16) << 11) | rk;
        unsigned kb = umx(key, dppu<0x128>(key));
        kb = umx(kb, dppu<0x124>(kb)); kb = umx(kb, dppu<0x122>(kb)); kb = umx(kb, dppu<0x121>(kb));
        stsu(aKw, kb);
        WAITL0();
        BAR();
        SCHED();
        pk = umx(wmax(*(const LAS v4u*)aKy), wmax(*(const LAS v4u*)(aKy + 16)));
        if (w == 0) ST4(pk, vK, sK);
        SCHED();
        const bool cand = key == pk;
        __builtin_amdgcn_s_setprio(0);
        v4u e[2];
#pragma unroll
        for (int i = 0; i < 8; i++) {
            const float h0 = bflo(hpk[i >> 2][i & 3]), h1 = bfhi(hpk[i >> 2][i & 3]);
            const unsigned d = pk2(h0 - mb2, h1 - mb2);
            e[i >> 2][i & 3] = nsent(pk2(bflo(d) * isd2, bfhi(d) * isd2));
        }
        if (cand) {
#pragma unroll
            for (int i = 0; i < 4; i++) ST16(e[i & 1], vC + (i & 1) * 64, sC + (i >> 1) * 8192);
#pragma unroll
            for (int i = 0; i < 4; i++)
                ST16(((v4u){nsent(hpk[i & 1][0]), nsent(hpk[i & 1][1]), nsent(hpk[i & 1][2]), nsent(hpk[i & 1][3])}), vC + (i & 1) * 64, sC + (WS_H - WS_C) + (i >> 1) * 8192);
        }
        SCHED();
#define KS 3
        unsigned k1;
        if (w == 1) {
            if (KS) SLEEP(KS);
            asm volatile("buffer_load_dword %0, %1, %2, %3 offen sc1" : "=v"(k1) : "v"(vP), "s"(rsWS), "s"(sRep) : "memory");
        }
        SCHED();
        STAMPW2(22, 0, 4);
#pragma unroll
        for (int i = 0; i < 8; i++)
            outv[i >> 2][i & 3] = pk2(bflo(hpk[i >> 2][i & 3]) + bflo(e[i >> 2][i & 3]), bfhi(hpk[i >> 2][i & 3]) + bfhi(e[i >> 2][i & 3]));
        {
#pragma unroll
            for (int j = 0; j < 4; j++) {
                const unsigned a = outv[0][j], b = outv[1][j];
                outv[0][j] = __builtin_amdgcn_update_dpp(a, b, 0x128, 0xf, 0xc, false);
                outv[1][j] = __builtin_amdgcn_update_dpp(b, a, 0x128, 0xf, 0x3, false);
            }
        }
        PIN(outv[0]); PIN(outv[1]);
        if (w == 1) {
            poll_words(k1, rsWS, vP, sRep);
            STAMPR(23, 2);
            unsigned gk = umx(k1, pk);
            gk = umx(gk, dppu<0x128>(gk)); gk = umx(gk, dppu<0x124>(gk)); gk = umx(gk, dppu<0x122>(gk)); gk = umx(gk, dppu<0x121>(gk));
            stsu(aG, gk);
        }
    }
    if (half) __builtin_amdgcn_s_setprio(1);
    STAMPW(24, 7);
    WL0();
    SCHED();
    BAR();
    SCHED();
    STAMPW2(11, 0, 4);
    const unsigned gv = ldsu(aG);
    unsigned vo[2];
    auto store_out = [&](unsigned srow) {
#pragma unroll
        for (int i = 0; i < 2; i++) __builtin_amdgcn_raw_buffer_store_b128(outv[i], rsO, vo[i], ct * 128 + i * 16384, AUX_SC1);
    };
    if (RARE(!ffn)) {
        const unsigned gkey = rfl(gv), srow = ~gkey & 2047u;
        vo[0] = (R2 & ~8u) != srow ? vOut : OOB; vo[1] = (R2 | 8u) != srow ? vOut : OOB;
        store_out(srow);
        if (w != 4) { RDUMP(); return; }
        const unsigned c9 = (bid & 3) * 4 + (idx & 3), col = c9 * 64 + (bid & 4) * 8 + (l & 7) * 4 + g;
        unsigned hl[2];
        sweepz<2, 2>([&](int k) { return LD4((col >> 1) * 4, (k ? WS_C : WS_H) + (((gkey & 0x780) + (gkey & 0x600)) << 4) + nr); }, hl);
        const unsigned ord = gkey >> 11;
        const unsigned rb = (ord & 0x8000u) ? (ord & 0x7fffu) : (~ord & 0xffffu);
        float sig = rbf(1.0f / (1.0f + __expf(-bf2f(rb))));
        float hc = (g & 1) ? bfhi(hl[0]) : bflo(hl[0]), lc = (g & 1) ? bfhi(hl[1]) : bflo(hl[1]);
        unsigned vY = l * 16, vZ = l < 48 ? l * 16 + 1024 : OOB, vO = (l & 8) ? OOB : col * 2, sY = WS_G9 + (c9 >> 2) * 16384 + (c9 & 3) * 2048 + nh, sO = srow * 2048;
        PIN(sig); PIN(hc); PIN(lc); PIN(vY); PIN(vZ); PIN(vO);
        asm volatile("" : "+s"(sY), "+s"(sO));
        v4u yp[2];
        SCHED();
        STAMPW(16, 4);
        SLEEP(S7);
        sweepz<2>([&](int k) { return LD16(k ? vZ : vY, sY); }, yp);
        const f32x4 ys = f4(yp[0]) + f4(yp[1]);
        float yt = xrow_scatter<false>(ys[0], ys[1], ys[2], ys[3]);
        yt += dppf<0x128>(yt);
        const float y = rbf(rbf(yt) * sig);
        __builtin_amdgcn_raw_buffer_store_b16((u16)pk2(hc + rbf(lc + y), 0.f), rsO, vO, sO, 0);
        STAMPW(17, 4);
        RDUMP();
        return;
    }
    if (w < 4) {
        const unsigned srow = ~rfl(gv) & 2047u;
        vo[0] = (R2 & ~8u) != srow ? vOut : OOB; vo[1] = (R2 | 8u) != srow ? vOut : OOB;
        PIN(vo[0]); PIN(vo[1]);
        BAR(); BAR(); BAR();
        store_out(srow);
        if (bid == 0 && tid == 0) __hip_atomic_store((gu32*)(ws + WS_EPOCH), tag, RLX_AGENT);
        STAMPW(12, 0);
        RDUMP();
        return;
    }

    auto tree16 = [&](unsigned a, unsigned stride) {
        f32x2 v[8];
#pragma unroll
        for (int i = 0; i < 8; i++) v[i] = (f32x2){ldsf(a + 2 * i * stride), ldsf(a + (2 * i + 1) * stride)};
        SCHED();
        const f32x2 t = ((v[0] + v[1]) + (v[2] + v[3])) + ((v[4] + v[5]) + (v[6] + v[7]));
        return t[0] + t[1];
    };
    vX = (((gv & 0x780) | vX) + (gv & 0x600)) << 4;
    v4u xq[1] = {LD16(vX, sX)};
    SCHED();
    const unsigned srow = ~rfl(gv) & 2047u;
    vo[0] = (R2 & ~8u) != srow ? vOut : OOB; vo[1] = (R2 | 8u) != srow ? vOut : OOB;
    PIN(vo[0]); PIN(vo[1]);
    SCHED();
    if (RARE(!__all(wmax(xq[0]) != SENT))) sweepz<1>([&](int) { return LD16(vX, sX); }, xq);
    STAMPW(13, 4);
#pragma unroll
    for (int ip = 0; ip < 4; ip++)
#pragma unroll
        for (int c = 0; c < 16; c++) dot2c(a1[c & 1][c >> 1], wp1[c & 1][ip][c >> 1], xq[0][ip]);
#pragma unroll
    for (int c = 0; c < 8; c++) fold8(a1[0][c], a1[1][c]);
    stsf4(aR1, (f32x4){a1[0][0], a1[0][1], a1[0][2], a1[0][3]});
    stsf4(aR1 + 16, (f32x4){a1[0][4], a1[0][5], a1[0][6], a1[0][7]});
    WL0();
    BAR();
    SCHED();
    ST4(f2s(tree16(aT1, 512)), v4, s8w);
    SCHED();
    STAMPW(14, 4);
#define S6 7
    perm2();
    float a2[8];
#pragma unroll
    for (int c = 0; c < 8; c++) { a2[c] = 0.f; PIN(a2[c]); }
    SCHED();
    SLEEP(S6);
    {
        unsigned pv[8];
        auto a_ld = [&](int k) { return LD4(v4 + (k & 3) * 1024, k < 4 ? s8r : s8s); };
        auto a_sum = [&](int m) { return (__uint_as_float(pv[m]) + __uint_as_float(pv[2 + m])) + (__uint_as_float(pv[4 + m]) + __uint_as_float(pv[6 + m])); };
        float gs, us;
        sweepn<8, 0, (FM & 2) != 0>(a_ld, pv, [&]() { gs = a_sum(0); us = a_sum(1); return gs + us; });
        STAMPW(15, 4);
        const unsigned gu = pk2(gs, us);
        const float gg = bflo(gu);
        const float act = rbf(gg * __builtin_amdgcn_rcpf(1.0f + __expf(-gg))) * bfhi(gu);
        const unsigned apk = pk2(act, dppf<0xB1>(act));
        unsigned ap[4];
#pragma unroll
        for (int ip = 0; ip < 4; ip++) ap[ip] = __builtin_amdgcn_ds_bpermute(aB + ip * 64, apk);
        SCHED();
#pragma unroll
        for (int ip = 0; ip < 4; ip++)
#pragma unroll
            for (int c = 0; c < 8; c++) dot2c(a2[(c & 1) * 4 + (c >> 1)], wp2[ip][(c & 1) * 4 + (c >> 1)], ap[ip]);
#pragma unroll
        for (int c = 0; c < 4; c++) fold8(a2[c], a2[4 + c]);
        stsf4(aR2, (f32x4){a2[0], a2[1], a2[2], a2[3]});
    }
    WL0();
    BAR();
    SCHED();
    if (lw == 0) ST4(f2s(tree16(aT2, 256)), v9, s9w);
    SCHED();
    BAR();
    SCHED();
    STAMPW(16, 4);
    store_out(srow);
    RDUMP();
}
__device__ __attribute__((used, noinline)) void fk_tail() { asm volatile(".Lfk_end:"); }

extern "C" void kernel_launch(void* const* d_in, const int* in_sizes, int n_in, void* d_out, int out_size, void* d_ws,
                              size_t ws_size, hipStream_t stream) {
    (void)in_sizes; (void)n_in; (void)out_size; (void)ws_size;
    (void)hipFuncSetAttribute((const void*)fused_kernel, hipFuncAttributeMaxDynamicSharedMemorySize, LDS_BYTES);
    fused_kernel<<<256, 512, LDS_BYTES, stream>>>((const u16*)d_in[0], (const u16*)d_in[1], (const u16*)d_in[2], (const u16*)d_in[3],
                                                  (const u16*)d_in[5], (const u16*)d_in[6], (const u16*)d_in[7], (const u16*)d_in[8],
                                                  (u16*)d_out, (char*)d_ws);
}
```

```cpp
#include <hip/hip_runtime.h>
#include <stdint.h>
#include <utility>

typedef unsigned short u16;
typedef unsigned long long u64;
typedef short bf16x8 __attribute__((ext_vector_type(8)));
typedef short s16x4 __attribute__((ext_vector_type(4)));
typedef float f32x4 __attribute__((ext_vector_type(4)));
typedef float f32x2 __attribute__((ext_vector_type(2)));
typedef __bf16 bf16x2 __attribute__((ext_vector_type(2)));
typedef unsigned v4u __attribute__((ext_vector_type(4)));
typedef unsigned v2u __attribute__((ext_vector_type(2)));
#define LAS __attribute__((address_space(3)))
#define GAS __attribute__((address_space(1)))
#define CAS __attribute__((address_space(4)))
typedef GAS u64 gu64;
typedef GAS unsigned gu32;
typedef __amdgpu_buffer_rsrc_t rsrc_t;
#define DI __device__ __forceinline__
#define RLX_AGENT __ATOMIC_RELAXED, __HIP_MEMORY_SCOPE_AGENT
#define BAR() __builtin_amdgcn_s_barrier()
#define SCHED() __builtin_amdgcn_sched_barrier(0)
#define SGB(m, n) __builtin_amdgcn_sched_group_barrier((m), (n), 0)
#define WAITL0() asm volatile("s_waitcnt lgkmcnt(0)" ::: "memory")
#define WL0() __builtin_amdgcn_s_waitcnt(0xc07f)
#define MFMA(a, b, c) __builtin_amdgcn_mfma_f32_16x16x32_bf16((a), (b), (c), 0, 0, 0)
#define AUX_SC1 16
template <int N> DI void waitv() { asm volatile("s_waitcnt vmcnt(%0)" ::"i"(N) : "memory"); }

#define WS_EPOCH 0
#define WS_G0 0x1000
#define WS_G1 0x8000
#define WS_F4 0x60000
#define WS_GC 0x70000
#define WS_PR 0x74000
#define WS_G3 0x80000
#define WS_GZ 0xC0000
#define SENT 0xFFFFFFFFu
#define WS_TL 0x100000
#define WS_G5 0x100000
#define WS_G8 0x180000
#define WS_GK 0x1B8000
#define WS_G9 0x1C0000
#define WS_C 0x1E0000
#define WS_H 0x1F0000
#define WS_P2 0x200000
#define P2_STRIDE 0x2180
#define WS_P4 0x600000
#define WS_USED 0xA00000
#define WS_STAMP 0x1000000

#define STAGE_B 40960
#define LDS_BYTES 163840
#define L_WO 32768
#define L_MB 161792
#define L_WPHI (L_MB - 8192)
#define L_SD (L_MB + 512)
#define L_CS (L_MB + 1024)
#define L_V L_WO
#define L_KH (L_WO + 65536)
#define L_XB 0
#define L_RED 4096

#define SPIN_CAP 400000u
#define DN0 3
#define W_AUX 2
#define LOG2E 1.4426950408889634f

DI unsigned rfl(unsigned x) { return __builtin_amdgcn_readfirstlane(x); }
DI float bf2f(unsigned u) { return __uint_as_float(u << 16); }
DI float bflo(unsigned u) { return __uint_as_float(u << 16); }
DI float bfhi(unsigned u) { return __uint_as_float(u & 0xffff0000u); }
DI unsigned pk2(float lo, float hi) { f32x2 v = {lo, hi}; return __builtin_bit_cast(unsigned, __builtin_convertvector(v, bf16x2)); }
DI unsigned nsent(unsigned x) { return __builtin_elementwise_min(x, SENT - 1); }
DI unsigned umx(unsigned a, unsigned b) { return __builtin_elementwise_max(a, b); }
DI float rbf(float f) { return bflo(pk2(f, f) & 0xffffu); }
DI bf16x8 mk8(unsigned a, unsigned b, unsigned c, unsigned d) { v4u v = {a, b, c, d}; return __builtin_bit_cast(bf16x8, v); }
DI void dma16(unsigned ldsaddr, unsigned voff, rsrc_t rs, unsigned soff) {
    asm volatile("s_mov_b32 m0, %0\n\ts_nop 0\n\tbuffer_load_dwordx4 %1, %2, %3 offen lds" ::"s"(ldsaddr), "v"(voff), "s"(rs), "s"(soff) : "memory");
}
DI void dma16x4(unsigned ldsaddr, const unsigned* v, rsrc_t rs, unsigned soff) {
    asm volatile("s_mov_b32 m0, %0\n\ts_nop 0\n\tbuffer_load_dwordx4 %1, %5, %6 offen lds\n\tbuffer_load_dwordx4 %2, %5, %6 offen offset:1024 lds\n\t"
                 "buffer_load_dwordx4 %3, %5, %6 offen offset:2048 lds\n\tbuffer_load_dwordx4 %4, %5, %6 offen offset:3072 lds"
                 ::"s"(ldsaddr), "v"(v[0]), "v"(v[1]), "v"(v[2]), "v"(v[3]), "s"(rs), "s"(soff) : "memory");
}
DI void touch(unsigned voff, rsrc_t rs, unsigned soff) { asm volatile("buffer_load_dword v255, %0, %1, %2 offen" ::"v"(voff), "s"(rs), "s"(soff) : "memory", "v255"); }
DI v4u ld16_asm(unsigned voff, rsrc_t rs, unsigned soff) {
    v4u r; asm volatile("buffer_load_dwordx4 %0, %1, %2, %3 offen" : "=v"(r) : "v"(voff), "s"(rs), "s"(soff) : "memory"); return r;
}
#define PIN(x) asm volatile("" : "+v"(x))
DI s16x4 tr_read(unsigned a) { return __builtin_amdgcn_ds_read_tr16_b64_v4i16((LAS s16x4*)(a)); }
DI bf16x8 cat8(s16x4 a, s16x4 b) { return __builtin_shufflevector(a, b, 0, 1, 2, 3, 4, 5, 6, 7); }
DI bf16x8 lds128(unsigned a) { return *(const LAS bf16x8*)(a); }
DI f32x4 ldsf4(unsigned a) { return *(const LAS f32x4*)(a); }
DI float ldsf(unsigned a) { return *(const LAS float*)(a); }
DI unsigned ldsu(unsigned a) { return *(const LAS unsigned*)(a); }
DI void stsf4(unsigned a, f32x4 v) { *(LAS f32x4*)(a) = v; }
DI void stsf(unsigned a, float v) { *(LAS float*)(a) = v; }
DI void stsu(unsigned a, unsigned v) { *(LAS unsigned*)(a) = v; }
template <int C> DI float dppf(float x) { return __uint_as_float(__builtin_amdgcn_update_dpp(0, __float_as_uint(x), C, 0xf, 0xf, true)); }
template <int C> DI unsigned dppu(unsigned x) { return __builtin_amdgcn_update_dpp(0, x, C, 0xf, 0xf, true); }
DI float max2f(float a, float b) { float r; asm("v_max_f32_e32 %0, %1, %2" : "=v"(r) : "v"(a), "v"(b)); return r; }
DI float max3f(float a, float b, float c) { float r; asm("v_max3_f32 %0, %1, %2, %3" : "=v"(r) : "v"(a), "v"(b), "v"(c)); return r; }
DI float row_sum(float x) { x += dppf<0x128>(x); x += dppf<0x124>(x); x += dppf<0x122>(x); x += dppf<0x121>(x); return x; }
DI float xrow_sum(float x) {
    auto a = __builtin_amdgcn_permlane16_swap(__float_as_uint(x), __float_as_uint(x), false, false);
    x = __uint_as_float(a[0]) + __uint_as_float(a[1]);
    auto b = __builtin_amdgcn_permlane32_swap(__float_as_uint(x), __float_as_uint(x), false, false);
    return __uint_as_float(b[0]) + __uint_as_float(b[1]);
}
DI float wave_sum(float x) { return xrow_sum(row_sum(x)); }
DI unsigned wave_maxu(unsigned x) {
    x = umx(x, dppu<0x128>(x)); x = umx(x, dppu<0x124>(x)); x = umx(x, dppu<0x122>(x)); x = umx(x, dppu<0x121>(x));
    auto a = __builtin_amdgcn_permlane16_swap(x, x, false, false);
    x = umx(a[0], a[1]);
    auto b = __builtin_amdgcn_permlane32_swap(x, x, false, false);
    return umx(b[0], b[1]);
}
DI float nmax3f(float a, float b, float c) { float r; asm("v_max3_f32 %0, -%1, -%2, -%3" : "=v"(r) : "v"(a), "v"(b), "v"(c)); return r; }
DI float nmax2f(float a, float b) { float r; asm("v_max_f32_e64 %0, -%1, -%2" : "=v"(r) : "v"(a), "v"(b)); return r; }
DI float mx2(float a, float b) { return __builtin_amdgcn_fmed3f(a, b, __builtin_inff()); }
template <bool MAX> DI float xrow_scatter(float a, float b, float c, float d) {
    const auto s = __builtin_amdgcn_permlane16_swap(__float_as_uint(a), __float_as_uint(b), false, false);
    const auto t = __builtin_amdgcn_permlane16_swap(__float_as_uint(c), __float_as_uint(d), false, false);
    const float u = MAX ? mx2(__uint_as_float(s[0]), __uint_as_float(s[1])) : __uint_as_float(s[0]) + __uint_as_float(s[1]);
    const float v = MAX ? mx2(__uint_as_float(t[0]), __uint_as_float(t[1])) : __uint_as_float(t[0]) + __uint_as_float(t[1]);
    const auto r = __builtin_amdgcn_permlane32_swap(__float_as_uint(u), __float_as_uint(v), false, false);
    return MAX ? mx2(__uint_as_float(r[0]), __uint_as_float(r[1])) : __uint_as_float(r[0]) + __uint_as_float(r[1]);
}
DI void xrow_gather(float w, float (&o)[4]) {
    const auto r = __builtin_amdgcn_permlane32_swap(__float_as_uint(w), __float_as_uint(w), false, false);
    const auto s = __builtin_amdgcn_permlane16_swap(r[0], r[0], false, false);
    const auto t = __builtin_amdgcn_permlane16_swap(r[1], r[1], false, false);
    o[0] = __uint_as_float(s[0]); o[1] = __uint_as_float(s[1]); o[2] = __uint_as_float(t[0]); o[3] = __uint_as_float(t[1]);
}
struct NoHook { DI void operator()(int, float&) const {} DI void operator()(int, float&, float&) const {} };
template <bool MUL, class H = NoHook> DI void softmax2(const f32x4 (&p)[2][2], float rs0, float rs1, bool hi, bf16x8 (&out)[4], H hook = H()) {
    float m[4];
#pragma unroll
    for (int i = 0; i < 2; i++) {
        const f32x4 &p0 = p[i][0], &p1 = p[i][1];
        m[2 * i] = max3f(max3f(p0[0], p0[1], p0[2]), max3f(p0[3], p1[0], p1[1]), max2f(p1[2], p1[3]));
        m[2 * i + 1] = max3f(nmax3f(p0[0], p0[1], p0[2]), nmax3f(p0[3], p1[0], p1[1]), nmax2f(p1[2], p1[3]));
    }
    asm volatile("s_nop 1" : "+v"(m[0]), "+v"(m[1]), "+v"(m[2]), "+v"(m[3]));
    hook(1, m[3]);
    const float rsr = hi ? rs1 : rs0;
    float a[4];
    xrow_gather(xrow_scatter<true>(m[0], m[1], m[2], m[3]) * (rsr * -LOG2E), a);
    f32x2 e[4][4];
    float s[4];
#pragma unroll
    for (int k = 0; k < 4; k++) {
        if (k == 2) hook(2, a[2], s[1]);
        const float sc = ((k >> 1) ? rs1 : rs0) * ((k & 1) ? -LOG2E : LOG2E);
#pragma unroll
        for (int j = 0; j < 4; j++) {
            const f32x4& q = p[k >> 1][j >> 1];
            const f32x2 t = __builtin_elementwise_fma((f32x2){q[2 * (j & 1)], q[2 * (j & 1) + 1]}, (f32x2){sc, sc}, (f32x2){a[k], a[k]});
            e[k][j] = (f32x2){__builtin_amdgcn_exp2f(t[0]), __builtin_amdgcn_exp2f(t[1])};
        }
        const f32x2 h = (e[k][0] + e[k][1]) + (e[k][2] + e[k][3]);
        s[k] = h[0] + h[1];
        if (k == 3) hook(3, s[k]);
    }
    float iv = __builtin_amdgcn_rcpf(xrow_scatter<false>(s[0], s[1], s[2], s[3]));
    if (MUL) iv *= rsr;
    xrow_gather(iv, a);
#pragma unroll
    for (int k = 0; k < 4; k++) {
        if (k == 2) hook(4, a[2]);
        const f32x2 c = {a[k], a[k]};
        const f32x2 x0 = e[k][0] * c, x1 = e[k][1] * c, x2 = e[k][2] * c, x3 = e[k][3] * c;
        out[k] = mk8(pk2(x0[0], x0[1]), pk2(x1[0], x1[1]), pk2(x2[0], x2[1]), pk2(x3[0], x3[1]));
    }
}
DI void swap16(v4u& x) {
    const auto a = __builtin_amdgcn_permlane16_swap(x[0], x[2], false, false), b = __builtin_amdgcn_permlane16_swap(x[1], x[3], false, false);
    x = (v4u){a[0], b[0], a[1], b[1]};
}
DI float dot2(unsigned a, unsigned b, float c) {
    return __builtin_amdgcn_fdot2_f32_bf16(__builtin_bit_cast(bf16x2, a), __builtin_bit_cast(bf16x2, b), c, false);
}
DI void st_gran(char* ws, unsigned off, unsigned tag, unsigned val) {
    __hip_atomic_store((gu64*)(ws + off), ((u64)tag << 32) | val, RLX_AGENT);
}
DI v2u ldg(rsrc_t rs, unsigned voff, unsigned soff) { return __builtin_amdgcn_raw_buffer_load_b64(rs, voff, soff, AUX_SC1); }
#define RARE(c) __builtin_expect(!!(c), 0)
#define FRESH() asm volatile("" ::: "memory")
template <int N> DI void sweep(rsrc_t rs, unsigned voff, unsigned soff, unsigned S, unsigned tag, unsigned (&v)[N]) {
#pragma nounroll
    for (unsigned spins = 0;;) {
        unsigned bad = 0;
        FRESH();
#pragma unroll
        for (int k = 0; k < N; ++k) { const v2u x = ldg(rs, voff, soff + k * S); v[k] = x[0]; bad |= x[1] ^ tag; }
        if (__all(bad == 0)) break;
        if (++spins > SPIN_CAP) break;
        __builtin_amdgcn_s_sleep(1);
    }
}
DI unsigned wmax(unsigned x) { return x; }
DI unsigned wmax(v2u x) { return umx(x[0], x[1]); }
DI unsigned wmax(v4u x) { return umx(umx(x[0], x[1]), umx(x[2], x[3])); }
template <int N, int SL = 0, class T, class F> DI void sweepz(F ld, T (&v)[N]) {
#pragma nounroll
    for (unsigned spins = 0;;) {
        FRESH();
        unsigned m = 0;
#pragma unroll
        for (int k = 0; k < N; ++k) v[k] = ld(k);
#pragma unroll
        for (int k = 0; k < N; ++k) m = umx(m, wmax(v[k]));
        if (__all(m != SENT)) break;
        if (++spins > SPIN_CAP) break;
        if (SL) __builtin_amdgcn_s_sleep(SL);
    }
}
DI f32x4 f4(v4u x) { return __builtin_bit_cast(f32x4, x); }
DI bool anynan(float x) { return __builtin_amdgcn_ballot_w64(x != x) != 0; }
DI void dot2c(float& c, unsigned a, unsigned b) { asm volatile("v_dot2c_f32_bf16 %0, %1, %2" : "+v"(c) : "v"(a), "v"(b)); }
DI void fold8(float& a, float b) {
    asm volatile("v_add_f32_dpp %0, %0, %0 row_ror:8 row_mask:0xf bank_mask:0x3\n\tv_add_f32_dpp %0, %1, %1 row_ror:8 row_mask:0xf bank_mask:0xc" : "+v"(a) : "v"(b));
}
template <int N, int SL, class T, class F, class S> DI void sweepn(F ld, T (&v)[N], S sum) {
    FRESH();
#pragma unroll
    for (int k = 0; k < N; ++k) v[k] = ld(k);
    if (RARE(anynan(sum()))) { sweepz<N, SL>(ld, v); sum(); }
}
DI unsigned f2s(float x) { return nsent(__float_as_uint(x)); }
#define OOB 0x4000000u
#define LD16(v, s) __builtin_amdgcn_raw_buffer_load_b128(rsWS, (v), (s), AUX_SC1)
#define LD4(v, s) __builtin_amdgcn_raw_buffer_load_b32(rsWS, (v), (s), AUX_SC1)
#define ST16(x, v, s) __builtin_amdgcn_raw_buffer_store_b128((x), rsWS, (v), (s), AUX_SC1)
#define ST4(x, v, s) __builtin_amdgcn_raw_buffer_store_b32((x), rsWS, (v), (s), AUX_SC1)
#define SLEEP(n) __builtin_amdgcn_s_sleep(n)
DI void poll_flags(rsrc_t rs, unsigned v, unsigned off, v2u want) {
    v2u x; unsigned n = SPIN_CAP;
    asm volatile("1:\n\tbuffer_load_dwordx2 %0, %2, %3, %4 offen sc1\n\ts_sub_u32 %1, %1, 1\n\ts_cbranch_scc1 2f\n\ts_waitcnt vmcnt(0)\n\t"
                 "v_cmp_ne_u64 vcc, %0, %5\n\ts_cbranch_vccnz 1b\n2:\n\ts_waitcnt vmcnt(0)" : "=&v"(x), "+s"(n) : "v"(v), "s"(rs), "s"(off), "v"(want) : "vcc", "scc", "memory");
}
DI void poll_words(unsigned& x, rsrc_t rs, unsigned v, unsigned off) {
    unsigned n = SPIN_CAP;
    asm volatile("s_branch 3f\n1:\n\tbuffer_load_dword %0, %2, %3, %4 offen sc1\n\ts_sub_u32 %1, %1, 1\n\ts_cbranch_scc1 2f\n3:\n\ts_waitcnt vmcnt(0)\n\t"
                 "v_cmp_eq_u32 vcc, -1, %0\n\ts_cbranch_vccnz 1b\n2:\n\ts_waitcnt vmcnt(0)" : "+v"(x), "+s"(n) : "v"(v), "s"(rs), "s"(off) : "vcc", "scc", "memory");
}
#define STAMP(i)
#define JIT(i) STAMP(i)
#define RINIT()
#define RDUMP()
#define KATTR
template <class F, int... Is> DI void static_for_impl(F&& f, std::integer_sequence<int, Is...>) { (f(std::integral_constant<int, Is>{}), ...); }
template <int N, class F> DI void static_for(F&& f) { static_for_impl(f, std::make_integer_sequence<int, N>{}); }

DI s16x4 bq(unsigned b0, unsigned b1, int j, int i) {
    const int ni = (j + 2) % 6;
    return tr_read(((ni & 1) ? b1 : b0) + (ni >> 1) * 8192 + (i >> 1) * 4096 + (i & 1) * 512);
}
template <class F> DI void group(f32x4 (&acc)[4][6], const bf16x8 (&A)[2][4], const bf16x8 (&B)[2], int j, F gap) {
    const int ni = (j + 2) % 6;
#pragma unroll
    for (int i = 0; i < 8; i++) {
        const int mi = i >> 1, ks = i & 1;
        if (ni < 4) asm volatile("v_mfma_f32_16x16x32_bf16 %0, %1, %2, %0" : "+v"(acc[mi][ni]) : "v"(B[ks]), "v"(A[ks][mi]));
        else asm volatile("v_mfma_f32_16x16x32_bf16 %0, %1, %2, %0" : "+v"(acc[mi][ni]) : "v"(A[ks][mi]), "v"(B[ks]));
        gap(i);
        SCHED();
    }
}
DI void img_write2(const unsigned (&ia)[4], const bf16x8 (&f)[4]) {
#pragma unroll
    for (int k = 0; k < 4; k++) {
        const v4u u = __builtin_bit_cast(v4u, f[k]);
        *(LAS v2u*)(ia[2 * (k & 1)] + (k >> 1) * 2048) = (v2u){u[0], u[1]};
        *(LAS v2u*)(ia[2 * (k & 1) + 1] + (k >> 1) * 2048) = (v2u){u[2], u[3]};
    }
}
DI float lds_bf(unsigned a) { return bf2f(*(const LAS u16*)(a)); }
#define MFMA_SETTLE2(p) asm volatile("s_nop 7\n\ts_nop 1" : "+v"(p[0][0]), "+v"(p[0][1]), "+v"(p[1][0]), "+v"(p[1][1]))

__global__ KATTR __launch_bounds__(512, 2) void fused_kernel(const u16* __restrict__ X, const u16* __restrict__ WQKV, const u16* __restrict__ WPHI,
                                                       const u16* __restrict__ WO, const u16* __restrict__ WR, const u16* __restrict__ WG,
                                                       const u16* __restrict__ WU, const u16* __restrict__ WD, u16* __restrict__ OUT,
                                                       char* __restrict__ ws) {
    extern __shared__ __attribute__((aligned(16))) char lds[];
    u64 pc0; unsigned clen;
    const int tid = threadIdx.x, l = tid & 63;
    asm volatile("s_getpc_b64 %0\n.Lfk_beg:\n\ts_lshr_b32 %1, %3, 3\n\tv_and_b32 v255, 0x1e0, %2\n\tv_add_lshl_u32 v255, v255, %1, 7\n\ts_mov_b32 %1, .Lfk_end-.Lfk_beg\n\t"
                 "v_min_u32 v255, %1, v255\n\tglobal_load_dword v255, v255, %0" : "=&s"(pc0), "=&s"(clen) : "v"(tid), "s"(blockIdx.x) : "v255", "scc");
    SCHED();
    const unsigned w = rfl(tid >> 6);
    const unsigned bid = blockIdx.x, xcd = (bid & 3) * 2 + ((bid >> 2) & 1), idx = bid >> 3;
    const unsigned rt = (xcd & 1) * 8 + (idx & 7), hp = (xcd >> 1) * 4 + (idx >> 3);
    const unsigned rt2 = (bid & 6) * 2 + (idx & 3), ct = (bid & 1) * 8 + (idx >> 2);
    const unsigned hq = (hp + 4) & 15;
    const unsigned rot = (rt >> 2) * 4;
    const unsigned rot2 = (2 * rt2 + 16 * ct) & 31;
    const unsigned ldsb = (unsigned)(size_t)lds;
    const int g = l >> 4, q4 = (l & 15) >> 2, p4 = l & 3, t16 = l & 15;
    const unsigned kr = l >> 3, gx = (g & 1) * 2 + (g >> 1);
    asm volatile("" ::"s"(X), "s"(WQKV), "s"(WPHI), "s"(WO), "s"(WR), "s"(WG), "s"(WU), "s"(WD), "s"(OUT), "s"(ws));
    unsigned tag;
#define READ_TAG() do { SCHED(); tag = *(const volatile CAS unsigned*)(ws + WS_EPOCH) + 1u; asm volatile("" : "+s"(tag)); } while (0)
    const rsrc_t rsWS = __builtin_amdgcn_make_buffer_rsrc((void*)ws, 0, WS_USED, 0x00020000);
    const rsrc_t rsX = __builtin_amdgcn_make_buffer_rsrc((void*)X, 0, 2048 * 1024 * 2, 0x00020000);
    const rsrc_t rsW = __builtin_amdgcn_make_buffer_rsrc((void*)WQKV, 0, 1024 * 3072 * 2, 0x00020000);
    const unsigned vA = (kr * 1024 + (((l & 7) ^ kr) * 8)) * 2;
    const unsigned vB0 = (kr * 3072 + (((l & 7) ^ (((kr >> 1) & 1) << 1)) * 8)) * 2;
    const unsigned vB1 = (kr * 3072 + (((l & 7) ^ ((((kr >> 1) & 1) | 2) << 1)) * 8)) * 2;
    const unsigned srow1 = rt * 128 + hp * 8 + (w & 3) * 2;
    v4u xr[4];
    if (w < 4) {
#pragma unroll
        for (int i = 0; i < 4; i++) xr[i] = ld16_asm(l * 16, rsX, srow1 * 2048 + i * 1024);
    }
    {
        const unsigned vBw = (w & 1) ? vB1 : vB0;
#pragma unroll
        for (int i = 0; i < 2; i++) dma16(ldsb + (w * 2 + i) * 1024, vA, rsX, (rt * 128 + w * 16 + i * 8) * 2048 + rot * 128);
#pragma unroll
        for (int i = 0; i < 3; i++) dma16(ldsb + 16384 + i * 8192 + w * 1024, vBw, rsW, (rot * 64 + w * 8) * 6144 + (i ? hp : hq) * 128 + i * 2048);
    }
    const f32x4 zero4 = {0.f, 0.f, 0.f, 0.f};
    const bf16x8 ones = mk8(0x3F803F80u, 0x3F803F80u, 0x3F803F80u, 0x3F803F80u);
    const unsigned lw = w & 3;
    const bool ffn = (ct & 7) != 3;
    const unsigned fb = !ffn ? 255u : (bid & 7) * 28 + (idx < 12 ? idx : idx - 4);
    const unsigned cg = (fb + 5) & 15, kc = fb >> 4;
    const unsigned kq = fb / 56, cg1 = fb % 56, cg1u = (cg1 + 3) % 56;
    v4u wreg[24];
    RINIT();
    STAMP(0);

    const rsrc_t rsWO = __builtin_amdgcn_make_buffer_rsrc((void*)WO, 0, 1024 * 1024 * 2, 0x00020000);
    const unsigned vWO = kr * 2048 + (((l & 7) ^ (((kr >> 1) & 3) << 1)) * 16);
    unsigned vwo[4];
    auto wo_so = [&](unsigned b) { return (((2 * b & 30) + (b >> 4) + rot2) & 31) * 65536 + ct * 128; };
    const unsigned fw = (((t16 >> 1) & 3) << 2) | (((t16 >> 3) & 1) << 1) | (t16 & 1);
    unsigned ad;
    unsigned ia[4];
    bf16x8 sdv[4];
    auto pre_addr = [&]() {
#pragma unroll
        for (int k = 0; k < 4; k++) { ia[k] = ldsb + L_XB + lw * 8192 + (w >> 2) * 4096 + t16 * 128 + (((4 * k + g) ^ fw) * 8); PIN(ia[k]); }
        const unsigned rr = 4 * g + q4, fr = (((rr >> 1) & 3) << 2) | (((rr >> 3) & 1) << 1) | (rr & 1);
        ad = ldsb + L_XB + (w & 1) * 16384 + rr * 128 + ((((w >> 1) * 4 + p4) ^ fr) * 8);
        PIN(ad);
    };
    auto sd_reads = [&]() {
        unsigned sa = ldsb + L_SD + g * 8;
        PIN(sa);
#pragma unroll
        for (int s = 0; s < 4; s++) {
            const v2u a = *(const LAS v2u*)(sa + s * 64), b = *(const LAS v2u*)(sa + s * 64 + 32);
            sdv[s] = mk8(a[0], a[1], b[0], b[1]);
        }
    };
    bf16x8 v0[4], v1[4];
    auto v_reads = [&]() {
        unsigned vd = ldsb + L_V + (w & 1) * 8192 + l * 16;
        PIN(vd);
#pragma unroll
        for (int s = 0; s < 4; s++) { v0[s] = lds128(vd + s * 2048); v1[s] = lds128(vd + s * 2048 + 1024); }
    };
    auto kv_tile = [&](auto hook6) {
        const unsigned h2 = w & 1, ft = w >> 1;
        WAITL0();
        BAR();
        SCHED();
        bf16x8 pf[4];
#pragma unroll
        for (int s = 0; s < 4; s++) pf[s] = cat8(tr_read(ad + s * 4096), tr_read(ad + s * 4096 + 2048));
        SCHED();
        hook6();
        SCHED();
        f32x4 k0 = zero4, k1 = zero4, kz = zero4;
#pragma unroll
        for (int s = 0; s < 4; s++) { k0 = MFMA(pf[s], v0[s], k0); k1 = MFMA(pf[s], v1[s], k1); }
#pragma unroll
        for (int s = 0; s < 4; s++) kz = MFMA(pf[s], sdv[s], kz);
        const unsigned pbase = WS_P2 + (hp * 16 + rt) * P2_STRIDE;
        const v4u o0 = {nsent(pk2(k0[0], k0[1])), nsent(pk2(k0[2], k0[3])), nsent(pk2(k1[0], k1[1])), nsent(pk2(k1[2], k1[3]))};
        __builtin_amdgcn_raw_buffer_store_b128(o0, rsWS, l * 16, pbase + h2 * 4096 + ft * 1024, AUX_SC1);
        asm volatile("s_nop 1" :: "v"(o0));
        __builtin_amdgcn_raw_buffer_store_b64((v2u){nsent(pk2(kz[0], kz[1])), nsent(pk2(kz[2], kz[3]))}, rsWS, t16 == 0 ? g * 8 : OOB,
                                              pbase + 8192 + h2 * 128 + ft * 32, AUX_SC1);
        SCHED();
        BAR();
    };

    v4u pp[2];
    auto red = [&]() {
        const v4u* p = pp;
        float lo = xrow_scatter<false>(bflo(p[0][0]) + bflo(p[1][0]), bflo(p[0][1]) + bflo(p[1][1]), bflo(p[0][2]) + bflo(p[1][2]), bflo(p[0][3]) + bflo(p[1][3]));
        float hi = xrow_scatter<false>(bfhi(p[0][0]) + bfhi(p[1][0]), bfhi(p[0][1]) + bfhi(p[1][1]), bfhi(p[0][2]) + bfhi(p[1][2]), bfhi(p[0][3]) + bfhi(p[1][3]));
        lo += dppf<0x128>(lo); hi += dppf<0x128>(hi);
        return nsent(pk2(lo, hi));
    };
    auto pp_so = [&](int k) { return WS_P2 + (hp * 16 + k * 8) * P2_STRIDE + (rt >> 2) * 2048 + (lw & 1) * 1024 + (rt & 3) * 256 + (lw >> 1) * 128; };
    auto red_issue = [&]() {
        const unsigned vpp = kr * P2_STRIDE + (l & 7) * 16;
#pragma unroll
        for (int k = 0; k < 2; k++) asm volatile("buffer_load_dwordx4 %0, %1, %2, %3 offen sc1" : "=v"(pp[k]) : "v"(vpp), "s"(rsWS), "s"(pp_so(k)) : "memory");
        SCHED();
    };
    auto kv_reduce = [&]() {
        waitv<5>();
        PIN(pp[0]); PIN(pp[1]);
        if (!__all(umx(wmax(pp[0]), wmax(pp[1])) != SENT)) sweepz<2, 1>([&](int k) { return LD16(kr * P2_STRIDE + (l & 7) * 16, pp_so(k)); }, pp);
        STAMP(25);
        ST4(red(), (l & 8) ? OOB : (l & 7) * 16 + g * 4, WS_G3 + (hp * 4 + (rt >> 2)) * 2048 + (lw & 1) * 1024 + (rt & 3) * 256 + (lw >> 1) * 128);
    };

    const unsigned half = w >> 2;
    const unsigned R2 = rt2 * 128 + lw * 32 + half * 16 + t16;

    auto dloads = [&](int i0, int i1) {
        const rsrc_t rsD = __builtin_amdgcn_make_buffer_rsrc((void*)WD, 0, 1024 * 3584 * 2, 0x00020000);
#pragma unroll
        for (int i = i0; i < i1; i++)
            wreg[16 + i] = __builtin_amdgcn_raw_buffer_load_b128(rsD, kr * 4096 + (l & 7) * 16, (kc * 256 + lw * 64 + (i >> 1) * 16 + (i & 1)) * 2048 + cg * 128, W_AUX);
        FRESH();
    };
    unsigned wp1[2][4][8];
    v4u xres[2], wrv[2];
    v2u pl;
    unsigned nh;
    unsigned so, va0, va1, wb[4], vFl, sFl, swc, sG5, sG5r, sRep, nr, aXw, aXr, vG5;
    f32x4 acc2[2][4];
    bf16x8 sop[2];
    unsigned pv;
    auto place = [&]() {
        SCHED();
        PIN(pl);
        if (RARE(rfl(pl[1]) != tag)) { unsigned c[1]; sweep<1>(rsWS, 0, WS_PR + 1024, 0, tag, c); pl[0] = c[0]; }
        pv = rfl(pl[0]);
        nh = ((bid ^ (pv >> 4)) & 4) * 2048;
        asm volatile("" : "+s"(pv), "+s"(nh));
    };
    auto prep2 = [&]() {
        SCHED();
        vFl = t16 * 128 + g * 8;
        sFl = WS_F4 + (rt2 & 12) * 4096 + (rt2 & 3) * 2048 + nh;
        va0 = WS_P4 + ((rt2 & 7) * 8 + lw * 2 + half) * 65536 + (nh ^ (~rt2 & 4) * 2048) + l * 16; va1 = va0 ^ 65536;
        so = (half + rot2 + (rot2 & 24)) * 1024;
#pragma unroll
        for (int nt = 0; nt < 4; nt++) { wb[nt] = ldsb + L_WO + half * 65536 + (4 * g + q4) * 128 + ((nt ^ ((2 * g + (q4 >> 1)) & 3)) * 32) + p4 * 8; PIN(wb[nt]); }
        swap16(xres[0]); swap16(xres[1]); swap16(wrv[0]); swap16(wrv[1]);
        asm volatile("s_getreg_b32 %0, hwreg(HW_REG_XCC_ID, 0, 3)" : "=s"(sRep));
        sRep ^= pv & 4;
        nr = (sRep & 4) * 2048;
        sRep = WS_GK + (sRep + (idx & 1) * 8) * 2048;
        sG5r = WS_G5 + (rt2 & 7) * 65536 + lw * 16384 + half * 4096 + nh;
        sG5 = sG5r + ct * 256;
        aXw = ldsb + L_XB + w * 4096 + l * 16; aXr = aXw ^ 16384; vG5 = g == q4 ? t16 * 16 : OOB;
        asm volatile("" : "+s"(sG5), "+s"(sG5r), "+s"(sRep), "+s"(nr), "+s"(sFl), "+s"(nh), "+s"(so));
        PIN(aXw); PIN(aXr); PIN(vG5); PIN(va0); PIN(va1); PIN(vFl);
#pragma unroll
        for (int nt = 0; nt < 4; nt++) {
            const unsigned x0 = xres[nt >> 1][2 * (nt & 1)], x1 = xres[nt >> 1][2 * (nt & 1) + 1];
            acc2[0][nt] = (f32x4){bflo(x0), bfhi(x0), bflo(x1), bfhi(x1)};
            PIN(acc2[0][nt]);
        }
        unsigned m0 = p4 == 0 ? 0x3F803F80u : 0u, m1 = p4 == 1 ? ~0u : 0u;
        PIN(m0); PIN(m1);
#pragma unroll
        for (int i = 0; i < 2; i++) {
            v4u u;
#pragma unroll
            for (int j = 0; j < 4; j++) u[j] = (wrv[i][j] & m1) | m0;
            sop[i] = __builtin_bit_cast(bf16x8, u);
            PIN(sop[i]);
        }
        float sw = 0.f;
#pragma unroll
        for (int i = 0; i < 8; i++) sw = dot2(wrv[i >> 2][i & 3], 0x3F803F80u, sw);
        swc = f2s(xrow_sum(sw));
        PIN(swc);
        SCHED();
    };
    auto res_loads = [&]() {
        pl = ldg(rsWS, 0, WS_PR + 1024);
#pragma unroll
        for (int i = 0; i < 2; i++) {
            xres[i] = *(const v4u*)(X + (size_t)R2 * 1024 + ct * 64 + i * 32 + 8 * gx);
            wrv[i] = *(const v4u*)(WR + ct * 64 + i * 32 + 8 * gx);
        }
        FRESH();
    };

    if (w < 4) {
        const unsigned wm = w & 1, hh = w >> 1;
        f32x4 acc[4][6];
        bf16x8 wf[2], wfq[2];
        float mbt[4], rst[4], csv[2];
        f32x4 mbv[4], csq[2], csk[2];
#pragma unroll
        for (int i = 0; i < 4; i++)
#pragma unroll
            for (int j = 0; j < 6; j++) { acc[i][j] = zero4; PIN(acc[i][j]); }
        {
            const unsigned aoff0 = ldsb + (wm * 64 + t16) * 128 + (((0 + g) ^ (l & 7)) * 16);
            const unsigned aoff1 = ldsb + (wm * 64 + t16) * 128 + (((4 + g) ^ (l & 7)) * 16);
            const unsigned sk = ((q4 >> 1) & 1) | ((g & 1) << 1);
            const unsigned boffA = ldsb + 16384 + (8 * g + q4) * 128 + (((2 * hh) ^ sk) * 32) + p4 * 8;
            const unsigned boffB = ldsb + 16384 + (8 * g + q4) * 128 + (((2 * hh + 1) ^ sk) * 32) + p4 * 8;
            bf16x8 A[2][4], B[3][2];
            s16x4 lo;
            unsigned a0[3], a1[3], bA[3], bB[3];
#pragma unroll
            for (int i = 0; i < 3; i++) {
                a0[i] = aoff0 + i * STAGE_B; a1[i] = aoff1 + i * STAGE_B; bA[i] = boffA + i * STAGE_B; bB[i] = boffB + i * STAGE_B;
                PIN(a0[i]); PIN(a1[i]); PIN(bA[i]); PIN(bB[i]);
            }
            unsigned aT = ldsb + L_MB + (wm * 64 + t16) * 4, aS = ldsb + L_SD + (wm * 64 + t16) * 2, aM = ldsb + L_MB + wm * 256 + g * 16;
            unsigned aC = ldsb + L_CS + hh * 128 + g * 16, aV = ldsb + L_CS + (128 + hh * 32 + t16) * 4;
            unsigned aW = ldsb + L_WPHI + hh * 2048 + (4 * g + q4) * 64 + p4 * 8, aK = ldsb + L_KH + w * 2048 + l * 16;
            PIN(aT); PIN(aS); PIN(aM); PIN(aC); PIN(aV); PIN(aW); PIN(aK);
            pre_addr();
            READ_TAG();
            waitv<5>();
            PIN(xr[0]); PIN(xr[1]); PIN(xr[2]); PIN(xr[3]);
            {
                float s[2], q[2], x0[2];
#pragma unroll
                for (int rr = 0; rr < 2; rr++) {
                    x0[rr] = bflo(rfl(xr[2 * rr][0]));
                    s[rr] = 0.f; q[rr] = 0.f;
#pragma unroll
                    for (int i = 0; i < 4; i++) {
                        const float a = bflo(xr[2 * rr][i]) - x0[rr], b = bfhi(xr[2 * rr][i]) - x0[rr];
                        const float c = bflo(xr[2 * rr + 1][i]) - x0[rr], d = bfhi(xr[2 * rr + 1][i]) - x0[rr];
                        s[rr] += (a + b) + (c + d);
                        q[rr] += (a * a + b * b) + (c * c + d * d);
                    }
                }
                const float sq = row_sum(xrow_scatter<false>(s[0], s[1], q[0], q[1]));
#define RLF(x, n) __uint_as_float(__builtin_amdgcn_readlane(__float_as_uint(x), (n)))
                const float r0 = RLF(sq, 0), r1 = RLF(sq, 16), r2 = RLF(sq, 32), r3 = RLF(sq, 48);
                const float sm = (l ? r1 : r0) * (1.0f / 1024.0f), qm = (l ? r3 : r2) * (1.0f / 1024.0f);
                const float var = fmaxf(qm - sm * sm, 0.f);
                if (l < 2) st_gran(ws, WS_G0 + (srow1 + l) * 8, tag, pk2((l ? x0[1] : x0[0]) + sm, sqrtf(rbf(rbf(var) + 1e-5f))));
            }
            waitv<1>();
            WAITL0();
            BAR();
            SCHED();
            STAMP(1);
            auto rdB = [&](int u, int j, int i) {
                if (i & 1) B[j % 3][i >> 1] = cat8(lo, bq(bA[u], bB[u], j, i)); else lo = bq(bA[u], bB[u], j, i);
            };
            auto rdA = [&](int u, int i) { A[i & 1][i >> 1] = lds128(((i & 1) ? a1[u] : a0[u]) + (i >> 1) * 2048); };
            auto head = [&](int u) {
                group(acc, A, B[0], 0, [&](int i) { rdB(u, 1 + (i >> 2), i & 3); });
                group(acc, A, B[1], 1, [&](int i) { if (i < 4) rdB(u, 3, i); });
                group(acc, A, B[2], 2, [&](int i) { if (i < 4) rdB(u, 4, i); });
                group(acc, A, B[0], 3, [&](int i) { if (i < 4) rdB(u, 5, i); });
                WL0();
            };
#pragma unroll
            for (int i = 0; i < 4; i++) rdB(0, 0, i);
#pragma unroll
            for (int i = 0; i < 8; i++) rdA(0, i);
            SCHED();
#pragma nounroll
            for (unsigned t = 0; t < 5; t++) {
#pragma unroll
                for (int u = 0; u < 3; u++) {
                    head(u);
                    BAR();
                    JIT(32 + t * 3 + u);
                    SCHED();
                    group(acc, A, B[1], 4, [&](int i) { if (i < 4) rdB((u + 1) % 3, 0, i); });
                    group(acc, A, B[2], 5, [&](int i) { rdA((u + 1) % 3, i); });
                }
            }
            unsigned kw[4][4];
            f32x2 kx;
            auto kpiece = [&](int n) {
                if (n > 0) { kw[((n - 1 >> 2) + 2) & 3][(n - 1) & 3] = pk2(kx[0], kx[1]); PIN(kw[((n - 1 >> 2) + 2) & 3][(n - 1) & 3]); }
                if (n < 16) {
                    const int mi = ((n >> 2) + 2) & 3, j = n & 3;
                    const f32x4 &c = acc[mi][2 + (j >> 1)], &s_ = csk[j >> 1];
                    float x0 = __builtin_fmaf(mbt[mi], s_[2 * (j & 1)], c[2 * (j & 1)]); PIN(x0);
                    float x1 = __builtin_fmaf(mbt[mi], s_[2 * (j & 1) + 1], c[2 * (j & 1) + 1]); PIN(x1);
                    kx = (f32x2){x0, x1};
                }
                if (n == 5 || n == 9) *(LAS v4u*)(aK + (n == 9) * 1024) = (v4u){kw[2 + (n == 9)][0], kw[2 + (n == 9)][1], kw[2 + (n == 9)][2], kw[2 + (n == 9)][3]};
            };
            group(acc, A, B[0], 0, [&](int i) { rdB(0, 1 + (i >> 2), i & 3); });
            group(acc, A, B[1], 1, [&](int i) {
                if (i < 4) { rdB(0, 3, i); mbt[(i + 2) & 3] = ldsf(aT + ((i + 2) & 3) * 64); }
                else if (i < 6) csk[i - 4] = ldsf4(aC + 256 + (i - 4) * 64);
                else wf[i - 6] = cat8(tr_read(aW + (i - 6) * 32), tr_read(aW + (i - 6) * 32 + 1024));
            });
            group(acc, A, B[2], 2, [&](int i) { if (i < 4) rdB(0, 4, i); else mbv[i - 4] = ldsf4(aM + (i - 4) * 64); kpiece(i); });
            group(acc, A, B[0], 3, [&](int i) { if (i < 4) rdB(0, 5, i); if (i == 2 || i == 3) csv[i - 2] = ldsf(aV + (i - 2) * 64); kpiece(8 + i); });
            WL0();
            BAR();
            SCHED();
            kpiece(16);
            f32x4 p[2][2];
#pragma unroll
            for (int i = 0; i < 4; i++) p[i >> 1][i & 1] = MFMA(wf[i & 1], mk8(kw[i >> 1][0], kw[i >> 1][1], kw[i >> 1][2], kw[i >> 1][3]), zero4);
            SCHED();
            STAMP(2);
#pragma unroll
            for (int mi = 0; mi < 4; mi++) rst[mi] = lds_bf(aS + mi * 32);
#pragma unroll
            for (int a = 0; a < 2; a++)
#pragma unroll
                for (int dt = 0; dt < 2; dt++) {
                    const f32x4 x0 = acc[2 * a][4 + dt] + mbv[2 * a] * csv[dt], x1 = acc[2 * a + 1][4 + dt] + mbv[2 * a + 1] * csv[dt];
                    *(LAS v4u*)(aK + (L_V - L_KH) + w * 2048 + (a * 2 + dt) * 1024) = (v4u){pk2(x0[0], x0[1]), pk2(x0[2], x0[3]), pk2(x1[0], x1[1]), pk2(x1[2], x1[3])};
                }
            SCHED();
            rst[0] = __builtin_amdgcn_rcpf(rst[0]); rst[1] = __builtin_amdgcn_rcpf(rst[1]);
            bf16x8 f[4];
            struct HookC {
                decltype(v_reads)& vr; decltype(sd_reads)& sr;
                DI void operator()(int k, float& x) const { if (k == 1) { PIN(x); WAITL0(); BAR(); PIN(x); } if (k == 3) { PIN(x); vr(); sr(); PIN(x); } }
                DI void operator()(int, float&, float&) const {}
            };
            softmax2<true>(p, rst[0], rst[1], g >= 2, f, HookC{v_reads, sd_reads});
            img_write2(ia, f);
#pragma unroll
            for (int ft = 0; ft < 2; ft++) wfq[ft] = cat8(tr_read(aW + ft * 32 + 4096), tr_read(aW + ft * 32 + 5120));
            csq[0] = ldsf4(aC); csq[1] = ldsf4(aC + 64);
            STAMP(3);
            kv_tile([]() {});
            STAMP(4);
            SCHED();
            group(acc, A, B[1], 4, [&](int i) { if (i == 2 || i == 3) { rst[i] = __builtin_amdgcn_rcpf(rst[i]); PIN(rst[i]); } });
            group(acc, A, B[2], 5, [&](int) {});
            asm volatile("s_nop 7\n\ts_nop 1");
            SCHED();
        }
        bf16x8 phq[2][4];
        v4u kg[6];
        auto kg_ld = [&](int k) { return k < 4 ? LD16(l * 16, WS_G3 + (hq * 2 + hh) * 4096 + k * 1024) : LD16(g * 16, WS_GZ + (hq * 2 + hh) * 128 + (k - 4) * 64); };
        {
            f32x4 pq[2][2][2];
#pragma unroll
            for (int mi = 0; mi < 4; mi++) {
                const f32x4 x0 = acc[mi][0] + mbt[mi] * csq[0], x1 = acc[mi][1] + mbt[mi] * csq[1];
                const bf16x8 qf = mk8(pk2(x0[0], x0[1]), pk2(x0[2], x0[3]), pk2(x1[0], x1[1]), pk2(x1[2], x1[3]));
                pq[mi >> 1][mi & 1][0] = MFMA(wfq[0], qf, zero4); pq[mi >> 1][mi & 1][1] = MFMA(wfq[1], qf, zero4);
            }
            MFMA_SETTLE2(pq[0]); MFMA_SETTLE2(pq[1]);
            softmax2<false>(pq[0], rst[0], rst[1], g >= 2, phq[0]);
#pragma unroll
            for (int i = 0; i < 4; i++) PIN(phq[0][i]);
            SCHED();
            red_issue();
            res_loads();
            SCHED();
            kv_reduce();
            SCHED();
            BAR();
            SCHED();
            softmax2<false>(pq[1], rst[2], rst[3], g >= 2, phq[1]);
#pragma unroll
            for (int i = 0; i < 4; i++) { PIN(phq[0][i]); PIN(phq[1][i]); }
            SCHED();
        }
        STAMP(5);
        v2u fv = {tag ^ 0x5bd1e995u, tag};
        place();
        PIN(xres[0]); PIN(xres[1]); PIN(wrv[0]); PIN(wrv[1]);
        unsigned sF = WS_F4 + (rt & 12) * 4096 + (rt & 3) * 2048 + hq * 128 + w * 8 + nh, vF = l == 0 ? 0 : OOB;
        {
            unsigned vO = l * 16, sO = WS_P4 + ((rt & 7) * 8 + wm * 4) * 65536 + (hq >> 2) * 16384 + ((hq & 3) * 2 + hh) * 1024 + (nh ^ (~rt & 4) * 2048);
            asm volatile("" : "+s"(sO), "+s"(sF));
            PIN(fv); PIN(vF);
            SLEEP(1);
            sweepz<6, 4>(kg_ld, kg);
            STAMP(6);
            bf16x8 kvA[2][2], zA[2];
            v4u ov[4];
#pragma unroll
            for (int s = 0; s < 2; s++) {
#pragma unroll
                for (int dt = 0; dt < 2; dt++) kvA[s][dt] = mk8(kg[2 * s][2 * dt], kg[2 * s][2 * dt + 1], kg[2 * s + 1][2 * dt], kg[2 * s + 1][2 * dt + 1]);
                zA[s] = __builtin_bit_cast(bf16x8, kg[4 + s]);
            }
#define PH(mi, s) phq[(mi) >> 1][2 * ((mi) & 1) + (s)]
            f32x4 dn[4], n0[4], n1[4];
            float id[4];
#pragma unroll
            for (int mi = 0; mi < 4; mi++) dn[mi] = MFMA(zA[0], PH(mi, 0), zero4);
#pragma unroll
            for (int mi = 0; mi < 4; mi++) dn[mi] = MFMA(zA[1], PH(mi, 1), dn[mi]);
            SCHED();
#pragma unroll
            for (int mi = 0; mi <= 4; mi++) {
                if (mi < 4) {
                    n0[mi] = MFMA(kvA[0][0], PH(mi, 0), zero4); n1[mi] = MFMA(kvA[0][1], PH(mi, 0), zero4);
                    n0[mi] = MFMA(kvA[1][0], PH(mi, 1), n0[mi]); n1[mi] = MFMA(kvA[1][1], PH(mi, 1), n1[mi]);
                    id[mi] = __builtin_amdgcn_rcpf(rbf(rbf(dn[mi][0]) + 1e-6f));
                }
                if (mi > 0) {
                    const f32x4 a = n0[mi - 1] * id[mi - 1], b = n1[mi - 1] * id[mi - 1];
                    ov[mi - 1] = (v4u){pk2(a[0], a[1]), pk2(a[2], a[3]), pk2(b[0], b[1]), pk2(b[2], b[3])};
                    ST16(ov[mi - 1], vO + (mi - 1) * 65536, sO);
                }
                if (mi < 4) { SGB(0x8, 1); SGB(0x2, 6); SGB(0x8, 1); SGB(0x2, 3); SGB(0x8, 1); SGB(0x2, 3); SGB(0x8, 1); SGB(0x2, 4); }
                SCHED();
            }
#pragma unroll
            for (int mi = 0; mi < 4; mi++) PIN(ov[mi]);
        }
        waitv<0>();
        __builtin_amdgcn_raw_buffer_store_b64(fv, rsWS, vF, sF, AUX_SC1);
        STAMP(7);
    } else {
        const unsigned pw = lw & 1;
        const rsrc_t rsG = __builtin_amdgcn_make_buffer_rsrc((void*)WG, 0, 1024 * 3584 * 2, 0x00020000);
        const rsrc_t rsU = __builtin_amdgcn_make_buffer_rsrc((void*)WU, 0, 1024 * 3584 * 2, 0x00020000);
        unsigned vwl = kr * 57344 + (l & 7) * 16;
        unsigned sg = (kq * 256 + lw * 64) * 7168 + cg1 * 128, su = (kq * 256 + lw * 64) * 7168 + cg1u * 128;
        auto wloads = [&](int n0, int n1) {
#pragma unroll
            for (int n = n0; n < n1; n++)
                asm volatile("buffer_load_dwordx4 %0, %1, %2, %3 offen nt" : "=v"(wreg[n]) : "v"(vwl), "s"(n < 8 ? rsG : rsU), "s"((n < 8 ? sg : su) + (n & 7) * 7168));
        };
        auto dl_asm = [&]() {
            const rsrc_t rsD = __builtin_amdgcn_make_buffer_rsrc((void*)WD, 0, 1024 * 3584 * 2, 0x00020000);
#pragma unroll
            for (int i = 0; i < DN0; i++)
                asm volatile("buffer_load_dwordx4 %0, %1, %2, %3 offen nt" : "=v"(wreg[16 + i]) : "v"(kr * 4096 + (l & 7) * 16), "s"(rsD),
                             "s"((kc * 256 + lw * 64 + (i >> 1) * 16 + (i & 1)) * 2048 + cg * 128));
        };
        unsigned aK, aW, aS;
        auto k_pre = [&]() {
            pre_addr();
            aK = ldsb + L_KH + lw * 2048 + l * 16; aW = ldsb + L_WPHI + (lw >> 1) * 2048 + (4 * g + q4) * 64 + p4 * 8; aS = ldsb + L_SD + (pw * 64 + 32 + t16) * 2;
            PIN(aK); PIN(aW); PIN(aS);
        };
        auto kfeat = [&](auto hook) {
            bf16x8 wf[2];
            float rst[2];
#pragma unroll
            for (int ft = 0; ft < 2; ft++) {
                wf[ft] = cat8(tr_read(aW + ft * 32), tr_read(aW + ft * 32 + 1024));
                rst[ft] = __builtin_amdgcn_rcpf(lds_bf(aS + ft * 32));
            }
            sd_reads();
            PIN(wf[0]); PIN(wf[1]); PIN(rst[0]); PIN(rst[1]);
            SCHED();
            BAR();
            SCHED();
            const bf16x8 kf0 = lds128(aK), kf1 = lds128(aK + 1024);
            SCHED();
            hook(-1, rst[0]);
            hook(0, rst[0]);
            SCHED();
            f32x4 p[2][2];
            p[0][0] = MFMA(wf[0], kf0, zero4); p[0][1] = MFMA(wf[1], kf0, zero4);
            p[1][0] = MFMA(wf[0], kf1, zero4); p[1][1] = MFMA(wf[1], kf1, zero4);
            MFMA_SETTLE2(p);
            bf16x8 f[4];
            softmax2<true>(p, rst[0], rst[1], g >= 2, f, hook);
            img_write2(ia, f);
            SCHED();
            hook(5, rst[0]);
            SCHED();
            STAMP(3);
            kv_tile([&]() { hook(6, rst[0]); });
            STAMP(4);
        };
        auto wblk = [&](int k) { return k == 0 ? 22 + lw : k == 1 ? (lw < 3 ? 26 + lw : 4) : k < 6 ? 4 * k - 3 + lw : k == 6 ? lw : (lw < 3 ? 29 + lw : 21); };
        auto wo_init = [&]() {
#pragma unroll
            for (int i = 0; i < 4; i++) { vwo[i] = vWO + i * (16384 - 1024); PIN(vwo[i]); }
        };
        unsigned hs[8];
        auto wl_pre = [&]() { asm volatile("" : "+s"(sg), "+s"(su)); PIN(vwl); };
        auto wo_pre = [&]() {
            wo_init();
#pragma unroll
            for (int k = 0; k < 8; k++) { hs[k] = wo_so(wblk(k)); asm volatile("" : "+s"(hs[k])); }
        };
        auto wo_rng = [&](int k0, int k1) {
#pragma unroll
            for (int k = k0; k < k1; k++) dma16x4(ldsb + L_WO + wblk(k) * 4096, vwo, rsWO, hs[k]);
        };
        auto pf = [&](int i) {
            if (i == 0) { wloads(0, 6); SCHED(); if (lw < 2) wo_pre(); SCHED(); }
            if (i < 3 && !ffn) wo_rng(i, i + 1);
            if (i == 1 || i == 2) wloads(6 * i, i == 1 ? 12 : 16);
            if (i == 6) dl_asm();
        };
        struct Hook {
            decltype(pf)& f; decltype(v_reads)& vr;
            DI void operator()(int k, float& x) const { PIN(x); if (k == 1) BAR(); if (k == 3) vr(); f(3 + k); PIN(x); }
            DI void operator()(int k, float& x, float& y) const { PIN(y); f(3 + k); PIN(x); }
        };
        auto tail16 = [&]() {
            kfeat(Hook{pf, v_reads});
            if (ffn) wo_rng(0, 3);
            wo_rng(3, 4);
            STAMP(27);
            if (ffn) waitv<18>(); else waitv<6>();
#pragma unroll
            for (int n = 0; n < 16 + DN0; n++) PIN(wreg[n]);
        };
        auto vT23 = [&]() {
            const unsigned n = (idx * 2 + pw) * 64 + l, r = n / 12, c = n % 12;
            return idx < 24 ? ((r & 127) + (r >> 7) * 256 + 128) * 6144 + (c >> 2) * 2048 + (((xcd >> 1) * 4 + (c >> 2 ? 0 : 4) + (c & 3)) & 15) * 128 : OOB;
        };
        if (lw < 2) {
            const unsigned sA = (rt * 128 + lw * 64) * 2048, sB = lw * 32 * 6144 + hp * 128, sBq = lw * 32 * 6144 + hq * 128;
            unsigned buf = 1;
            unsigned va[4], vb[4];
#pragma unroll
            for (int i = 0; i < 4; i++) { va[i] = vA + i * (16384 - 1024); vb[i] = ((i & 1) ? vB1 : vB0) + i * (49152 - 1024); PIN(va[i]); PIN(vb[i]); }
            auto stage = [&](unsigned s, unsigned bf, int i0, int i1) {
                const unsigned kt = (s + rot) & 15;
                const unsigned lb = ldsb + bf * STAGE_B;
                const unsigned sa = sA + kt * 128, sb = kt * 393216;
                if (i0 <= 0 && 0 < i1) dma16x4(lb + lw * 8192, va, rsX, sa);
                if (i0 <= 1 && 1 < i1) dma16x4(lb + lw * 8192 + 4096, va, rsX, sa + 65536);
                if (i0 <= 2 && 2 < i1) dma16x4(lb + 16384 + 8192 + lw * 4096, vb, rsW, sb + sB + 2048);
                if (i0 <= 3 && 3 < i1) dma16x4(lb + 16384 + 16384 + lw * 4096, vb, rsW, sb + sB + 4096);
                if (i0 <= 4 && 4 < i1) dma16x4(lb + 16384 + lw * 4096, vb, rsW, sb + sBq);
            };
            stage(1, 1, 0, 4);
            SCHED();
            unsigned vt = vT23();
            PIN(vt);
            wl_pre(); k_pre();
            waitv<16>(); BAR();
            stage(1, 1, 4, 5);
            touch(vt, rsW, (xcd & 1) * 512 * 6144);
            waitv<1>(); BAR();
            stage(2, 2, 0, 5); stage(3, 0, 0, 3);
            waitv<12>(); BAR();
            stage(3, 0, 3, 5); stage(4, 1, 0, 4);
            waitv<16>(); BAR();
            stage(4, 1, 4, 5);
            buf = 2;
#pragma nounroll
            for (unsigned s = 5; s < 16; s++) {
                if (s > 5) { waitv<20>(); BAR(); }
                stage(s, buf, 0, 5);
                buf = buf == 2 ? 0 : buf + 1;
            }
            waitv<20>(); BAR();
            pf(0);
            READ_TAG();
            if (ffn) waitv<6>(); else waitv<10>();
            BAR();
            pf(1);
            tail16();
        } else {
            SCHED();
            unsigned v3;
            const unsigned h3 = (xcd >> 1) * 4 + 3, kv3 = !(xcd & 2), wv = idx * 2 + pw;
            auto line3 = [&](unsigned per, unsigned rows, auto rowf) {
                const unsigned n = wv * (kv3 ? per : (per + 2) / 3) + l, r3 = kv3 ? n / 3 : n, c3 = kv3 ? n % 3 : 0;
                return (idx < 24 && l < (kv3 ? per : (per + 2) / 3) && r3 < rows) ? rowf(r3) * 6144 + c3 * 2048 + ((c3 ? h3 : h3 + 4) & 15) * 128 : OOB;
            };
            v3 = line3(32, 512, [](unsigned r) { return r; });
            touch(line3(16, 256, [](unsigned r) { return (r & 127) + (r >> 7) * 256; }), rsW, (xcd & 1) * 512 * 6144);
            {
                const rsrc_t rsP = __builtin_amdgcn_make_buffer_rsrc((void*)WPHI, 0, 32 * 1024 * 2, 0x00020000);
#pragma unroll
                for (int i = 0; i < 4; i++) dma16(ldsb + L_WPHI + pw * 4096 + i * 1024, l * 16, rsP, (pw ? hq : hp) * 4096 + i * 1024);
            }
            v4u sv = {SENT, SENT, SENT, SENT};
            unsigned vc4 = l < 16 ? WS_G3 + (hp * 4 + (rt >> 2)) * 2048 + pw * 1024 + (rt & 3) * 256 + l * 16
                         : l < 24 ? WS_P2 + (hp * 16 + rt) * P2_STRIDE + 8192 + pw * 128 + (l - 16) * 16
                         : (l < 32 && pw == 0 && (rt & 7) == 0) ? WS_GZ + (hp * 2 + (rt >> 3)) * 128 + (l - 24) * 16
                         : l >= 56 ? WS_F4 + (rt & 12) * 4096 + (rt & 3) * 2048 + hq * 128 + pw * 8192 + (l & 7) * 16 : OOB;
            auto clears = [&](int i0, int i1) {
#pragma unroll
                for (int i = i0; i < i1; i++) {
                    if (i < 4) ST16(sv, l * 16, WS_P2 + (hp * 16 + rt) * P2_STRIDE + pw * 4096 + i * 1024);
                    if (i == 4) ST16(sv, vc4, 0);
                    if (i > 4) ST16(sv, l * 16, WS_TL + (bid * 4 + pw * 2 + i - 5) * 1024);
                }
                FRESH();
            };
            PIN(vc4); PIN(sv);
            unsigned ca = ldsb + 16384 + pw * 8192 + (8 * g + q4) * 128 + (((rt & 3) ^ (((q4 >> 1) & 1) | ((g & 1) << 1))) * 32) + p4 * 8;
            PIN(ca);
            bf16x8 cf[4];
            f32x4 cd[2] = {zero4, zero4};
            auto cs_read = [&](int st) {
#pragma unroll
                for (int i = 0; i < 4; i++) cf[i] = cat8(tr_read(ca + (st % 3) * STAGE_B + i * 4096), tr_read(ca + (st % 3) * STAGE_B + i * 4096 + 512));
                SCHED();
            };
            READ_TAG();
            waitv<5>();
            BAR();
            SCHED();
            cs_read(0);
            FRESH();
            const rsrc_t rsC = __builtin_amdgcn_make_buffer_rsrc((void*)pc0, 0, clen, 0x00020000);
            auto code_pf = [&]() { touch(l < 6 ? l * 128 : OOB, rsC, (idx * 2 + pw) * 768); };
            v4u gl[4];
            float s0 = 0.f, s1 = 0.f;
            unsigned bad;
            v2u gs, gc;
            unsigned vgc = WS_GC + (pw ? hq : hp) * 256 + (l & 31) * 8;
            unsigned lat = 0;
            unsigned vgl = (l < 48 ? l : 0) * 16;
            unsigned aw0 = ldsb + L_CS + pw * 384 + (vgl >> 1), aw1 = ldsb + L_MB + (pw * 64 + l) * 4, aw2 = ldsb + L_SD + (pw * 64 + l) * 2;
            static_for<15>([&](auto tc) {
                constexpr int t = decltype(tc)::value;
                WAITL0();
                JIT(32 + t);
                BAR();
                SCHED();
                if constexpr (t < 4) {
                    __builtin_amdgcn_s_setprio(2);
#pragma unroll
                    for (int i = 0; i < 4; i++) cd[i & 1] = MFMA(ones, cf[(i & 1) * 2 + (i >> 1)], cd[i & 1]);
                    SCHED();
                    __builtin_amdgcn_s_setprio(0);
                    if constexpr (t < 3) cs_read(t + 1);
                    else __builtin_amdgcn_raw_buffer_store_b64((v2u){__float_as_uint(g ? cd[1][0] : cd[0][0]), tag}, rsWS, (g == 1 || g + pw == 0) ? g * 512 + t16 * 8 : OOB,
                                                               WS_G1 + ((hp * 4 + (rt >> 2)) * 192 + pw * 64 + (rt & 3) * 16) * 8, AUX_SC1);
                }
                if constexpr (t == 0) touch(v3, rsW, (~xcd & 1) * 512 * 6144);
                if constexpr (t == 12) code_pf();
                if constexpr (t == 8) {
                    waitv<0>();
                    __builtin_amdgcn_raw_buffer_store_b64((v2u){0u, tag}, rsWS, l == 0 ? 0 : OOB, WS_GC + ((hp * 2 + pw) * 16 + rt) * 8, AUX_SC1);
                }
                if constexpr (t == 8 || t == 9) {
#pragma unroll
                    for (int r = (t - 8) * 2; r < (t - 8) * 2 + 2; r++)
                        asm volatile("buffer_load_dwordx4 %0, %1, %2, %3 offen sc1" : "=v"(gl[r]) : "v"(vgl), "s"(rsWS), "s"(WS_G1 + ((hp * 4 + r) * 192 + pw * 96) * 8) : "memory");
                }
                if constexpr (t == 6) {
                    asm volatile("buffer_load_dwordx2 %0, %1, %2, %3 offen sc1" : "=v"(gs) : "v"(l * 8), "s"(rsWS), "s"(WS_G0 + (rt * 128 + pw * 64) * 8) : "memory");
                }
                if constexpr (t == 11) {
                    waitv<0>();
                    PIN(gs);
                    bad = gs[1] ^ tag;
#pragma unroll
                    for (int r = 0; r < 4; r++) {
                        PIN(gl[r]);
                        bad |= (gl[r][1] ^ tag) | (gl[r][3] ^ tag);
                        s0 += __uint_as_float(gl[r][0]); s1 += __uint_as_float(gl[r][2]);
                    }
                    PIN(s0); PIN(s1); PIN(bad);
                }
                if constexpr (t == 12) {
                    if (RARE(!__all(bad == 0))) {
                        s0 = 0.f; s1 = 0.f;
#pragma nounroll
                        for (unsigned r = 0; r < 4; r++) {
                            unsigned a[2];
                            sweep<2>(rsWS, vgl, WS_G1 + ((hp * 4 + r) * 192 + pw * 96) * 8, 8, tag, a);
                            s0 += __uint_as_float(a[0]); s1 += __uint_as_float(a[1]);
                        }
                        unsigned c[1];
                        sweep<1>(rsWS, l * 8, WS_G0 + (rt * 128 + pw * 64) * 8, 0, tag, c);
                        gs[0] = c[0];
                    }
                    *(LAS f32x2*)(aw0) = (f32x2){-s0, -s1};
                    stsf(aw1, bflo(gs[0]));
                    *(LAS u16*)(aw2) = (u16)(gs[0] >> 16);
                    asm volatile("buffer_load_dwordx2 %0, %1, %2, 0 offen sc1" : "=v"(gc) : "v"(vgc), "s"(rsWS) : "memory");
                    SCHED();
                }
                if constexpr (t == 8) { if (RARE(bid + pw == 0)) ST16(sv, l < 16 ? (l & 8) * 1024 + (l & 7) * 16 : OOB, WS_PR); }
                if constexpr (t == 10 || t == 11) {
                    if (RARE(bid + pw == 0)) {
                        u64 c0, c1; unsigned x;
                        asm volatile("s_memtime %0\n\ts_load_dword %2, %3, %4\n\ts_waitcnt lgkmcnt(0)\n\ts_memtime %1\n\ts_waitcnt lgkmcnt(0)"
                                     : "=&s"(c0), "=&s"(c1), "=&s"(x) : "s"(ws), "i"(WS_PR + (t & 1) * 8192) : "memory");
                        if constexpr (t == 10) lat = (unsigned)c1 - (unsigned)c0;
                        else {
                            asm volatile("s_getreg_b32 %0, hwreg(HW_REG_XCC_ID, 0, 3)" : "=s"(x));
                            __builtin_amdgcn_raw_buffer_store_b64((v2u){((unsigned)c1 - (unsigned)c0 < lat ? 0x44 : 0) ^ (x & 4), tag}, rsWS, l == 0 ? 0 : OOB, WS_PR + 1024, AUX_SC1);
                        }
                    }
                }
                if constexpr (t == 5) k_pre();
                if constexpr (t == 6) { wl_pre(); wo_pre(); }
                if constexpr (t == 7) { PIN(vgc); PIN(vgl); PIN(aw0); PIN(aw1); PIN(aw2); }
                clears(t < 7 ? t : 7, t < 7 ? t + 1 : 7);
                if constexpr (t == 13) {
                    waitv<0>();
                    PIN(gc);
                    if (RARE(!__all(gc[1] == tag))) { unsigned c[1]; sweep<1>(rsWS, vgc, 0, 0, tag, c); }
                    SCHED();
                    pf(0);
                }
                if constexpr (t == 14) pf(1);
            });
            tail16();
        }
        STAMP(28);
        auto perm1 = [&]() {
#pragma unroll
            for (int ip = 0; ip < 4; ip++)
#pragma unroll
                for (int c = 0; c < 4; c++) {
#pragma unroll
                    for (int m = 0; m < 2; m++) {
                        const unsigned a = wreg[m * 8 + 2 * ip][c], b = wreg[m * 8 + 2 * ip + 1][c];
                        wp1[m][ip][2 * c] = __builtin_amdgcn_perm(b, a, 0x05040100);
                        wp1[m][ip][2 * c + 1] = __builtin_amdgcn_perm(b, a, 0x07060302);
                    }
                }
#pragma unroll
            for (int ip = 0; ip < 4; ip++)
#pragma unroll
                for (int c = 0; c < 8; c++) { PIN(wp1[0][ip][c]); PIN(wp1[1][ip][c]); }
            SCHED();
            STAMP(29);
        };
        if (RARE(lw == 0 && (rt & 7) == 0)) {
            __builtin_amdgcn_s_setprio(1);
            sweepz<2, 1>([&](int k) { return LD16(kr * P2_STRIDE + (l & 7) * 16, WS_P2 + (hp * 16 + k * 8) * P2_STRIDE + 8192 + (rt >> 3) * 128); }, pp);
            ST4(red(), (l & 8) ? OOB : ((l >> 2) & 1) * 64 + ((l & 1) * 2 + (g >> 1)) * 16 + ((l >> 1) & 1) * 8 + (g & 1) * 4, WS_GZ + (hp * 2 + (rt >> 3)) * 128);
            __builtin_amdgcn_s_setprio(0);
        }
        SCHED();
        perm1();
        BAR();
        STAMP(30);
        res_loads();
        wo_rng(4, 8);
        STAMP(31);
        waitv<0>();
        PIN(xres[0]); PIN(xres[1]); PIN(wrv[0]); PIN(wrv[1]);
        place();
        STAMP(7);
    }
    prep2();
    if (w == 0) { SLEEP(4); poll_flags(rsWS, vFl, sFl, (v2u){tag ^ 0x5bd1e995u, tag}); }
    BAR();
    FRESH();
    SCHED();
    STAMP(8);
    bf16x8 bb[2][4];
    {
        auto aload = [&](int st, int m2) {
            const v4u r = LD16(m2 ? va1 : va0, so);
            if (m2 == 0) so = ((so | 8192) + 2048) & 0xDC00;
            return r;
        };
        constexpr int PD = 2;
        v4u af[PD][2];
#pragma unroll
        for (int i = 0; i < PD; i++) {
#pragma unroll
            for (int m2 = 1; m2 >= 0; m2--) af[i][m2] = aload(i, m2);
            SCHED();
        }
#pragma unroll
        for (int nt = 0; nt < 4; nt++) bb[0][nt] = cat8(tr_read(wb[nt]), tr_read(wb[nt] + 2048));
#pragma unroll
        for (int nt = 0; nt < 4; nt++) acc2[1][nt] = zero4;
        SCHED();
#pragma unroll
        for (int st = 0; st < 16; st++) {
            if (st + 1 < 16) {
#pragma unroll
                for (int nt = 0; nt < 4; nt++)
                    bb[(st + 1) % (2)][nt] = cat8(tr_read(wb[nt] + (st + 1) * 4096), tr_read(wb[nt] + (st + 1) * 4096 + 2048));
            }
#pragma unroll
            for (int mm = 0; mm < 2; mm++) {
                const int m2 = mm ^ 1;
                const bf16x8 a = __builtin_bit_cast(bf16x8, af[st % PD][m2]);
#pragma unroll
                for (int nt = 0; nt < 4; nt++) acc2[m2][nt] = MFMA(bb[st % (2)][nt], a, acc2[m2][nt]);
            }
            if (st + PD < 16) {
#pragma unroll
                for (int m2 = 1; m2 >= 0; m2--) af[st % PD][m2] = aload(PD + st, m2);
            }
#pragma unroll
            for (int i = 0; i < 4; i++) { SGB(0x8, 1); SGB(0x100, 2); }
            SCHED();
        }
    }
    STAMP(9);
#pragma unroll
    for (int nt = 0; nt < 4; nt++) stsf4(aXw + nt * 1024, acc2[1][nt]);
    SCHED();
    if (half) dloads(DN0, 8);
    SCHED();
    WAITL0();
    if (half) __builtin_amdgcn_s_setprio(1);
    BAR();
    SCHED();
    v4u hpk[2];
    {
        f32x4 pa[4];
#pragma unroll
        for (int nt = 0; nt < 4; nt++) pa[nt] = ldsf4(aXr + nt * 1024);
        SCHED();
#pragma unroll
        for (int i = 0; i < 8; i++) {
            const f32x4 a = acc2[0][i >> 1] + pa[i >> 1];
            hpk[i >> 2][i & 3] = pk2(a[2 * (i & 1)], a[2 * (i & 1) + 1]);
        }
        const bf16x8 h0 = __builtin_bit_cast(bf16x8, hpk[0]), h1 = __builtin_bit_cast(bf16x8, hpk[1]);
        f32x4 gq = MFMA(h0, h0, zero4), sd = MFMA(sop[0], h0, zero4);
        gq = MFMA(h1, h1, gq); sd = MFMA(sop[1], h1, sd);
        float q0 = (l & 1) ? gq[1] : gq[0], q1 = (l & 1) ? gq[3] : gq[2];
        PIN(q0); PIN(q1);
        const v4u pv = {f2s(sd[0]), f2s((l & 2) ? q1 : q0), f2s(sd[1]), swc};
        ST16(pv, vG5, sG5);
        asm volatile("s_nop 1" :: "v"(pv));
    }
    __builtin_amdgcn_s_setprio(0);
    SCHED();
    swap16(hpk[0]); swap16(hpk[1]);
    PIN(hpk[0]); PIN(hpk[1]);
    STAMP(10);
    unsigned wp2[4][8];
    auto perm2 = [&]() {
#pragma unroll
        for (int i = 16; i < 24; i++) PIN(wreg[i]);
#pragma unroll
        for (int ip = 0; ip < 4; ip++)
#pragma unroll
            for (int c = 0; c < 4; c++) {
                const unsigned a = wreg[16 + 2 * ip][c], b = wreg[16 + 2 * ip + 1][c];
                wp2[ip][2 * c] = __builtin_amdgcn_perm(b, a, 0x05040100);
                wp2[ip][2 * c + 1] = __builtin_amdgcn_perm(b, a, 0x07060302);
            }
#pragma unroll
        for (int ip = 0; ip < 4; ip++)
#pragma unroll
            for (int c = 0; c < 8; c++) PIN(wp2[ip][c]);
    };
    v4u outv[2];
    const rsrc_t rsO = __builtin_amdgcn_make_buffer_rsrc((void*)OUT, 0, 2048 * 1024 * 2, 0x00020000);
    unsigned vOut = (R2 & ~8u) * 2048 + (t16 >> 3) * 64 + gx * 16;
    PIN(vOut);
    unsigned aG = ldsb + L_WO + 512, vX = kr, v4 = l * 4, sX = WS_C + kq * 512 + lw * 128 + nr;
    unsigned aR1 = ldsb + L_RED + (lw * 4 + g) * 512 + t16 * 32, aT1 = ldsb + L_RED + (lw & 1) * 256 + l * 4;
    unsigned aR2 = ldsb + L_RED + 8192 + (lw * 4 + g) * 256 + (t16 & 7) * 32 + (t16 >> 3) * 16, aT2 = ldsb + L_RED + 8192 + l * 4, aB = kr * 8;
    const unsigned cx = (lw & 1) ? cg1u : cg1;
    unsigned s8w = WS_G8 + (cx >> 2) * 16384 + (lw >> 1) * 8192 + (kq * 2 + (lw & 1)) * 1024 + (cx & 3) * 256;
    unsigned s8r = WS_G8 + kc * 16384 + lw * 256 + nr, s8s = s8r + 4096;
    unsigned s9w = WS_G9 + (cg >> 2) * 16384 + (cg & 3) * 2048 + kc * 128, v9 = (l & 31) * 4 + (nh ^ (((l >> 3) ^ bid) & 4) * 2048);
    float a1[2][8];
    if (half) {
#pragma unroll
        for (int c = 0; c < 16; c++) { a1[c >> 3][c & 7] = 0.f; PIN(a1[c >> 3][c & 7]); }
        PIN(vX); PIN(v4); PIN(aR1); PIN(aT1); PIN(aR2); PIN(aT2); PIN(aB); PIN(v9);
    }
    PIN(aG);
    asm volatile("" : "+s"(sX), "+s"(s8w), "+s"(s8r), "+s"(s8s), "+s"(s9w));
    unsigned vP = t16 == rt2 ? OOB : t16 * 128, pk;
    {
        v4u pv[4];
        unsigned vK = tid < 4 ? ((ct + 4 * tid) & 15) * 2048 : OOB, vS = l * 16;
        PIN(vK); PIN(vS);
        unsigned sK = WS_GK + rt2 * 128, rk = 2047u - R2, aKy = ldsb + L_WO + 576, aKw = aKy + w * 4;
        unsigned vC = gx * 16, sC = WS_C + (~rt2 & 12) * 4096 + (~rt2 & 3) * 2048 + ct * 128;
        PIN(vC);
        asm volatile("" : "+s"(sK), "+s"(sC));
        PIN(rk); PIN(aKy); PIN(aKw); PIN(vP);
        auto st_ld = [&](int k) { return LD16(vS + k * 1024, sG5r); };
        auto st_sum = [&]() { const f32x4 ps = (f4(pv[0]) + f4(pv[1])) + (f4(pv[2]) + f4(pv[3])); return xrow_scatter<false>(ps[0], ps[1], ps[2], ps[3]); };
        SCHED();
        SLEEP(13);
        __builtin_amdgcn_s_setprio(1);
        float ts;
        sweepn<4, 4>(st_ld, pv, [&]() { return ts = st_sum(); });
        STAMP(21);
        float T[4];
        xrow_gather(ts, T);
        const float mean = T[0] * (1.0f / 1024.0f), var = fmaxf(T[1] * (1.0f / 1024.0f) - mean * mean, 0.f);
        const unsigned sg = pk2(mean, __builtin_amdgcn_sqrtf(rbf(rbf(var) + 1e-5f)));
        const float mb2 = bflo(sg), isd2 = __builtin_amdgcn_rcpf(bfhi(sg));
        const float rsc = (T[2] - mb2 * T[3]) * isd2;
        const unsigned rb = pk2(rsc, rsc);
        const unsigned key = (((rb ^ (((int)rb >> 31) | 0x80000000u)) >> 16) << 11) | rk;
        unsigned kb = umx(key, dppu<0x128>(key));
        kb = umx(kb, dppu<0x124>(kb)); kb = umx(kb, dppu<0x122>(kb)); kb = umx(kb, dppu<0x121>(kb));
        stsu(aKw, kb);
        WAITL0();
        BAR();
        SCHED();
        pk = umx(wmax(*(const LAS v4u*)aKy), wmax(*(const LAS v4u*)(aKy + 16)));
        if (w == 0) ST4(pk, vK, sK);
        SCHED();
        const bool cand = key == pk;
        __builtin_amdgcn_s_setprio(0);
        v4u e[2];
#pragma unroll
        for (int i = 0; i < 8; i++) {
            const float h0 = bflo(hpk[i >> 2][i & 3]), h1 = bfhi(hpk[i >> 2][i & 3]);
            const unsigned d = pk2(h0 - mb2, h1 - mb2);
            e[i >> 2][i & 3] = nsent(pk2(bflo(d) * isd2, bfhi(d) * isd2));
        }
        if (cand) {
#pragma unroll
            for (int i = 0; i < 4; i++) ST16(e[i & 1], vC + (i & 1) * 64, sC + (i >> 1) * 8192);
#pragma unroll
            for (int i = 0; i < 4; i++)
                ST16(((v4u){nsent(hpk[i & 1][0]), nsent(hpk[i & 1][1]), nsent(hpk[i & 1][2]), nsent(hpk[i & 1][3])}), vC + (i & 1) * 64, sC + (WS_H - WS_C) + (i >> 1) * 8192);
        }
        SCHED();
        unsigned k1;
        if (w == 1) {
            SLEEP(3);
            asm volatile("buffer_load_dword %0, %1, %2, %3 offen sc1" : "=v"(k1) : "v"(vP), "s"(rsWS), "s"(sRep) : "memory");
        }
        SCHED();
        STAMP(22);
#pragma unroll
        for (int i = 0; i < 8; i++)
            outv[i >> 2][i & 3] = pk2(bflo(hpk[i >> 2][i & 3]) + bflo(e[i >> 2][i & 3]), bfhi(hpk[i >> 2][i & 3]) + bfhi(e[i >> 2][i & 3]));
        {
#pragma unroll
            for (int j = 0; j < 4; j++) {
                const unsigned a = outv[0][j], b = outv[1][j];
                outv[0][j] = __builtin_amdgcn_update_dpp(a, b, 0x128, 0xf, 0xc, false);
                outv[1][j] = __builtin_amdgcn_update_dpp(b, a, 0x128, 0xf, 0x3, false);
            }
        }
        PIN(outv[0]); PIN(outv[1]);
        if (w == 1) {
            poll_words(k1, rsWS, vP, sRep);
            STAMP(23);
            unsigned gk = umx(k1, pk);
            gk = umx(gk, dppu<0x128>(gk)); gk = umx(gk, dppu<0x124>(gk)); gk = umx(gk, dppu<0x122>(gk)); gk = umx(gk, dppu<0x121>(gk));
            stsu(aG, gk);
        }
    }
    if (half) __builtin_amdgcn_s_setprio(1);
    STAMP(24);
    WL0();
    SCHED();
    BAR();
    SCHED();
    STAMP(11);
    const unsigned gv = ldsu(aG);
    unsigned vo[2];
    auto store_out = [&](unsigned srow) {
#pragma unroll
        for (int i = 0; i < 2; i++) __builtin_amdgcn_raw_buffer_store_b128(outv[i], rsO, vo[i], ct * 128 + i * 16384, AUX_SC1);
    };
    if (RARE(!ffn)) {
        const unsigned gkey = rfl(gv), srow = ~gkey & 2047u;
        vo[0] = (R2 & ~8u) != srow ? vOut : OOB; vo[1] = (R2 | 8u) != srow ? vOut : OOB;
        store_out(srow);
        if (w != 4) { RDUMP(); return; }
        const unsigned c9 = (bid & 3) * 4 + (idx & 3), col = c9 * 64 + (bid & 4) * 8 + (l & 7) * 4 + g;
        unsigned hl[2];
        sweepz<2, 2>([&](int k) { return LD4((col >> 1) * 4, (k ? WS_C : WS_H) + (((gkey & 0x780) + (gkey & 0x600)) << 4) + nr); }, hl);
        const unsigned ord = gkey >> 11;
        const unsigned rb = (ord & 0x8000u) ? (ord & 0x7fffu) : (~ord & 0xffffu);
        float sig = rbf(1.0f / (1.0f + __expf(-bf2f(rb))));
        float hc = (g & 1) ? bfhi(hl[0]) : bflo(hl[0]), lc = (g & 1) ? bfhi(hl[1]) : bflo(hl[1]);
        unsigned vY = l * 16, vZ = l < 48 ? l * 16 + 1024 : OOB, vO = (l & 8) ? OOB : col * 2, sY = WS_G9 + (c9 >> 2) * 16384 + (c9 & 3) * 2048 + nh, sO = srow * 2048;
        PIN(sig); PIN(hc); PIN(lc); PIN(vY); PIN(vZ); PIN(vO);
        asm volatile("" : "+s"(sY), "+s"(sO));
        v4u yp[2];
        SCHED();
        STAMP(16);
        SLEEP(24);
        sweepz<2>([&](int k) { return LD16(k ? vZ : vY, sY); }, yp);
        const f32x4 ys = f4(yp[0]) + f4(yp[1]);
        float yt = xrow_scatter<false>(ys[0], ys[1], ys[2], ys[3]);
        yt += dppf<0x128>(yt);
        const float y = rbf(rbf(yt) * sig);
        __builtin_amdgcn_raw_buffer_store_b16((u16)pk2(hc + rbf(lc + y), 0.f), rsO, vO, sO, 0);
        STAMP(17);
        RDUMP();
        return;
    }
    if (w < 4) {
        const unsigned srow = ~rfl(gv) & 2047u;
        vo[0] = (R2 & ~8u) != srow ? vOut : OOB; vo[1] = (R2 | 8u) != srow ? vOut : OOB;
        PIN(vo[0]); PIN(vo[1]);
        BAR(); BAR(); BAR();
        store_out(srow);
        if (bid == 0 && tid == 0) __hip_atomic_store((gu32*)(ws + WS_EPOCH), tag, RLX_AGENT);
        STAMP(12);
        RDUMP();
        return;
    }

    auto tree16 = [&](unsigned a, unsigned stride) {
        f32x2 v[8];
#pragma unroll
        for (int i = 0; i < 8; i++) v[i] = (f32x2){ldsf(a + 2 * i * stride), ldsf(a + (2 * i + 1) * stride)};
        SCHED();
        const f32x2 t = ((v[0] + v[1]) + (v[2] + v[3])) + ((v[4] + v[5]) + (v[6] + v[7]));
        return t[0] + t[1];
    };
    vX = (((gv & 0x780) | vX) + (gv & 0x600)) << 4;
    v4u xq[1] = {LD16(vX, sX)};
    SCHED();
    const unsigned srow = ~rfl(gv) & 2047u;
    vo[0] = (R2 & ~8u) != srow ? vOut : OOB; vo[1] = (R2 | 8u) != srow ? vOut : OOB;
    PIN(vo[0]); PIN(vo[1]);
    SCHED();
    if (RARE(!__all(wmax(xq[0]) != SENT))) sweepz<1>([&](int) { return LD16(vX, sX); }, xq);
    STAMP(13);
#pragma unroll
    for (int ip = 0; ip < 4; ip++)
#pragma unroll
        for (int c = 0; c < 16; c++) dot2c(a1[c & 1][c >> 1], wp1[c & 1][ip][c >> 1], xq[0][ip]);
#pragma unroll
    for (int c = 0; c < 8; c++) fold8(a1[0][c], a1[1][c]);
    stsf4(aR1, (f32x4){a1[0][0], a1[0][1], a1[0][2], a1[0][3]});
    stsf4(aR1 + 16, (f32x4){a1[0][4], a1[0][5], a1[0][6], a1[0][7]});
    WL0();
    BAR();
    SCHED();
    ST4(f2s(tree16(aT1, 512)), v4, s8w);
    SCHED();
    STAMP(14);
    perm2();
    float a2[8];
#pragma unroll
    for (int c = 0; c < 8; c++) { a2[c] = 0.f; PIN(a2[c]); }
    SCHED();
    SLEEP(7);
    {
        unsigned pv[8];
        auto a_ld = [&](int k) { return LD4(v4 + (k & 3) * 1024, k < 4 ? s8r : s8s); };
        auto a_sum = [&](int m) { return (__uint_as_float(pv[m]) + __uint_as_float(pv[2 + m])) + (__uint_as_float(pv[4 + m]) + __uint_as_float(pv[6 + m])); };
        float gs, us;
        sweepn<8, 0>(a_ld, pv, [&]() { gs = a_sum(0); us = a_sum(1); return gs + us; });
        STAMP(15);
        const unsigned gu = pk2(gs, us);
        const float gg = bflo(gu);
        const float act = rbf(gg * __builtin_amdgcn_rcpf(1.0f + __expf(-gg))) * bfhi(gu);
        const unsigned apk = pk2(act, dppf<0xB1>(act));
        unsigned ap[4];
#pragma unroll
        for (int ip = 0; ip < 4; ip++) ap[ip] = __builtin_amdgcn_ds_bpermute(aB + ip * 64, apk);
        SCHED();
#pragma unroll
        for (int ip = 0; ip < 4; ip++)
#pragma unroll
            for (int c = 0; c < 8; c++) dot2c(a2[(c & 1) * 4 + (c >> 1)], wp2[ip][(c & 1) * 4 + (c >> 1)], ap[ip]);
#pragma unroll
        for (int c = 0; c < 4; c++) fold8(a2[c], a2[4 + c]);
        stsf4(aR2, (f32x4){a2[0], a2[1], a2[2], a2[3]});
    }
    WL0();
    BAR();
    SCHED();
    if (lw == 0) ST4(f2s(tree16(aT2, 256)), v9, s9w);
    SCHED();
    BAR();
    SCHED();
    STAMP(16);
    store_out(srow);
    RDUMP();
}
__device__ __attribute__((used, noinline)) void fk_tail() { asm volatile(".Lfk_end:"); }

extern "C" void kernel_launch(void* const* d_in, const int* in_sizes, int n_in, void* d_out, int out_size, void* d_ws,
                              size_t ws_size, hipStream_t stream) {
    (void)in_sizes; (void)n_in; (void)out_size; (void)ws_size;
    (void)hipFuncSetAttribute((const void*)fused_kernel, hipFuncAttributeMaxDynamicSharedMemorySize, LDS_BYTES);
    fused_kernel<<<256, 512, LDS_BYTES, stream>>>((const u16*)d_in[0], (const u16*)d_in[1], (const u16*)d_in[2], (const u16*)d_in[3],
                                                  (const u16*)d_in[5], (const u16*)d_in[6], (const u16*)d_in[7], (const u16*)d_in[8],
                                                  (u16*)d_out, (char*)d_ws + (-(size_t)d_ws & 0x1FFFFF));
}
```

```cpp
#include <hip/hip_runtime.h>
#include <stdint.h>
#include <utility>

typedef unsigned short u16;
typedef unsigned long long u64;
typedef short bf16x8 __attribute__((ext_vector_type(8)));
typedef short s16x4 __attribute__((ext_vector_type(4)));
typedef float f32x4 __attribute__((ext_vector_type(4)));
typedef float f32x2 __attribute__((ext_vector_type(2)));
typedef __bf16 bf16x2 __attribute__((ext_vector_type(2)));
typedef unsigned v4u __attribute__((ext_vector_type(4)));
typedef unsigned v2u __attribute__((ext_vector_type(2)));
#define LAS __attribute__((address_space(3)))
#define GAS __attribute__((address_space(1)))
#define CAS __attribute__((address_space(4)))
typedef GAS u64 gu64;
typedef GAS unsigned gu32;
typedef __amdgpu_buffer_rsrc_t rsrc_t;
#define DI __device__ __forceinline__
#define RLX_AGENT __ATOMIC_RELAXED, __HIP_MEMORY_SCOPE_AGENT
#define BAR() __builtin_amdgcn_s_barrier()
#define SCHED() __builtin_amdgcn_sched_barrier(0)
#define SGB(m, n) __builtin_amdgcn_sched_group_barrier((m), (n), 0)
#define WAITL0() asm volatile("s_waitcnt lgkmcnt(0)" ::: "memory")
#define WL0() __builtin_amdgcn_s_waitcnt(0xc07f)
#define MFMA(a, b, c) __builtin_amdgcn_mfma_f32_16x16x32_bf16((a), (b), (c), 0, 0, 0)
#define AUX_SC1 16
template <int N> DI void waitv() { asm volatile("s_waitcnt vmcnt(%0)" ::"i"(N) : "memory"); }

#define WS_EPOCH 0
#define WS_G0 0x1000
#define WS_G1 0x8000
#define WS_F4 0x60000
#define WS_GC 0x70000
#define WS_PR 0x74000
#define WS_G3 0x80000
#define WS_GZ 0xC0000
#define SENT 0xFFFFFFFFu
#define WS_TL 0x100000
#define WS_G5 0x100000
#define WS_G8 0x180000
#define WS_GK 0x1B8000
#define WS_G9 0x1C0000
#define WS_C 0x1E0000
#define WS_H 0x1F0000
#define WS_P2 0x200000
#define P2_STRIDE 0x2180
#define WS_P4 0x600000
#define WS_USED 0xA00000
#define WS_STAMP 0x1000000

#define STAGE_B 40960
#define LDS_BYTES 163840
#define L_WO 32768
#define L_MB 161792
#define L_WPHI (L_MB - 8192)
#define L_SD (L_MB + 512)
#define L_CS (L_MB + 1024)
#define L_V L_WO
#define L_KH (L_WO + 65536)
#define L_XB 0
#define L_RED 4096

#define SPIN_CAP 400000u
#define DN0 3
#define W_AUX 2
#define LOG2E 1.4426950408889634f

DI unsigned rfl(unsigned x) { return __builtin_amdgcn_readfirstlane(x); }
DI float bf2f(unsigned u) { return __uint_as_float(u << 16); }
DI float bflo(unsigned u) { return __uint_as_float(u << 16); }
DI float bfhi(unsigned u) { return __uint_as_float(u & 0xffff0000u); }
DI unsigned pk2(float lo, float hi) { f32x2 v = {lo, hi}; return __builtin_bit_cast(unsigned, __builtin_convertvector(v, bf16x2)); }
DI unsigned nsent(unsigned x) { return __builtin_elementwise_min(x, SENT - 1); }
DI unsigned umx(unsigned a, unsigned b) { return __builtin_elementwise_max(a, b); }
DI float rbf(float f) { return bflo(pk2(f, f) & 0xffffu); }
DI bf16x8 mk8(unsigned a, unsigned b, unsigned c, unsigned d) { v4u v = {a, b, c, d}; return __builtin_bit_cast(bf16x8, v); }
DI void dma16(unsigned ldsaddr, unsigned voff, rsrc_t rs, unsigned soff) {
    asm volatile("s_mov_b32 m0, %0\n\ts_nop 0\n\tbuffer_load_dwordx4 %1, %2, %3 offen lds" ::"s"(ldsaddr), "v"(voff), "s"(rs), "s"(soff) : "memory");
}
DI void dma16x4(unsigned ldsaddr, const unsigned* v, rsrc_t rs, unsigned soff) {
    asm volatile("s_mov_b32 m0, %0\n\ts_nop 0\n\tbuffer_load_dwordx4 %1, %5, %6 offen lds\n\tbuffer_load_dwordx4 %2, %5, %6 offen offset:1024 lds\n\t"
                 "buffer_load_dwordx4 %3, %5, %6 offen offset:2048 lds\n\tbuffer_load_dwordx4 %4, %5, %6 offen offset:3072 lds"
                 ::"s"(ldsaddr), "v"(v[0]), "v"(v[1]), "v"(v[2]), "v"(v[3]), "s"(rs), "s"(soff) : "memory");
}
DI void touch(unsigned voff, rsrc_t rs, unsigned soff) { asm volatile("buffer_load_dword v255, %0, %1, %2 offen" ::"v"(voff), "s"(rs), "s"(soff) : "memory", "v255"); }
DI v4u ld16_asm(unsigned voff, rsrc_t rs, unsigned soff) {
    v4u r; asm volatile("buffer_load_dwordx4 %0, %1, %2, %3 offen" : "=v"(r) : "v"(voff), "s"(rs), "s"(soff) : "memory"); return r;
}
#define PIN(x) asm volatile("" : "+v"(x))
DI s16x4 tr_read(unsigned a) { return __builtin_amdgcn_ds_read_tr16_b64_v4i16((LAS s16x4*)(a)); }
DI bf16x8 cat8(s16x4 a, s16x4 b) { return __builtin_shufflevector(a, b, 0, 1, 2, 3, 4, 5, 6, 7); }
DI bf16x8 lds128(unsigned a) { return *(const LAS bf16x8*)(a); }
DI f32x4 ldsf4(unsigned a) { return *(const LAS f32x4*)(a); }
DI float ldsf(unsigned a) { return *(const LAS float*)(a); }
DI unsigned ldsu(unsigned a) { return *(const LAS unsigned*)(a); }
DI void stsf4(unsigned a, f32x4 v) { *(LAS f32x4*)(a) = v; }
DI void stsf(unsigned a, float v) { *(LAS float*)(a) = v; }
DI void stsu(unsigned a, unsigned v) { *(LAS unsigned*)(a) = v; }
template <int C> DI float dppf(float x) { return __uint_as_float(__builtin_amdgcn_update_dpp(0, __float_as_uint(x), C, 0xf, 0xf, true)); }
template <int C> DI unsigned dppu(unsigned x) { return __builtin_amdgcn_update_dpp(0, x, C, 0xf, 0xf, true); }
DI float max2f(float a, float b) { float r; asm("v_max_f32_e32 %0, %1, %2" : "=v"(r) : "v"(a), "v"(b)); return r; }
DI float max3f(float a, float b, float c) { float r; asm("v_max3_f32 %0, %1, %2, %3" : "=v"(r) : "v"(a), "v"(b), "v"(c)); return r; }
DI float row_sum(float x) { x += dppf<0x128>(x); x += dppf<0x124>(x); x += dppf<0x122>(x); x += dppf<0x121>(x); return x; }
DI float xrow_sum(float x) {
    auto a = __builtin_amdgcn_permlane16_swap(__float_as_uint(x), __float_as_uint(x), false, false);
    x = __uint_as_float(a[0]) + __uint_as_float(a[1]);
    auto b = __builtin_amdgcn_permlane32_swap(__float_as_uint(x), __float_as_uint(x), false, false);
    return __uint_as_float(b[0]) + __uint_as_float(b[1]);
}
DI float wave_sum(float x) { return xrow_sum(row_sum(x)); }
DI unsigned wave_maxu(unsigned x) {
    x = umx(x, dppu<0x128>(x)); x = umx(x, dppu<0x124>(x)); x = umx(x, dppu<0x122>(x)); x = umx(x, dppu<0x121>(x));
    auto a = __builtin_amdgcn_permlane16_swap(x, x, false, false);
    x = umx(a[0], a[1]);
    auto b = __builtin_amdgcn_permlane32_swap(x, x, false, false);
    return umx(b[0], b[1]);
}
DI float nmax3f(float a, float b, float c) { float r; asm("v_max3_f32 %0, -%1, -%2, -%3" : "=v"(r) : "v"(a), "v"(b), "v"(c)); return r; }
DI float nmax2f(float a, float b) { float r; asm("v_max_f32_e64 %0, -%1, -%2" : "=v"(r) : "v"(a), "v"(b)); return r; }
DI float mx2(float a, float b) { return __builtin_amdgcn_fmed3f(a, b, __builtin_inff()); }
template <bool MAX> DI float xrow_scatter(float a, float b, float c, float d) {
    const auto s = __builtin_amdgcn_permlane16_swap(__float_as_uint(a), __float_as_uint(b), false, false);
    const auto t = __builtin_amdgcn_permlane16_swap(__float_as_uint(c), __float_as_uint(d), false, false);
    const float u = MAX ? mx2(__uint_as_float(s[0]), __uint_as_float(s[1])) : __uint_as_float(s[0]) + __uint_as_float(s[1]);
    const float v = MAX ? mx2(__uint_as_float(t[0]), __uint_as_float(t[1])) : __uint_as_float(t[0]) + __uint_as_float(t[1]);
    const auto r = __builtin_amdgcn_permlane32_swap(__float_as_uint(u), __float_as_uint(v), false, false);
    return MAX ? mx2(__uint_as_float(r[0]), __uint_as_float(r[1])) : __uint_as_float(r[0]) + __uint_as_float(r[1]);
}
DI void xrow_gather(float w, float (&o)[4]) {
    const auto r = __builtin_amdgcn_permlane32_swap(__float_as_uint(w), __float_as_uint(w), false, false);
    const auto s = __builtin_amdgcn_permlane16_swap(r[0], r[0], false, false);
    const auto t = __builtin_amdgcn_permlane16_swap(r[1], r[1], false, false);
    o[0] = __uint_as_float(s[0]); o[1] = __uint_as_float(s[1]); o[2] = __uint_as_float(t[0]); o[3] = __uint_as_float(t[1]);
}
struct NoHook { DI void operator()(int, float&) const {} DI void operator()(int, float&, float&) const {} };
template <bool MUL, class H = NoHook> DI void softmax2(const f32x4 (&p)[2][2], float rs0, float rs1, bool hi, bf16x8 (&out)[4], H hook = H()) {
    float m[4];
#pragma unroll
    for (int i = 0; i < 2; i++) {
        const f32x4 &p0 = p[i][0], &p1 = p[i][1];
        m[2 * i] = max3f(max3f(p0[0], p0[1], p0[2]), max3f(p0[3], p1[0], p1[1]), max2f(p1[2], p1[3]));
        m[2 * i + 1] = max3f(nmax3f(p0[0], p0[1], p0[2]), nmax3f(p0[3], p1[0], p1[1]), nmax2f(p1[2], p1[3]));
    }
    asm volatile("s_nop 1" : "+v"(m[0]), "+v"(m[1]), "+v"(m[2]), "+v"(m[3]));
    hook(1, m[3]);
    const float rsr = hi ? rs1 : rs0;
    float a[4];
    xrow_gather(xrow_scatter<true>(m[0], m[1], m[2], m[3]) * (rsr * -LOG2E), a);
    f32x2 e[4][4];
    float s[4];
#pragma unroll
    for (int k = 0; k < 4; k++) {
        if (k == 2) hook(2, a[2], s[1]);
        const float sc = ((k >> 1) ? rs1 : rs0) * ((k & 1) ? -LOG2E : LOG2E);
#pragma unroll
        for (int j = 0; j < 4; j++) {
            const f32x4& q = p[k >> 1][j >> 1];
            const f32x2 t = __builtin_elementwise_fma((f32x2){q[2 * (j & 1)], q[2 * (j & 1) + 1]}, (f32x2){sc, sc}, (f32x2){a[k], a[k]});
            e[k][j] = (f32x2){__builtin_amdgcn_exp2f(t[0]), __builtin_amdgcn_exp2f(t[1])};
        }
        const f32x2 h = (e[k][0] + e[k][1]) + (e[k][2] + e[k][3]);
        s[k] = h[0] + h[1];
        if (k == 3) hook(3, s[k]);
    }
    float iv = __builtin_amdgcn_rcpf(xrow_scatter<false>(s[0], s[1], s[2], s[3]));
    if (MUL) iv *= rsr;
    xrow_gather(iv, a);
#pragma unroll
    for (int k = 0; k < 4; k++) {
        if (k == 2) hook(4, a[2]);
        const f32x2 c = {a[k], a[k]};
        const f32x2 x0 = e[k][0] * c, x1 = e[k][1] * c, x2 = e[k][2] * c, x3 = e[k][3] * c;
        out[k] = mk8(pk2(x0[0], x0[1]), pk2(x1[0], x1[1]), pk2(x2[0], x2[1]), pk2(x3[0], x3[1]));
    }
}
DI void swap16(v4u& x) {
    const auto a = __builtin_amdgcn_permlane16_swap(x[0], x[2], false, false), b = __builtin_amdgcn_permlane16_swap(x[1], x[3], false, false);
    x = (v4u){a[0], b[0], a[1], b[1]};
}
DI float dot2(unsigned a, unsigned b, float c) {
    return __builtin_amdgcn_fdot2_f32_bf16(__builtin_bit_cast(bf16x2, a), __builtin_bit_cast(bf16x2, b), c, false);
}
DI void st_gran(char* ws, unsigned off, unsigned tag, unsigned val) {
    __hip_atomic_store((gu64*)(ws + off), ((u64)tag << 32) | val, RLX_AGENT);
}
DI v2u ldg(rsrc_t rs, unsigned voff, unsigned soff) { return __builtin_amdgcn_raw_buffer_load_b64(rs, voff, soff, AUX_SC1); }
#define RARE(c) __builtin_expect(!!(c), 0)
#define FRESH() asm volatile("" ::: "memory")
template <int N> DI void sweep(rsrc_t rs, unsigned voff, unsigned soff, unsigned S, unsigned tag, unsigned (&v)[N]) {
#pragma nounroll
    for (unsigned spins = 0;;) {
        unsigned bad = 0;
        FRESH();
#pragma unroll
        for (int k = 0; k < N; ++k) { const v2u x = ldg(rs, voff, soff + k * S); v[k] = x[0]; bad |= x[1] ^ tag; }
        if (__all(bad == 0)) break;
        if (++spins > SPIN_CAP) break;
        __builtin_amdgcn_s_sleep(1);
    }
}
DI unsigned wmax(unsigned x) { return x; }
DI unsigned wmax(v2u x) { return umx(x[0], x[1]); }
DI unsigned wmax(v4u x) { return umx(umx(x[0], x[1]), umx(x[2], x[3])); }
template <int N, int SL = 0, class T, class F> DI void sweepz(F ld, T (&v)[N]) {
#pragma nounroll
    for (unsigned spins = 0;;) {
        FRESH();
        unsigned m = 0;
#pragma unroll
        for (int k = 0; k < N; ++k) v[k] = ld(k);
#pragma unroll
        for (int k = 0; k < N; ++k) m = umx(m, wmax(v[k]));
        if (__all(m != SENT)) break;
        if (++spins > SPIN_CAP) break;
        if (SL) __builtin_amdgcn_s_sleep(SL);
    }
}
DI f32x4 f4(v4u x) { return __builtin_bit_cast(f32x4, x); }
DI bool anynan(float x) { return __builtin_amdgcn_ballot_w64(x != x) != 0; }
DI void dot2c(float& c, unsigned a, unsigned b) { asm volatile("v_dot2c_f32_bf16 %0, %1, %2" : "+v"(c) : "v"(a), "v"(b)); }
DI void fold8(float& a, float b) {
    asm volatile("v_add_f32_dpp %0, %0, %0 row_ror:8 row_mask:0xf bank_mask:0x3\n\tv_add_f32_dpp %0, %1, %1 row_ror:8 row_mask:0xf bank_mask:0xc" : "+v"(a) : "v"(b));
}
template <int N, int SL, class T, class F, class S> DI void sweepn(F ld, T (&v)[N], S sum) {
    FRESH();
#pragma unroll
    for (int k = 0; k < N; ++k) v[k] = ld(k);
    if (RARE(anynan(sum()))) { sweepz<N, SL>(ld, v); sum(); }
}
DI unsigned f2s(float x) { return nsent(__float_as_uint(x)); }
#define OOB 0x4000000u
#define LD16(v, s) __builtin_amdgcn_raw_buffer_load_b128(rsWS, (v), (s), AUX_SC1)
#define LD4(v, s) __builtin_amdgcn_raw_buffer_load_b32(rsWS, (v), (s), AUX_SC1)
#define ST16(x, v, s) __builtin_amdgcn_raw_buffer_store_b128((x), rsWS, (v), (s), AUX_SC1)
#define ST4(x, v, s) __builtin_amdgcn_raw_buffer_store_b32((x), rsWS, (v), (s), AUX_SC1)
#define SLEEP(n) __builtin_amdgcn_s_sleep(n)
DI void poll_flags(rsrc_t rs, unsigned v, unsigned off, v2u want) {
    v2u x; unsigned n = SPIN_CAP;
    asm volatile("1:\n\tbuffer_load_dwordx2 %0, %2, %3, %4 offen sc1\n\ts_sub_u32 %1, %1, 1\n\ts_cbranch_scc1 2f\n\ts_waitcnt vmcnt(0)\n\t"
                 "v_cmp_ne_u64 vcc, %0, %5\n\ts_cbranch_vccnz 1b\n2:\n\ts_waitcnt vmcnt(0)" : "=&v"(x), "+s"(n) : "v"(v), "s"(rs), "s"(off), "v"(want) : "vcc", "scc", "memory");
}
DI void poll_words(unsigned& x, rsrc_t rs, unsigned v, unsigned off) {
    unsigned n = SPIN_CAP;
    asm volatile("s_branch 3f\n1:\n\tbuffer_load_dword %0, %2, %3, %4 offen sc1\n\ts_sub_u32 %1, %1, 1\n\ts_cbranch_scc1 2f\n3:\n\ts_waitcnt vmcnt(0)\n\t"
                 "v_cmp_eq_u32 vcc, -1, %0\n\ts_cbranch_vccnz 1b\n2:\n\ts_waitcnt vmcnt(0)" : "+v"(x), "+s"(n) : "v"(v), "s"(rs), "s"(off) : "vcc", "scc", "memory");
}
#define STAMP(i)
#define JIT(i) STAMP(i)
#define RINIT()
#define RDUMP()
#define KATTR
template <class F, int... Is> DI void static_for_impl(F&& f, std::integer_sequence<int, Is...>) { (f(std::integral_constant<int, Is>{}), ...); }
template <int N, class F> DI void static_for(F&& f) { static_for_impl(f, std::make_integer_sequence<int, N>{}); }

DI s16x4 bq(unsigned b0, unsigned b1, int j, int i) {
    const int ni = (j + 2) % 6;
    return tr_read(((ni & 1) ? b1 : b0) + (ni >> 1) * 8192 + (i >> 1) * 4096 + (i & 1) * 512);
}
template <class F> DI void group(f32x4 (&acc)[4][6], const bf16x8 (&A)[2][4], const bf16x8 (&B)[2], int j, F gap) {
    const int ni = (j + 2) % 6;
#pragma unroll
    for (int i = 0; i < 8; i++) {
        const int mi = i >> 1, ks = i & 1;
        if (ni < 4) asm volatile("v_mfma_f32_16x16x32_bf16 %0, %1, %2, %0" : "+v"(acc[mi][ni]) : "v"(B[ks]), "v"(A[ks][mi]));
        else asm volatile("v_mfma_f32_16x16x32_bf16 %0, %1, %2, %0" : "+v"(acc[mi][ni]) : "v"(A[ks][mi]), "v"(B[ks]));
        gap(i);
        SCHED();
    }
}
DI void img_write2(const unsigned (&ia)[4], const bf16x8 (&f)[4]) {
#pragma unroll
    for (int k = 0; k < 4; k++) {
        const v4u u = __builtin_bit_cast(v4u, f[k]);
        *(LAS v2u*)(ia[2 * (k & 1)] + (k >> 1) * 2048) = (v2u){u[0], u[1]};
        *(LAS v2u*)(ia[2 * (k & 1) + 1] + (k >> 1) * 2048) = (v2u){u[2], u[3]};
    }
}
DI float lds_bf(unsigned a) { return bf2f(*(const LAS u16*)(a)); }
#define MFMA_SETTLE2(p) asm volatile("s_nop 7\n\ts_nop 1" : "+v"(p[0][0]), "+v"(p[0][1]), "+v"(p[1][0]), "+v"(p[1][1]))

__global__ KATTR __launch_bounds__(512, 2) void fused_kernel(const u16* __restrict__ X, const u16* __restrict__ WQKV, const u16* __restrict__ WPHI,
                                                       const u16* __restrict__ WO, const u16* __restrict__ WR, const u16* __restrict__ WG,
                                                       const u16* __restrict__ WU, const u16* __restrict__ WD, u16* __restrict__ OUT,
                                                       char* __restrict__ ws) {
    extern __shared__ __attribute__((aligned(16))) char lds[];
    u64 pc0; unsigned clen;
    const int tid = threadIdx.x, l = tid & 63;
    asm volatile("s_getpc_b64 %0\n.Lfk_beg:\n\ts_lshr_b32 %1, %3, 3\n\tv_and_b32 v255, 0x1e0, %2\n\tv_add_lshl_u32 v255, v255, %1, 7\n\ts_mov_b32 %1, .Lfk_end-.Lfk_beg\n\t"
                 "v_min_u32 v255, %1, v255\n\tglobal_load_dword v255, v255, %0" : "=&s"(pc0), "=&s"(clen) : "v"(tid), "s"(blockIdx.x) : "v255", "scc");
    SCHED();
    const unsigned w = rfl(tid >> 6);
    const unsigned bid = blockIdx.x, xcd = (bid & 3) * 2 + ((bid >> 2) & 1), idx = bid >> 3;
    const unsigned rt = (xcd & 1) * 8 + (idx & 7), hp = (xcd >> 1) * 4 + (idx >> 3);
    const unsigned rt2 = (bid & 6) * 2 + (idx & 3), ct = (bid & 1) * 8 + (idx >> 2);
    const unsigned hq = (hp + 4) & 15;
    const unsigned rot = (rt >> 2) * 4;
    const unsigned rot2 = (2 * rt2 + 16 * ct) & 31;
    const unsigned ldsb = (unsigned)(size_t)lds;
    const int g = l >> 4, q4 = (l & 15) >> 2, p4 = l & 3, t16 = l & 15;
    const unsigned kr = l >> 3, gx = (g & 1) * 2 + (g >> 1);
    asm volatile("" ::"s"(X), "s"(WQKV), "s"(WPHI), "s"(WO), "s"(WR), "s"(WG), "s"(WU), "s"(WD), "s"(OUT), "s"(ws));
    unsigned tag;
#define READ_TAG() do { SCHED(); tag = *(const volatile CAS unsigned*)(ws + WS_EPOCH) + 1u; asm volatile("" : "+s"(tag)); } while (0)
    const rsrc_t rsWS = __builtin_amdgcn_make_buffer_rsrc((void*)ws, 0, WS_USED, 0x00020000);
    const rsrc_t rsX = __builtin_amdgcn_make_buffer_rsrc((void*)X, 0, 2048 * 1024 * 2, 0x00020000);
    const rsrc_t rsW = __builtin_amdgcn_make_buffer_rsrc((void*)WQKV, 0, 1024 * 3072 * 2, 0x00020000);
    const unsigned vA = (kr * 1024 + (((l & 7) ^ kr) * 8)) * 2;
    const unsigned vB0 = (kr * 3072 + (((l & 7) ^ (((kr >> 1) & 1) << 1)) * 8)) * 2;
    const unsigned vB1 = (kr * 3072 + (((l & 7) ^ ((((kr >> 1) & 1) | 2) << 1)) * 8)) * 2;
    const unsigned srow1 = rt * 128 + hp * 8 + (w & 3) * 2;
    v4u xr[4];
    if (w < 4) {
#pragma unroll
        for (int i = 0; i < 4; i++) xr[i] = ld16_asm(l * 16, rsX, srow1 * 2048 + i * 1024);
    }
    {
        const unsigned vBw = (w & 1) ? vB1 : vB0;
#pragma unroll
        for (int i = 0; i < 2; i++) dma16(ldsb + (w * 2 + i) * 1024, vA, rsX, (rt * 128 + w * 16 + i * 8) * 2048 + rot * 128);
#pragma unroll
        for (int i = 0; i < 3; i++) dma16(ldsb + 16384 + i * 8192 + w * 1024, vBw, rsW, (rot * 64 + w * 8) * 6144 + (i ? hp : hq) * 128 + i * 2048);
    }
    const f32x4 zero4 = {0.f, 0.f, 0.f, 0.f};
    const bf16x8 ones = mk8(0x3F803F80u, 0x3F803F80u, 0x3F803F80u, 0x3F803F80u);
    const unsigned lw = w & 3;
    const bool ffn = (ct & 7) != 3;
    const unsigned fm = idx < 12 ? idx : idx - 4;
    const unsigned fj = fm < 19 ? fm / 3 * 4 + (fm % 3 ? fm % 3 + 1 : 0) : fm == 19 ? 27 - (bid & 1) : fm < 27 ? 4 * fm - 79 : 26 + (bid & 1);
    const unsigned fb = !ffn ? 255u : (bid & 7) * 28 + fj;
    const unsigned cg = (fb + 5) & 15, kc = fb >> 4;
    const unsigned kq = fb / 56, cg1 = fb % 56, cg1u = (cg1 + 3) % 56;
    v4u wreg[24];
    RINIT();
    STAMP(0);

    const rsrc_t rsWO = __builtin_amdgcn_make_buffer_rsrc((void*)WO, 0, 1024 * 1024 * 2, 0x00020000);
    const unsigned vWO = kr * 2048 + (((l & 7) ^ (((kr >> 1) & 3) << 1)) * 16);
    unsigned vwo[4];
    auto wo_so = [&](unsigned b) { return (((2 * b & 30) + (b >> 4) + rot2) & 31) * 65536 + ct * 128; };
    const unsigned fw = (((t16 >> 1) & 3) << 2) | (((t16 >> 3) & 1) << 1) | (t16 & 1);
    unsigned ad;
    unsigned ia[4];
    bf16x8 sdv[4];
    auto pre_addr = [&]() {
#pragma unroll
        for (int k = 0; k < 4; k++) { ia[k] = ldsb + L_XB + lw * 8192 + (w >> 2) * 4096 + t16 * 128 + (((4 * k + g) ^ fw) * 8); PIN(ia[k]); }
        const unsigned rr = 4 * g + q4, fr = (((rr >> 1) & 3) << 2) | (((rr >> 3) & 1) << 1) | (rr & 1);
        ad = ldsb + L_XB + (w & 1) * 16384 + rr * 128 + ((((w >> 1) * 4 + p4) ^ fr) * 8);
        PIN(ad);
    };
    auto sd_reads = [&]() {
        unsigned sa = ldsb + L_SD + g * 8;
        PIN(sa);
#pragma unroll
        for (int s = 0; s < 4; s++) {
            const v2u a = *(const LAS v2u*)(sa + s * 64), b = *(const LAS v2u*)(sa + s * 64 + 32);
            sdv[s] = mk8(a[0], a[1], b[0], b[1]);
        }
    };
    bf16x8 v0[4], v1[4];
    auto v_reads = [&]() {
        unsigned vd = ldsb + L_V + (w & 1) * 8192 + l * 16;
        PIN(vd);
#pragma unroll
        for (int s = 0; s < 4; s++) { v0[s] = lds128(vd + s * 2048); v1[s] = lds128(vd + s * 2048 + 1024); }
    };
    auto kv_tile = [&](auto hook6) {
        const unsigned h2 = w & 1, ft = w >> 1;
        WAITL0();
        BAR();
        SCHED();
        bf16x8 pf[4];
#pragma unroll
        for (int s = 0; s < 4; s++) pf[s] = cat8(tr_read(ad + s * 4096), tr_read(ad + s * 4096 + 2048));
        SCHED();
        hook6();
        SCHED();
        f32x4 k0 = zero4, k1 = zero4, kz = zero4;
#pragma unroll
        for (int s = 0; s < 4; s++) { k0 = MFMA(pf[s], v0[s], k0); k1 = MFMA(pf[s], v1[s], k1); }
#pragma unroll
        for (int s = 0; s < 4; s++) kz = MFMA(pf[s], sdv[s], kz);
        const unsigned pbase = WS_P2 + (hp * 16 + rt) * P2_STRIDE;
        const v4u o0 = {nsent(pk2(k0[0], k0[1])), nsent(pk2(k0[2], k0[3])), nsent(pk2(k1[0], k1[1])), nsent(pk2(k1[2], k1[3]))};
        __builtin_amdgcn_raw_buffer_store_b128(o0, rsWS, l * 16, pbase + h2 * 4096 + ft * 1024, AUX_SC1);
        asm volatile("s_nop 1" :: "v"(o0));
        __builtin_amdgcn_raw_buffer_store_b64((v2u){nsent(pk2(kz[0], kz[1])), nsent(pk2(kz[2], kz[3]))}, rsWS, t16 == 0 ? g * 8 : OOB,
                                              pbase + 8192 + h2 * 128 + ft * 32, AUX_SC1);
        SCHED();
        BAR();
    };

    v4u pp[2];
    auto red = [&]() {
        const v4u* p = pp;
        float lo = xrow_scatter<false>(bflo(p[0][0]) + bflo(p[1][0]), bflo(p[0][1]) + bflo(p[1][1]), bflo(p[0][2]) + bflo(p[1][2]), bflo(p[0][3]) + bflo(p[1][3]));
        float hi = xrow_scatter<false>(bfhi(p[0][0]) + bfhi(p[1][0]), bfhi(p[0][1]) + bfhi(p[1][1]), bfhi(p[0][2]) + bfhi(p[1][2]), bfhi(p[0][3]) + bfhi(p[1][3]));
        lo += dppf<0x128>(lo); hi += dppf<0x128>(hi);
        return nsent(pk2(lo, hi));
    };
    auto pp_so = [&](int k) { return WS_P2 + (hp * 16 + k * 8) * P2_STRIDE + (rt >> 2) * 2048 + (lw & 1) * 1024 + (rt & 3) * 256 + (lw >> 1) * 128; };
    auto red_issue = [&]() {
        const unsigned vpp = kr * P2_STRIDE + (l & 7) * 16;
#pragma unroll
        for (int k = 0; k < 2; k++) asm volatile("buffer_load_dwordx4 %0, %1, %2, %3 offen sc1" : "=v"(pp[k]) : "v"(vpp), "s"(rsWS), "s"(pp_so(k)) : "memory");
        SCHED();
    };
    auto kv_reduce = [&]() {
        waitv<5>();
        PIN(pp[0]); PIN(pp[1]);
        if (!__all(umx(wmax(pp[0]), wmax(pp[1])) != SENT)) sweepz<2, 1>([&](int k) { return LD16(kr * P2_STRIDE + (l & 7) * 16, pp_so(k)); }, pp);
        STAMP(25);
        ST4(red(), (l & 8) ? OOB : (l & 7) * 16 + g * 4, WS_G3 + (hp * 4 + (rt >> 2)) * 2048 + (lw & 1) * 1024 + (rt & 3) * 256 + (lw >> 1) * 128);
    };

    const unsigned half = w >> 2;
    const unsigned R2 = rt2 * 128 + lw * 32 + half * 16 + t16;

    auto dloads = [&](int i0, int i1) {
        const rsrc_t rsD = __builtin_amdgcn_make_buffer_rsrc((void*)WD, 0, 1024 * 3584 * 2, 0x00020000);
#pragma unroll
        for (int i = i0; i < i1; i++)
            wreg[16 + i] = __builtin_amdgcn_raw_buffer_load_b128(rsD, kr * 4096 + (l & 7) * 16, (kc * 256 + lw * 64 + (i >> 1) * 16 + (i & 1)) * 2048 + cg * 128, W_AUX);
        FRESH();
    };
    unsigned wp1[2][4][8];
    v4u xres[2], wrv[2];
    v2u pl;
    unsigned nh;
    unsigned so, va0, va1, wb[4], vFl, sFl, swc, sG5, sG5r, sRep, nr, aXw, aXr, vG5;
    f32x4 acc2[2][4];
    bf16x8 sop[2];
    unsigned pv;
    auto place = [&]() {
        SCHED();
        PIN(pl);
        if (RARE(rfl(pl[1]) != tag)) { unsigned c[1]; sweep<1>(rsWS, 0, WS_PR + 1024, 0, tag, c); pl[0] = c[0]; }
        pv = rfl(pl[0]);
        nh = ((bid ^ (pv >> 4)) & 4) * 2048;
        asm volatile("" : "+s"(pv), "+s"(nh));
    };
    auto prep2 = [&]() {
        SCHED();
        vFl = t16 * 128 + g * 8;
        sFl = WS_F4 + (rt2 & 12) * 4096 + (rt2 & 3) * 2048 + nh;
        va0 = WS_P4 + ((rt2 & 7) * 8 + lw * 2 + half) * 65536 + (nh ^ (~rt2 & 4) * 2048) + l * 16; va1 = va0 ^ 65536;
        so = (half + rot2 + (rot2 & 24)) * 1024;
#pragma unroll
        for (int nt = 0; nt < 4; nt++) { wb[nt] = ldsb + L_WO + half * 65536 + (4 * g + q4) * 128 + ((nt ^ ((2 * g + (q4 >> 1)) & 3)) * 32) + p4 * 8; PIN(wb[nt]); }
        swap16(xres[0]); swap16(xres[1]); swap16(wrv[0]); swap16(wrv[1]);
        asm volatile("s_getreg_b32 %0, hwreg(HW_REG_XCC_ID, 0, 3)" : "=s"(sRep));
        sRep ^= pv & 4;
        nr = (sRep & 4) * 2048;
        sRep = WS_GK + (sRep + (idx & 1) * 8) * 2048;
        sG5r = WS_G5 + (rt2 & 7) * 65536 + lw * 16384 + half * 4096 + nh;
        sG5 = sG5r + ct * 256;
        aXw = ldsb + L_XB + w * 4096 + l * 16; aXr = aXw ^ 16384; vG5 = g == q4 ? t16 * 16 : OOB;
        asm volatile("" : "+s"(sG5), "+s"(sG5r), "+s"(sRep), "+s"(nr), "+s"(sFl), "+s"(nh), "+s"(so));
        PIN(aXw); PIN(aXr); PIN(vG5); PIN(va0); PIN(va1); PIN(vFl);
#pragma unroll
        for (int nt = 0; nt < 4; nt++) {
            const unsigned x0 = xres[nt >> 1][2 * (nt & 1)], x1 = xres[nt >> 1][2 * (nt & 1) + 1];
            acc2[0][nt] = (f32x4){bflo(x0), bfhi(x0), bflo(x1), bfhi(x1)};
            PIN(acc2[0][nt]);
        }
        unsigned m0 = p4 == 0 ? 0x3F803F80u : 0u, m1 = p4 == 1 ? ~0u : 0u;
        PIN(m0); PIN(m1);
#pragma unroll
        for (int i = 0; i < 2; i++) {
            v4u u;
#pragma unroll
            for (int j = 0; j < 4; j++) u[j] = (wrv[i][j] & m1) | m0;
            sop[i] = __builtin_bit_cast(bf16x8, u);
            PIN(sop[i]);
        }
        float sw = 0.f;
#pragma unroll
        for (int i = 0; i < 8; i++) sw = dot2(wrv[i >> 2][i & 3], 0x3F803F80u, sw);
        swc = f2s(xrow_sum(sw));
        PIN(swc);
        SCHED();
    };
    auto res_loads = [&]() {
        pl = ldg(rsWS, 0, WS_PR + 1024);
#pragma unroll
        for (int i = 0; i < 2; i++) {
            xres[i] = *(const v4u*)(X + (size_t)R2 * 1024 + ct * 64 + i * 32 + 8 * gx);
            wrv[i] = *(const v4u*)(WR + ct * 64 + i * 32 + 8 * gx);
        }
        FRESH();
    };

    if (w < 4) {
        const unsigned wm = w & 1, hh = w >> 1;
        f32x4 acc[4][6];
        bf16x8 wf[2], wfq[2];
        float mbt[4], rst[4], csv[2];
        f32x4 mbv[4], csq[2], csk[2];
#pragma unroll
        for (int i = 0; i < 4; i++)
#pragma unroll
            for (int j = 0; j < 6; j++) { acc[i][j] = zero4; PIN(acc[i][j]); }
        {
            const unsigned aoff0 = ldsb + (wm * 64 + t16) * 128 + (((0 + g) ^ (l & 7)) * 16);
            const unsigned aoff1 = ldsb + (wm * 64 + t16) * 128 + (((4 + g) ^ (l & 7)) * 16);
            const unsigned sk = ((q4 >> 1) & 1) | ((g & 1) << 1);
            const unsigned boffA = ldsb + 16384 + (8 * g + q4) * 128 + (((2 * hh) ^ sk) * 32) + p4 * 8;
            const unsigned boffB = ldsb + 16384 + (8 * g + q4) * 128 + (((2 * hh + 1) ^ sk) * 32) + p4 * 8;
            bf16x8 A[2][4], B[3][2];
            s16x4 lo;
            unsigned a0[3], a1[3], bA[3], bB[3];
#pragma unroll
            for (int i = 0; i < 3; i++) {
                a0[i] = aoff0 + i * STAGE_B; a1[i] = aoff1 + i * STAGE_B; bA[i] = boffA + i * STAGE_B; bB[i] = boffB + i * STAGE_B;
                PIN(a0[i]); PIN(a1[i]); PIN(bA[i]); PIN(bB[i]);
            }
            unsigned aT = ldsb + L_MB + (wm * 64 + t16) * 4, aS = ldsb + L_SD + (wm * 64 + t16) * 2, aM = ldsb + L_MB + wm * 256 + g * 16;
            unsigned aC = ldsb + L_CS + hh * 128 + g * 16, aV = ldsb + L_CS + (128 + hh * 32 + t16) * 4;
            unsigned aW = ldsb + L_WPHI + hh * 2048 + (4 * g + q4) * 64 + p4 * 8, aK = ldsb + L_KH + w * 2048 + l * 16;
            PIN(aT); PIN(aS); PIN(aM); PIN(aC); PIN(aV); PIN(aW); PIN(aK);
            pre_addr();
            READ_TAG();
            waitv<5>();
            PIN(xr[0]); PIN(xr[1]); PIN(xr[2]); PIN(xr[3]);
            {
                float s[2], q[2], x0[2];
#pragma unroll
                for (int rr = 0; rr < 2; rr++) {
                    x0[rr] = bflo(rfl(xr[2 * rr][0]));
                    s[rr] = 0.f; q[rr] = 0.f;
#pragma unroll
                    for (int i = 0; i < 4; i++) {
                        const float a = bflo(xr[2 * rr][i]) - x0[rr], b = bfhi(xr[2 * rr][i]) - x0[rr];
                        const float c = bflo(xr[2 * rr + 1][i]) - x0[rr], d = bfhi(xr[2 * rr + 1][i]) - x0[rr];
                        s[rr] += (a + b) + (c + d);
                        q[rr] += (a * a + b * b) + (c * c + d * d);
                    }
                }
                const float sq = row_sum(xrow_scatter<false>(s[0], s[1], q[0], q[1]));
#define RLF(x, n) __uint_as_float(__builtin_amdgcn_readlane(__float_as_uint(x), (n)))
                const float r0 = RLF(sq, 0), r1 = RLF(sq, 16), r2 = RLF(sq, 32), r3 = RLF(sq, 48);
                const float sm = (l ? r1 : r0) * (1.0f / 1024.0f), qm = (l ? r3 : r2) * (1.0f / 1024.0f);
                const float var = fmaxf(qm - sm * sm, 0.f);
                if (l < 2) st_gran(ws, WS_G0 + (srow1 + l) * 8, tag, pk2((l ? x0[1] : x0[0]) + sm, sqrtf(rbf(rbf(var) + 1e-5f))));
            }
            waitv<1>();
            WAITL0();
            BAR();
            SCHED();
            STAMP(1);
            auto rdB = [&](int u, int j, int i) {
                if (i & 1) B[j % 3][i >> 1] = cat8(lo, bq(bA[u], bB[u], j, i)); else lo = bq(bA[u], bB[u], j, i);
            };
            auto rdA = [&](int u, int i) { A[i & 1][i >> 1] = lds128(((i & 1) ? a1[u] : a0[u]) + (i >> 1) * 2048); };
            auto head = [&](int u) {
                group(acc, A, B[0], 0, [&](int i) { rdB(u, 1 + (i >> 2), i & 3); });
                group(acc, A, B[1], 1, [&](int i) { if (i < 4) rdB(u, 3, i); });
                group(acc, A, B[2], 2, [&](int i) { if (i < 4) rdB(u, 4, i); });
                group(acc, A, B[0], 3, [&](int i) { if (i < 4) rdB(u, 5, i); });
                WL0();
            };
#pragma unroll
            for (int i = 0; i < 4; i++) rdB(0, 0, i);
#pragma unroll
            for (int i = 0; i < 8; i++) rdA(0, i);
            SCHED();
#pragma nounroll
            for (unsigned t = 0; t < 5; t++) {
#pragma unroll
                for (int u = 0; u < 3; u++) {
                    head(u);
                    BAR();
                    JIT(32 + t * 3 + u);
                    SCHED();
                    group(acc, A, B[1], 4, [&](int i) { if (i < 4) rdB((u + 1) % 3, 0, i); });
                    group(acc, A, B[2], 5, [&](int i) { rdA((u + 1) % 3, i); });
                }
            }
            unsigned kw[4][4];
            f32x2 kx;
            auto kpiece = [&](int n) {
                if (n > 0) { kw[((n - 1 >> 2) + 2) & 3][(n - 1) & 3] = pk2(kx[0], kx[1]); PIN(kw[((n - 1 >> 2) + 2) & 3][(n - 1) & 3]); }
                if (n < 16) {
                    const int mi = ((n >> 2) + 2) & 3, j = n & 3;
                    const f32x4 &c = acc[mi][2 + (j >> 1)], &s_ = csk[j >> 1];
                    float x0 = __builtin_fmaf(mbt[mi], s_[2 * (j & 1)], c[2 * (j & 1)]); PIN(x0);
                    float x1 = __builtin_fmaf(mbt[mi], s_[2 * (j & 1) + 1], c[2 * (j & 1) + 1]); PIN(x1);
                    kx = (f32x2){x0, x1};
                }
                if (n == 5 || n == 9) *(LAS v4u*)(aK + (n == 9) * 1024) = (v4u){kw[2 + (n == 9)][0], kw[2 + (n == 9)][1], kw[2 + (n == 9)][2], kw[2 + (n == 9)][3]};
            };
            group(acc, A, B[0], 0, [&](int i) { rdB(0, 1 + (i >> 2), i & 3); });
            group(acc, A, B[1], 1, [&](int i) {
                if (i < 4) { rdB(0, 3, i); mbt[(i + 2) & 3] = ldsf(aT + ((i + 2) & 3) * 64); }
                else if (i < 6) csk[i - 4] = ldsf4(aC + 256 + (i - 4) * 64);
                else wf[i - 6] = cat8(tr_read(aW + (i - 6) * 32), tr_read(aW + (i - 6) * 32 + 1024));
            });
            group(acc, A, B[2], 2, [&](int i) { if (i < 4) rdB(0, 4, i); else mbv[i - 4] = ldsf4(aM + (i - 4) * 64); kpiece(i); });
            group(acc, A, B[0], 3, [&](int i) { if (i < 4) rdB(0, 5, i); if (i == 2 || i == 3) csv[i - 2] = ldsf(aV + (i - 2) * 64); kpiece(8 + i); });
            WL0();
            BAR();
            SCHED();
            kpiece(16);
            f32x4 p[2][2];
#pragma unroll
            for (int i = 0; i < 4; i++) p[i >> 1][i & 1] = MFMA(wf[i & 1], mk8(kw[i >> 1][0], kw[i >> 1][1], kw[i >> 1][2], kw[i >> 1][3]), zero4);
            SCHED();
            STAMP(2);
#pragma unroll
            for (int mi = 0; mi < 4; mi++) rst[mi] = lds_bf(aS + mi * 32);
#pragma unroll
            for (int a = 0; a < 2; a++)
#pragma unroll
                for (int dt = 0; dt < 2; dt++) {
                    const f32x4 x0 = acc[2 * a][4 + dt] + mbv[2 * a] * csv[dt], x1 = acc[2 * a + 1][4 + dt] + mbv[2 * a + 1] * csv[dt];
                    *(LAS v4u*)(aK + (L_V - L_KH) + w * 2048 + (a * 2 + dt) * 1024) = (v4u){pk2(x0[0], x0[1]), pk2(x0[2], x0[3]), pk2(x1[0], x1[1]), pk2(x1[2], x1[3])};
                }
            SCHED();
            rst[0] = __builtin_amdgcn_rcpf(rst[0]); rst[1] = __builtin_amdgcn_rcpf(rst[1]);
            bf16x8 f[4];
            struct HookC {
                decltype(v_reads)& vr; decltype(sd_reads)& sr;
                DI void operator()(int k, float& x) const { if (k == 1) { PIN(x); WAITL0(); BAR(); PIN(x); } if (k == 3) { PIN(x); vr(); sr(); PIN(x); } }
                DI void operator()(int, float&, float&) const {}
            };
            softmax2<true>(p, rst[0], rst[1], g >= 2, f, HookC{v_reads, sd_reads});
            img_write2(ia, f);
#pragma unroll
            for (int ft = 0; ft < 2; ft++) wfq[ft] = cat8(tr_read(aW + ft * 32 + 4096), tr_read(aW + ft * 32 + 5120));
            csq[0] = ldsf4(aC); csq[1] = ldsf4(aC + 64);
            STAMP(3);
            kv_tile([]() {});
            STAMP(4);
            SCHED();
            group(acc, A, B[1], 4, [&](int i) { if (i == 2 || i == 3) { rst[i] = __builtin_amdgcn_rcpf(rst[i]); PIN(rst[i]); } });
            group(acc, A, B[2], 5, [&](int) {});
            asm volatile("s_nop 7\n\ts_nop 1");
            SCHED();
        }
        bf16x8 phq[2][4];
        v4u kg[6];
        auto kg_ld = [&](int k) { return k < 4 ? LD16(l * 16, WS_G3 + (hq * 2 + hh) * 4096 + k * 1024) : LD16(g * 16, WS_GZ + (hq * 2 + hh) * 128 + (k - 4) * 64); };
        {
            f32x4 pq[2][2][2];
#pragma unroll
            for (int mi = 0; mi < 4; mi++) {
                const f32x4 x0 = acc[mi][0] + mbt[mi] * csq[0], x1 = acc[mi][1] + mbt[mi] * csq[1];
                const bf16x8 qf = mk8(pk2(x0[0], x0[1]), pk2(x0[2], x0[3]), pk2(x1[0], x1[1]), pk2(x1[2], x1[3]));
                pq[mi >> 1][mi & 1][0] = MFMA(wfq[0], qf, zero4); pq[mi >> 1][mi & 1][1] = MFMA(wfq[1], qf, zero4);
            }
            MFMA_SETTLE2(pq[0]); MFMA_SETTLE2(pq[1]);
            softmax2<false>(pq[0], rst[0], rst[1], g >= 2, phq[0]);
#pragma unroll
            for (int i = 0; i < 4; i++) PIN(phq[0][i]);
            SCHED();
            red_issue();
            res_loads();
            SCHED();
            kv_reduce();
            SCHED();
            BAR();
            SCHED();
            softmax2<false>(pq[1], rst[2], rst[3], g >= 2, phq[1]);
#pragma unroll
            for (int i = 0; i < 4; i++) { PIN(phq[0][i]); PIN(phq[1][i]); }
            SCHED();
        }
        STAMP(5);
        v2u fv = {tag ^ 0x5bd1e995u, tag};
        place();
        PIN(xres[0]); PIN(xres[1]); PIN(wrv[0]); PIN(wrv[1]);
        unsigned sF = WS_F4 + (rt & 12) * 4096 + (rt & 3) * 2048 + hq * 128 + w * 8 + nh, vF = l == 0 ? 0 : OOB;
        {
            unsigned vO = l * 16, sO = WS_P4 + ((rt & 7) * 8 + wm * 4) * 65536 + (hq >> 2) * 16384 + ((hq & 3) * 2 + hh) * 1024 + (nh ^ (~rt & 4) * 2048);
            asm volatile("" : "+s"(sO), "+s"(sF));
            PIN(fv); PIN(vF);
            SLEEP(1);
            sweepz<6, 4>(kg_ld, kg);
            STAMP(6);
            bf16x8 kvA[2][2], zA[2];
            v4u ov[4];
#pragma unroll
            for (int s = 0; s < 2; s++) {
#pragma unroll
                for (int dt = 0; dt < 2; dt++) kvA[s][dt] = mk8(kg[2 * s][2 * dt], kg[2 * s][2 * dt + 1], kg[2 * s + 1][2 * dt], kg[2 * s + 1][2 * dt + 1]);
                zA[s] = __builtin_bit_cast(bf16x8, kg[4 + s]);
            }
#define PH(mi, s) phq[(mi) >> 1][2 * ((mi) & 1) + (s)]
            f32x4 dn[4], n0[4], n1[4];
            float id[4];
#pragma unroll
            for (int mi = 0; mi < 4; mi++) dn[mi] = MFMA(zA[0], PH(mi, 0), zero4);
#pragma unroll
            for (int mi = 0; mi < 4; mi++) dn[mi] = MFMA(zA[1], PH(mi, 1), dn[mi]);
            SCHED();
#pragma unroll
            for (int mi = 0; mi <= 4; mi++) {
                if (mi < 4) {
                    n0[mi] = MFMA(kvA[0][0], PH(mi, 0), zero4); n1[mi] = MFMA(kvA[0][1], PH(mi, 0), zero4);
                    n0[mi] = MFMA(kvA[1][0], PH(mi, 1), n0[mi]); n1[mi] = MFMA(kvA[1][1], PH(mi, 1), n1[mi]);
                    id[mi] = __builtin_amdgcn_rcpf(rbf(rbf(dn[mi][0]) + 1e-6f));
                }
                if (mi > 0) {
                    const f32x4 a = n0[mi - 1] * id[mi - 1], b = n1[mi - 1] * id[mi - 1];
                    ov[mi - 1] = (v4u){pk2(a[0], a[1]), pk2(a[2], a[3]), pk2(b[0], b[1]), pk2(b[2], b[3])};
                    ST16(ov[mi - 1], vO + (mi - 1) * 65536, sO);
                }
                if (mi < 4) { SGB(0x8, 1); SGB(0x2, 6); SGB(0x8, 1); SGB(0x2, 3); SGB(0x8, 1); SGB(0x2, 3); SGB(0x8, 1); SGB(0x2, 4); }
                SCHED();
            }
#pragma unroll
            for (int mi = 0; mi < 4; mi++) PIN(ov[mi]);
        }
        waitv<0>();
        __builtin_amdgcn_raw_buffer_store_b64(fv, rsWS, vF, sF, AUX_SC1);
        STAMP(7);
    } else {
        const unsigned pw = lw & 1;
        const rsrc_t rsG = __builtin_amdgcn_make_buffer_rsrc((void*)WG, 0, 1024 * 3584 * 2, 0x00020000);
        const rsrc_t rsU = __builtin_amdgcn_make_buffer_rsrc((void*)WU, 0, 1024 * 3584 * 2, 0x00020000);
        unsigned vwl = kr * 57344 + (l & 7) * 16;
        unsigned sg = (kq * 256 + lw * 64) * 7168 + cg1 * 128, su = (kq * 256 + lw * 64) * 7168 + cg1u * 128;
        auto wloads = [&](int n0, int n1) {
#pragma unroll
            for (int n = n0; n < n1; n++)
                asm volatile("buffer_load_dwordx4 %0, %1, %2, %3 offen nt" : "=v"(wreg[n]) : "v"(vwl), "s"(n < 8 ? rsG : rsU), "s"((n < 8 ? sg : su) + (n & 7) * 7168));
        };
        auto dl_asm = [&]() {
            const rsrc_t rsD = __builtin_amdgcn_make_buffer_rsrc((void*)WD, 0, 1024 * 3584 * 2, 0x00020000);
#pragma unroll
            for (int i = 0; i < DN0; i++)
                asm volatile("buffer_load_dwordx4 %0, %1, %2, %3 offen nt" : "=v"(wreg[16 + i]) : "v"(kr * 4096 + (l & 7) * 16), "s"(rsD),
                             "s"((kc * 256 + lw * 64 + (i >> 1) * 16 + (i & 1)) * 2048 + cg * 128));
        };
        unsigned aK, aW, aS;
        auto k_pre = [&]() {
            pre_addr();
            aK = ldsb + L_KH + lw * 2048 + l * 16; aW = ldsb + L_WPHI + (lw >> 1) * 2048 + (4 * g + q4) * 64 + p4 * 8; aS = ldsb + L_SD + (pw * 64 + 32 + t16) * 2;
            PIN(aK); PIN(aW); PIN(aS);
        };
        auto kfeat = [&](auto hook) {
            bf16x8 wf[2];
            float rst[2];
#pragma unroll
            for (int ft = 0; ft < 2; ft++) {
                wf[ft] = cat8(tr_read(aW + ft * 32), tr_read(aW + ft * 32 + 1024));
                rst[ft] = __builtin_amdgcn_rcpf(lds_bf(aS + ft * 32));
            }
            sd_reads();
            PIN(wf[0]); PIN(wf[1]); PIN(rst[0]); PIN(rst[1]);
            SCHED();
            BAR();
            SCHED();
            const bf16x8 kf0 = lds128(aK), kf1 = lds128(aK + 1024);
            SCHED();
            hook(-1, rst[0]);
            hook(0, rst[0]);
            SCHED();
            f32x4 p[2][2];
            p[0][0] = MFMA(wf[0], kf0, zero4); p[0][1] = MFMA(wf[1], kf0, zero4);
            p[1][0] = MFMA(wf[0], kf1, zero4); p[1][1] = MFMA(wf[1], kf1, zero4);
            MFMA_SETTLE2(p);
            bf16x8 f[4];
            softmax2<true>(p, rst[0], rst[1], g >= 2, f, hook);
            img_write2(ia, f);
            SCHED();
            hook(5, rst[0]);
            SCHED();
            STAMP(3);
            kv_tile([&]() { hook(6, rst[0]); });
            STAMP(4);
        };
        auto wblk = [&](int k) { return k == 0 ? 22 + lw : k == 1 ? (lw < 3 ? 26 + lw : 4) : k < 6 ? 4 * k - 3 + lw : k == 6 ? lw : (lw < 3 ? 29 + lw : 21); };
        auto wo_init = [&]() {
#pragma unroll
            for (int i = 0; i < 4; i++) { vwo[i] = vWO + i * (16384 - 1024); PIN(vwo[i]); }
        };
        unsigned hs[8];
        auto wl_pre = [&]() { asm volatile("" : "+s"(sg), "+s"(su)); PIN(vwl); };
        auto wo_pre = [&]() {
            wo_init();
#pragma unroll
            for (int k = 0; k < 8; k++) { hs[k] = wo_so(wblk(k)); asm volatile("" : "+s"(hs[k])); }
        };
        auto wo_rng = [&](int k0, int k1) {
#pragma unroll
            for (int k = k0; k < k1; k++) dma16x4(ldsb + L_WO + wblk(k) * 4096, vwo, rsWO, hs[k]);
        };
        auto pf = [&](int i) {
            if (i == 0) { wloads(0, 6); SCHED(); if (lw < 2) wo_pre(); SCHED(); }
            if (i < 3 && !ffn) wo_rng(i, i + 1);
            if (i == 1 || i == 2) wloads(6 * i, i == 1 ? 12 : 16);
            if (i == 6) dl_asm();
        };
        struct Hook {
            decltype(pf)& f; decltype(v_reads)& vr;
            DI void operator()(int k, float& x) const { PIN(x); if (k == 1) BAR(); if (k == 3) vr(); f(3 + k); PIN(x); }
            DI void operator()(int k, float& x, float& y) const { PIN(y); f(3 + k); PIN(x); }
        };
        auto tail16 = [&]() {
            kfeat(Hook{pf, v_reads});
            if (ffn) wo_rng(0, 3);
            wo_rng(3, 4);
            STAMP(27);
            if (ffn) waitv<18>(); else waitv<6>();
#pragma unroll
            for (int n = 0; n < 16 + DN0; n++) PIN(wreg[n]);
        };
        auto vT23 = [&]() {
            const unsigned n = (idx * 2 + pw) * 64 + l, r = n / 12, c = n % 12;
            return idx < 24 ? ((r & 127) + (r >> 7) * 256 + 128) * 6144 + (c >> 2) * 2048 + (((xcd >> 1) * 4 + (c >> 2 ? 0 : 4) + (c & 3)) & 15) * 128 : OOB;
        };
        if (lw < 2) {
            const unsigned sA = (rt * 128 + lw * 64) * 2048, sB = lw * 32 * 6144 + hp * 128, sBq = lw * 32 * 6144 + hq * 128;
            unsigned buf = 1;
            unsigned va[4], vb[4];
#pragma unroll
            for (int i = 0; i < 4; i++) { va[i] = vA + i * (16384 - 1024); vb[i] = ((i & 1) ? vB1 : vB0) + i * (49152 - 1024); PIN(va[i]); PIN(vb[i]); }
            auto stage = [&](unsigned s, unsigned bf, int i0, int i1) {
                const unsigned kt = (s + rot) & 15;
                const unsigned lb = ldsb + bf * STAGE_B;
                const unsigned sa = sA + kt * 128, sb = kt * 393216;
                if (i0 <= 0 && 0 < i1) dma16x4(lb + lw * 8192, va, rsX, sa);
                if (i0 <= 1 && 1 < i1) dma16x4(lb + lw * 8192 + 4096, va, rsX, sa + 65536);
                if (i0 <= 2 && 2 < i1) dma16x4(lb + 16384 + 8192 + lw * 4096, vb, rsW, sb + sB + 2048);
                if (i0 <= 3 && 3 < i1) dma16x4(lb + 16384 + 16384 + lw * 4096, vb, rsW, sb + sB + 4096);
                if (i0 <= 4 && 4 < i1) dma16x4(lb + 16384 + lw * 4096, vb, rsW, sb + sBq);
            };
            stage(1, 1, 0, 4);
            SCHED();
            unsigned vt = vT23();
            PIN(vt);
            wl_pre(); k_pre();
            waitv<16>(); BAR();
            stage(1, 1, 4, 5);
            touch(vt, rsW, (xcd & 1) * 512 * 6144);
            waitv<1>(); BAR();
            stage(2, 2, 0, 5); stage(3, 0, 0, 3);
            waitv<12>(); BAR();
            stage(3, 0, 3, 5); stage(4, 1, 0, 4);
            waitv<16>(); BAR();
            stage(4, 1, 4, 5);
            buf = 2;
#pragma nounroll
            for (unsigned s = 5; s < 16; s++) {
                if (s > 5) { waitv<20>(); BAR(); }
                stage(s, buf, 0, 5);
                buf = buf == 2 ? 0 : buf + 1;
            }
            waitv<20>(); BAR();
            pf(0);
            READ_TAG();
            if (ffn) waitv<6>(); else waitv<10>();
            BAR();
            pf(1);
            tail16();
        } else {
            SCHED();
            unsigned v3;
            const unsigned h3 = (xcd >> 1) * 4 + 3, kv3 = !(xcd & 2), wv = idx * 2 + pw;
            auto line3 = [&](unsigned per, unsigned rows, auto rowf) {
                const unsigned n = wv * (kv3 ? per : (per + 2) / 3) + l, r3 = kv3 ? n / 3 : n, c3 = kv3 ? n % 3 : 0;
                return (idx < 24 && l < (kv3 ? per : (per + 2) / 3) && r3 < rows) ? rowf(r3) * 6144 + c3 * 2048 + ((c3 ? h3 : h3 + 4) & 15) * 128 : OOB;
            };
            v3 = line3(32, 512, [](unsigned r) { return r; });
            touch(line3(16, 256, [](unsigned r) { return (r & 127) + (r >> 7) * 256; }), rsW, (xcd & 1) * 512 * 6144);
            {
                const rsrc_t rsP = __builtin_amdgcn_make_buffer_rsrc((void*)WPHI, 0, 32 * 1024 * 2, 0x00020000);
#pragma unroll
                for (int i = 0; i < 4; i++) dma16(ldsb + L_WPHI + pw * 4096 + i * 1024, l * 16, rsP, (pw ? hq : hp) * 4096 + i * 1024);
            }
            v4u sv = {SENT, SENT, SENT, SENT};
            unsigned vc4 = l < 16 ? WS_G3 + (hp * 4 + (rt >> 2)) * 2048 + pw * 1024 + (rt & 3) * 256 + l * 16
                         : l < 24 ? WS_P2 + (hp * 16 + rt) * P2_STRIDE + 8192 + pw * 128 + (l - 16) * 16
                         : (l < 32 && pw == 0 && (rt & 7) == 0) ? WS_GZ + (hp * 2 + (rt >> 3)) * 128 + (l - 24) * 16
                         : l >= 56 ? WS_F4 + (rt & 12) * 4096 + (rt & 3) * 2048 + hq * 128 + pw * 8192 + (l & 7) * 16 : OOB;
            auto clears = [&](int i0, int i1) {
#pragma unroll
                for (int i = i0; i < i1; i++) {
                    if (i < 4) ST16(sv, l * 16, WS_P2 + (hp * 16 + rt) * P2_STRIDE + pw * 4096 + i * 1024);
                    if (i == 4) ST16(sv, vc4, 0);
                    if (i > 4) ST16(sv, l * 16, WS_TL + (bid * 4 + pw * 2 + i - 5) * 1024);
                }
                FRESH();
            };
            PIN(vc4); PIN(sv);
            unsigned ca = ldsb + 16384 + pw * 8192 + (8 * g + q4) * 128 + (((rt & 3) ^ (((q4 >> 1) & 1) | ((g & 1) << 1))) * 32) + p4 * 8;
            PIN(ca);
            bf16x8 cf[4];
            f32x4 cd[2] = {zero4, zero4};
            auto cs_read = [&](int st) {
#pragma unroll
                for (int i = 0; i < 4; i++) cf[i] = cat8(tr_read(ca + (st % 3) * STAGE_B + i * 4096), tr_read(ca + (st % 3) * STAGE_B + i * 4096 + 512));
                SCHED();
            };
            READ_TAG();
            waitv<5>();
            BAR();
            SCHED();
            cs_read(0);
            FRESH();
            const rsrc_t rsC = __builtin_amdgcn_make_buffer_rsrc((void*)pc0, 0, clen, 0x00020000);
            auto code_pf = [&]() { touch(l < 6 ? l * 128 : OOB, rsC, (idx * 2 + pw) * 768); };
            v4u gl[4];
            float s0 = 0.f, s1 = 0.f;
            unsigned bad;
            v2u gs, gc;
            unsigned vgc = WS_GC + (pw ? hq : hp) * 256 + (l & 31) * 8;
            unsigned lat = 0;
            unsigned vgl = (l < 48 ? l : 0) * 16;
            unsigned aw0 = ldsb + L_CS + pw * 384 + (vgl >> 1), aw1 = ldsb + L_MB + (pw * 64 + l) * 4, aw2 = ldsb + L_SD + (pw * 64 + l) * 2;
            static_for<15>([&](auto tc) {
                constexpr int t = decltype(tc)::value;
                WAITL0();
                JIT(32 + t);
                BAR();
                SCHED();
                if constexpr (t < 4) {
                    __builtin_amdgcn_s_setprio(2);
#pragma unroll
                    for (int i = 0; i < 4; i++) cd[i & 1] = MFMA(ones, cf[(i & 1) * 2 + (i >> 1)], cd[i & 1]);
                    SCHED();
                    __builtin_amdgcn_s_setprio(0);
                    if constexpr (t < 3) cs_read(t + 1);
                    else __builtin_amdgcn_raw_buffer_store_b64((v2u){__float_as_uint(g ? cd[1][0] : cd[0][0]), tag}, rsWS, (g == 1 || g + pw == 0) ? g * 512 + t16 * 8 : OOB,
                                                               WS_G1 + ((hp * 4 + (rt >> 2)) * 192 + pw * 64 + (rt & 3) * 16) * 8, AUX_SC1);
                }
                if constexpr (t == 0) touch(v3, rsW, (~xcd & 1) * 512 * 6144);
                if constexpr (t == 12) code_pf();
                if constexpr (t == 8) {
                    waitv<0>();
                    __builtin_amdgcn_raw_buffer_store_b64((v2u){0u, tag}, rsWS, l == 0 ? 0 : OOB, WS_GC + ((hp * 2 + pw) * 16 + rt) * 8, AUX_SC1);
                }
                if constexpr (t == 8 || t == 9) {
#pragma unroll
                    for (int r = (t - 8) * 2; r < (t - 8) * 2 + 2; r++)
                        asm volatile("buffer_load_dwordx4 %0, %1, %2, %3 offen sc1" : "=v"(gl[r]) : "v"(vgl), "s"(rsWS), "s"(WS_G1 + ((hp * 4 + r) * 192 + pw * 96) * 8) : "memory");
                }
                if constexpr (t == 6) {
                    asm volatile("buffer_load_dwordx2 %0, %1, %2, %3 offen sc1" : "=v"(gs) : "v"(l * 8), "s"(rsWS), "s"(WS_G0 + (rt * 128 + pw * 64) * 8) : "memory");
                }
                if constexpr (t == 11) {
                    waitv<0>();
                    PIN(gs);
                    bad = gs[1] ^ tag;
#pragma unroll
                    for (int r = 0; r < 4; r++) {
                        PIN(gl[r]);
                        bad |= (gl[r][1] ^ tag) | (gl[r][3] ^ tag);
                        s0 += __uint_as_float(gl[r][0]); s1 += __uint_as_float(gl[r][2]);
                    }
                    PIN(s0); PIN(s1); PIN(bad);
                }
                if constexpr (t == 12) {
                    if (RARE(!__all(bad == 0))) {
                        s0 = 0.f; s1 = 0.f;
#pragma nounroll
                        for (unsigned r = 0; r < 4; r++) {
                            unsigned a[2];
                            sweep<2>(rsWS, vgl, WS_G1 + ((hp * 4 + r) * 192 + pw * 96) * 8, 8, tag, a);
                            s0 += __uint_as_float(a[0]); s1 += __uint_as_float(a[1]);
                        }
                        unsigned c[1];
                        sweep<1>(rsWS, l * 8, WS_G0 + (rt * 128 + pw * 64) * 8, 0, tag, c);
                        gs[0] = c[0];
                    }
                    *(LAS f32x2*)(aw0) = (f32x2){-s0, -s1};
                    stsf(aw1, bflo(gs[0]));
                    *(LAS u16*)(aw2) = (u16)(gs[0] >> 16);
                    asm volatile("buffer_load_dwordx2 %0, %1, %2, 0 offen sc1" : "=v"(gc) : "v"(vgc), "s"(rsWS) : "memory");
                    SCHED();
                }
                if constexpr (t == 8) { if (RARE(bid + pw == 0)) ST16(sv, l < 16 ? (l & 8) * 1024 + (l & 7) * 16 : OOB, WS_PR); }
                if constexpr (t == 10 || t == 11) {
                    if (RARE(bid + pw == 0)) {
                        u64 c0, c1; unsigned x;
                        asm volatile("s_memtime %0\n\ts_load_dword %2, %3, %4\n\ts_waitcnt lgkmcnt(0)\n\ts_memtime %1\n\ts_waitcnt lgkmcnt(0)"
                                     : "=&s"(c0), "=&s"(c1), "=&s"(x) : "s"(ws), "i"(WS_PR + (t & 1) * 8192) : "memory");
                        if constexpr (t == 10) lat = (unsigned)c1 - (unsigned)c0;
                        else {
                            asm volatile("s_getreg_b32 %0, hwreg(HW_REG_XCC_ID, 0, 3)" : "=s"(x));
                            __builtin_amdgcn_raw_buffer_store_b64((v2u){((unsigned)c1 - (unsigned)c0 < lat ? 0x44 : 0) ^ (x & 4), tag}, rsWS, l == 0 ? 0 : OOB, WS_PR + 1024, AUX_SC1);
                        }
                    }
                }
                if constexpr (t == 5) k_pre();
                if constexpr (t == 6) { wl_pre(); wo_pre(); }
                if constexpr (t == 7) { PIN(vgc); PIN(vgl); PIN(aw0); PIN(aw1); PIN(aw2); }
                clears(t < 7 ? t : 7, t < 7 ? t + 1 : 7);
                if constexpr (t == 13) {
                    waitv<0>();
                    PIN(gc);
                    if (RARE(!__all(gc[1] == tag))) { unsigned c[1]; sweep<1>(rsWS, vgc, 0, 0, tag, c); }
                    SCHED();
                    pf(0);
                }
                if constexpr (t == 14) pf(1);
            });
            tail16();
        }
        STAMP(28);
        auto perm1 = [&]() {
#pragma unroll
            for (int ip = 0; ip < 4; ip++)
#pragma unroll
                for (int c = 0; c < 4; c++) {
#pragma unroll
                    for (int m = 0; m < 2; m++) {
                        const unsigned a = wreg[m * 8 + 2 * ip][c], b = wreg[m * 8 + 2 * ip + 1][c];
                        wp1[m][ip][2 * c] = __builtin_amdgcn_perm(b, a, 0x05040100);
                        wp1[m][ip][2 * c + 1] = __builtin_amdgcn_perm(b, a, 0x07060302);
                    }
                }
#pragma unroll
            for (int ip = 0; ip < 4; ip++)
#pragma unroll
                for (int c = 0; c < 8; c++) { PIN(wp1[0][ip][c]); PIN(wp1[1][ip][c]); }
            SCHED();
            STAMP(29);
        };
        if (RARE(lw == 0 && (rt & 7) == 0)) {
            __builtin_amdgcn_s_setprio(1);
            sweepz<2, 1>([&](int k) { return LD16(kr * P2_STRIDE + (l & 7) * 16, WS_P2 + (hp * 16 + k * 8) * P2_STRIDE + 8192 + (rt >> 3) * 128); }, pp);
            ST4(red(), (l & 8) ? OOB : ((l >> 2) & 1) * 64 + ((l & 1) * 2 + (g >> 1)) * 16 + ((l >> 1) & 1) * 8 + (g & 1) * 4, WS_GZ + (hp * 2 + (rt >> 3)) * 128);
            __builtin_amdgcn_s_setprio(0);
        }
        SCHED();
        perm1();
        BAR();
        STAMP(30);
        res_loads();
        wo_rng(4, 8);
        STAMP(31);
        waitv<0>();
        PIN(xres[0]); PIN(xres[1]); PIN(wrv[0]); PIN(wrv[1]);
        place();
        STAMP(7);
    }
    prep2();
    if (w == 0) { SLEEP(4); poll_flags(rsWS, vFl, sFl, (v2u){tag ^ 0x5bd1e995u, tag}); }
    BAR();
    FRESH();
    SCHED();
    STAMP(8);
    bf16x8 bb[2][4];
    {
        auto aload = [&](int st, int m2) {
            const v4u r = LD16(m2 ? va1 : va0, so);
            if (m2 == 0) so = ((so | 8192) + 2048) & 0xDC00;
            return r;
        };
        constexpr int PD = 2;
        v4u af[PD][2];
#pragma unroll
        for (int i = 0; i < PD; i++) {
#pragma unroll
            for (int m2 = 1; m2 >= 0; m2--) af[i][m2] = aload(i, m2);
            SCHED();
        }
#pragma unroll
        for (int nt = 0; nt < 4; nt++) bb[0][nt] = cat8(tr_read(wb[nt]), tr_read(wb[nt] + 2048));
#pragma unroll
        for (int nt = 0; nt < 4; nt++) acc2[1][nt] = zero4;
        SCHED();
#pragma unroll
        for (int st = 0; st < 16; st++) {
            if (st + 1 < 16) {
#pragma unroll
                for (int nt = 0; nt < 4; nt++)
                    bb[(st + 1) % (2)][nt] = cat8(tr_read(wb[nt] + (st + 1) * 4096), tr_read(wb[nt] + (st + 1) * 4096 + 2048));
            }
#pragma unroll
            for (int mm = 0; mm < 2; mm++) {
                const int m2 = mm ^ 1;
                const bf16x8 a = __builtin_bit_cast(bf16x8, af[st % PD][m2]);
#pragma unroll
                for (int nt = 0; nt < 4; nt++) acc2[m2][nt] = MFMA(bb[st % (2)][nt], a, acc2[m2][nt]);
            }
            if (st + PD < 16) {
#pragma unroll
                for (int m2 = 1; m2 >= 0; m2--) af[st % PD][m2] = aload(PD + st, m2);
            }
#pragma unroll
            for (int i = 0; i < 4; i++) { SGB(0x8, 1); SGB(0x100, 2); }
            SCHED();
        }
    }
    STAMP(9);
#pragma unroll
    for (int nt = 0; nt < 4; nt++) stsf4(aXw + nt * 1024, acc2[1][nt]);
    SCHED();
    if (half) dloads(DN0, 8);
    SCHED();
    WAITL0();
    if (half) __builtin_amdgcn_s_setprio(1);
    BAR();
    SCHED();
    v4u hpk[2];
    {
        f32x4 pa[4];
#pragma unroll
        for (int nt = 0; nt < 4; nt++) pa[nt] = ldsf4(aXr + nt * 1024);
        SCHED();
#pragma unroll
        for (int i = 0; i < 8; i++) {
            const f32x4 a = acc2[0][i >> 1] + pa[i >> 1];
            hpk[i >> 2][i & 3] = pk2(a[2 * (i & 1)], a[2 * (i & 1) + 1]);
        }
        const bf16x8 h0 = __builtin_bit_cast(bf16x8, hpk[0]), h1 = __builtin_bit_cast(bf16x8, hpk[1]);
        f32x4 gq = MFMA(h0, h0, zero4), sd = MFMA(sop[0], h0, zero4);
        gq = MFMA(h1, h1, gq); sd = MFMA(sop[1], h1, sd);
        float q0 = (l & 1) ? gq[1] : gq[0], q1 = (l & 1) ? gq[3] : gq[2];
        PIN(q0); PIN(q1);
        const v4u pv = {f2s(sd[0]), f2s((l & 2) ? q1 : q0), f2s(sd[1]), swc};
        ST16(pv, vG5, sG5);
        asm volatile("s_nop 1" :: "v"(pv));
    }
    __builtin_amdgcn_s_setprio(0);
    SCHED();
    swap16(hpk[0]); swap16(hpk[1]);
    PIN(hpk[0]); PIN(hpk[1]);
    STAMP(10);
    unsigned wp2[4][8];
    auto perm2 = [&]() {
#pragma unroll
        for (int i = 16; i < 24; i++) PIN(wreg[i]);
#pragma unroll
        for (int ip = 0; ip < 4; ip++)
#pragma unroll
            for (int c = 0; c < 4; c++) {
                const unsigned a = wreg[16 + 2 * ip][c], b = wreg[16 + 2 * ip + 1][c];
                wp2[ip][2 * c] = __builtin_amdgcn_perm(b, a, 0x05040100);
                wp2[ip][2 * c + 1] = __builtin_amdgcn_perm(b, a, 0x07060302);
            }
#pragma unroll
        for (int ip = 0; ip < 4; ip++)
#pragma unroll
            for (int c = 0; c < 8; c++) PIN(wp2[ip][c]);
    };
    v4u outv[2];
    const rsrc_t rsO = __builtin_amdgcn_make_buffer_rsrc((void*)OUT, 0, 2048 * 1024 * 2, 0x00020000);
    unsigned vOut = (R2 & ~8u) * 2048 + (t16 >> 3) * 64 + gx * 16;
    PIN(vOut);
    unsigned aG = ldsb + L_WO + 512, vX = kr, v4 = l * 4, sX = WS_C + kq * 512 + lw * 128 + nr;
    unsigned aR1 = ldsb + L_RED + (lw * 4 + g) * 512 + t16 * 32, aT1 = ldsb + L_RED + (lw & 1) * 256 + l * 4;
    unsigned aR2 = ldsb + L_RED + 8192 + (lw * 4 + g) * 256 + (t16 & 7) * 32 + (t16 >> 3) * 16, aT2 = ldsb + L_RED + 8192 + l * 4, aB = kr * 8;
    const unsigned cx = (lw & 1) ? cg1u : cg1;
    unsigned s8w = WS_G8 + (cx >> 2) * 16384 + (lw >> 1) * 8192 + (kq * 2 + (lw & 1)) * 1024 + (cx & 3) * 256;
    unsigned s8r = WS_G8 + kc * 16384 + lw * 256 + nr, s8s = s8r + 4096;
    unsigned s9w = WS_G9 + (cg >> 2) * 16384 + (cg & 3) * 2048 + kc * 128, v9 = (l & 31) * 4 + (nh ^ (((l >> 3) ^ bid) & 4) * 2048);
    float a1[2][8];
    if (half) {
#pragma unroll
        for (int c = 0; c < 16; c++) { a1[c >> 3][c & 7] = 0.f; PIN(a1[c >> 3][c & 7]); }
        PIN(vX); PIN(v4); PIN(aR1); PIN(aT1); PIN(aR2); PIN(aT2); PIN(aB); PIN(v9);
    }
    PIN(aG);
    asm volatile("" : "+s"(sX), "+s"(s8w), "+s"(s8r), "+s"(s8s), "+s"(s9w));
    unsigned vP = t16 == rt2 ? OOB : t16 * 128, pk;
    {
        v4u pv[4];
        unsigned vK = tid < 4 ? ((ct + 4 * tid) & 15) * 2048 : OOB, vS = l * 16;
        PIN(vK); PIN(vS);
        unsigned sK = WS_GK + rt2 * 128, rk = 2047u - R2, aKy = ldsb + L_WO + 576, aKw = aKy + w * 4;
        unsigned vC = gx * 16, sC = WS_C + (~rt2 & 12) * 4096 + (~rt2 & 3) * 2048 + ct * 128;
        PIN(vC);
        asm volatile("" : "+s"(sK), "+s"(sC));
        PIN(rk); PIN(aKy); PIN(aKw); PIN(vP);
        auto st_ld = [&](int k) { return LD16(vS + k * 1024, sG5r); };
        auto st_sum = [&]() { const f32x4 ps = (f4(pv[0]) + f4(pv[1])) + (f4(pv[2]) + f4(pv[3])); return xrow_scatter<false>(ps[0], ps[1], ps[2], ps[3]); };
        SCHED();
        if (half) SLEEP(4); else SLEEP(7);
        __builtin_amdgcn_s_setprio(1);
        float ts;
        sweepn<4, 4>(st_ld, pv, [&]() { return ts = st_sum(); });
        STAMP(21);
        float T[4];
        xrow_gather(ts, T);
        const float mean = T[0] * (1.0f / 1024.0f), var = fmaxf(T[1] * (1.0f / 1024.0f) - mean * mean, 0.f);
        const unsigned sg = pk2(mean, __builtin_amdgcn_sqrtf(rbf(rbf(var) + 1e-5f)));
        const float mb2 = bflo(sg), isd2 = __builtin_amdgcn_rcpf(bfhi(sg));
        const float rsc = (T[2] - mb2 * T[3]) * isd2;
        const unsigned rb = pk2(rsc, rsc);
        const unsigned key = (((rb ^ (((int)rb >> 31) | 0x80000000u)) >> 16) << 11) | rk;
        unsigned kb = umx(key, dppu<0x128>(key));
        kb = umx(kb, dppu<0x124>(kb)); kb = umx(kb, dppu<0x122>(kb)); kb = umx(kb, dppu<0x121>(kb));
        stsu(aKw, kb);
        WAITL0();
        BAR();
        SCHED();
        pk = umx(wmax(*(const LAS v4u*)aKy), wmax(*(const LAS v4u*)(aKy + 16)));
        if (w == 0) ST4(pk, vK, sK);
        SCHED();
        const bool cand = key == pk;
        __builtin_amdgcn_s_setprio(0);
        v4u e[2];
#pragma unroll
        for (int i = 0; i < 8; i++) {
            const float h0 = bflo(hpk[i >> 2][i & 3]), h1 = bfhi(hpk[i >> 2][i & 3]);
            const unsigned d = pk2(h0 - mb2, h1 - mb2);
            e[i >> 2][i & 3] = nsent(pk2(bflo(d) * isd2, bfhi(d) * isd2));
        }
        if (cand) {
#pragma unroll
            for (int i = 0; i < 4; i++) ST16(e[i & 1], vC + (i & 1) * 64, sC + (i >> 1) * 8192);
#pragma unroll
            for (int i = 0; i < 4; i++)
                ST16(((v4u){nsent(hpk[i & 1][0]), nsent(hpk[i & 1][1]), nsent(hpk[i & 1][2]), nsent(hpk[i & 1][3])}), vC + (i & 1) * 64, sC + (WS_H - WS_C) + (i >> 1) * 8192);
        }
        SCHED();
        unsigned k1;
        if (w == 1) {
            SLEEP(3);
            asm volatile("buffer_load_dword %0, %1, %2, %3 offen sc1" : "=v"(k1) : "v"(vP), "s"(rsWS), "s"(sRep) : "memory");
        }
        SCHED();
        STAMP(22);
#pragma unroll
        for (int i = 0; i < 8; i++)
            outv[i >> 2][i & 3] = pk2(bflo(hpk[i >> 2][i & 3]) + bflo(e[i >> 2][i & 3]), bfhi(hpk[i >> 2][i & 3]) + bfhi(e[i >> 2][i & 3]));
        {
#pragma unroll
            for (int j = 0; j < 4; j++) {
                const unsigned a = outv[0][j], b = outv[1][j];
                outv[0][j] = __builtin_amdgcn_update_dpp(a, b, 0x128, 0xf, 0xc, false);
                outv[1][j] = __builtin_amdgcn_update_dpp(b, a, 0x128, 0xf, 0x3, false);
            }
        }
        PIN(outv[0]); PIN(outv[1]);
        if (w == 1) {
            poll_words(k1, rsWS, vP, sRep);
            STAMP(23);
            unsigned gk = umx(k1, pk);
            gk = umx(gk, dppu<0x128>(gk)); gk = umx(gk, dppu<0x124>(gk)); gk = umx(gk, dppu<0x122>(gk)); gk = umx(gk, dppu<0x121>(gk));
            stsu(aG, gk);
        }
    }
    if (half) __builtin_amdgcn_s_setprio(1);
    STAMP(24);
    WL0();
    SCHED();
    BAR();
    SCHED();
    STAMP(11);
    const unsigned gv = ldsu(aG);
    unsigned vo[2];
    auto store_out = [&](unsigned srow) {
#pragma unroll
        for (int i = 0; i < 2; i++) __builtin_amdgcn_raw_buffer_store_b128(outv[i], rsO, vo[i], ct * 128 + i * 16384, AUX_SC1);
    };
    if (RARE(!ffn)) {
        const unsigned gkey = rfl(gv), srow = ~gkey & 2047u;
        vo[0] = (R2 & ~8u) != srow ? vOut : OOB; vo[1] = (R2 | 8u) != srow ? vOut : OOB;
        store_out(srow);
        if (w != 4) { RDUMP(); return; }
        const unsigned c9 = (bid & 3) * 4 + (idx & 3), col = c9 * 64 + (bid & 4) * 8 + (l & 7) * 4 + g;
        unsigned hl[2];
        sweepz<2, 2>([&](int k) { return LD4((col >> 1) * 4, (k ? WS_C : WS_H) + (((gkey & 0x780) + (gkey & 0x600)) << 4) + nr); }, hl);
        const unsigned ord = gkey >> 11;
        const unsigned rb = (ord & 0x8000u) ? (ord & 0x7fffu) : (~ord & 0xffffu);
        float sig = rbf(1.0f / (1.0f + __expf(-bf2f(rb))));
        float hc = (g & 1) ? bfhi(hl[0]) : bflo(hl[0]), lc = (g & 1) ? bfhi(hl[1]) : bflo(hl[1]);
        unsigned vY = l * 16, vZ = l < 48 ? l * 16 + 1024 : OOB, vO = (l & 8) ? OOB : col * 2, sY = WS_G9 + (c9 >> 2) * 16384 + (c9 & 3) * 2048 + nh, sO = srow * 2048;
        PIN(sig); PIN(hc); PIN(lc); PIN(vY); PIN(vZ); PIN(vO);
        asm volatile("" : "+s"(sY), "+s"(sO));
        v4u yp[2];
        SCHED();
        STAMP(16);
        SLEEP(24);
        sweepz<2>([&](int k) { return LD16(k ? vZ : vY, sY); }, yp);
        const f32x4 ys = f4(yp[0]) + f4(yp[1]);
        float yt = xrow_scatter<false>(ys[0], ys[1], ys[2], ys[3]);
        yt += dppf<0x128>(yt);
        const float y = rbf(rbf(yt) * sig);
        __builtin_amdgcn_raw_buffer_store_b16((u16)pk2(hc + rbf(lc + y), 0.f), rsO, vO, sO, 0);
        STAMP(17);
        RDUMP();
        return;
    }
    if (w < 4) {
        const unsigned srow = ~rfl(gv) & 2047u;
        vo[0] = (R2 & ~8u) != srow ? vOut : OOB; vo[1] = (R2 | 8u) != srow ? vOut : OOB;
        PIN(vo[0]); PIN(vo[1]);
        BAR(); BAR(); BAR();
        store_out(srow);
        if (bid == 0 && tid == 0) __hip_atomic_store((gu32*)(ws + WS_EPOCH), tag, RLX_AGENT);
        STAMP(12);
        RDUMP();
        return;
    }

    auto tree16 = [&](unsigned a, unsigned stride) {
        f32x2 v[8];
#pragma unroll
        for (int i = 0; i < 8; i++) v[i] = (f32x2){ldsf(a + 2 * i * stride), ldsf(a + (2 * i + 1) * stride)};
        SCHED();
        const f32x2 t = ((v[0] + v[1]) + (v[2] + v[3])) + ((v[4] + v[5]) + (v[6] + v[7]));
        return t[0] + t[1];
    };
    vX = (((gv & 0x780) | vX) + (gv & 0x600)) << 4;
    v4u xq[1] = {LD16(vX, sX)};
    SCHED();
    const unsigned srow = ~rfl(gv) & 2047u;
    vo[0] = (R2 & ~8u) != srow ? vOut : OOB; vo[1] = (R2 | 8u) != srow ? vOut : OOB;
    PIN(vo[0]); PIN(vo[1]);
    SCHED();
    if (RARE(!__all(wmax(xq[0]) != SENT))) sweepz<1>([&](int) { return LD16(vX, sX); }, xq);
    STAMP(13);
#pragma unroll
    for (int ip = 0; ip < 4; ip++)
#pragma unroll
        for (int c = 0; c < 16; c++) dot2c(a1[c & 1][c >> 1], wp1[c & 1][ip][c >> 1], xq[0][ip]);
#pragma unroll
    for (int c = 0; c < 8; c++) fold8(a1[0][c], a1[1][c]);
    stsf4(aR1, (f32x4){a1[0][0], a1[0][1], a1[0][2], a1[0][3]});
    stsf4(aR1 + 16, (f32x4){a1[0][4], a1[0][5], a1[0][6], a1[0][7]});
    WL0();
    BAR();
    SCHED();
    ST4(f2s(tree16(aT1, 512)), v4, s8w);
    SCHED();
    STAMP(14);
    perm2();
    float a2[8];
#pragma unroll
    for (int c = 0; c < 8; c++) { a2[c] = 0.f; PIN(a2[c]); }
    SCHED();
    SLEEP(7);
    {
        unsigned pv[8];
        auto a_ld = [&](int k) { return LD4(v4 + (k & 3) * 1024, k < 4 ? s8r : s8s); };
        auto a_sum = [&](int m) { return (__uint_as_float(pv[m]) + __uint_as_float(pv[2 + m])) + (__uint_as_float(pv[4 + m]) + __uint_as_float(pv[6 + m])); };
        float gs, us;
        sweepn<8, 0>(a_ld, pv, [&]() { gs = a_sum(0); us = a_sum(1); return gs + us; });
        STAMP(15);
        const unsigned gu = pk2(gs, us);
        const float gg = bflo(gu);
        const float act = rbf(gg * __builtin_amdgcn_rcpf(1.0f + __expf(-gg))) * bfhi(gu);
        const unsigned apk = pk2(act, dppf<0xB1>(act));
        unsigned ap[4];
#pragma unroll
        for (int ip = 0; ip < 4; ip++) ap[ip] = __builtin_amdgcn_ds_bpermute(aB + ip * 64, apk);
        SCHED();
#pragma unroll
        for (int ip = 0; ip < 4; ip++)
#pragma unroll
            for (int c = 0; c < 8; c++) dot2c(a2[(c & 1) * 4 + (c >> 1)], wp2[ip][(c & 1) * 4 + (c >> 1)], ap[ip]);
#pragma unroll
        for (int c = 0; c < 4; c++) fold8(a2[c], a2[4 + c]);
        stsf4(aR2, (f32x4){a2[0], a2[1], a2[2], a2[3]});
    }
    WL0();
    BAR();
    SCHED();
    if (lw == 0) ST4(f2s(tree16(aT2, 256)), v9, s9w);
    SCHED();
    BAR();
    SCHED();
    STAMP(16);
    store_out(srow);
    RDUMP();
}
__device__ __attribute__((used, noinline)) void fk_tail() { asm volatile(".Lfk_end:"); }

extern "C" void kernel_launch(void* const* d_in, const int* in_sizes, int n_in, void* d_out, int out_size, void* d_ws,
                              size_t ws_size, hipStream_t stream) {
    (void)in_sizes; (void)n_in; (void)out_size; (void)ws_size;
    (void)hipFuncSetAttribute((const void*)fused_kernel, hipFuncAttributeMaxDynamicSharedMemorySize, LDS_BYTES);
    fused_kernel<<<256, 512, LDS_BYTES, stream>>>((const u16*)d_in[0], (const u16*)d_in[1], (const u16*)d_in[2], (const u16*)d_in[3],
                                                  (const u16*)d_in[5], (const u16*)d_in[6], (const u16*)d_in[7], (const u16*)d_in[8],
                                                  (u16*)d_out, (char*)d_ws + (-(size_t)d_ws & 0x1FFFFF));
}
```

```cpp
#include <hip/hip_runtime.h>
#include <stdint.h>
#include <utility>

typedef unsigned short u16;
typedef unsigned long long u64;
typedef short bf16x8 __attribute__((ext_vector_type(8)));
typedef short s16x4 __attribute__((ext_vector_type(4)));
typedef float f32x4 __attribute__((ext_vector_type(4)));
typedef float f32x2 __attribute__((ext_vector_type(2)));
typedef __bf16 bf16x2 __attribute__((ext_vector_type(2)));
typedef unsigned v4u __attribute__((ext_vector_type(4)));
typedef unsigned v2u __attribute__((ext_vector_type(2)));
#define LAS __attribute__((address_space(3)))
#define GAS __attribute__((address_space(1)))
#define CAS __attribute__((address_space(4)))
typedef GAS u64 gu64;
typedef GAS unsigned gu32;
typedef __amdgpu_buffer_rsrc_t rsrc_t;
#define DI __device__ __forceinline__
#define RLX_AGENT __ATOMIC_RELAXED, __HIP_MEMORY_SCOPE_AGENT
#define BAR() __builtin_amdgcn_s_barrier()
#define SCHED() __builtin_amdgcn_sched_barrier(0)
#define SGB(m, n) __builtin_amdgcn_sched_group_barrier((m), (n), 0)
#define WAITL0() asm volatile("s_waitcnt lgkmcnt(0)" ::: "memory")
#define WL0() __builtin_amdgcn_s_waitcnt(0xc07f)
#define MFMA(a, b, c) __builtin_amdgcn_mfma_f32_16x16x32_bf16((a), (b), (c), 0, 0, 0)
#define AUX_SC1 16
template <int N> DI void waitv() { asm volatile("s_waitcnt vmcnt(%0)" ::"i"(N) : "memory"); }

#define WS_EPOCH 0
#define WS_G0 0x1000
#define WS_G1 0x8000
#define WS_F4 0x60000
#define WS_GC 0x70000
#define WS_PR 0x74000
#define WS_G3 0x80000
#define WS_GZ 0xC0000
#define SENT 0xFFFFFFFFu
#define WS_TL 0x100000
#define WS_G5 0x100000
#define WS_G8 0x180000
#define WS_GK 0x1B8000
#define WS_G9 0x1C0000
#define WS_C 0x1E0000
#define WS_H 0x1F0000
#define WS_P2 0x200000
#define P2_STRIDE 0x2180
#define WS_P4 0x600000
#define WS_USED 0xA00000
#define WS_STAMP 0x1000000

#define STAGE_B 40960
#define LDS_BYTES 163840
#define L_WO 32768
#define L_MB 161792
#define L_WPHI (L_MB - 8192)
#define L_SD (L_MB + 512)
#define L_CS (L_MB + 1024)
#define L_V L_WO
#define L_KH (L_WO + 65536)
#define L_XB 0
#define L_RED 4096

#define SPIN_CAP 400000u
#define DN0 3
#define W_AUX 2
#define LOG2E 1.4426950408889634f

DI unsigned rfl(unsigned x) { return __builtin_amdgcn_readfirstlane(x); }
DI float bf2f(unsigned u) { return __uint_as_float(u << 16); }
DI float bflo(unsigned u) { return __uint_as_float(u << 16); }
DI float bfhi(unsigned u) { return __uint_as_float(u & 0xffff0000u); }
DI unsigned pk2(float lo, float hi) { f32x2 v = {lo, hi}; return __builtin_bit_cast(unsigned, __builtin_convertvector(v, bf16x2)); }
DI unsigned nsent(unsigned x) { return __builtin_elementwise_min(x, SENT - 1); }
DI unsigned umx(unsigned a, unsigned b) { return __builtin_elementwise_max(a, b); }
DI float rbf(float f) { return bflo(pk2(f, f) & 0xffffu); }
DI bf16x8 mk8(unsigned a, unsigned b, unsigned c, unsigned d) { v4u v = {a, b, c, d}; return __builtin_bit_cast(bf16x8, v); }
DI void dma16(unsigned ldsaddr, unsigned voff, rsrc_t rs, unsigned soff) {
    asm volatile("s_mov_b32 m0, %0\n\ts_nop 0\n\tbuffer_load_dwordx4 %1, %2, %3 offen lds" ::"s"(ldsaddr), "v"(voff), "s"(rs), "s"(soff) : "memory");
}
DI void dma16x4(unsigned ldsaddr, const unsigned* v, rsrc_t rs, unsigned soff) {
    asm volatile("s_mov_b32 m0, %0\n\ts_nop 0\n\tbuffer_load_dwordx4 %1, %5, %6 offen lds\n\tbuffer_load_dwordx4 %2, %5, %6 offen offset:1024 lds\n\t"
                 "buffer_load_dwordx4 %3, %5, %6 offen offset:2048 lds\n\tbuffer_load_dwordx4 %4, %5, %6 offen offset:3072 lds"
                 ::"s"(ldsaddr), "v"(v[0]), "v"(v[1]), "v"(v[2]), "v"(v[3]), "s"(rs), "s"(soff) : "memory");
}
DI void touch(unsigned voff, rsrc_t rs, unsigned soff) { asm volatile("buffer_load_dword v255, %0, %1, %2 offen" ::"v"(voff), "s"(rs), "s"(soff) : "memory", "v255"); }
DI v4u ld16_asm(unsigned voff, rsrc_t rs, unsigned soff) {
    v4u r; asm volatile("buffer_load_dwordx4 %0, %1, %2, %3 offen" : "=v"(r) : "v"(voff), "s"(rs), "s"(soff) : "memory"); return r;
}
#define PIN(x) asm volatile("" : "+v"(x))
DI s16x4 tr_read(unsigned a) { return __builtin_amdgcn_ds_read_tr16_b64_v4i16((LAS s16x4*)(a)); }
DI bf16x8 cat8(s16x4 a, s16x4 b) { return __builtin_shufflevector(a, b, 0, 1, 2, 3, 4, 5, 6, 7); }
DI bf16x8 lds128(unsigned a) { return *(const LAS bf16x8*)(a); }
DI f32x4 ldsf4(unsigned a) { return *(const LAS f32x4*)(a); }
DI float ldsf(unsigned a) { return *(const LAS float*)(a); }
DI unsigned ldsu(unsigned a) { return *(const LAS unsigned*)(a); }
DI void stsf4(unsigned a, f32x4 v) { *(LAS f32x4*)(a) = v; }
DI void stsf(unsigned a, float v) { *(LAS float*)(a) = v; }
DI void stsu(unsigned a, unsigned v) { *(LAS unsigned*)(a) = v; }
template <int C> DI float dppf(float x) { return __uint_as_float(__builtin_amdgcn_update_dpp(0, __float_as_uint(x), C, 0xf, 0xf, true)); }
template <int C> DI unsigned dppu(unsigned x) { return __builtin_amdgcn_update_dpp(0, x, C, 0xf, 0xf, true); }
DI float max2f(float a, float b) { float r; asm("v_max_f32_e32 %0, %1, %2" : "=v"(r) : "v"(a), "v"(b)); return r; }
DI float max3f(float a, float b, float c) { float r; asm("v_max3_f32 %0, %1, %2, %3" : "=v"(r) : "v"(a), "v"(b), "v"(c)); return r; }
DI float row_sum(float x) { x += dppf<0x128>(x); x += dppf<0x124>(x); x += dppf<0x122>(x); x += dppf<0x121>(x); return x; }
DI float xrow_sum(float x) {
    auto a = __builtin_amdgcn_permlane16_swap(__float_as_uint(x), __float_as_uint(x), false, false);
    x = __uint_as_float(a[0]) + __uint_as_float(a[1]);
    auto b = __builtin_amdgcn_permlane32_swap(__float_as_uint(x), __float_as_uint(x), false, false);
    return __uint_as_float(b[0]) + __uint_as_float(b[1]);
}
DI float wave_sum(float x) { return xrow_sum(row_sum(x)); }
DI unsigned wave_maxu(unsigned x) {
    x = umx(x, dppu<0x128>(x)); x = umx(x, dppu<0x124>(x)); x = umx(x, dppu<0x122>(x)); x = umx(x, dppu<0x121>(x));
    auto a = __builtin_amdgcn_permlane16_swap(x, x, false, false);
    x = umx(a[0], a[1]);
    auto b = __builtin_amdgcn_permlane32_swap(x, x, false, false);
    return umx(b[0], b[1]);
}
DI float nmax3f(float a, float b, float c) { float r; asm("v_max3_f32 %0, -%1, -%2, -%3" : "=v"(r) : "v"(a), "v"(b), "v"(c)); return r; }
DI float nmax2f(float a, float b) { float r; asm("v_max_f32_e64 %0, -%1, -%2" : "=v"(r) : "v"(a), "v"(b)); return r; }
DI float mx2(float a, float b) { return __builtin_amdgcn_fmed3f(a, b, __builtin_inff()); }
template <bool MAX> DI float xrow_scatter(float a, float b, float c, float d) {
    const auto s = __builtin_amdgcn_permlane16_swap(__float_as_uint(a), __float_as_uint(b), false, false);
    const auto t = __builtin_amdgcn_permlane16_swap(__float_as_uint(c), __float_as_uint(d), false, false);
    const float u = MAX ? mx2(__uint_as_float(s[0]), __uint_as_float(s[1])) : __uint_as_float(s[0]) + __uint_as_float(s[1]);
    const float v = MAX ? mx2(__uint_as_float(t[0]), __uint_as_float(t[1])) : __uint_as_float(t[0]) + __uint_as_float(t[1]);
    const auto r = __builtin_amdgcn_permlane32_swap(__float_as_uint(u), __float_as_uint(v), false, false);
    return MAX ? mx2(__uint_as_float(r[0]), __uint_as_float(r[1])) : __uint_as_float(r[0]) + __uint_as_float(r[1]);
}
DI void xrow_gather(float w, float (&o)[4]) {
    const auto r = __builtin_amdgcn_permlane32_swap(__float_as_uint(w), __float_as_uint(w), false, false);
    const auto s = __builtin_amdgcn_permlane16_swap(r[0], r[0], false, false);
    const auto t = __builtin_amdgcn_permlane16_swap(r[1], r[1], false, false);
    o[0] = __uint_as_float(s[0]); o[1] = __uint_as_float(s[1]); o[2] = __uint_as_float(t[0]); o[3] = __uint_as_float(t[1]);
}
struct NoHook { DI void operator()(int, float&) const {} DI void operator()(int, float&, float&) const {} };
template <bool MUL, class H = NoHook> DI void softmax2(const f32x4 (&p)[2][2], float rs0, float rs1, bool hi, bf16x8 (&out)[4], H hook = H()) {
    float m[4];
#pragma unroll
    for (int i = 0; i < 2; i++) {
        const f32x4 &p0 = p[i][0], &p1 = p[i][1];
        m[2 * i] = max3f(max3f(p0[0], p0[1], p0[2]), max3f(p0[3], p1[0], p1[1]), max2f(p1[2], p1[3]));
        m[2 * i + 1] = max3f(nmax3f(p0[0], p0[1], p0[2]), nmax3f(p0[3], p1[0], p1[1]), nmax2f(p1[2], p1[3]));
    }
    asm volatile("s_nop 1" : "+v"(m[0]), "+v"(m[1]), "+v"(m[2]), "+v"(m[3]));
    hook(1, m[3]);
    const float rsr = hi ? rs1 : rs0;
    float a[4];
    xrow_gather(xrow_scatter<true>(m[0], m[1], m[2], m[3]) * (rsr * -LOG2E), a);
    f32x2 e[4][4];
    float s[4];
#pragma unroll
    for (int k = 0; k < 4; k++) {
        if (k == 2) hook(2, a[2], s[1]);
        const float sc = ((k >> 1) ? rs1 : rs0) * ((k & 1) ? -LOG2E : LOG2E);
#pragma unroll
        for (int j = 0; j < 4; j++) {
            const f32x4& q = p[k >> 1][j >> 1];
            const f32x2 t = __builtin_elementwise_fma((f32x2){q[2 * (j & 1)], q[2 * (j & 1) + 1]}, (f32x2){sc, sc}, (f32x2){a[k], a[k]});
            e[k][j] = (f32x2){__builtin_amdgcn_exp2f(t[0]), __builtin_amdgcn_exp2f(t[1])};
        }
        const f32x2 h = (e[k][0] + e[k][1]) + (e[k][2] + e[k][3]);
        s[k] = h[0] + h[1];
        if (k == 3) hook(3, s[k]);
    }
    float iv = __builtin_amdgcn_rcpf(xrow_scatter<false>(s[0], s[1], s[2], s[3]));
    if (MUL) iv *= rsr;
    xrow_gather(iv, a);
#pragma unroll
    for (int k = 0; k < 4; k++) {
        if (k == 2) hook(4, a[2]);
        const f32x2 c = {a[k], a[k]};
        const f32x2 x0 = e[k][0] * c, x1 = e[k][1] * c, x2 = e[k][2] * c, x3 = e[k][3] * c;
        out[k] = mk8(pk2(x0[0], x0[1]), pk2(x1[0], x1[1]), pk2(x2[0], x2[1]), pk2(x3[0], x3[1]));
    }
}
DI void swap16(v4u& x) {
    const auto a = __builtin_amdgcn_permlane16_swap(x[0], x[2], false, false), b = __builtin_amdgcn_permlane16_swap(x[1], x[3], false, false);
    x = (v4u){a[0], b[0], a[1], b[1]};
}
DI float dot2(unsigned a, unsigned b, float c) {
    return __builtin_amdgcn_fdot2_f32_bf16(__builtin_bit_cast(bf16x2, a), __builtin_bit_cast(bf16x2, b), c, false);
}
DI void st_gran(char* ws, unsigned off, unsigned tag, unsigned val) {
    __hip_atomic_store((gu64*)(ws + off), ((u64)tag << 32) | val, RLX_AGENT);
}
DI v2u ldg(rsrc_t rs, unsigned voff, unsigned soff) { return __builtin_amdgcn_raw_buffer_load_b64(rs, voff, soff, AUX_SC1); }
#define RARE(c) __builtin_expect(!!(c), 0)
#define FRESH() asm volatile("" ::: "memory")
template <int N> DI void sweep(rsrc_t rs, unsigned voff, unsigned soff, unsigned S, unsigned tag, unsigned (&v)[N]) {
#pragma nounroll
    for (unsigned spins = 0;;) {
        unsigned bad = 0;
        FRESH();
#pragma unroll
        for (int k = 0; k < N; ++k) { const v2u x = ldg(rs, voff, soff + k * S); v[k] = x[0]; bad |= x[1] ^ tag; }
        if (__all(bad == 0)) break;
        if (++spins > SPIN_CAP) break;
        __builtin_amdgcn_s_sleep(1);
    }
}
DI unsigned wmax(unsigned x) { return x; }
DI unsigned wmax(v2u x) { return umx(x[0], x[1]); }
DI unsigned wmax(v4u x) { return umx(umx(x[0], x[1]), umx(x[2], x[3])); }
template <int N, int SL = 0, class T, class F> DI void sweepz(F ld, T (&v)[N]) {
#pragma nounroll
    for (unsigned spins = 0;;) {
        FRESH();
        unsigned m = 0;
#pragma unroll
        for (int k = 0; k < N; ++k) v[k] = ld(k);
#pragma unroll
        for (int k = 0; k < N; ++k) m = umx(m, wmax(v[k]));
        if (__all(m != SENT)) break;
        if (++spins > SPIN_CAP) break;
        if (SL) __builtin_amdgcn_s_sleep(SL);
    }
}
DI f32x4 f4(v4u x) { return __builtin_bit_cast(f32x4, x); }
DI bool anynan(float x) { return __builtin_amdgcn_ballot_w64(x != x) != 0; }
DI void dot2c(float& c, unsigned a, unsigned b) { asm volatile("v_dot2c_f32_bf16 %0, %1, %2" : "+v"(c) : "v"(a), "v"(b)); }
DI void fold8(float& a, float b) {
    asm volatile("v_add_f32_dpp %0, %0, %0 row_ror:8 row_mask:0xf bank_mask:0x3\n\tv_add_f32_dpp %0, %1, %1 row_ror:8 row_mask:0xf bank_mask:0xc" : "+v"(a) : "v"(b));
}
template <int N, int SL, class T, class F, class S> DI void sweepn(F ld, T (&v)[N], S sum) {
    FRESH();
#pragma unroll
    for (int k = 0; k < N; ++k) v[k] = ld(k);
    if (RARE(anynan(sum()))) { sweepz<N, SL>(ld, v); sum(); }
}
DI unsigned f2s(float x) { return nsent(__float_as_uint(x)); }
#define OOB 0x4000000u
#define LD16(v, s) __builtin_amdgcn_raw_buffer_load_b128(rsWS, (v), (s), AUX_SC1)
#define LD4(v, s) __builtin_amdgcn_raw_buffer_load_b32(rsWS, (v), (s), AUX_SC1)
#define ST16(x, v, s) __builtin_amdgcn_raw_buffer_store_b128((x), rsWS, (v), (s), AUX_SC1)
#define ST4(x, v, s) __builtin_amdgcn_raw_buffer_store_b32((x), rsWS, (v), (s), AUX_SC1)
#define SLEEP(n) __builtin_amdgcn_s_sleep(n)
DI void poll_flags(rsrc_t rs, unsigned v, unsigned off, v2u want) {
    v2u x; unsigned n = SPIN_CAP;
    asm volatile("1:\n\tbuffer_load_dwordx2 %0, %2, %3, %4 offen sc1\n\ts_sub_u32 %1, %1, 1\n\ts_cbranch_scc1 2f\n\ts_waitcnt vmcnt(0)\n\t"
                 "v_cmp_ne_u64 vcc, %0, %5\n\ts_cbranch_vccnz 1b\n2:\n\ts_waitcnt vmcnt(0)" : "=&v"(x), "+s"(n) : "v"(v), "s"(rs), "s"(off), "v"(want) : "vcc", "scc", "memory");
}
DI void poll_words(unsigned& x, rsrc_t rs, unsigned v, unsigned off) {
    unsigned n = SPIN_CAP;
    asm volatile("s_branch 3f\n1:\n\tbuffer_load_dword %0, %2, %3, %4 offen sc1\n\ts_sub_u32 %1, %1, 1\n\ts_cbranch_scc1 2f\n3:\n\ts_waitcnt vmcnt(0)\n\t"
                 "v_cmp_eq_u32 vcc, -1, %0\n\ts_cbranch_vccnz 1b\n2:\n\ts_waitcnt vmcnt(0)" : "+v"(x), "+s"(n) : "v"(v), "s"(rs), "s"(off) : "vcc", "scc", "memory");
}
#define STAMP(i)
#define JIT(i) STAMP(i)
#define RINIT()
#define RDUMP()
#define KATTR
template <class F, int... Is> DI void static_for_impl(F&& f, std::integer_sequence<int, Is...>) { (f(std::integral_constant<int, Is>{}), ...); }
template <int N, class F> DI void static_for(F&& f) { static_for_impl(f, std::make_integer_sequence<int, N>{}); }

DI s16x4 bq(unsigned b0, unsigned b1, int j, int i) {
    const int ni = (j + 2) % 6;
    return tr_read(((ni & 1) ? b1 : b0) + (ni >> 1) * 8192 + (i >> 1) * 4096 + (i & 1) * 512);
}
template <class F> DI void group(f32x4 (&acc)[4][6], const bf16x8 (&A)[2][4], const bf16x8 (&B)[2], int j, F gap) {
    const int ni = (j + 2) % 6;
#pragma unroll
    for (int i = 0; i < 8; i++) {
        const int mi = i >> 1, ks = i & 1;
        if (ni < 4) asm volatile("v_mfma_f32_16x16x32_bf16 %0, %1, %2, %0" : "+v"(acc[mi][ni]) : "v"(B[ks]), "v"(A[ks][mi]));
        else asm volatile("v_mfma_f32_16x16x32_bf16 %0, %1, %2, %0" : "+v"(acc[mi][ni]) : "v"(A[ks][mi]), "v"(B[ks]));
        gap(i);
        SCHED();
    }
}
DI void img_write2(const unsigned (&ia)[4], const bf16x8 (&f)[4]) {
#pragma unroll
    for (int k = 0; k < 4; k++) {
        const v4u u = __builtin_bit_cast(v4u, f[k]);
        *(LAS v2u*)(ia[2 * (k & 1)] + (k >> 1) * 2048) = (v2u){u[0], u[1]};
        *(LAS v2u*)(ia[2 * (k & 1) + 1] + (k >> 1) * 2048) = (v2u){u[2], u[3]};
    }
}
DI float lds_bf(unsigned a) { return bf2f(*(const LAS u16*)(a)); }
#define MFMA_SETTLE2(p) asm volatile("s_nop 7\n\ts_nop 1" : "+v"(p[0][0]), "+v"(p[0][1]), "+v"(p[1][0]), "+v"(p[1][1]))

__global__ KATTR __launch_bounds__(512, 2) void fused_kernel(const u16* __restrict__ X, const u16* __restrict__ WQKV, const u16* __restrict__ WPHI,
                                                       const u16* __restrict__ WO, const u16* __restrict__ WR, const u16* __restrict__ WG,
                                                       const u16* __restrict__ WU, const u16* __restrict__ WD, u16* __restrict__ OUT,
                                                       char* __restrict__ ws) {
    extern __shared__ __attribute__((aligned(16))) char lds[];
    u64 pc0; unsigned clen;
    const int tid = threadIdx.x, l = tid & 63;
    asm volatile("s_getpc_b64 %0\n.Lfk_beg:\n\ts_lshr_b32 %1, %3, 3\n\tv_and_b32 v255, 0x1e0, %2\n\tv_add_lshl_u32 v255, v255, %1, 7\n\ts_mov_b32 %1, .Lfk_end-.Lfk_beg\n\t"
                 "v_min_u32 v255, %1, v255\n\tglobal_load_dword v255, v255, %0" : "=&s"(pc0), "=&s"(clen) : "v"(tid), "s"(blockIdx.x) : "v255", "scc");
    SCHED();
    const unsigned w = rfl(tid >> 6);
    const unsigned bid = blockIdx.x, xcd = (bid & 3) * 2 + ((bid >> 2) & 1), idx = bid >> 3;
    const unsigned rt = (xcd & 1) * 8 + (idx & 7), hp = (xcd >> 1) * 4 + (idx >> 3);
    const unsigned rt2 = (bid & 6) * 2 + (idx & 3), ct = (bid & 1) * 8 + (idx >> 2);
    const unsigned hq = (hp + 4) & 15;
    const unsigned rot = (rt >> 2) * 4;
    const unsigned rot2 = (2 * rt2 + 16 * ct) & 31;
    const unsigned ldsb = (unsigned)(size_t)lds;
    const int g = l >> 4, q4 = (l & 15) >> 2, p4 = l & 3, t16 = l & 15;
    const unsigned kr = l >> 3, gx = (g & 1) * 2 + (g >> 1);
    asm volatile("" ::"s"(X), "s"(WQKV), "s"(WPHI), "s"(WO), "s"(WR), "s"(WG), "s"(WU), "s"(WD), "s"(OUT), "s"(ws));
    unsigned tag;
#define READ_TAG() do { SCHED(); tag = *(const volatile CAS unsigned*)(ws + WS_EPOCH) + 1u; asm volatile("" : "+s"(tag)); } while (0)
    const rsrc_t rsWS = __builtin_amdgcn_make_buffer_rsrc((void*)ws, 0, WS_USED, 0x00020000);
    const rsrc_t rsX = __builtin_amdgcn_make_buffer_rsrc((void*)X, 0, 2048 * 1024 * 2, 0x00020000);
    const rsrc_t rsW = __builtin_amdgcn_make_buffer_rsrc((void*)WQKV, 0, 1024 * 3072 * 2, 0x00020000);
    const unsigned vA = (kr * 1024 + (((l & 7) ^ kr) * 8)) * 2;
    const unsigned vB0 = (kr * 3072 + (((l & 7) ^ (((kr >> 1) & 1) << 1)) * 8)) * 2;
    const unsigned vB1 = (kr * 3072 + (((l & 7) ^ ((((kr >> 1) & 1) | 2) << 1)) * 8)) * 2;
    const unsigned srow1 = rt * 128 + hp * 8 + (w & 3) * 2;
    v4u xr[4];
    if (w < 4) {
#pragma unroll
        for (int i = 0; i < 4; i++) xr[i] = ld16_asm(l * 16, rsX, srow1 * 2048 + i * 1024);
    }
    {
        const unsigned vBw = (w & 1) ? vB1 : vB0;
#pragma unroll
        for (int i = 0; i < 2; i++) dma16(ldsb + (w * 2 + i) * 1024, vA, rsX, (rt * 128 + w * 16 + i * 8) * 2048 + rot * 128);
#pragma unroll
        for (int i = 0; i < 3; i++) dma16(ldsb + 16384 + i * 8192 + w * 1024, vBw, rsW, (rot * 64 + w * 8) * 6144 + (i ? hp : hq) * 128 + i * 2048);
    }
    const f32x4 zero4 = {0.f, 0.f, 0.f, 0.f};
    const bf16x8 ones = mk8(0x3F803F80u, 0x3F803F80u, 0x3F803F80u, 0x3F803F80u);
    const unsigned lw = w & 3;
    const bool ffn = (ct & 7) != 3;
    const unsigned fm = idx < 12 ? idx : idx - 4;
    const unsigned fj = fm < 19 ? fm / 3 * 4 + (fm % 3 ? fm % 3 + 1 : 0) : fm == 19 ? 27 - (bid & 1) : fm < 27 ? 4 * fm - 79 : 26 + (bid & 1);
    const unsigned fb = !ffn ? 255u : (bid & 7) * 28 + fj;
    const unsigned cg = (fb + 5) & 15, kc = fb >> 4;
    const unsigned kq = fb / 56, cg1 = fb % 56, cg1u = (cg1 + 3) % 56;
    v4u wreg[24];
    RINIT();
    STAMP(0);

    const rsrc_t rsWO = __builtin_amdgcn_make_buffer_rsrc((void*)WO, 0, 1024 * 1024 * 2, 0x00020000);
    const unsigned vWO = kr * 2048 + (((l & 7) ^ (((kr >> 1) & 3) << 1)) * 16);
    unsigned vwo[4];
    auto wo_so = [&](unsigned b) { return (((2 * b & 30) + (b >> 4) + rot2) & 31) * 65536 + ct * 128; };
    const unsigned fw = (((t16 >> 1) & 3) << 2) | (((t16 >> 3) & 1) << 1) | (t16 & 1);
    unsigned ad;
    unsigned ia[4];
    bf16x8 sdv[4];
    auto pre_addr = [&]() {
#pragma unroll
        for (int k = 0; k < 4; k++) { ia[k] = ldsb + L_XB + lw * 8192 + (w >> 2) * 4096 + t16 * 128 + (((4 * k + g) ^ fw) * 8); PIN(ia[k]); }
        const unsigned rr = 4 * g + q4, fr = (((rr >> 1) & 3) << 2) | (((rr >> 3) & 1) << 1) | (rr & 1);
        ad = ldsb + L_XB + (w & 1) * 16384 + rr * 128 + ((((w >> 1) * 4 + p4) ^ fr) * 8);
        PIN(ad);
    };
    auto sd_reads = [&]() {
        unsigned sa = ldsb + L_SD + g * 8;
        PIN(sa);
#pragma unroll
        for (int s = 0; s < 4; s++) {
            const v2u a = *(const LAS v2u*)(sa + s * 64), b = *(const LAS v2u*)(sa + s * 64 + 32);
            sdv[s] = mk8(a[0], a[1], b[0], b[1]);
        }
    };
    bf16x8 v0[4], v1[4];
    auto v_reads = [&]() {
        unsigned vd = ldsb + L_V + (w & 1) * 8192 + l * 16;
        PIN(vd);
#pragma unroll
        for (int s = 0; s < 4; s++) { v0[s] = lds128(vd + s * 2048); v1[s] = lds128(vd + s * 2048 + 1024); }
    };
    auto kv_tile = [&](auto hook6) {
        const unsigned h2 = w & 1, ft = w >> 1;
        WAITL0();
        BAR();
        SCHED();
        bf16x8 pf[4];
#pragma unroll
        for (int s = 0; s < 4; s++) pf[s] = cat8(tr_read(ad + s * 4096), tr_read(ad + s * 4096 + 2048));
        SCHED();
        hook6();
        SCHED();
        f32x4 k0 = zero4, k1 = zero4, kz = zero4;
#pragma unroll
        for (int s = 0; s < 4; s++) { k0 = MFMA(pf[s], v0[s], k0); k1 = MFMA(pf[s], v1[s], k1); }
#pragma unroll
        for (int s = 0; s < 4; s++) kz = MFMA(pf[s], sdv[s], kz);
        const unsigned pbase = WS_P2 + (hp * 16 + rt) * P2_STRIDE;
        const v4u o0 = {nsent(pk2(k0[0], k0[1])), nsent(pk2(k0[2], k0[3])), nsent(pk2(k1[0], k1[1])), nsent(pk2(k1[2], k1[3]))};
        __builtin_amdgcn_raw_buffer_store_b128(o0, rsWS, l * 16, pbase + h2 * 4096 + ft * 1024, AUX_SC1);
        asm volatile("s_nop 1" :: "v"(o0));
        __builtin_amdgcn_raw_buffer_store_b64((v2u){nsent(pk2(kz[0], kz[1])), nsent(pk2(kz[2], kz[3]))}, rsWS, t16 == 0 ? g * 8 : OOB,
                                              pbase + 8192 + h2 * 128 + ft * 32, AUX_SC1);
        SCHED();
        BAR();
    };

    v4u pp[2];
    auto red = [&]() {
        const v4u* p = pp;
        float lo = xrow_scatter<false>(bflo(p[0][0]) + bflo(p[1][0]), bflo(p[0][1]) + bflo(p[1][1]), bflo(p[0][2]) + bflo(p[1][2]), bflo(p[0][3]) + bflo(p[1][3]));
        float hi = xrow_scatter<false>(bfhi(p[0][0]) + bfhi(p[1][0]), bfhi(p[0][1]) + bfhi(p[1][1]), bfhi(p[0][2]) + bfhi(p[1][2]), bfhi(p[0][3]) + bfhi(p[1][3]));
        lo += dppf<0x128>(lo); hi += dppf<0x128>(hi);
        return nsent(pk2(lo, hi));
    };
    auto pp_so = [&](int k) { return WS_P2 + (hp * 16 + k * 8) * P2_STRIDE + (rt >> 2) * 2048 + (lw & 1) * 1024 + (rt & 3) * 256 + (lw >> 1) * 128; };
    auto red_issue = [&]() {
        const unsigned vpp = kr * P2_STRIDE + (l & 7) * 16;
#pragma unroll
        for (int k = 0; k < 2; k++) asm volatile("buffer_load_dwordx4 %0, %1, %2, %3 offen sc1" : "=v"(pp[k]) : "v"(vpp), "s"(rsWS), "s"(pp_so(k)) : "memory");
        SCHED();
    };
    auto kv_reduce = [&]() {
        waitv<5>();
        PIN(pp[0]); PIN(pp[1]);
        if (!__all(umx(wmax(pp[0]), wmax(pp[1])) != SENT)) sweepz<2, 1>([&](int k) { return LD16(kr * P2_STRIDE + (l & 7) * 16, pp_so(k)); }, pp);
        STAMP(25);
        ST4(red(), (l & 8) ? OOB : (l & 7) * 16 + g * 4, WS_G3 + (hp * 4 + (rt >> 2)) * 2048 + (lw & 1) * 1024 + (rt & 3) * 256 + (lw >> 1) * 128);
    };

    const unsigned half = w >> 2;
    const unsigned R2 = rt2 * 128 + lw * 32 + half * 16 + t16;

    auto dloads = [&](int i0, int i1) {
        const rsrc_t rsD = __builtin_amdgcn_make_buffer_rsrc((void*)WD, 0, 1024 * 3584 * 2, 0x00020000);
#pragma unroll
        for (int i = i0; i < i1; i++)
            wreg[16 + i] = __builtin_amdgcn_raw_buffer_load_b128(rsD, kr * 4096 + (l & 7) * 16, (kc * 256 + lw * 64 + (i >> 1) * 16 + (i & 1)) * 2048 + cg * 128, W_AUX);
        FRESH();
    };
    unsigned wp1[2][4][8];
    v4u xres[2], wrv[2];
    v2u pl;
    unsigned nh;
    unsigned so, va0, va1, wb[4], vFl, sFl, swc, sG5, sG5r, sRep, nr, aXw, aXr, vG5;
    f32x4 acc2[2][4];
    bf16x8 sop[2];
    unsigned pv;
    auto place = [&]() {
        SCHED();
        PIN(pl);
        if (RARE(rfl(pl[1]) != tag)) { unsigned c[1]; sweep<1>(rsWS, 0, WS_PR + 1024, 0, tag, c); pl[0] = c[0]; }
        pv = rfl(pl[0]);
        nh = ((bid ^ (pv >> 4)) & 4) * 2048;
        asm volatile("" : "+s"(pv), "+s"(nh));
    };
    auto prep2 = [&]() {
        SCHED();
        vFl = t16 * 128 + g * 8;
        sFl = WS_F4 + (rt2 & 12) * 4096 + (rt2 & 3) * 2048 + nh;
        va0 = WS_P4 + ((rt2 & 7) * 8 + lw * 2 + half) * 65536 + (nh ^ (~rt2 & 4) * 2048) + l * 16; va1 = va0 ^ 65536;
        so = (half + rot2 + (rot2 & 24)) * 1024;
#pragma unroll
        for (int nt = 0; nt < 4; nt++) { wb[nt] = ldsb + L_WO + half * 65536 + (4 * g + q4) * 128 + ((nt ^ ((2 * g + (q4 >> 1)) & 3)) * 32) + p4 * 8; PIN(wb[nt]); }
        swap16(xres[0]); swap16(xres[1]); swap16(wrv[0]); swap16(wrv[1]);
        asm volatile("s_getreg_b32 %0, hwreg(HW_REG_XCC_ID, 0, 3)" : "=s"(sRep));
        sRep ^= pv & 4;
        nr = (sRep & 4) * 2048;
        sRep = WS_GK + (sRep + (idx & 1) * 8) * 2048;
        sG5r = WS_G5 + (rt2 & 7) * 65536 + lw * 16384 + half * 4096 + nh;
        sG5 = sG5r + ct * 256;
        aXw = ldsb + L_XB + w * 4096 + l * 16; aXr = aXw ^ 16384; vG5 = g == q4 ? t16 * 16 : OOB;
        asm volatile("" : "+s"(sG5), "+s"(sG5r), "+s"(sRep), "+s"(nr), "+s"(sFl), "+s"(nh), "+s"(so));
        PIN(aXw); PIN(aXr); PIN(vG5); PIN(va0); PIN(va1); PIN(vFl);
#pragma unroll
        for (int nt = 0; nt < 4; nt++) {
            const unsigned x0 = xres[nt >> 1][2 * (nt & 1)], x1 = xres[nt >> 1][2 * (nt & 1) + 1];
            acc2[0][nt] = (f32x4){bflo(x0), bfhi(x0), bflo(x1), bfhi(x1)};
            PIN(acc2[0][nt]);
        }
        unsigned m0 = p4 == 0 ? 0x3F803F80u : 0u, m1 = p4 == 1 ? ~0u : 0u;
        PIN(m0); PIN(m1);
#pragma unroll
        for (int i = 0; i < 2; i++) {
            v4u u;
#pragma unroll
            for (int j = 0; j < 4; j++) u[j] = (wrv[i][j] & m1) | m0;
            sop[i] = __builtin_bit_cast(bf16x8, u);
            PIN(sop[i]);
        }
        float sw = 0.f;
#pragma unroll
        for (int i = 0; i < 8; i++) sw = dot2(wrv[i >> 2][i & 3], 0x3F803F80u, sw);
        swc = f2s(xrow_sum(sw));
        PIN(swc);
        SCHED();
    };
    auto res_loads = [&]() {
        pl = ldg(rsWS, 0, WS_PR + 1024);
#pragma unroll
        for (int i = 0; i < 2; i++) {
            xres[i] = *(const v4u*)(X + (size_t)R2 * 1024 + ct * 64 + i * 32 + 8 * gx);
            wrv[i] = *(const v4u*)(WR + ct * 64 + i * 32 + 8 * gx);
        }
        FRESH();
    };

    if (w < 4) {
        const unsigned wm = w & 1, hh = w >> 1;
        f32x4 acc[4][6];
        bf16x8 wf[2], wfq[2];
        float mbt[4], rst[4], csv[2];
        f32x4 mbv[4], csq[2], csk[2];
#pragma unroll
        for (int i = 0; i < 4; i++)
#pragma unroll
            for (int j = 0; j < 6; j++) { acc[i][j] = zero4; PIN(acc[i][j]); }
        {
            const unsigned aoff0 = ldsb + (wm * 64 + t16) * 128 + (((0 + g) ^ (l & 7)) * 16);
            const unsigned aoff1 = ldsb + (wm * 64 + t16) * 128 + (((4 + g) ^ (l & 7)) * 16);
            const unsigned sk = ((q4 >> 1) & 1) | ((g & 1) << 1);
            const unsigned boffA = ldsb + 16384 + (8 * g + q4) * 128 + (((2 * hh) ^ sk) * 32) + p4 * 8;
            const unsigned boffB = ldsb + 16384 + (8 * g + q4) * 128 + (((2 * hh + 1) ^ sk) * 32) + p4 * 8;
            bf16x8 A[2][4], B[3][2];
            s16x4 lo;
            unsigned a0[3], a1[3], bA[3], bB[3];
#pragma unroll
            for (int i = 0; i < 3; i++) {
                a0[i] = aoff0 + i * STAGE_B; a1[i] = aoff1 + i * STAGE_B; bA[i] = boffA + i * STAGE_B; bB[i] = boffB + i * STAGE_B;
                PIN(a0[i]); PIN(a1[i]); PIN(bA[i]); PIN(bB[i]);
            }
            unsigned aT = ldsb + L_MB + (wm * 64 + t16) * 4, aS = ldsb + L_SD + (wm * 64 + t16) * 2, aM = ldsb + L_MB + wm * 256 + g * 16;
            unsigned aC = ldsb + L_CS + hh * 128 + g * 16, aV = ldsb + L_CS + (128 + hh * 32 + t16) * 4;
            unsigned aW = ldsb + L_WPHI + hh * 2048 + (4 * g + q4) * 64 + p4 * 8, aK = ldsb + L_KH + w * 2048 + l * 16;
            PIN(aT); PIN(aS); PIN(aM); PIN(aC); PIN(aV); PIN(aW); PIN(aK);
            pre_addr();
            READ_TAG();
            waitv<5>();
            PIN(xr[0]); PIN(xr[1]); PIN(xr[2]); PIN(xr[3]);
            {
                float s[2], q[2], x0[2];
#pragma unroll
                for (int rr = 0; rr < 2; rr++) {
                    x0[rr] = bflo(rfl(xr[2 * rr][0]));
                    s[rr] = 0.f; q[rr] = 0.f;
#pragma unroll
                    for (int i = 0; i < 4; i++) {
                        const float a = bflo(xr[2 * rr][i]) - x0[rr], b = bfhi(xr[2 * rr][i]) - x0[rr];
                        const float c = bflo(xr[2 * rr + 1][i]) - x0[rr], d = bfhi(xr[2 * rr + 1][i]) - x0[rr];
                        s[rr] += (a + b) + (c + d);
                        q[rr] += (a * a + b * b) + (c * c + d * d);
                    }
                }
                const float sq = row_sum(xrow_scatter<false>(s[0], s[1], q[0], q[1]));
#define RLF(x, n) __uint_as_float(__builtin_amdgcn_readlane(__float_as_uint(x), (n)))
                const float r0 = RLF(sq, 0), r1 = RLF(sq, 16), r2 = RLF(sq, 32), r3 = RLF(sq, 48);
                const float sm = (l ? r1 : r0) * (1.0f / 1024.0f), qm = (l ? r3 : r2) * (1.0f / 1024.0f);
                const float var = fmaxf(qm - sm * sm, 0.f);
                if (l < 2) st_gran(ws, WS_G0 + (srow1 + l) * 8, tag, pk2((l ? x0[1] : x0[0]) + sm, sqrtf(rbf(rbf(var) + 1e-5f))));
            }
            waitv<1>();
            WAITL0();
            BAR();
            SCHED();
            STAMP(1);
            auto rdB = [&](int u, int j, int i) {
                if (i & 1) B[j % 3][i >> 1] = cat8(lo, bq(bA[u], bB[u], j, i)); else lo = bq(bA[u], bB[u], j, i);
            };
            auto rdA = [&](int u, int i) { A[i & 1][i >> 1] = lds128(((i & 1) ? a1[u] : a0[u]) + (i >> 1) * 2048); };
            auto head = [&](int u) {
                group(acc, A, B[0], 0, [&](int i) { rdB(u, 1 + (i >> 2), i & 3); });
                group(acc, A, B[1], 1, [&](int i) { if (i < 4) rdB(u, 3, i); });
                group(acc, A, B[2], 2, [&](int i) { if (i < 4) rdB(u, 4, i); });
                group(acc, A, B[0], 3, [&](int i) { if (i < 4) rdB(u, 5, i); });
                WL0();
            };
#pragma unroll
            for (int i = 0; i < 4; i++) rdB(0, 0, i);
#pragma unroll
            for (int i = 0; i < 8; i++) rdA(0, i);
            SCHED();
#pragma nounroll
            for (unsigned t = 0; t < 5; t++) {
#pragma unroll
                for (int u = 0; u < 3; u++) {
                    head(u);
                    BAR();
                    JIT(32 + t * 3 + u);
                    SCHED();
                    group(acc, A, B[1], 4, [&](int i) { if (i < 4) rdB((u + 1) % 3, 0, i); });
                    group(acc, A, B[2], 5, [&](int i) { rdA((u + 1) % 3, i); });
                }
            }
            unsigned kw[4][4];
            f32x2 kx;
            auto kpiece = [&](int n) {
                if (n > 0) { kw[((n - 1 >> 2) + 2) & 3][(n - 1) & 3] = pk2(kx[0], kx[1]); PIN(kw[((n - 1 >> 2) + 2) & 3][(n - 1) & 3]); }
                if (n < 16) {
                    const int mi = ((n >> 2) + 2) & 3, j = n & 3;
                    const f32x4 &c = acc[mi][2 + (j >> 1)], &s_ = csk[j >> 1];
                    float x0 = __builtin_fmaf(mbt[mi], s_[2 * (j & 1)], c[2 * (j & 1)]); PIN(x0);
                    float x1 = __builtin_fmaf(mbt[mi], s_[2 * (j & 1) + 1], c[2 * (j & 1) + 1]); PIN(x1);
                    kx = (f32x2){x0, x1};
                }
                if (n == 5 || n == 9) *(LAS v4u*)(aK + (n == 9) * 1024) = (v4u){kw[2 + (n == 9)][0], kw[2 + (n == 9)][1], kw[2 + (n == 9)][2], kw[2 + (n == 9)][3]};
            };
            group(acc, A, B[0], 0, [&](int i) { rdB(0, 1 + (i >> 2), i & 3); });
            group(acc, A, B[1], 1, [&](int i) {
                if (i < 4) { rdB(0, 3, i); mbt[(i + 2) & 3] = ldsf(aT + ((i + 2) & 3) * 64); }
                else if (i < 6) csk[i - 4] = ldsf4(aC + 256 + (i - 4) * 64);
                else wf[i - 6] = cat8(tr_read(aW + (i - 6) * 32), tr_read(aW + (i - 6) * 32 + 1024));
            });
            group(acc, A, B[2], 2, [&](int i) { if (i < 4) rdB(0, 4, i); else mbv[i - 4] = ldsf4(aM + (i - 4) * 64); kpiece(i); });
            group(acc, A, B[0], 3, [&](int i) { if (i < 4) rdB(0, 5, i); if (i == 2 || i == 3) csv[i - 2] = ldsf(aV + (i - 2) * 64); kpiece(8 + i); });
            WL0();
            BAR();
            SCHED();
            kpiece(16);
            f32x4 p[2][2];
#pragma unroll
            for (int i = 0; i < 4; i++) p[i >> 1][i & 1] = MFMA(wf[i & 1], mk8(kw[i >> 1][0], kw[i >> 1][1], kw[i >> 1][2], kw[i >> 1][3]), zero4);
            SCHED();
            STAMP(2);
#pragma unroll
            for (int mi = 0; mi < 4; mi++) rst[mi] = lds_bf(aS + mi * 32);
#pragma unroll
            for (int a = 0; a < 2; a++)
#pragma unroll
                for (int dt = 0; dt < 2; dt++) {
                    const f32x4 x0 = acc[2 * a][4 + dt] + mbv[2 * a] * csv[dt], x1 = acc[2 * a + 1][4 + dt] + mbv[2 * a + 1] * csv[dt];
                    *(LAS v4u*)(aK + (L_V - L_KH) + w * 2048 + (a * 2 + dt) * 1024) = (v4u){pk2(x0[0], x0[1]), pk2(x0[2], x0[3]), pk2(x1[0], x1[1]), pk2(x1[2], x1[3])};
                }
            SCHED();
            rst[0] = __builtin_amdgcn_rcpf(rst[0]); rst[1] = __builtin_amdgcn_rcpf(rst[1]);
            bf16x8 f[4];
            struct HookC {
                decltype(v_reads)& vr; decltype(sd_reads)& sr;
                DI void operator()(int k, float& x) const { if (k == 1) { PIN(x); WAITL0(); BAR(); PIN(x); } if (k == 3) { PIN(x); vr(); sr(); PIN(x); } }
                DI void operator()(int, float&, float&) const {}
            };
            softmax2<true>(p, rst[0], rst[1], g >= 2, f, HookC{v_reads, sd_reads});
            img_write2(ia, f);
#pragma unroll
            for (int ft = 0; ft < 2; ft++) wfq[ft] = cat8(tr_read(aW + ft * 32 + 4096), tr_read(aW + ft * 32 + 5120));
            csq[0] = ldsf4(aC); csq[1] = ldsf4(aC + 64);
            STAMP(3);
            kv_tile([]() {});
            STAMP(4);
            SCHED();
            group(acc, A, B[1], 4, [&](int i) { if (i == 2 || i == 3) { rst[i] = __builtin_amdgcn_rcpf(rst[i]); PIN(rst[i]); } });
            group(acc, A, B[2], 5, [&](int) {});
            asm volatile("s_nop 7\n\ts_nop 1");
            SCHED();
        }
        bf16x8 phq[2][4];
        v4u kg[6];
        auto kg_ld = [&](int k) { return k < 4 ? LD16(l * 16, WS_G3 + (hq * 2 + hh) * 4096 + k * 1024) : LD16(g * 16, WS_GZ + (hq * 2 + hh) * 128 + (k - 4) * 64); };
        {
            f32x4 pq[2][2][2];
#pragma unroll
            for (int mi = 0; mi < 4; mi++) {
                const f32x4 x0 = acc[mi][0] + mbt[mi] * csq[0], x1 = acc[mi][1] + mbt[mi] * csq[1];
                const bf16x8 qf = mk8(pk2(x0[0], x0[1]), pk2(x0[2], x0[3]), pk2(x1[0], x1[1]), pk2(x1[2], x1[3]));
                pq[mi >> 1][mi & 1][0] = MFMA(wfq[0], qf, zero4); pq[mi >> 1][mi & 1][1] = MFMA(wfq[1], qf, zero4);
            }
            MFMA_SETTLE2(pq[0]); MFMA_SETTLE2(pq[1]);
            softmax2<false>(pq[0], rst[0], rst[1], g >= 2, phq[0]);
#pragma unroll
            for (int i = 0; i < 4; i++) PIN(phq[0][i]);
            SCHED();
            red_issue();
            res_loads();
            SCHED();
            kv_reduce();
            SCHED();
            BAR();
            SCHED();
            softmax2<false>(pq[1], rst[2], rst[3], g >= 2, phq[1]);
#pragma unroll
            for (int i = 0; i < 4; i++) { PIN(phq[0][i]); PIN(phq[1][i]); }
            SCHED();
        }
        STAMP(5);
        v2u fv = {tag ^ 0x5bd1e995u, tag};
        place();
        PIN(xres[0]); PIN(xres[1]); PIN(wrv[0]); PIN(wrv[1]);
        unsigned sF = WS_F4 + (rt & 12) * 4096 + (rt & 3) * 2048 + hq * 128 + w * 8 + nh, vF = l == 0 ? 0 : OOB;
        {
            unsigned vO = l * 16, sO = WS_P4 + ((rt & 7) * 8 + wm * 4) * 65536 + (hq >> 2) * 16384 + ((hq & 3) * 2 + hh) * 1024 + (nh ^ (~rt & 4) * 2048);
            asm volatile("" : "+s"(sO), "+s"(sF));
            PIN(fv); PIN(vF);
            SLEEP(1);
            sweepz<6, 4>(kg_ld, kg);
            STAMP(6);
            bf16x8 kvA[2][2], zA[2];
            v4u ov[4];
#pragma unroll
            for (int s = 0; s < 2; s++) {
#pragma unroll
                for (int dt = 0; dt < 2; dt++) kvA[s][dt] = mk8(kg[2 * s][2 * dt], kg[2 * s][2 * dt + 1], kg[2 * s + 1][2 * dt], kg[2 * s + 1][2 * dt + 1]);
                zA[s] = __builtin_bit_cast(bf16x8, kg[4 + s]);
            }
#define PH(mi, s) phq[(mi) >> 1][2 * ((mi) & 1) + (s)]
            f32x4 dn[4], n0[4], n1[4];
            float id[4];
#pragma unroll
            for (int mi = 0; mi < 4; mi++) dn[mi] = MFMA(zA[0], PH(mi, 0), zero4);
#pragma unroll
            for (int mi = 0; mi < 4; mi++) dn[mi] = MFMA(zA[1], PH(mi, 1), dn[mi]);
            SCHED();
#pragma unroll
            for (int mi = 0; mi <= 4; mi++) {
                if (mi < 4) {
                    n0[mi] = MFMA(kvA[0][0], PH(mi, 0), zero4); n1[mi] = MFMA(kvA[0][1], PH(mi, 0), zero4);
                    n0[mi] = MFMA(kvA[1][0], PH(mi, 1), n0[mi]); n1[mi] = MFMA(kvA[1][1], PH(mi, 1), n1[mi]);
                    id[mi] = __builtin_amdgcn_rcpf(rbf(rbf(dn[mi][0]) + 1e-6f));
                }
                if (mi > 0) {
                    const f32x4 a = n0[mi - 1] * id[mi - 1], b = n1[mi - 1] * id[mi - 1];
                    ov[mi - 1] = (v4u){pk2(a[0], a[1]), pk2(a[2], a[3]), pk2(b[0], b[1]), pk2(b[2], b[3])};
                    ST16(ov[mi - 1], vO + (mi - 1) * 65536, sO);
                }
                if (mi < 4) { SGB(0x8, 1); SGB(0x2, 6); SGB(0x8, 1); SGB(0x2, 3); SGB(0x8, 1); SGB(0x2, 3); SGB(0x8, 1); SGB(0x2, 4); }
                SCHED();
            }
#pragma unroll
            for (int mi = 0; mi < 4; mi++) PIN(ov[mi]);
        }
        waitv<0>();
        __builtin_amdgcn_raw_buffer_store_b64(fv, rsWS, vF, sF, AUX_SC1);
        STAMP(7);
    } else {
        const unsigned pw = lw & 1;
        const rsrc_t rsG = __builtin_amdgcn_make_buffer_rsrc((void*)WG, 0, 1024 * 3584 * 2, 0x00020000);
        const rsrc_t rsU = __builtin_amdgcn_make_buffer_rsrc((void*)WU, 0, 1024 * 3584 * 2, 0x00020000);
        unsigned vwl = kr * 57344 + (l & 7) * 16;
        unsigned sg = (kq * 256 + lw * 64) * 7168 + cg1 * 128, su = (kq * 256 + lw * 64) * 7168 + cg1u * 128;
        auto wloads = [&](int n0, int n1) {
#pragma unroll
            for (int n = n0; n < n1; n++)
                asm volatile("buffer_load_dwordx4 %0, %1, %2, %3 offen nt" : "=v"(wreg[n]) : "v"(vwl), "s"(n < 8 ? rsG : rsU), "s"((n < 8 ? sg : su) + (n & 7) * 7168));
        };
        auto dl_asm = [&]() {
            const rsrc_t rsD = __builtin_amdgcn_make_buffer_rsrc((void*)WD, 0, 1024 * 3584 * 2, 0x00020000);
#pragma unroll
            for (int i = 0; i < DN0; i++)
                asm volatile("buffer_load_dwordx4 %0, %1, %2, %3 offen nt" : "=v"(wreg[16 + i]) : "v"(kr * 4096 + (l & 7) * 16), "s"(rsD),
                             "s"((kc * 256 + lw * 64 + (i >> 1) * 16 + (i & 1)) * 2048 + cg * 128));
        };
        unsigned aK, aW, aS;
        auto k_pre = [&]() {
            pre_addr();
            aK = ldsb + L_KH + lw * 2048 + l * 16; aW = ldsb + L_WPHI + (lw >> 1) * 2048 + (4 * g + q4) * 64 + p4 * 8; aS = ldsb + L_SD + (pw * 64 + 32 + t16) * 2;
            PIN(aK); PIN(aW); PIN(aS);
        };
        auto kfeat = [&](auto hook) {
            bf16x8 wf[2];
            float rst[2];
#pragma unroll
            for (int ft = 0; ft < 2; ft++) {
                wf[ft] = cat8(tr_read(aW + ft * 32), tr_read(aW + ft * 32 + 1024));
                rst[ft] = __builtin_amdgcn_rcpf(lds_bf(aS + ft * 32));
            }
            sd_reads();
            PIN(wf[0]); PIN(wf[1]); PIN(rst[0]); PIN(rst[1]);
            SCHED();
            BAR();
            SCHED();
            const bf16x8 kf0 = lds128(aK), kf1 = lds128(aK + 1024);
            SCHED();
            hook(-1, rst[0]);
            hook(0, rst[0]);
            SCHED();
            f32x4 p[2][2];
            p[0][0] = MFMA(wf[0], kf0, zero4); p[0][1] = MFMA(wf[1], kf0, zero4);
            p[1][0] = MFMA(wf[0], kf1, zero4); p[1][1] = MFMA(wf[1], kf1, zero4);
            MFMA_SETTLE2(p);
            bf16x8 f[4];
            softmax2<true>(p, rst[0], rst[1], g >= 2, f, hook);
            img_write2(ia, f);
            SCHED();
            hook(5, rst[0]);
            SCHED();
            STAMP(3);
            kv_tile([&]() { hook(6, rst[0]); });
            STAMP(4);
        };
        auto wblk = [&](int k) { return k == 0 ? 22 + lw : k == 1 ? (lw < 3 ? 26 + lw : 4) : k < 6 ? 4 * k - 3 + lw : k == 6 ? lw : (lw < 3 ? 29 + lw : 21); };
        auto wo_init = [&]() {
#pragma unroll
            for (int i = 0; i < 4; i++) { vwo[i] = vWO + i * (16384 - 1024); PIN(vwo[i]); }
        };
        unsigned hs[8];
        auto wl_pre = [&]() { asm volatile("" : "+s"(sg), "+s"(su)); PIN(vwl); };
        auto wo_pre = [&]() {
            wo_init();
#pragma unroll
            for (int k = 0; k < 8; k++) { hs[k] = wo_so(wblk(k)); asm volatile("" : "+s"(hs[k])); }
        };
        auto wo_rng = [&](int k0, int k1) {
#pragma unroll
            for (int k = k0; k < k1; k++) dma16x4(ldsb + L_WO + wblk(k) * 4096, vwo, rsWO, hs[k]);
        };
        auto pf = [&](int i) {
            if (i == 0) { wloads(0, 6); SCHED(); if (lw < 2) wo_pre(); SCHED(); }
            if (i < 3 && !ffn) wo_rng(i, i + 1);
            if (i == 1 || i == 2) wloads(6 * i, i == 1 ? 12 : 16);
            if (i == 6) dl_asm();
        };
        struct Hook {
            decltype(pf)& f; decltype(v_reads)& vr;
            DI void operator()(int k, float& x) const { PIN(x); if (k == 1) BAR(); if (k == 3) vr(); f(3 + k); PIN(x); }
            DI void operator()(int k, float& x, float& y) const { PIN(y); f(3 + k); PIN(x); }
        };
        auto tail16 = [&]() {
            kfeat(Hook{pf, v_reads});
            if (ffn) wo_rng(0, 3);
            wo_rng(3, 4);
            STAMP(27);
            if (ffn) waitv<18>(); else waitv<6>();
#pragma unroll
            for (int n = 0; n < 16 + DN0; n++) PIN(wreg[n]);
        };
        auto vT23 = [&]() {
            const unsigned n = (idx * 2 + pw) * 64 + l, r = n / 12, c = n % 12;
            return idx < 24 ? ((r & 127) + (r >> 7) * 256 + 128) * 6144 + (c >> 2) * 2048 + (((xcd >> 1) * 4 + (c >> 2 ? 0 : 4) + (c & 3)) & 15) * 128 : OOB;
        };
        if (lw < 2) {
            const unsigned sA = (rt * 128 + lw * 64) * 2048, sB = lw * 32 * 6144 + hp * 128, sBq = lw * 32 * 6144 + hq * 128;
            unsigned buf = 1;
            unsigned va[4], vb[4];
#pragma unroll
            for (int i = 0; i < 4; i++) { va[i] = vA + i * (16384 - 1024); vb[i] = ((i & 1) ? vB1 : vB0) + i * (49152 - 1024); PIN(va[i]); PIN(vb[i]); }
            auto stage = [&](unsigned s, unsigned bf, int i0, int i1) {
                const unsigned kt = (s + rot) & 15;
                const unsigned lb = ldsb + bf * STAGE_B;
                const unsigned sa = sA + kt * 128, sb = kt * 393216;
                if (i0 <= 0 && 0 < i1) dma16x4(lb + lw * 8192, va, rsX, sa);
                if (i0 <= 1 && 1 < i1) dma16x4(lb + lw * 8192 + 4096, va, rsX, sa + 65536);
                if (i0 <= 2 && 2 < i1) dma16x4(lb + 16384 + 8192 + lw * 4096, vb, rsW, sb + sB + 2048);
                if (i0 <= 3 && 3 < i1) dma16x4(lb + 16384 + 16384 + lw * 4096, vb, rsW, sb + sB + 4096);
                if (i0 <= 4 && 4 < i1) dma16x4(lb + 16384 + lw * 4096, vb, rsW, sb + sBq);
            };
            stage(1, 1, 0, 4);
            SCHED();
            unsigned vt = vT23();
            PIN(vt);
            wl_pre(); k_pre();
            waitv<16>(); BAR();
            stage(1, 1, 4, 5);
            touch(vt, rsW, (xcd & 1) * 512 * 6144);
            waitv<1>(); BAR();
            stage(2, 2, 0, 5); stage(3, 0, 0, 3);
            waitv<12>(); BAR();
            stage(3, 0, 3, 5); stage(4, 1, 0, 4);
            waitv<16>(); BAR();
            stage(4, 1, 4, 5);
            buf = 2;
#pragma nounroll
            for (unsigned s = 5; s < 16; s++) {
                if (s > 5) { waitv<20>(); BAR(); }
                stage(s, buf, 0, 5);
                buf = buf == 2 ? 0 : buf + 1;
            }
            waitv<20>(); BAR();
            pf(0);
            READ_TAG();
            if (ffn) waitv<6>(); else waitv<10>();
            BAR();
            pf(1);
            tail16();
        } else {
            SCHED();
            unsigned v3;
            const unsigned h3 = (xcd >> 1) * 4 + 3, kv3 = !(xcd & 2), wv = idx * 2 + pw;
            auto line3 = [&](unsigned per, unsigned rows, auto rowf) {
                const unsigned n = wv * (kv3 ? per : (per + 2) / 3) + l, r3 = kv3 ? n / 3 : n, c3 = kv3 ? n % 3 : 0;
                return (idx < 24 && l < (kv3 ? per : (per + 2) / 3) && r3 < rows) ? rowf(r3) * 6144 + c3 * 2048 + ((c3 ? h3 : h3 + 4) & 15) * 128 : OOB;
            };
            v3 = line3(32, 512, [](unsigned r) { return r; });
            touch(line3(16, 256, [](unsigned r) { return (r & 127) + (r >> 7) * 256; }), rsW, (xcd & 1) * 512 * 6144);
            {
                const rsrc_t rsP = __builtin_amdgcn_make_buffer_rsrc((void*)WPHI, 0, 32 * 1024 * 2, 0x00020000);
#pragma unroll
                for (int i = 0; i < 4; i++) dma16(ldsb + L_WPHI + pw * 4096 + i * 1024, l * 16, rsP, (pw ? hq : hp) * 4096 + i * 1024);
            }
            v4u sv = {SENT, SENT, SENT, SENT};
            unsigned vc4 = l < 16 ? WS_G3 + (hp * 4 + (rt >> 2)) * 2048 + pw * 1024 + (rt & 3) * 256 + l * 16
                         : l < 24 ? WS_P2 + (hp * 16 + rt) * P2_STRIDE + 8192 + pw * 128 + (l - 16) * 16
                         : (l < 32 && pw == 0 && (rt & 7) == 0) ? WS_GZ + (hp * 2 + (rt >> 3)) * 128 + (l - 24) * 16
                         : l >= 56 ? WS_F4 + (rt & 12) * 4096 + (rt & 3) * 2048 + hq * 128 + pw * 8192 + (l & 7) * 16 : OOB;
            auto clears = [&](int i0, int i1) {
#pragma unroll
                for (int i = i0; i < i1; i++) {
                    if (i < 4) ST16(sv, l * 16, WS_P2 + (hp * 16 + rt) * P2_STRIDE + pw * 4096 + i * 1024);
                    if (i == 4) ST16(sv, vc4, 0);
                    if (i > 4) ST16(sv, l * 16, WS_TL + (bid * 4 + pw * 2 + i - 5) * 1024);
                }
                FRESH();
            };
            PIN(vc4); PIN(sv);
            unsigned ca = ldsb + 16384 + pw * 8192 + (8 * g + q4) * 128 + (((rt & 3) ^ (((q4 >> 1) & 1) | ((g & 1) << 1))) * 32) + p4 * 8;
            PIN(ca);
            bf16x8 cf[4];
            f32x4 cd[2] = {zero4, zero4};
            auto cs_read = [&](int st) {
#pragma unroll
                for (int i = 0; i < 4; i++) cf[i] = cat8(tr_read(ca + (st % 3) * STAGE_B + i * 4096), tr_read(ca + (st % 3) * STAGE_B + i * 4096 + 512));
                SCHED();
            };
            READ_TAG();
            waitv<5>();
            BAR();
            SCHED();
            cs_read(0);
            FRESH();
            const rsrc_t rsC = __builtin_amdgcn_make_buffer_rsrc((void*)pc0, 0, clen, 0x00020000);
            auto code_pf = [&]() { touch(l < 6 ? l * 128 : OOB, rsC, (idx * 2 + pw) * 768); };
            v4u gl[4];
            float s0 = 0.f, s1 = 0.f;
            unsigned bad;
            v2u gs, gc;
            unsigned vgc = WS_GC + (pw ? hq : hp) * 256 + (l & 31) * 8;
            unsigned lat[2] = {~0u, ~0u};
            unsigned vgl = (l < 48 ? l : 0) * 16;
            unsigned aw0 = ldsb + L_CS + pw * 384 + (vgl >> 1), aw1 = ldsb + L_MB + (pw * 64 + l) * 4, aw2 = ldsb + L_SD + (pw * 64 + l) * 2;
            static_for<15>([&](auto tc) {
                constexpr int t = decltype(tc)::value;
                WAITL0();
                JIT(32 + t);
                BAR();
                SCHED();
                if constexpr (t < 4) {
                    __builtin_amdgcn_s_setprio(2);
#pragma unroll
                    for (int i = 0; i < 4; i++) cd[i & 1] = MFMA(ones, cf[(i & 1) * 2 + (i >> 1)], cd[i & 1]);
                    SCHED();
                    __builtin_amdgcn_s_setprio(0);
                    if constexpr (t < 3) cs_read(t + 1);
                    else __builtin_amdgcn_raw_buffer_store_b64((v2u){__float_as_uint(g ? cd[1][0] : cd[0][0]), tag}, rsWS, (g == 1 || g + pw == 0) ? g * 512 + t16 * 8 : OOB,
                                                               WS_G1 + ((hp * 4 + (rt >> 2)) * 192 + pw * 64 + (rt & 3) * 16) * 8, AUX_SC1);
                }
                if constexpr (t == 0) touch(v3, rsW, (~xcd & 1) * 512 * 6144);
                if constexpr (t == 12) code_pf();
                if constexpr (t == 8) {
                    waitv<0>();
                    __builtin_amdgcn_raw_buffer_store_b64((v2u){0u, tag}, rsWS, l == 0 ? 0 : OOB, WS_GC + ((hp * 2 + pw) * 16 + rt) * 8, AUX_SC1);
                }
                if constexpr (t == 8 || t == 9) {
#pragma unroll
                    for (int r = (t - 8) * 2; r < (t - 8) * 2 + 2; r++)
                        asm volatile("buffer_load_dwordx4 %0, %1, %2, %3 offen sc1" : "=v"(gl[r]) : "v"(vgl), "s"(rsWS), "s"(WS_G1 + ((hp * 4 + r) * 192 + pw * 96) * 8) : "memory");
                }
                if constexpr (t == 6) {
                    asm volatile("buffer_load_dwordx2 %0, %1, %2, %3 offen sc1" : "=v"(gs) : "v"(l * 8), "s"(rsWS), "s"(WS_G0 + (rt * 128 + pw * 64) * 8) : "memory");
                }
                if constexpr (t == 11) {
                    waitv<0>();
                    PIN(gs);
                    bad = gs[1] ^ tag;
#pragma unroll
                    for (int r = 0; r < 4; r++) {
                        PIN(gl[r]);
                        bad |= (gl[r][1] ^ tag) | (gl[r][3] ^ tag);
                        s0 += __uint_as_float(gl[r][0]); s1 += __uint_as_float(gl[r][2]);
                    }
                    PIN(s0); PIN(s1); PIN(bad);
                }
                if constexpr (t == 12) {
                    if (RARE(!__all(bad == 0))) {
                        s0 = 0.f; s1 = 0.f;
#pragma nounroll
                        for (unsigned r = 0; r < 4; r++) {
                            unsigned a[2];
                            sweep<2>(rsWS, vgl, WS_G1 + ((hp * 4 + r) * 192 + pw * 96) * 8, 8, tag, a);
                            s0 += __uint_as_float(a[0]); s1 += __uint_as_float(a[1]);
                        }
                        unsigned c[1];
                        sweep<1>(rsWS, l * 8, WS_G0 + (rt * 128 + pw * 64) * 8, 0, tag, c);
                        gs[0] = c[0];
                    }
                    *(LAS f32x2*)(aw0) = (f32x2){-s0, -s1};
                    stsf(aw1, bflo(gs[0]));
                    *(LAS u16*)(aw2) = (u16)(gs[0] >> 16);
                    asm volatile("buffer_load_dwordx2 %0, %1, %2, 0 offen sc1" : "=v"(gc) : "v"(vgc), "s"(rsWS) : "memory");
                    SCHED();
                }
                if constexpr (t == 6) { if (RARE(bid * 2 + pw == 16)) ST16(sv, l < 32 ? g * 8192 + t16 * 16 : OOB, WS_PR); }
                if constexpr (t >= 8 && t < 12) {
                    if (RARE(bid * 2 + pw == 16)) {
                        u64 c0, c1; unsigned x;
                        asm volatile(".p2align 6\n\ts_memtime %0\n\ts_load_dword %2, %3, %4\n\ts_waitcnt lgkmcnt(0)\n\ts_memtime %1\n\ts_waitcnt lgkmcnt(0)"
                                     : "=&s"(c0), "=&s"(c1), "=&s"(x) : "s"(ws), "i"(WS_PR + (t & 1) * 8192 + (t & 2) * 64) : "memory");
                        lat[t & 1] = __builtin_elementwise_min(lat[t & 1], (unsigned)c1 - (unsigned)c0);
                        if constexpr (t == 11) {
                            asm volatile("s_getreg_b32 %0, hwreg(HW_REG_XCC_ID, 0, 3)" : "=s"(x));
                            __builtin_amdgcn_raw_buffer_store_b64((v2u){(lat[1] < lat[0] ? 0x44 : 0) ^ (x & 4), tag}, rsWS, l == 0 ? 0 : OOB, WS_PR + 1024, AUX_SC1);
                        }
                    }
                }
                if constexpr (t == 5) k_pre();
                if constexpr (t == 6) { wl_pre(); wo_pre(); }
                if constexpr (t == 7) { PIN(vgc); PIN(vgl); PIN(aw0); PIN(aw1); PIN(aw2); }
                clears(t < 7 ? t : 7, t < 7 ? t + 1 : 7);
                if constexpr (t == 13) {
                    waitv<0>();
                    PIN(gc);
                    if (RARE(!__all(gc[1] == tag))) { unsigned c[1]; sweep<1>(rsWS, vgc, 0, 0, tag, c); }
                    SCHED();
                    pf(0);
                }
                if constexpr (t == 14) pf(1);
            });
            tail16();
        }
        STAMP(28);
        auto perm1 = [&]() {
#pragma unroll
            for (int ip = 0; ip < 4; ip++)
#pragma unroll
                for (int c = 0; c < 4; c++) {
#pragma unroll
                    for (int m = 0; m < 2; m++) {
                        const unsigned a = wreg[m * 8 + 2 * ip][c], b = wreg[m * 8 + 2 * ip + 1][c];
                        wp1[m][ip][2 * c] = __builtin_amdgcn_perm(b, a, 0x05040100);
                        wp1[m][ip][2 * c + 1] = __builtin_amdgcn_perm(b, a, 0x07060302);
                    }
                }
#pragma unroll
            for (int ip = 0; ip < 4; ip++)
#pragma unroll
                for (int c = 0; c < 8; c++) { PIN(wp1[0][ip][c]); PIN(wp1[1][ip][c]); }
            SCHED();
            STAMP(29);
        };
        if (RARE(lw == 0 && (rt & 7) == 0)) {
            __builtin_amdgcn_s_setprio(1);
            sweepz<2, 1>([&](int k) { return LD16(kr * P2_STRIDE + (l & 7) * 16, WS_P2 + (hp * 16 + k * 8) * P2_STRIDE + 8192 + (rt >> 3) * 128); }, pp);
            ST4(red(), (l & 8) ? OOB : ((l >> 2) & 1) * 64 + ((l & 1) * 2 + (g >> 1)) * 16 + ((l >> 1) & 1) * 8 + (g & 1) * 4, WS_GZ + (hp * 2 + (rt >> 3)) * 128);
            __builtin_amdgcn_s_setprio(0);
        }
        SCHED();
        perm1();
        BAR();
        STAMP(30);
        res_loads();
        wo_rng(4, 8);
        STAMP(31);
        waitv<0>();
        PIN(xres[0]); PIN(xres[1]); PIN(wrv[0]); PIN(wrv[1]);
        place();
        STAMP(7);
    }
    prep2();
    if (w == 0) { SLEEP(4); poll_flags(rsWS, vFl, sFl, (v2u){tag ^ 0x5bd1e995u, tag}); }
    BAR();
    FRESH();
    SCHED();
    STAMP(8);
    bf16x8 bb[2][4];
    {
        auto aload = [&](int st, int m2) {
            const v4u r = LD16(m2 ? va1 : va0, so);
            if (m2 == 0) so = ((so | 8192) + 2048) & 0xDC00;
            return r;
        };
        constexpr int PD = 2;
        v4u af[PD][2];
#pragma unroll
        for (int i = 0; i < PD; i++) {
#pragma unroll
            for (int m2 = 1; m2 >= 0; m2--) af[i][m2] = aload(i, m2);
            SCHED();
        }
#pragma unroll
        for (int nt = 0; nt < 4; nt++) bb[0][nt] = cat8(tr_read(wb[nt]), tr_read(wb[nt] + 2048));
#pragma unroll
        for (int nt = 0; nt < 4; nt++) acc2[1][nt] = zero4;
        SCHED();
#pragma unroll
        for (int st = 0; st < 16; st++) {
            if (st + 1 < 16) {
#pragma unroll
                for (int nt = 0; nt < 4; nt++)
                    bb[(st + 1) % (2)][nt] = cat8(tr_read(wb[nt] + (st + 1) * 4096), tr_read(wb[nt] + (st + 1) * 4096 + 2048));
            }
#pragma unroll
            for (int mm = 0; mm < 2; mm++) {
                const int m2 = mm ^ 1;
                const bf16x8 a = __builtin_bit_cast(bf16x8, af[st % PD][m2]);
#pragma unroll
                for (int nt = 0; nt < 4; nt++) acc2[m2][nt] = MFMA(bb[st % (2)][nt], a, acc2[m2][nt]);
            }
            if (st + PD < 16) {
#pragma unroll
                for (int m2 = 1; m2 >= 0; m2--) af[st % PD][m2] = aload(PD + st, m2);
            }
#pragma unroll
            for (int i = 0; i < 4; i++) { SGB(0x8, 1); SGB(0x100, 2); }
            SCHED();
        }
    }
    STAMP(9);
#pragma unroll
    for (int nt = 0; nt < 4; nt++) stsf4(aXw + nt * 1024, acc2[1][nt]);
    SCHED();
    if (half) dloads(DN0, 8);
    SCHED();
    WAITL0();
    if (half) __builtin_amdgcn_s_setprio(1);
    BAR();
    SCHED();
    v4u hpk[2];
    {
        f32x4 pa[4];
#pragma unroll
        for (int nt = 0; nt < 4; nt++) pa[nt] = ldsf4(aXr + nt * 1024);
        SCHED();
#pragma unroll
        for (int i = 0; i < 8; i++) {
            const f32x4 a = acc2[0][i >> 1] + pa[i >> 1];
            hpk[i >> 2][i & 3] = pk2(a[2 * (i & 1)], a[2 * (i & 1) + 1]);
        }
        const bf16x8 h0 = __builtin_bit_cast(bf16x8, hpk[0]), h1 = __builtin_bit_cast(bf16x8, hpk[1]);
        f32x4 gq = MFMA(h0, h0, zero4), sd = MFMA(sop[0], h0, zero4);
        gq = MFMA(h1, h1, gq); sd = MFMA(sop[1], h1, sd);
        float q0 = (l & 1) ? gq[1] : gq[0], q1 = (l & 1) ? gq[3] : gq[2];
        PIN(q0); PIN(q1);
        const v4u pv = {f2s(sd[0]), f2s((l & 2) ? q1 : q0), f2s(sd[1]), swc};
        ST16(pv, vG5, sG5);
        asm volatile("s_nop 1" :: "v"(pv));
    }
    __builtin_amdgcn_s_setprio(0);
    SCHED();
    swap16(hpk[0]); swap16(hpk[1]);
    PIN(hpk[0]); PIN(hpk[1]);
    STAMP(10);
    unsigned wp2[4][8];
    auto perm2 = [&]() {
#pragma unroll
        for (int i = 16; i < 24; i++) PIN(wreg[i]);
#pragma unroll
        for (int ip = 0; ip < 4; ip++)
#pragma unroll
            for (int c = 0; c < 4; c++) {
                const unsigned a = wreg[16 + 2 * ip][c], b = wreg[16 + 2 * ip + 1][c];
                wp2[ip][2 * c] = __builtin_amdgcn_perm(b, a, 0x05040100);
                wp2[ip][2 * c + 1] = __builtin_amdgcn_perm(b, a, 0x07060302);
            }
#pragma unroll
        for (int ip = 0; ip < 4; ip++)
#pragma unroll
            for (int c = 0; c < 8; c++) PIN(wp2[ip][c]);
    };
    v4u outv[2];
    const rsrc_t rsO = __builtin_amdgcn_make_buffer_rsrc((void*)OUT, 0, 2048 * 1024 * 2, 0x00020000);
    unsigned vOut = (R2 & ~8u) * 2048 + (t16 >> 3) * 64 + gx * 16;
    PIN(vOut);
    unsigned aG = ldsb + L_WO + 512, vX = kr, v4 = l * 4, sX = WS_C + kq * 512 + lw * 128 + nr;
    unsigned aR1 = ldsb + L_RED + (lw * 4 + g) * 512 + t16 * 32, aT1 = ldsb + L_RED + (lw & 1) * 256 + l * 4;
    unsigned aR2 = ldsb + L_RED + 8192 + (lw * 4 + g) * 256 + (t16 & 7) * 32 + (t16 >> 3) * 16, aT2 = ldsb + L_RED + 8192 + l * 4, aB = kr * 8;
    const unsigned cx = (lw & 1) ? cg1u : cg1;
    unsigned s8w = WS_G8 + (cx >> 2) * 16384 + (lw >> 1) * 8192 + (kq * 2 + (lw & 1)) * 1024 + (cx & 3) * 256;
    unsigned s8r = WS_G8 + kc * 16384 + lw * 256 + nr, s8s = s8r + 4096;
    unsigned s9w = WS_G9 + (cg >> 2) * 16384 + (cg & 3) * 2048 + kc * 128, v9 = (l & 31) * 4 + (nh ^ (((l >> 3) ^ bid) & 4) * 2048);
    float a1[2][8];
    if (half) {
#pragma unroll
        for (int c = 0; c < 16; c++) { a1[c >> 3][c & 7] = 0.f; PIN(a1[c >> 3][c & 7]); }
        PIN(vX); PIN(v4); PIN(aR1); PIN(aT1); PIN(aR2); PIN(aT2); PIN(aB); PIN(v9);
    }
    PIN(aG);
    asm volatile("" : "+s"(sX), "+s"(s8w), "+s"(s8r), "+s"(s8s), "+s"(s9w));
    unsigned vP = t16 == rt2 ? OOB : t16 * 128, pk;
    {
        v4u pv[4];
        unsigned vK = tid < 4 ? ((ct + 4 * tid) & 15) * 2048 : OOB, vS = l * 16;
        PIN(vK); PIN(vS);
        unsigned sK = WS_GK + rt2 * 128, rk = 2047u - R2, aKy = ldsb + L_WO + 576, aKw = aKy + w * 4;
        unsigned vC = gx * 16, sC = WS_C + (~rt2 & 12) * 4096 + (~rt2 & 3) * 2048 + ct * 128;
        PIN(vC);
        asm volatile("" : "+s"(sK), "+s"(sC));
        PIN(rk); PIN(aKy); PIN(aKw); PIN(vP);
        auto st_ld = [&](int k) { return LD16(vS + k * 1024, sG5r); };
        auto st_sum = [&]() { const f32x4 ps = (f4(pv[0]) + f4(pv[1])) + (f4(pv[2]) + f4(pv[3])); return xrow_scatter<false>(ps[0], ps[1], ps[2], ps[3]); };
        SCHED();
        if (half) SLEEP(4); else SLEEP(7);
        __builtin_amdgcn_s_setprio(1);
        float ts;
        sweepn<4, 4>(st_ld, pv, [&]() { return ts = st_sum(); });
        STAMP(21);
        float T[4];
        xrow_gather(ts, T);
        const float mean = T[0] * (1.0f / 1024.0f), var = fmaxf(T[1] * (1.0f / 1024.0f) - mean * mean, 0.f);
        const unsigned sg = pk2(mean, __builtin_amdgcn_sqrtf(rbf(rbf(var) + 1e-5f)));
        const float mb2 = bflo(sg), isd2 = __builtin_amdgcn_rcpf(bfhi(sg));
        const float rsc = (T[2] - mb2 * T[3]) * isd2;
        const unsigned rb = pk2(rsc, rsc);
        const unsigned key = (((rb ^ (((int)rb >> 31) | 0x80000000u)) >> 16) << 11) | rk;
        unsigned kb = umx(key, dppu<0x128>(key));
        kb = umx(kb, dppu<0x124>(kb)); kb = umx(kb, dppu<0x122>(kb)); kb = umx(kb, dppu<0x121>(kb));
        stsu(aKw, kb);
        WAITL0();
        BAR();
        SCHED();
        pk = umx(wmax(*(const LAS v4u*)aKy), wmax(*(const LAS v4u*)(aKy + 16)));
        if (w == 0) ST4(pk, vK, sK);
        SCHED();
        const bool cand = key == pk;
        __builtin_amdgcn_s_setprio(0);
        v4u e[2];
#pragma unroll
        for (int i = 0; i < 8; i++) {
            const float h0 = bflo(hpk[i >> 2][i & 3]), h1 = bfhi(hpk[i >> 2][i & 3]);
            const unsigned d = pk2(h0 - mb2, h1 - mb2);
            e[i >> 2][i & 3] = nsent(pk2(bflo(d) * isd2, bfhi(d) * isd2));
        }
        if (cand) {
#pragma unroll
            for (int i = 0; i < 4; i++) ST16(e[i & 1], vC + (i & 1) * 64, sC + (i >> 1) * 8192);
#pragma unroll
            for (int i = 0; i < 4; i++)
                ST16(((v4u){nsent(hpk[i & 1][0]), nsent(hpk[i & 1][1]), nsent(hpk[i & 1][2]), nsent(hpk[i & 1][3])}), vC + (i & 1) * 64, sC + (WS_H - WS_C) + (i >> 1) * 8192);
        }
        SCHED();
        unsigned k1;
        if (w == 1) {
            SLEEP(3);
            asm volatile("buffer_load_dword %0, %1, %2, %3 offen sc1" : "=v"(k1) : "v"(vP), "s"(rsWS), "s"(sRep) : "memory");
        }
        SCHED();
        STAMP(22);
#pragma unroll
        for (int i = 0; i < 8; i++)
            outv[i >> 2][i & 3] = pk2(bflo(hpk[i >> 2][i & 3]) + bflo(e[i >> 2][i & 3]), bfhi(hpk[i >> 2][i & 3]) + bfhi(e[i >> 2][i & 3]));
        {
#pragma unroll
            for (int j = 0; j < 4; j++) {
                const unsigned a = outv[0][j], b = outv[1][j];
                outv[0][j] = __builtin_amdgcn_update_dpp(a, b, 0x128, 0xf, 0xc, false);
                outv[1][j] = __builtin_amdgcn_update_dpp(b, a, 0x128, 0xf, 0x3, false);
            }
        }
        PIN(outv[0]); PIN(outv[1]);
        if (w == 1) {
            poll_words(k1, rsWS, vP, sRep);
            STAMP(23);
            unsigned gk = umx(k1, pk);
            gk = umx(gk, dppu<0x128>(gk)); gk = umx(gk, dppu<0x124>(gk)); gk = umx(gk, dppu<0x122>(gk)); gk = umx(gk, dppu<0x121>(gk));
            stsu(aG, gk);
        }
    }
    if (half) __builtin_amdgcn_s_setprio(1);
    STAMP(24);
    WL0();
    SCHED();
    BAR();
    SCHED();
    STAMP(11);
    const unsigned gv = ldsu(aG);
    unsigned vo[2];
    auto store_out = [&](unsigned srow) {
#pragma unroll
        for (int i = 0; i < 2; i++) __builtin_amdgcn_raw_buffer_store_b128(outv[i], rsO, vo[i], ct * 128 + i * 16384, AUX_SC1);
    };
    if (RARE(!ffn)) {
        const unsigned gkey = rfl(gv), srow = ~gkey & 2047u;
        vo[0] = (R2 & ~8u) != srow ? vOut : OOB; vo[1] = (R2 | 8u) != srow ? vOut : OOB;
        store_out(srow);
        if (w != 4) { RDUMP(); return; }
        const unsigned c9 = (bid & 3) * 4 + (idx & 3), col = c9 * 64 + (bid & 4) * 8 + (l & 7) * 4 + g;
        unsigned hl[2];
        sweepz<2, 2>([&](int k) { return LD4((col >> 1) * 4, (k ? WS_C : WS_H) + (((gkey & 0x780) + (gkey & 0x600)) << 4) + nr); }, hl);
        const unsigned ord = gkey >> 11;
        const unsigned rb = (ord & 0x8000u) ? (ord & 0x7fffu) : (~ord & 0xffffu);
        float sig = rbf(1.0f / (1.0f + __expf(-bf2f(rb))));
        float hc = (g & 1) ? bfhi(hl[0]) : bflo(hl[0]), lc = (g & 1) ? bfhi(hl[1]) : bflo(hl[1]);
        unsigned vY = l * 16, vZ = l < 48 ? l * 16 + 1024 : OOB, vO = (l & 8) ? OOB : col * 2, sY = WS_G9 + (c9 >> 2) * 16384 + (c9 & 3) * 2048 + nh, sO = srow * 2048;
        PIN(sig); PIN(hc); PIN(lc); PIN(vY); PIN(vZ); PIN(vO);
        asm volatile("" : "+s"(sY), "+s"(sO));
        v4u yp[2];
        SCHED();
        STAMP(16);
        SLEEP(24);
        sweepz<2>([&](int k) { return LD16(k ? vZ : vY, sY); }, yp);
        const f32x4 ys = f4(yp[0]) + f4(yp[1]);
        float yt = xrow_scatter<false>(ys[0], ys[1], ys[2], ys[3]);
        yt += dppf<0x128>(yt);
        const float y = rbf(rbf(yt) * sig);
        __builtin_amdgcn_raw_buffer_store_b16((u16)pk2(hc + rbf(lc + y), 0.f), rsO, vO, sO, 0);
        STAMP(17);
        RDUMP();
        return;
    }
    if (w < 4) {
        const unsigned srow = ~rfl(gv) & 2047u;
        vo[0] = (R2 & ~8u) != srow ? vOut : OOB; vo[1] = (R2 | 8u) != srow ? vOut : OOB;
        PIN(vo[0]); PIN(vo[1]);
        BAR(); BAR(); BAR();
        store_out(srow);
        if (bid == 0 && tid == 0) __hip_atomic_store((gu32*)(ws + WS_EPOCH), tag, RLX_AGENT);
        STAMP(12);
        RDUMP();
        return;
    }

    auto tree16 = [&](unsigned a, unsigned stride) {
        f32x2 v[8];
#pragma unroll
        for (int i = 0; i < 8; i++) v[i] = (f32x2){ldsf(a + 2 * i * stride), ldsf(a + (2 * i + 1) * stride)};
        SCHED();
        const f32x2 t = ((v[0] + v[1]) + (v[2] + v[3])) + ((v[4] + v[5]) + (v[6] + v[7]));
        return t[0] + t[1];
    };
    vX = (((gv & 0x780) | vX) + (gv & 0x600)) << 4;
    v4u xq[1] = {LD16(vX, sX)};
    SCHED();
    const unsigned srow = ~rfl(gv) & 2047u;
    vo[0] = (R2 & ~8u) != srow ? vOut : OOB; vo[1] = (R2 | 8u) != srow ? vOut : OOB;
    PIN(vo[0]); PIN(vo[1]);
    SCHED();
    if (RARE(!__all(wmax(xq[0]) != SENT))) sweepz<1>([&](int) { return LD16(vX, sX); }, xq);
    STAMP(13);
#pragma unroll
    for (int ip = 0; ip < 4; ip++)
#pragma unroll
        for (int c = 0; c < 16; c++) dot2c(a1[c & 1][c >> 1], wp1[c & 1][ip][c >> 1], xq[0][ip]);
#pragma unroll
    for (int c = 0; c < 8; c++) fold8(a1[0][c], a1[1][c]);
    stsf4(aR1, (f32x4){a1[0][0], a1[0][1], a1[0][2], a1[0][3]});
    stsf4(aR1 + 16, (f32x4){a1[0][4], a1[0][5], a1[0][6], a1[0][7]});
    WL0();
    BAR();
    SCHED();
    ST4(f2s(tree16(aT1, 512)), v4, s8w);
    SCHED();
    STAMP(14);
    perm2();
    float a2[8];
#pragma unroll
    for (int c = 0; c < 8; c++) { a2[c] = 0.f; PIN(a2[c]); }
    SCHED();
    SLEEP(7);
    {
        unsigned pv[8];
        auto a_ld = [&](int k) { return LD4(v4 + (k & 3) * 1024, k < 4 ? s8r : s8s); };
        auto a_sum = [&](int m) { return (__uint_as_float(pv[m]) + __uint_as_float(pv[2 + m])) + (__uint_as_float(pv[4 + m]) + __uint_as_float(pv[6 + m])); };
        float gs, us;
        sweepn<8, 0>(a_ld, pv, [&]() { gs = a_sum(0); us = a_sum(1); return gs + us; });
        STAMP(15);
        const unsigned gu = pk2(gs, us);
        const float gg = bflo(gu);
        const float act = rbf(gg * __builtin_amdgcn_rcpf(1.0f + __expf(-gg))) * bfhi(gu);
        const unsigned apk = pk2(act, dppf<0xB1>(act));
        unsigned ap[4];
#pragma unroll
        for (int ip = 0; ip < 4; ip++) ap[ip] = __builtin_amdgcn_ds_bpermute(aB + ip * 64, apk);
        SCHED();
#pragma unroll
        for (int ip = 0; ip < 4; ip++)
#pragma unroll
            for (int c = 0; c < 8; c++) dot2c(a2[(c & 1) * 4 + (c >> 1)], wp2[ip][(c & 1) * 4 + (c >> 1)], ap[ip]);
#pragma unroll
        for (int c = 0; c < 4; c++) fold8(a2[c], a2[4 + c]);
        stsf4(aR2, (f32x4){a2[0], a2[1], a2[2], a2[3]});
    }
    WL0();
    BAR();
    SCHED();
    if (lw == 0) ST4(f2s(tree16(aT2, 256)), v9, s9w);
    SCHED();
    BAR();
    SCHED();
    STAMP(16);
    store_out(srow);
    RDUMP();
}
__device__ __attribute__((used, noinline)) void fk_tail() { asm volatile(".Lfk_end:"); }

extern "C" void kernel_launch(void* const* d_in, const int* in_sizes, int n_in, void* d_out, int out_size, void* d_ws,
                              size_t ws_size, hipStream_t stream) {
    (void)in_sizes; (void)n_in; (void)out_size; (void)ws_size;
    (void)hipFuncSetAttribute((const void*)fused_kernel, hipFuncAttributeMaxDynamicSharedMemorySize, LDS_BYTES);
    fused_kernel<<<256, 512, LDS_BYTES, stream>>>((const u16*)d_in[0], (const u16*)d_in[1], (const u16*)d_in[2], (const u16*)d_in[3],
                                                  (const u16*)d_in[5], (const u16*)d_in[6], (const u16*)d_in[7], (const u16*)d_in[8],
                                                  (u16*)d_out, (char*)d_ws + (-(size_t)d_ws & 0x1FFFFF));
}
```

```cpp
#include <hip/hip_runtime.h>
#include <stdint.h>
#include <utility>

typedef unsigned short u16;
typedef unsigned long long u64;
typedef short bf16x8 __attribute__((ext_vector_type(8)));
typedef short s16x4 __attribute__((ext_vector_type(4)));
typedef float f32x4 __attribute__((ext_vector_type(4)));
typedef float f32x2 __attribute__((ext_vector_type(2)));
typedef __bf16 bf16x2 __attribute__((ext_vector_type(2)));
typedef unsigned v4u __attribute__((ext_vector_type(4)));
typedef unsigned v2u __attribute__((ext_vector_type(2)));
#define LAS __attribute__((address_space(3)))
#define GAS __attribute__((address_space(1)))
#define CAS __attribute__((address_space(4)))
typedef GAS u64 gu64;
typedef GAS unsigned gu32;
typedef __amdgpu_buffer_rsrc_t rsrc_t;
#define DI __device__ __forceinline__
#define RLX_AGENT __ATOMIC_RELAXED, __HIP_MEMORY_SCOPE_AGENT
#define BAR() __builtin_amdgcn_s_barrier()
#define SCHED() __builtin_amdgcn_sched_barrier(0)
#define SGB(m, n) __builtin_amdgcn_sched_group_barrier((m), (n), 0)
#define WAITL0() asm volatile("s_waitcnt lgkmcnt(0)" ::: "memory")
#define WL0() __builtin_amdgcn_s_waitcnt(0xc07f)
#define MFMA(a, b, c) __builtin_amdgcn_mfma_f32_16x16x32_bf16((a), (b), (c), 0, 0, 0)
#define AUX_SC1 16
template <int N> DI void waitv() { asm volatile("s_waitcnt vmcnt(%0)" ::"i"(N) : "memory"); }

#define WS_EPOCH 0
#define WS_G0 0x1000
#define WS_G1 0x8000
#define WS_F4 0x60000
#define WS_GC 0x70000
#define WS_PR 0x74000
#define WS_G3 0x80000
#define WS_GZ 0xC0000
#define SENT 0xFFFFFFFFu
#define WS_TL 0x100000
#define WS_G5 0x100000
#define WS_G8 0x180000
#define WS_GK 0x1B8000
#define WS_G9 0x1C0000
#define WS_C 0x1E0000
#define WS_H 0x1F0000
#define WS_P2 0x200000
#define P2_STRIDE 0x2180
#define WS_P4 0x600000
#define WS_USED 0xA00000
#define WS_STAMP 0x1000000

#define STAGE_B 40960
#define LDS_BYTES 163840
#define L_WO 32768
#define L_MB 161792
#define L_WPHI (L_MB - 8192)
#define L_SD (L_MB + 512)
#define L_CS (L_MB + 1024)
#define L_V L_WO
#define L_KH (L_WO + 65536)
#define L_XB 0
#define L_RED 4096

#define SPIN_CAP 400000u
#define DN0 3
#define W_AUX 2
#define LOG2E 1.4426950408889634f

DI unsigned rfl(unsigned x) { return __builtin_amdgcn_readfirstlane(x); }
DI float bf2f(unsigned u) { return __uint_as_float(u << 16); }
DI float bflo(unsigned u) { return __uint_as_float(u << 16); }
DI float bfhi(unsigned u) { return __uint_as_float(u & 0xffff0000u); }
DI unsigned pk2(float lo, float hi) { f32x2 v = {lo, hi}; return __builtin_bit_cast(unsigned, __builtin_convertvector(v, bf16x2)); }
DI unsigned nsent(unsigned x) { return __builtin_elementwise_min(x, SENT - 1); }
DI unsigned umx(unsigned a, unsigned b) { return __builtin_elementwise_max(a, b); }
DI float rbf(float f) { return bflo(pk2(f, f) & 0xffffu); }
DI bf16x8 mk8(unsigned a, unsigned b, unsigned c, unsigned d) { v4u v = {a, b, c, d}; return __builtin_bit_cast(bf16x8, v); }
DI void dma16(unsigned ldsaddr, unsigned voff, rsrc_t rs, unsigned soff) {
    asm volatile("s_mov_b32 m0, %0\n\ts_nop 0\n\tbuffer_load_dwordx4 %1, %2, %3 offen lds" ::"s"(ldsaddr), "v"(voff), "s"(rs), "s"(soff) : "memory");
}
DI void dma16x4(unsigned ldsaddr, const unsigned* v, rsrc_t rs, unsigned soff) {
    asm volatile("s_mov_b32 m0, %0\n\ts_nop 0\n\tbuffer_load_dwordx4 %1, %5, %6 offen lds\n\tbuffer_load_dwordx4 %2, %5, %6 offen offset:1024 lds\n\t"
                 "buffer_load_dwordx4 %3, %5, %6 offen offset:2048 lds\n\tbuffer_load_dwordx4 %4, %5, %6 offen offset:3072 lds"
                 ::"s"(ldsaddr), "v"(v[0]), "v"(v[1]), "v"(v[2]), "v"(v[3]), "s"(rs), "s"(soff) : "memory");
}
DI void touch(unsigned voff, rsrc_t rs, unsigned soff) { asm volatile("buffer_load_dword v255, %0, %1, %2 offen" ::"v"(voff), "s"(rs), "s"(soff) : "memory", "v255"); }
DI v4u ld16_asm(unsigned voff, rsrc_t rs, unsigned soff) {
    v4u r; asm volatile("buffer_load_dwordx4 %0, %1, %2, %3 offen" : "=v"(r) : "v"(voff), "s"(rs), "s"(soff) : "memory"); return r;
}
#define PIN(x) asm volatile("" : "+v"(x))
DI s16x4 tr_read(unsigned a) { return __builtin_amdgcn_ds_read_tr16_b64_v4i16((LAS s16x4*)(a)); }
DI bf16x8 cat8(s16x4 a, s16x4 b) { return __builtin_shufflevector(a, b, 0, 1, 2, 3, 4, 5, 6, 7); }
DI bf16x8 lds128(unsigned a) { return *(const LAS bf16x8*)(a); }
DI f32x4 ldsf4(unsigned a) { return *(const LAS f32x4*)(a); }
DI float ldsf(unsigned a) { return *(const LAS float*)(a); }
DI unsigned ldsu(unsigned a) { return *(const LAS unsigned*)(a); }
DI void stsf4(unsigned a, f32x4 v) { *(LAS f32x4*)(a) = v; }
DI void stsf(unsigned a, float v) { *(LAS float*)(a) = v; }
DI void stsu(unsigned a, unsigned v) { *(LAS unsigned*)(a) = v; }
template <int C> DI float dppf(float x) { return __uint_as_float(__builtin_amdgcn_update_dpp(0, __float_as_uint(x), C, 0xf, 0xf, true)); }
template <int C> DI unsigned dppu(unsigned x) { return __builtin_amdgcn_update_dpp(0, x, C, 0xf, 0xf, true); }
DI float max2f(float a, float b) { float r; asm("v_max_f32_e32 %0, %1, %2" : "=v"(r) : "v"(a), "v"(b)); return r; }
DI float max3f(float a, float b, float c) { float r; asm("v_max3_f32 %0, %1, %2, %3" : "=v"(r) : "v"(a), "v"(b), "v"(c)); return r; }
DI float row_sum(float x) { x += dppf<0x128>(x); x += dppf<0x124>(x); x += dppf<0x122>(x); x += dppf<0x121>(x); return x; }
DI float xrow_sum(float x) {
    auto a = __builtin_amdgcn_permlane16_swap(__float_as_uint(x), __float_as_uint(x), false, false);
    x = __uint_as_float(a[0]) + __uint_as_float(a[1]);
    auto b = __builtin_amdgcn_permlane32_swap(__float_as_uint(x), __float_as_uint(x), false, false);
    return __uint_as_float(b[0]) + __uint_as_float(b[1]);
}
DI float wave_sum(float x) { return xrow_sum(row_sum(x)); }
DI unsigned wave_maxu(unsigned x) {
    x = umx(x, dppu<0x128>(x)); x = umx(x, dppu<0x124>(x)); x = umx(x, dppu<0x122>(x)); x = umx(x, dppu<0x121>(x));
    auto a = __builtin_amdgcn_permlane16_swap(x, x, false, false);
    x = umx(a[0], a[1]);
    auto b = __builtin_amdgcn_permlane32_swap(x, x, false, false);
    return umx(b[0], b[1]);
}
DI float nmax3f(float a, float b, float c) { float r; asm("v_max3_f32 %0, -%1, -%2, -%3" : "=v"(r) : "v"(a), "v"(b), "v"(c)); return r; }
DI float nmax2f(float a, float b) { float r; asm("v_max_f32_e64 %0, -%1, -%2" : "=v"(r) : "v"(a), "v"(b)); return r; }
DI float mx2(float a, float b) { return __builtin_amdgcn_fmed3f(a, b, __builtin_inff()); }
template <bool MAX> DI float xrow_scatter(float a, float b, float c, float d) {
    const auto s = __builtin_amdgcn_permlane16_swap(__float_as_uint(a), __float_as_uint(b), false, false);
    const auto t = __builtin_amdgcn_permlane16_swap(__float_as_uint(c), __float_as_uint(d), false, false);
    const float u = MAX ? mx2(__uint_as_float(s[0]), __uint_as_float(s[1])) : __uint_as_float(s[0]) + __uint_as_float(s[1]);
    const float v = MAX ? mx2(__uint_as_float(t[0]), __uint_as_float(t[1])) : __uint_as_float(t[0]) + __uint_as_float(t[1]);
    const auto r = __builtin_amdgcn_permlane32_swap(__float_as_uint(u), __float_as_uint(v), false, false);
    return MAX ? mx2(__uint_as_float(r[0]), __uint_as_float(r[1])) : __uint_as_float(r[0]) + __uint_as_float(r[1]);
}
DI void xrow_gather(float w, float (&o)[4]) {
    const auto r = __builtin_amdgcn_permlane32_swap(__float_as_uint(w), __float_as_uint(w), false, false);
    const auto s = __builtin_amdgcn_permlane16_swap(r[0], r[0], false, false);
    const auto t = __builtin_amdgcn_permlane16_swap(r[1], r[1], false, false);
    o[0] = __uint_as_float(s[0]); o[1] = __uint_as_float(s[1]); o[2] = __uint_as_float(t[0]); o[3] = __uint_as_float(t[1]);
}
struct NoHook { DI void operator()(int, float&) const {} DI void operator()(int, float&, float&) const {} };
template <bool MUL, class H = NoHook> DI void softmax2(const f32x4 (&p)[2][2], float rs0, float rs1, bool hi, bf16x8 (&out)[4], H hook = H()) {
    float m[4];
#pragma unroll
    for (int i = 0; i < 2; i++) {
        const f32x4 &p0 = p[i][0], &p1 = p[i][1];
        m[2 * i] = max3f(max3f(p0[0], p0[1], p0[2]), max3f(p0[3], p1[0], p1[1]), max2f(p1[2], p1[3]));
        m[2 * i + 1] = max3f(nmax3f(p0[0], p0[1], p0[2]), nmax3f(p0[3], p1[0], p1[1]), nmax2f(p1[2], p1[3]));
    }
    asm volatile("s_nop 1" : "+v"(m[0]), "+v"(m[1]), "+v"(m[2]), "+v"(m[3]));
    hook(1, m[3]);
    const float rsr = hi ? rs1 : rs0;
    float a[4];
    xrow_gather(xrow_scatter<true>(m[0], m[1], m[2], m[3]) * (rsr * -LOG2E), a);
    f32x2 e[4][4];
    float s[4];
#pragma unroll
    for (int k = 0; k < 4; k++) {
        if (k == 2) hook(2, a[2], s[1]);
        const float sc = ((k >> 1) ? rs1 : rs0) * ((k & 1) ? -LOG2E : LOG2E);
#pragma unroll
        for (int j = 0; j < 4; j++) {
            const f32x4& q = p[k >> 1][j >> 1];
            const f32x2 t = __builtin_elementwise_fma((f32x2){q[2 * (j & 1)], q[2 * (j & 1) + 1]}, (f32x2){sc, sc}, (f32x2){a[k], a[k]});
            e[k][j] = (f32x2){__builtin_amdgcn_exp2f(t[0]), __builtin_amdgcn_exp2f(t[1])};
        }
        const f32x2 h = (e[k][0] + e[k][1]) + (e[k][2] + e[k][3]);
        s[k] = h[0] + h[1];
        if (k == 3) hook(3, s[k]);
    }
    float iv = __builtin_amdgcn_rcpf(xrow_scatter<false>(s[0], s[1], s[2], s[3]));
    if (MUL) iv *= rsr;
    xrow_gather(iv, a);
#pragma unroll
    for (int k = 0; k < 4; k++) {
        if (k == 2) hook(4, a[2]);
        const f32x2 c = {a[k], a[k]};
        const f32x2 x0 = e[k][0] * c, x1 = e[k][1] * c, x2 = e[k][2] * c, x3 = e[k][3] * c;
        out[k] = mk8(pk2(x0[0], x0[1]), pk2(x1[0], x1[1]), pk2(x2[0], x2[1]), pk2(x3[0], x3[1]));
    }
}
DI void swap16(v4u& x) {
    const auto a = __builtin_amdgcn_permlane16_swap(x[0], x[2], false, false), b = __builtin_amdgcn_permlane16_swap(x[1], x[3], false, false);
    x = (v4u){a[0], b[0], a[1], b[1]};
}
DI float dot2(unsigned a, unsigned b, float c) {
    return __builtin_amdgcn_fdot2_f32_bf16(__builtin_bit_cast(bf16x2, a), __builtin_bit_cast(bf16x2, b), c, false);
}
DI void st_gran(char* ws, unsigned off, unsigned tag, unsigned val) {
    __hip_atomic_store((gu64*)(ws + off), ((u64)tag << 32) | val, RLX_AGENT);
}
DI v2u ldg(rsrc_t rs, unsigned voff, unsigned soff) { return __builtin_amdgcn_raw_buffer_load_b64(rs, voff, soff, AUX_SC1); }
#define RARE(c) __builtin_expect(!!(c), 0)
#define FRESH() asm volatile("" ::: "memory")
template <int N> DI void sweep(rsrc_t rs, unsigned voff, unsigned soff, unsigned S, unsigned tag, unsigned (&v)[N]) {
#pragma nounroll
    for (unsigned spins = 0;;) {
        unsigned bad = 0;
        FRESH();
#pragma unroll
        for (int k = 0; k < N; ++k) { const v2u x = ldg(rs, voff, soff + k * S); v[k] = x[0]; bad |= x[1] ^ tag; }
        if (__all(bad == 0)) break;
        if (++spins > SPIN_CAP) break;
        __builtin_amdgcn_s_sleep(1);
    }
}
DI unsigned wmax(unsigned x) { return x; }
DI unsigned wmax(v2u x) { return umx(x[0], x[1]); }
DI unsigned wmax(v4u x) { return umx(umx(x[0], x[1]), umx(x[2], x[3])); }
template <int N, int SL = 0, class T, class F> DI void sweepz(F ld, T (&v)[N]) {
#pragma nounroll
    for (unsigned spins = 0;;) {
        FRESH();
        unsigned m = 0;
#pragma unroll
        for (int k = 0; k < N; ++k) v[k] = ld(k);
#pragma unroll
        for (int k = 0; k < N; ++k) m = umx(m, wmax(v[k]));
        if (__all(m != SENT)) break;
        if (++spins > SPIN_CAP) break;
        if (SL) __builtin_amdgcn_s_sleep(SL);
    }
}
DI f32x4 f4(v4u x) { return __builtin_bit_cast(f32x4, x); }
DI bool anynan(float x) { return __builtin_amdgcn_ballot_w64(x != x) != 0; }
DI void dot2c(float& c, unsigned a, unsigned b) { asm volatile("v_dot2c_f32_bf16 %0, %1, %2" : "+v"(c) : "v"(a), "v"(b)); }
DI void fold8(float& a, float b) {
    asm volatile("v_add_f32_dpp %0, %0, %0 row_ror:8 row_mask:0xf bank_mask:0x3\n\tv_add_f32_dpp %0, %1, %1 row_ror:8 row_mask:0xf bank_mask:0xc" : "+v"(a) : "v"(b));
}
template <int N, int SL, class T, class F, class S> DI void sweepn(F ld, T (&v)[N], S sum) {
    FRESH();
#pragma unroll
    for (int k = 0; k < N; ++k) v[k] = ld(k);
    if (RARE(anynan(sum()))) { sweepz<N, SL>(ld, v); sum(); }
}
DI unsigned f2s(float x) { return nsent(__float_as_uint(x)); }
#define OOB 0x4000000u
#define LD16(v, s) __builtin_amdgcn_raw_buffer_load_b128(rsWS, (v), (s), AUX_SC1)
#define LD4(v, s) __builtin_amdgcn_raw_buffer_load_b32(rsWS, (v), (s), AUX_SC1)
#define ST16(x, v, s) __builtin_amdgcn_raw_buffer_store_b128((x), rsWS, (v), (s), AUX_SC1)
#define ST4(x, v, s) __builtin_amdgcn_raw_buffer_store_b32((x), rsWS, (v), (s), AUX_SC1)
#define SLEEP(n) __builtin_amdgcn_s_sleep(n)
DI void poll_flags(rsrc_t rs, unsigned v, unsigned off, v2u want) {
    v2u x; unsigned n = SPIN_CAP;
    asm volatile("1:\n\tbuffer_load_dwordx2 %0, %2, %3, %4 offen sc1\n\ts_sub_u32 %1, %1, 1\n\ts_cbranch_scc1 2f\n\ts_waitcnt vmcnt(0)\n\t"
                 "v_cmp_ne_u64 vcc, %0, %5\n\ts_cbranch_vccnz 1b\n2:\n\ts_waitcnt vmcnt(0)" : "=&v"(x), "+s"(n) : "v"(v), "s"(rs), "s"(off), "v"(want) : "vcc", "scc", "memory");
}
DI void poll_words(unsigned& x, rsrc_t rs, unsigned v, unsigned off) {
    unsigned n = SPIN_CAP;
    asm volatile("s_branch 3f\n1:\n\tbuffer_load_dword %0, %2, %3, %4 offen sc1\n\ts_sub_u32 %1, %1, 1\n\ts_cbranch_scc1 2f\n3:\n\ts_waitcnt vmcnt(0)\n\t"
                 "v_cmp_eq_u32 vcc, -1, %0\n\ts_cbranch_vccnz 1b\n2:\n\ts_waitcnt vmcnt(0)" : "+v"(x), "+s"(n) : "v"(v), "s"(rs), "s"(off) : "vcc", "scc", "memory");
}
#define STAMP(i)
#define JIT(i) STAMP(i)
#define RINIT()
#define RDUMP()
#define KATTR
template <class F, int... Is> DI void static_for_impl(F&& f, std::integer_sequence<int, Is...>) { (f(std::integral_constant<int, Is>{}), ...); }
template <int N, class F> DI void static_for(F&& f) { static_for_impl(f, std::make_integer_sequence<int, N>{}); }

DI s16x4 bq(unsigned b0, unsigned b1, int j, int i) {
    const int ni = (j + 2) % 6;
    return tr_read(((ni & 1) ? b1 : b0) + (ni >> 1) * 8192 + (i >> 1) * 4096 + (i & 1) * 512);
}
template <class F> DI void group(f32x4 (&acc)[4][6], const bf16x8 (&A)[2][4], const bf16x8 (&B)[2], int j, F gap) {
    const int ni = (j + 2) % 6;
#pragma unroll
    for (int i = 0; i < 8; i++) {
        const int mi = i >> 1, ks = i & 1;
        if (ni < 4) asm volatile("v_mfma_f32_16x16x32_bf16 %0, %1, %2, %0" : "+v"(acc[mi][ni]) : "v"(B[ks]), "v"(A[ks][mi]));
        else asm volatile("v_mfma_f32_16x16x32_bf16 %0, %1, %2, %0" : "+v"(acc[mi][ni]) : "v"(A[ks][mi]), "v"(B[ks]));
        gap(i);
        SCHED();
    }
}
DI void img_write2(const unsigned (&ia)[4], const bf16x8 (&f)[4]) {
#pragma unroll
    for (int k = 0; k < 4; k++) {
        const v4u u = __builtin_bit_cast(v4u, f[k]);
        *(LAS v2u*)(ia[2 * (k & 1)] + (k >> 1) * 2048) = (v2u){u[0], u[1]};
        *(LAS v2u*)(ia[2 * (k & 1) + 1] + (k >> 1) * 2048) = (v2u){u[2], u[3]};
    }
}
DI float lds_bf(unsigned a) { return bf2f(*(const LAS u16*)(a)); }
#define MFMA_SETTLE2(p) asm volatile("s_nop 7\n\ts_nop 1" : "+v"(p[0][0]), "+v"(p[0][1]), "+v"(p[1][0]), "+v"(p[1][1]))

__global__ KATTR __launch_bounds__(512, 2) void fused_kernel(const u16* __restrict__ X, const u16* __restrict__ WQKV, const u16* __restrict__ WPHI,
                                                       const u16* __restrict__ WO, const u16* __restrict__ WR, const u16* __restrict__ WG,
                                                       const u16* __restrict__ WU, const u16* __restrict__ WD, u16* __restrict__ OUT,
                                                       char* __restrict__ ws) {
    extern __shared__ __attribute__((aligned(16))) char lds[];
    u64 pc0; unsigned clen;
    const int tid = threadIdx.x, l = tid & 63;
    asm volatile("s_getpc_b64 %0\n.Lfk_beg:\n\ts_lshr_b32 %1, %3, 3\n\tv_and_b32 v255, 0x1e0, %2\n\tv_add_lshl_u32 v255, v255, %1, 7\n\ts_mov_b32 %1, .Lfk_end-.Lfk_beg\n\t"
                 "v_min_u32 v255, %1, v255\n\tglobal_load_dword v255, v255, %0" : "=&s"(pc0), "=&s"(clen) : "v"(tid), "s"(blockIdx.x) : "v255", "scc");
    SCHED();
    const unsigned w = rfl(tid >> 6);
    const unsigned bid = blockIdx.x, xcd = (bid & 3) * 2 + ((bid >> 2) & 1), idx = bid >> 3;
    const unsigned rt = (xcd & 1) * 8 + (idx & 7), hp = (xcd >> 1) * 4 + (idx >> 3);
    const unsigned rt2 = (bid & 6) * 2 + (idx & 3), ct = (bid & 1) * 8 + (idx >> 2);
    const unsigned hq = (hp + 4) & 15;
    const unsigned rot = (rt >> 2) * 4;
    const unsigned rot2 = (2 * rt2 + 16 * ct) & 31;
    const unsigned ldsb = (unsigned)(size_t)lds;
    const int g = l >> 4, q4 = (l & 15) >> 2, p4 = l & 3, t16 = l & 15;
    const unsigned kr = l >> 3, gx = (g & 1) * 2 + (g >> 1);
    asm volatile("" ::"s"(X), "s"(WQKV), "s"(WPHI), "s"(WO), "s"(WR), "s"(WG), "s"(WU), "s"(WD), "s"(OUT), "s"(ws));
    unsigned tag;
#define READ_TAG() do { SCHED(); tag = *(const volatile CAS unsigned*)(ws + WS_EPOCH) + 1u; asm volatile("" : "+s"(tag)); } while (0)
    const rsrc_t rsWS = __builtin_amdgcn_make_buffer_rsrc((void*)ws, 0, WS_USED, 0x00020000);
    const rsrc_t rsX = __builtin_amdgcn_make_buffer_rsrc((void*)X, 0, 2048 * 1024 * 2, 0x00020000);
    const rsrc_t rsW = __builtin_amdgcn_make_buffer_rsrc((void*)WQKV, 0, 1024 * 3072 * 2, 0x00020000);
    const unsigned vA = (kr * 1024 + (((l & 7) ^ kr) * 8)) * 2;
    const unsigned vB0 = (kr * 3072 + (((l & 7) ^ (((kr >> 1) & 1) << 1)) * 8)) * 2;
    const unsigned vB1 = (kr * 3072 + (((l & 7) ^ ((((kr >> 1) & 1) | 2) << 1)) * 8)) * 2;
    const unsigned srow1 = rt * 128 + hp * 8 + (w & 3) * 2;
    v4u xr[4];
    if (w < 4) {
#pragma unroll
        for (int i = 0; i < 4; i++) xr[i] = ld16_asm(l * 16, rsX, srow1 * 2048 + i * 1024);
    }
    {
        const unsigned vBw = (w & 1) ? vB1 : vB0;
#pragma unroll
        for (int i = 0; i < 2; i++) dma16(ldsb + (w * 2 + i) * 1024, vA, rsX, (rt * 128 + w * 16 + i * 8) * 2048 + rot * 128);
#pragma unroll
        for (int i = 0; i < 3; i++) dma16(ldsb + 16384 + i * 8192 + w * 1024, vBw, rsW, (rot * 64 + w * 8) * 6144 + (i ? hp : hq) * 128 + i * 2048);
    }
    const f32x4 zero4 = {0.f, 0.f, 0.f, 0.f};
    const bf16x8 ones = mk8(0x3F803F80u, 0x3F803F80u, 0x3F803F80u, 0x3F803F80u);
    const unsigned lw = w & 3;
    const bool ffn = (ct & 7) != 3;
    const unsigned fm = idx < 12 ? idx : idx - 4;
    const unsigned fj = fm < 19 ? fm / 3 * 4 + (fm % 3 ? fm % 3 + 1 : 0) : fm == 19 ? 27 - (bid & 1) : fm < 27 ? 4 * fm - 79 : 26 + (bid & 1);
    const unsigned fb = !ffn ? 255u : (bid & 7) * 28 + fj;
    const unsigned cg = (fb + 5) & 15, kc = fb >> 4;
    const unsigned kq = fb / 56, cg1 = fb % 56, cg1u = (cg1 + 3) % 56;
    v4u wreg[24];
    RINIT();
    STAMP(0);

    const rsrc_t rsWO = __builtin_amdgcn_make_buffer_rsrc((void*)WO, 0, 1024 * 1024 * 2, 0x00020000);
    const unsigned vWO = kr * 2048 + (((l & 7) ^ (((kr >> 1) & 3) << 1)) * 16);
    unsigned vwo[4];
    auto wo_so = [&](unsigned b) { return (((2 * b & 30) + (b >> 4) + rot2) & 31) * 65536 + ct * 128; };
    const unsigned fw = (((t16 >> 1) & 3) << 2) | (((t16 >> 3) & 1) << 1) | (t16 & 1);
    unsigned ad;
    unsigned ia[4];
    bf16x8 sdv[4];
    auto pre_addr = [&]() {
#pragma unroll
        for (int k = 0; k < 4; k++) { ia[k] = ldsb + L_XB + lw * 8192 + (w >> 2) * 4096 + t16 * 128 + (((4 * k + g) ^ fw) * 8); PIN(ia[k]); }
        const unsigned rr = 4 * g + q4, fr = (((rr >> 1) & 3) << 2) | (((rr >> 3) & 1) << 1) | (rr & 1);
        ad = ldsb + L_XB + (w & 1) * 16384 + rr * 128 + ((((w >> 1) * 4 + p4) ^ fr) * 8);
        PIN(ad);
    };
    auto sd_reads = [&]() {
        unsigned sa = ldsb + L_SD + g * 8;
        PIN(sa);
#pragma unroll
        for (int s = 0; s < 4; s++) {
            const v2u a = *(const LAS v2u*)(sa + s * 64), b = *(const LAS v2u*)(sa + s * 64 + 32);
            sdv[s] = mk8(a[0], a[1], b[0], b[1]);
        }
    };
    bf16x8 v0[4], v1[4];
    auto v_reads = [&]() {
        unsigned vd = ldsb + L_V + (w & 1) * 8192 + l * 16;
        PIN(vd);
#pragma unroll
        for (int s = 0; s < 4; s++) { v0[s] = lds128(vd + s * 2048); v1[s] = lds128(vd + s * 2048 + 1024); }
    };
    auto kv_tile = [&](auto hook6) {
        const unsigned h2 = w & 1, ft = w >> 1;
        WAITL0();
        BAR();
        SCHED();
        bf16x8 pf[4];
#pragma unroll
        for (int s = 0; s < 4; s++) pf[s] = cat8(tr_read(ad + s * 4096), tr_read(ad + s * 4096 + 2048));
        SCHED();
        hook6();
        SCHED();
        f32x4 k0 = zero4, k1 = zero4, kz = zero4;
#pragma unroll
        for (int s = 0; s < 4; s++) { k0 = MFMA(pf[s], v0[s], k0); k1 = MFMA(pf[s], v1[s], k1); }
#pragma unroll
        for (int s = 0; s < 4; s++) kz = MFMA(pf[s], sdv[s], kz);
        const unsigned pbase = WS_P2 + (hp * 16 + rt) * P2_STRIDE;
        const v4u o0 = {nsent(pk2(k0[0], k0[1])), nsent(pk2(k0[2], k0[3])), nsent(pk2(k1[0], k1[1])), nsent(pk2(k1[2], k1[3]))};
        __builtin_amdgcn_raw_buffer_store_b128(o0, rsWS, l * 16, pbase + h2 * 4096 + ft * 1024, AUX_SC1);
        asm volatile("s_nop 1" :: "v"(o0));
        __builtin_amdgcn_raw_buffer_store_b64((v2u){nsent(pk2(kz[0], kz[1])), nsent(pk2(kz[2], kz[3]))}, rsWS, t16 == 0 ? g * 8 : OOB,
                                              pbase + 8192 + h2 * 128 + ft * 32, AUX_SC1);
        SCHED();
        BAR();
    };

    v4u pp[2];
    auto red = [&]() {
        const v4u* p = pp;
        float lo = xrow_scatter<false>(bflo(p[0][0]) + bflo(p[1][0]), bflo(p[0][1]) + bflo(p[1][1]), bflo(p[0][2]) + bflo(p[1][2]), bflo(p[0][3]) + bflo(p[1][3]));
        float hi = xrow_scatter<false>(bfhi(p[0][0]) + bfhi(p[1][0]), bfhi(p[0][1]) + bfhi(p[1][1]), bfhi(p[0][2]) + bfhi(p[1][2]), bfhi(p[0][3]) + bfhi(p[1][3]));
        lo += dppf<0x128>(lo); hi += dppf<0x128>(hi);
        return nsent(pk2(lo, hi));
    };
    auto pp_so = [&](int k) { return WS_P2 + (hp * 16 + k * 8) * P2_STRIDE + (rt >> 2) * 2048 + (lw & 1) * 1024 + (rt & 3) * 256 + (lw >> 1) * 128; };
    auto red_issue = [&]() {
        const unsigned vpp = kr * P2_STRIDE + (l & 7) * 16;
#pragma unroll
        for (int k = 0; k < 2; k++) asm volatile("buffer_load_dwordx4 %0, %1, %2, %3 offen sc1" : "=v"(pp[k]) : "v"(vpp), "s"(rsWS), "s"(pp_so(k)) : "memory");
        SCHED();
    };
    auto kv_reduce = [&]() {
        waitv<5>();
        PIN(pp[0]); PIN(pp[1]);
        if (!__all(umx(wmax(pp[0]), wmax(pp[1])) != SENT)) sweepz<2, 1>([&](int k) { return LD16(kr * P2_STRIDE + (l & 7) * 16, pp_so(k)); }, pp);
        STAMP(25);
        ST4(red(), (l & 8) ? OOB : (l & 7) * 16 + g * 4, WS_G3 + (hp * 4 + (rt >> 2)) * 2048 + (lw & 1) * 1024 + (rt & 3) * 256 + (lw >> 1) * 128);
    };

    const unsigned half = w >> 2;
    const unsigned R2 = rt2 * 128 + lw * 32 + half * 16 + t16;

    auto dloads = [&](int i0, int i1) {
        const rsrc_t rsD = __builtin_amdgcn_make_buffer_rsrc((void*)WD, 0, 1024 * 3584 * 2, 0x00020000);
#pragma unroll
        for (int i = i0; i < i1; i++)
            wreg[16 + i] = __builtin_amdgcn_raw_buffer_load_b128(rsD, kr * 4096 + (l & 7) * 16, (kc * 256 + lw * 64 + (i >> 1) * 16 + (i & 1)) * 2048 + cg * 128, W_AUX);
        FRESH();
    };
    unsigned wp1[2][4][8];
    v4u xres[2], wrv[2];
    v2u pl;
    unsigned nh;
    unsigned so, va0, va1, wb[4], vFl, sFl, swc, sG5, sG5r, sRep, nr, aXw, aXr, vG5;
    f32x4 acc2[2][4];
    bf16x8 sop[2];
    unsigned pv;
    auto place = [&]() {
        SCHED();
        PIN(pl);
        if (RARE(rfl(pl[1]) != tag)) { unsigned c[1]; sweep<1>(rsWS, 0, WS_PR + 1024, 0, tag, c); pl[0] = c[0]; }
        pv = rfl(pl[0]);
        nh = ((bid ^ (pv >> 4)) & 4) * 2048;
        asm volatile("" : "+s"(pv), "+s"(nh));
    };
    auto prep2 = [&]() {
        SCHED();
        vFl = t16 * 128 + g * 8;
        sFl = WS_F4 + (rt2 & 12) * 4096 + (rt2 & 3) * 2048 + nh;
        va0 = WS_P4 + ((rt2 & 7) * 8 + lw * 2 + half) * 65536 + (nh ^ (~rt2 & 4) * 2048) + l * 16; va1 = va0 ^ 65536;
        so = (half + rot2 + (rot2 & 24)) * 1024;
#pragma unroll
        for (int nt = 0; nt < 4; nt++) { wb[nt] = ldsb + L_WO + half * 65536 + (4 * g + q4) * 128 + ((nt ^ ((2 * g + (q4 >> 1)) & 3)) * 32) + p4 * 8; PIN(wb[nt]); }
        swap16(xres[0]); swap16(xres[1]); swap16(wrv[0]); swap16(wrv[1]);
        asm volatile("s_getreg_b32 %0, hwreg(HW_REG_XCC_ID, 0, 3)" : "=s"(sRep));
        sRep ^= pv & 4;
        nr = (sRep & 4) * 2048;
        sRep = WS_GK + (sRep + (idx & 1) * 8) * 2048;
        sG5r = WS_G5 + (rt2 & 7) * 65536 + lw * 16384 + half * 4096 + nh;
        sG5 = sG5r + ct * 256;
        aXw = ldsb + L_XB + w * 4096 + l * 16; aXr = aXw ^ 16384; vG5 = g == q4 ? t16 * 16 : OOB;
        asm volatile("" : "+s"(sG5), "+s"(sG5r), "+s"(sRep), "+s"(nr), "+s"(sFl), "+s"(nh), "+s"(so));
        PIN(aXw); PIN(aXr); PIN(vG5); PIN(va0); PIN(va1); PIN(vFl);
#pragma unroll
        for (int nt = 0; nt < 4; nt++) {
            const unsigned x0 = xres[nt >> 1][2 * (nt & 1)], x1 = xres[nt >> 1][2 * (nt & 1) + 1];
            acc2[0][nt] = (f32x4){bflo(x0), bfhi(x0), bflo(x1), bfhi(x1)};
            PIN(acc2[0][nt]);
        }
        unsigned m0 = p4 == 0 ? 0x3F803F80u : 0u, m1 = p4 == 1 ? ~0u : 0u;
        PIN(m0); PIN(m1);
#pragma unroll
        for (int i = 0; i < 2; i++) {
            v4u u;
#pragma unroll
            for (int j = 0; j < 4; j++) u[j] = (wrv[i][j] & m1) | m0;
            sop[i] = __builtin_bit_cast(bf16x8, u);
            PIN(sop[i]);
        }
        float sw = 0.f;
#pragma unroll
        for (int i = 0; i < 8; i++) sw = dot2(wrv[i >> 2][i & 3], 0x3F803F80u, sw);
        swc = f2s(xrow_sum(sw));
        PIN(swc);
        SCHED();
    };
    auto res_loads = [&]() {
        pl = ldg(rsWS, 0, WS_PR + 1024);
#pragma unroll
        for (int i = 0; i < 2; i++) {
            xres[i] = *(const v4u*)(X + (size_t)R2 * 1024 + ct * 64 + i * 32 + 8 * gx);
            wrv[i] = *(const v4u*)(WR + ct * 64 + i * 32 + 8 * gx);
        }
        FRESH();
    };

    if (w < 4) {
        const unsigned wm = w & 1, hh = w >> 1;
        f32x4 acc[4][6];
        bf16x8 wf[2], wfq[2];
        float mbt[4], rst[4], csv[2];
        f32x4 mbv[4], csq[2], csk[2];
#pragma unroll
        for (int i = 0; i < 4; i++)
#pragma unroll
            for (int j = 0; j < 6; j++) { acc[i][j] = zero4; PIN(acc[i][j]); }
        {
            const unsigned aoff0 = ldsb + (wm * 64 + t16) * 128 + (((0 + g) ^ (l & 7)) * 16);
            const unsigned aoff1 = ldsb + (wm * 64 + t16) * 128 + (((4 + g) ^ (l & 7)) * 16);
            const unsigned sk = ((q4 >> 1) & 1) | ((g & 1) << 1);
            const unsigned boffA = ldsb + 16384 + (8 * g + q4) * 128 + (((2 * hh) ^ sk) * 32) + p4 * 8;
            const unsigned boffB = ldsb + 16384 + (8 * g + q4) * 128 + (((2 * hh + 1) ^ sk) * 32) + p4 * 8;
            bf16x8 A[2][4], B[3][2];
            s16x4 lo;
            unsigned a0[3], a1[3], bA[3], bB[3];
#pragma unroll
            for (int i = 0; i < 3; i++) {
                a0[i] = aoff0 + i * STAGE_B; a1[i] = aoff1 + i * STAGE_B; bA[i] = boffA + i * STAGE_B; bB[i] = boffB + i * STAGE_B;
                PIN(a0[i]); PIN(a1[i]); PIN(bA[i]); PIN(bB[i]);
            }
            unsigned aT = ldsb + L_MB + (wm * 64 + t16) * 4, aS = ldsb + L_SD + (wm * 64 + t16) * 2, aM = ldsb + L_MB + wm * 256 + g * 16;
            unsigned aC = ldsb + L_CS + hh * 128 + g * 16, aV = ldsb + L_CS + (128 + hh * 32 + t16) * 4;
            unsigned aW = ldsb + L_WPHI + hh * 2048 + (4 * g + q4) * 64 + p4 * 8, aK = ldsb + L_KH + w * 2048 + l * 16;
            PIN(aT); PIN(aS); PIN(aM); PIN(aC); PIN(aV); PIN(aW); PIN(aK);
            pre_addr();
            READ_TAG();
            waitv<5>();
            PIN(xr[0]); PIN(xr[1]); PIN(xr[2]); PIN(xr[3]);
            {
                float s[2], q[2], x0[2];
#pragma unroll
                for (int rr = 0; rr < 2; rr++) {
                    x0[rr] = bflo(rfl(xr[2 * rr][0]));
                    s[rr] = 0.f; q[rr] = 0.f;
#pragma unroll
                    for (int i = 0; i < 4; i++) {
                        const float a = bflo(xr[2 * rr][i]) - x0[rr], b = bfhi(xr[2 * rr][i]) - x0[rr];
                        const float c = bflo(xr[2 * rr + 1][i]) - x0[rr], d = bfhi(xr[2 * rr + 1][i]) - x0[rr];
                        s[rr] += (a + b) + (c + d);
                        q[rr] += (a * a + b * b) + (c * c + d * d);
                    }
                }
                const float sq = row_sum(xrow_scatter<false>(s[0], s[1], q[0], q[1]));
#define RLF(x, n) __uint_as_float(__builtin_amdgcn_readlane(__float_as_uint(x), (n)))
                const float r0 = RLF(sq, 0), r1 = RLF(sq, 16), r2 = RLF(sq, 32), r3 = RLF(sq, 48);
                const float sm = (l ? r1 : r0) * (1.0f / 1024.0f), qm = (l ? r3 : r2) * (1.0f / 1024.0f);
                const float var = fmaxf(qm - sm * sm, 0.f);
                if (l < 2) st_gran(ws, WS_G0 + (srow1 + l) * 8, tag, pk2((l ? x0[1] : x0[0]) + sm, sqrtf(rbf(rbf(var) + 1e-5f))));
            }
            waitv<1>();
            WAITL0();
            BAR();
            SCHED();
            STAMP(1);
            auto rdB = [&](int u, int j, int i) {
                if (i & 1) B[j % 3][i >> 1] = cat8(lo, bq(bA[u], bB[u], j, i)); else lo = bq(bA[u], bB[u], j, i);
            };
            auto rdA = [&](int u, int i) { A[i & 1][i >> 1] = lds128(((i & 1) ? a1[u] : a0[u]) + (i >> 1) * 2048); };
            auto head = [&](int u) {
                group(acc, A, B[0], 0, [&](int i) { rdB(u, 1 + (i >> 2), i & 3); });
                group(acc, A, B[1], 1, [&](int i) { if (i < 4) rdB(u, 3, i); });
                group(acc, A, B[2], 2, [&](int i) { if (i < 4) rdB(u, 4, i); });
                group(acc, A, B[0], 3, [&](int i) { if (i < 4) rdB(u, 5, i); });
                WL0();
            };
#pragma unroll
            for (int i = 0; i < 4; i++) rdB(0, 0, i);
#pragma unroll
            for (int i = 0; i < 8; i++) rdA(0, i);
            SCHED();
#pragma nounroll
            for (unsigned t = 0; t < 5; t++) {
#pragma unroll
                for (int u = 0; u < 3; u++) {
                    head(u);
                    BAR();
                    JIT(32 + t * 3 + u);
                    SCHED();
                    group(acc, A, B[1], 4, [&](int i) { if (i < 4) rdB((u + 1) % 3, 0, i); });
                    group(acc, A, B[2], 5, [&](int i) { rdA((u + 1) % 3, i); });
                }
            }
            unsigned kw[4][4];
            f32x2 kx;
            auto kpiece = [&](int n) {
                if (n > 0) { kw[((n - 1 >> 2) + 2) & 3][(n - 1) & 3] = pk2(kx[0], kx[1]); PIN(kw[((n - 1 >> 2) + 2) & 3][(n - 1) & 3]); }
                if (n < 16) {
                    const int mi = ((n >> 2) + 2) & 3, j = n & 3;
                    const f32x4 &c = acc[mi][2 + (j >> 1)], &s_ = csk[j >> 1];
                    float x0 = __builtin_fmaf(mbt[mi], s_[2 * (j & 1)], c[2 * (j & 1)]); PIN(x0);
                    float x1 = __builtin_fmaf(mbt[mi], s_[2 * (j & 1) + 1], c[2 * (j & 1) + 1]); PIN(x1);
                    kx = (f32x2){x0, x1};
                }
                if (n == 5 || n == 9) *(LAS v4u*)(aK + (n == 9) * 1024) = (v4u){kw[2 + (n == 9)][0], kw[2 + (n == 9)][1], kw[2 + (n == 9)][2], kw[2 + (n == 9)][3]};
            };
            group(acc, A, B[0], 0, [&](int i) { rdB(0, 1 + (i >> 2), i & 3); });
            group(acc, A, B[1], 1, [&](int i) {
                if (i < 4) { rdB(0, 3, i); mbt[(i + 2) & 3] = ldsf(aT + ((i + 2) & 3) * 64); }
                else if (i < 6) csk[i - 4] = ldsf4(aC + 256 + (i - 4) * 64);
                else wf[i - 6] = cat8(tr_read(aW + (i - 6) * 32), tr_read(aW + (i - 6) * 32 + 1024));
            });
            group(acc, A, B[2], 2, [&](int i) { if (i < 4) rdB(0, 4, i); else mbv[i - 4] = ldsf4(aM + (i - 4) * 64); kpiece(i); });
            group(acc, A, B[0], 3, [&](int i) { if (i < 4) rdB(0, 5, i); if (i == 2 || i == 3) csv[i - 2] = ldsf(aV + (i - 2) * 64); kpiece(8 + i); });
            WL0();
            BAR();
            SCHED();
            kpiece(16);
            f32x4 p[2][2];
#pragma unroll
            for (int i = 0; i < 4; i++) p[i >> 1][i & 1] = MFMA(wf[i & 1], mk8(kw[i >> 1][0], kw[i >> 1][1], kw[i >> 1][2], kw[i >> 1][3]), zero4);
            SCHED();
            STAMP(2);
#pragma unroll
            for (int mi = 0; mi < 4; mi++) rst[mi] = lds_bf(aS + mi * 32);
#pragma unroll
            for (int a = 0; a < 2; a++)
#pragma unroll
                for (int dt = 0; dt < 2; dt++) {
                    const f32x4 x0 = acc[2 * a][4 + dt] + mbv[2 * a] * csv[dt], x1 = acc[2 * a + 1][4 + dt] + mbv[2 * a + 1] * csv[dt];
                    *(LAS v4u*)(aK + (L_V - L_KH) + w * 2048 + (a * 2 + dt) * 1024) = (v4u){pk2(x0[0], x0[1]), pk2(x0[2], x0[3]), pk2(x1[0], x1[1]), pk2(x1[2], x1[3])};
                }
            SCHED();
            rst[0] = __builtin_amdgcn_rcpf(rst[0]); rst[1] = __builtin_amdgcn_rcpf(rst[1]);
            bf16x8 f[4];
            struct HookC {
                decltype(v_reads)& vr; decltype(sd_reads)& sr;
                DI void operator()(int k, float& x) const { if (k == 1) { PIN(x); WAITL0(); BAR(); PIN(x); } if (k == 3) { PIN(x); vr(); sr(); PIN(x); } }
                DI void operator()(int, float&, float&) const {}
            };
            softmax2<true>(p, rst[0], rst[1], g >= 2, f, HookC{v_reads, sd_reads});
            img_write2(ia, f);
#pragma unroll
            for (int ft = 0; ft < 2; ft++) wfq[ft] = cat8(tr_read(aW + ft * 32 + 4096), tr_read(aW + ft * 32 + 5120));
            csq[0] = ldsf4(aC); csq[1] = ldsf4(aC + 64);
            STAMP(3);
            kv_tile([]() {});
            STAMP(4);
            SCHED();
            group(acc, A, B[1], 4, [&](int i) { if (i == 2 || i == 3) { rst[i] = __builtin_amdgcn_rcpf(rst[i]); PIN(rst[i]); } });
            group(acc, A, B[2], 5, [&](int) {});
            asm volatile("s_nop 7\n\ts_nop 1");
            SCHED();
        }
        bf16x8 phq[2][4];
        v4u kg[6];
        unsigned vO = l * 16, vZ3 = g * 16, sK3 = WS_G3 + (hq * 2 + hh) * 4096, sZ3 = WS_GZ + (hq * 2 + hh) * 128;
        auto kg_ld = [&](int k) { return k < 2 ? LD16(vZ3, sZ3 + k * 64) : LD16(vO, sK3 + (k - 2) * 1024); };
        {
            f32x4 pq[2][2][2];
#pragma unroll
            for (int mi = 0; mi < 4; mi++) {
                const f32x4 x0 = acc[mi][0] + mbt[mi] * csq[0], x1 = acc[mi][1] + mbt[mi] * csq[1];
                const bf16x8 qf = mk8(pk2(x0[0], x0[1]), pk2(x0[2], x0[3]), pk2(x1[0], x1[1]), pk2(x1[2], x1[3]));
                pq[mi >> 1][mi & 1][0] = MFMA(wfq[0], qf, zero4); pq[mi >> 1][mi & 1][1] = MFMA(wfq[1], qf, zero4);
            }
            MFMA_SETTLE2(pq[0]); MFMA_SETTLE2(pq[1]);
            softmax2<false>(pq[0], rst[0], rst[1], g >= 2, phq[0]);
#pragma unroll
            for (int i = 0; i < 4; i++) PIN(phq[0][i]);
            SCHED();
            red_issue();
            res_loads();
            SCHED();
            kv_reduce();
            SCHED();
            BAR();
            SCHED();
            softmax2<false>(pq[1], rst[2], rst[3], g >= 2, phq[1]);
#pragma unroll
            for (int i = 0; i < 4; i++) { PIN(phq[0][i]); PIN(phq[1][i]); }
            SCHED();
        }
        STAMP(5);
        v2u fv = {tag ^ 0x5bd1e995u, tag};
        place();
        PIN(xres[0]); PIN(xres[1]); PIN(wrv[0]); PIN(wrv[1]);
        unsigned sF = WS_F4 + (rt & 12) * 4096 + (rt & 3) * 2048 + hq * 128 + w * 8 + nh, vF = l == 0 ? 0 : OOB;
        {
            unsigned sO[4];
#pragma unroll
            for (int mi = 0; mi < 4; mi++) sO[mi] = WS_P4 + ((rt & 7) * 8 + wm * 4 + mi) * 65536 + (hq >> 2) * 16384 + ((hq & 3) * 2 + hh) * 1024 + (nh ^ (~rt & 4) * 2048);
            asm volatile("" : "+s"(sO[0]), "+s"(sO[1]), "+s"(sO[2]), "+s"(sO[3]), "+s"(sF));
            PIN(fv); PIN(vF); PIN(vO); PIN(vZ3);
            SCHED();
            bf16x8 kvA[2][2];
            v4u ov[4];
#define PH(mi, s) phq[(mi) >> 1][2 * ((mi) & 1) + (s)]
            f32x4 n0[4], n1[4];
            float id[4];
            auto num = [&](int mi) {
                n0[mi] = MFMA(kvA[0][0], PH(mi, 0), zero4); n1[mi] = MFMA(kvA[0][1], PH(mi, 0), zero4);
                n0[mi] = MFMA(kvA[1][0], PH(mi, 1), n0[mi]); n1[mi] = MFMA(kvA[1][1], PH(mi, 1), n1[mi]);
            };
            sweepz<6, 4>(kg_ld, kg);
            STAMP(6);
            f32x4 dn[4];
#pragma unroll
            for (int mi = 0; mi < 4; mi++) dn[mi] = MFMA(__builtin_bit_cast(bf16x8, kg[0]), PH(mi, 0), zero4);
            SCHED();
#pragma unroll
            for (int mi = 0; mi < 4; mi++) {
                dn[mi] = MFMA(__builtin_bit_cast(bf16x8, kg[1]), PH(mi, 1), dn[mi]);
                SCHED();
                const int s = mi >> 1, dt = mi & 1;
                kvA[s][dt] = mk8(kg[2 + 2 * s][2 * dt], kg[2 + 2 * s][2 * dt + 1], kg[3 + 2 * s][2 * dt], kg[3 + 2 * s][2 * dt + 1]);
                PIN(kvA[s][dt]);
                SCHED();
            }
#pragma unroll
            for (int i = 0; i < 2; i++) {
                num(i);
                const unsigned d = pk2(dn[2 * i][0], dn[2 * i + 1][0]), e = pk2(bflo(d) + 1e-6f, bfhi(d) + 1e-6f);
                id[2 * i] = __builtin_amdgcn_rcpf(bflo(e)); id[2 * i + 1] = __builtin_amdgcn_rcpf(bfhi(e));
                PIN(id[2 * i]); PIN(id[2 * i + 1]);
                SGB(0x8, 1); SGB(0x2, 3); SGB(0x8, 1); SGB(0x2, 2); SGB(0x8, 1); SGB(0x2, 3); SGB(0x8, 1); SGB(0x2, 2);
                SCHED();
            }
#pragma unroll
            for (int mi = 0; mi < 4; mi++) {
                if (mi < 2) num(mi + 2);
                const f32x4 a = n0[mi] * id[mi], b = n1[mi] * id[mi];
                ov[mi] = (v4u){pk2(a[0], a[1]), pk2(a[2], a[3]), pk2(b[0], b[1]), pk2(b[2], b[3])};
                ST16(ov[mi], vO, sO[mi]);
                if (mi < 2) { SGB(0x8, 1); SGB(0x2, 2); SGB(0x8, 1); SGB(0x2, 2); SGB(0x8, 1); SGB(0x2, 2); SGB(0x8, 1); SGB(0x2, 2); }
                SCHED();
            }
#pragma unroll
            for (int mi = 0; mi < 4; mi++) PIN(ov[mi]);
        }
        waitv<0>();
        __builtin_amdgcn_raw_buffer_store_b64(fv, rsWS, vF, sF, AUX_SC1);
        STAMP(7);
    } else {
        const unsigned pw = lw & 1;
        const rsrc_t rsG = __builtin_amdgcn_make_buffer_rsrc((void*)WG, 0, 1024 * 3584 * 2, 0x00020000);
        const rsrc_t rsU = __builtin_amdgcn_make_buffer_rsrc((void*)WU, 0, 1024 * 3584 * 2, 0x00020000);
        unsigned vwl = kr * 57344 + (l & 7) * 16;
        unsigned sg = (kq * 256 + lw * 64) * 7168 + cg1 * 128, su = (kq * 256 + lw * 64) * 7168 + cg1u * 128;
        auto wloads = [&](int n0, int n1) {
#pragma unroll
            for (int n = n0; n < n1; n++)
                asm volatile("buffer_load_dwordx4 %0, %1, %2, %3 offen nt" : "=v"(wreg[n]) : "v"(vwl), "s"(n < 8 ? rsG : rsU), "s"((n < 8 ? sg : su) + (n & 7) * 7168));
        };
        auto dl_asm = [&]() {
            const rsrc_t rsD = __builtin_amdgcn_make_buffer_rsrc((void*)WD, 0, 1024 * 3584 * 2, 0x00020000);
#pragma unroll
            for (int i = 0; i < DN0; i++)
                asm volatile("buffer_load_dwordx4 %0, %1, %2, %3 offen nt" : "=v"(wreg[16 + i]) : "v"(kr * 4096 + (l & 7) * 16), "s"(rsD),
                             "s"((kc * 256 + lw * 64 + (i >> 1) * 16 + (i & 1)) * 2048 + cg * 128));
        };
        unsigned aK, aW, aS;
        auto k_pre = [&]() {
            pre_addr();
            aK = ldsb + L_KH + lw * 2048 + l * 16; aW = ldsb + L_WPHI + (lw >> 1) * 2048 + (4 * g + q4) * 64 + p4 * 8; aS = ldsb + L_SD + (pw * 64 + 32 + t16) * 2;
            PIN(aK); PIN(aW); PIN(aS);
        };
        auto kfeat = [&](auto hook) {
            bf16x8 wf[2];
            float rst[2];
#pragma unroll
            for (int ft = 0; ft < 2; ft++) {
                wf[ft] = cat8(tr_read(aW + ft * 32), tr_read(aW + ft * 32 + 1024));
                rst[ft] = __builtin_amdgcn_rcpf(lds_bf(aS + ft * 32));
            }
            sd_reads();
            PIN(wf[0]); PIN(wf[1]); PIN(rst[0]); PIN(rst[1]);
            SCHED();
            BAR();
            SCHED();
            const bf16x8 kf0 = lds128(aK), kf1 = lds128(aK + 1024);
            SCHED();
            hook(-1, rst[0]);
            hook(0, rst[0]);
            SCHED();
            f32x4 p[2][2];
            p[0][0] = MFMA(wf[0], kf0, zero4); p[0][1] = MFMA(wf[1], kf0, zero4);
            p[1][0] = MFMA(wf[0], kf1, zero4); p[1][1] = MFMA(wf[1], kf1, zero4);
            MFMA_SETTLE2(p);
            bf16x8 f[4];
            softmax2<true>(p, rst[0], rst[1], g >= 2, f, hook);
            img_write2(ia, f);
            SCHED();
            hook(5, rst[0]);
            SCHED();
            STAMP(3);
            kv_tile([&]() { hook(6, rst[0]); });
            STAMP(4);
        };
        auto wblk = [&](int k) { return k == 0 ? 22 + lw : k == 1 ? (lw < 3 ? 26 + lw : 4) : k < 6 ? 4 * k - 3 + lw : k == 6 ? lw : (lw < 3 ? 29 + lw : 21); };
        auto wo_init = [&]() {
#pragma unroll
            for (int i = 0; i < 4; i++) { vwo[i] = vWO + i * (16384 - 1024); PIN(vwo[i]); }
        };
        unsigned hs[8];
        auto wl_pre = [&]() { asm volatile("" : "+s"(sg), "+s"(su)); PIN(vwl); };
        auto wo_pre = [&]() {
            wo_init();
#pragma unroll
            for (int k = 0; k < 8; k++) { hs[k] = wo_so(wblk(k)); asm volatile("" : "+s"(hs[k])); }
        };
        auto wo_rng = [&](int k0, int k1) {
#pragma unroll
            for (int k = k0; k < k1; k++) dma16x4(ldsb + L_WO + wblk(k) * 4096, vwo, rsWO, hs[k]);
        };
        auto pf = [&](int i) {
            if (i == 0) { wloads(0, 6); SCHED(); if (lw < 2) wo_pre(); SCHED(); }
            if (i < 3 && !ffn) wo_rng(i, i + 1);
            if (i == 1 || i == 2) wloads(6 * i, i == 1 ? 12 : 16);
            if (i == 6) dl_asm();
        };
        struct Hook {
            decltype(pf)& f; decltype(v_reads)& vr;
            DI void operator()(int k, float& x) const { PIN(x); if (k == 1) BAR(); if (k == 3) vr(); f(3 + k); PIN(x); }
            DI void operator()(int k, float& x, float& y) const { PIN(y); f(3 + k); PIN(x); }
        };
        auto tail16 = [&]() {
            kfeat(Hook{pf, v_reads});
            if (ffn) wo_rng(0, 3);
            wo_rng(3, 4);
            STAMP(27);
            if (ffn) waitv<18>(); else waitv<6>();
#pragma unroll
            for (int n = 0; n < 16 + DN0; n++) PIN(wreg[n]);
        };
        auto vT23 = [&]() {
            const unsigned n = (idx * 2 + pw) * 64 + l, r = n / 12, c = n % 12;
            return idx < 24 ? ((r & 127) + (r >> 7) * 256 + 128) * 6144 + (c >> 2) * 2048 + (((xcd >> 1) * 4 + (c >> 2 ? 0 : 4) + (c & 3)) & 15) * 128 : OOB;
        };
        if (lw < 2) {
            const unsigned sA = (rt * 128 + lw * 64) * 2048, sB = lw * 32 * 6144 + hp * 128, sBq = lw * 32 * 6144 + hq * 128;
            unsigned buf = 1;
            unsigned va[4], vb[4];
#pragma unroll
            for (int i = 0; i < 4; i++) { va[i] = vA + i * (16384 - 1024); vb[i] = ((i & 1) ? vB1 : vB0) + i * (49152 - 1024); PIN(va[i]); PIN(vb[i]); }
            auto stage = [&](unsigned s, unsigned bf, int i0, int i1) {
                const unsigned kt = (s + rot) & 15;
                const unsigned lb = ldsb + bf * STAGE_B;
                const unsigned sa = sA + kt * 128, sb = kt * 393216;
                if (i0 <= 0 && 0 < i1) dma16x4(lb + lw * 8192, va, rsX, sa);
                if (i0 <= 1 && 1 < i1) dma16x4(lb + lw * 8192 + 4096, va, rsX, sa + 65536);
                if (i0 <= 2 && 2 < i1) dma16x4(lb + 16384 + 8192 + lw * 4096, vb, rsW, sb + sB + 2048);
                if (i0 <= 3 && 3 < i1) dma16x4(lb + 16384 + 16384 + lw * 4096, vb, rsW, sb + sB + 4096);
                if (i0 <= 4 && 4 < i1) dma16x4(lb + 16384 + lw * 4096, vb, rsW, sb + sBq);
            };
            stage(1, 1, 0, 4);
            SCHED();
            unsigned vt = vT23();
            PIN(vt);
            wl_pre(); k_pre();
            waitv<16>(); BAR();
            stage(1, 1, 4, 5);
            touch(vt, rsW, (xcd & 1) * 512 * 6144);
            waitv<1>(); BAR();
            stage(2, 2, 0, 5); stage(3, 0, 0, 3);
            waitv<12>(); BAR();
            stage(3, 0, 3, 5); stage(4, 1, 0, 4);
            waitv<16>(); BAR();
            stage(4, 1, 4, 5);
            buf = 2;
#pragma nounroll
            for (unsigned s = 5; s < 16; s++) {
                if (s > 5) { waitv<20>(); BAR(); }
                stage(s, buf, 0, 5);
                buf = buf == 2 ? 0 : buf + 1;
            }
            waitv<20>(); BAR();
            pf(0);
            READ_TAG();
            if (ffn) waitv<6>(); else waitv<10>();
            BAR();
            pf(1);
            tail16();
        } else {
            SCHED();
            unsigned v3;
            const unsigned h3 = (xcd >> 1) * 4 + 3, kv3 = !(xcd & 2), wv = idx * 2 + pw;
            auto line3 = [&](unsigned per, unsigned rows, auto rowf) {
                const unsigned n = wv * (kv3 ? per : (per + 2) / 3) + l, r3 = kv3 ? n / 3 : n, c3 = kv3 ? n % 3 : 0;
                return (idx < 24 && l < (kv3 ? per : (per + 2) / 3) && r3 < rows) ? rowf(r3) * 6144 + c3 * 2048 + ((c3 ? h3 : h3 + 4) & 15) * 128 : OOB;
            };
            v3 = line3(32, 512, [](unsigned r) { return r; });
            touch(line3(16, 256, [](unsigned r) { return (r & 127) + (r >> 7) * 256; }), rsW, (xcd & 1) * 512 * 6144);
            {
                const rsrc_t rsP = __builtin_amdgcn_make_buffer_rsrc((void*)WPHI, 0, 32 * 1024 * 2, 0x00020000);
#pragma unroll
                for (int i = 0; i < 4; i++) dma16(ldsb + L_WPHI + pw * 4096 + i * 1024, l * 16, rsP, (pw ? hq : hp) * 4096 + i * 1024);
            }
            v4u sv = {SENT, SENT, SENT, SENT};
            unsigned vc4 = l < 16 ? WS_G3 + (hp * 4 + (rt >> 2)) * 2048 + pw * 1024 + (rt & 3) * 256 + l * 16
                         : l < 24 ? WS_P2 + (hp * 16 + rt) * P2_STRIDE + 8192 + pw * 128 + (l - 16) * 16
                         : (l < 32 && pw == 0 && (rt & 7) == 0) ? WS_GZ + (hp * 2 + (rt >> 3)) * 128 + (l - 24) * 16
                         : l >= 56 ? WS_F4 + (rt & 12) * 4096 + (rt & 3) * 2048 + hq * 128 + pw * 8192 + (l & 7) * 16 : OOB;
            auto clears = [&](int i0, int i1) {
#pragma unroll
                for (int i = i0; i < i1; i++) {
                    if (i < 4) ST16(sv, l * 16, WS_P2 + (hp * 16 + rt) * P2_STRIDE + pw * 4096 + i * 1024);
                    if (i == 4) ST16(sv, vc4, 0);
                    if (i > 4) ST16(sv, l * 16, WS_TL + (bid * 4 + pw * 2 + i - 5) * 1024);
                }
                FRESH();
            };
            PIN(vc4); PIN(sv);
            unsigned ca = ldsb + 16384 + pw * 8192 + (8 * g + q4) * 128 + (((rt & 3) ^ (((q4 >> 1) & 1) | ((g & 1) << 1))) * 32) + p4 * 8;
            PIN(ca);
            bf16x8 cf[4];
            f32x4 cd[2] = {zero4, zero4};
            auto cs_read = [&](int st) {
#pragma unroll
                for (int i = 0; i < 4; i++) cf[i] = cat8(tr_read(ca + (st % 3) * STAGE_B + i * 4096), tr_read(ca + (st % 3) * STAGE_B + i * 4096 + 512));
                SCHED();
            };
            READ_TAG();
            waitv<5>();
            BAR();
            SCHED();
            cs_read(0);
            FRESH();
            const rsrc_t rsC = __builtin_amdgcn_make_buffer_rsrc((void*)pc0, 0, clen, 0x00020000);
            auto code_pf = [&]() { touch(l < 6 ? l * 128 : OOB, rsC, (idx * 2 + pw) * 768); };
            v4u gl[4];
            float s0 = 0.f, s1 = 0.f;
            unsigned bad;
            v2u gs, gc;
            unsigned vgc = WS_GC + (pw ? hq : hp) * 256 + (l & 31) * 8;
            unsigned lat[2] = {~0u, ~0u};
            unsigned vgl = (l < 48 ? l : 0) * 16;
            unsigned aw0 = ldsb + L_CS + pw * 384 + (vgl >> 1), aw1 = ldsb + L_MB + (pw * 64 + l) * 4, aw2 = ldsb + L_SD + (pw * 64 + l) * 2;
            static_for<15>([&](auto tc) {
                constexpr int t = decltype(tc)::value;
                WAITL0();
                JIT(32 + t);
                BAR();
                SCHED();
                if constexpr (t < 4) {
                    __builtin_amdgcn_s_setprio(2);
#pragma unroll
                    for (int i = 0; i < 4; i++) cd[i & 1] = MFMA(ones, cf[(i & 1) * 2 + (i >> 1)], cd[i & 1]);
                    SCHED();
                    __builtin_amdgcn_s_setprio(0);
                    if constexpr (t < 3) cs_read(t + 1);
                    else __builtin_amdgcn_raw_buffer_store_b64((v2u){__float_as_uint(g ? cd[1][0] : cd[0][0]), tag}, rsWS, (g == 1 || g + pw == 0) ? g * 512 + t16 * 8 : OOB,
                                                               WS_G1 + ((hp * 4 + (rt >> 2)) * 192 + pw * 64 + (rt & 3) * 16) * 8, AUX_SC1);
                }
                if constexpr (t == 0) touch(v3, rsW, (~xcd & 1) * 512 * 6144);
                if constexpr (t == 12) code_pf();
                if constexpr (t == 8) {
                    waitv<0>();
                    __builtin_amdgcn_raw_buffer_store_b64((v2u){0u, tag}, rsWS, l == 0 ? 0 : OOB, WS_GC + ((hp * 2 + pw) * 16 + rt) * 8, AUX_SC1);
                }
                if constexpr (t == 8 || t == 9) {
#pragma unroll
                    for (int r = (t - 8) * 2; r < (t - 8) * 2 + 2; r++)
                        asm volatile("buffer_load_dwordx4 %0, %1, %2, %3 offen sc1" : "=v"(gl[r]) : "v"(vgl), "s"(rsWS), "s"(WS_G1 + ((hp * 4 + r) * 192 + pw * 96) * 8) : "memory");
                }
                if constexpr (t == 6) {
                    asm volatile("buffer_load_dwordx2 %0, %1, %2, %3 offen sc1" : "=v"(gs) : "v"(l * 8), "s"(rsWS), "s"(WS_G0 + (rt * 128 + pw * 64) * 8) : "memory");
                }
                if constexpr (t == 11) {
                    waitv<0>();
                    PIN(gs);
                    bad = gs[1] ^ tag;
#pragma unroll
                    for (int r = 0; r < 4; r++) {
                        PIN(gl[r]);
                        bad |= (gl[r][1] ^ tag) | (gl[r][3] ^ tag);
                        s0 += __uint_as_float(gl[r][0]); s1 += __uint_as_float(gl[r][2]);
                    }
                    PIN(s0); PIN(s1); PIN(bad);
                }
                if constexpr (t == 12) {
                    if (RARE(!__all(bad == 0))) {
                        s0 = 0.f; s1 = 0.f;
#pragma nounroll
                        for (unsigned r = 0; r < 4; r++) {
                            unsigned a[2];
                            sweep<2>(rsWS, vgl, WS_G1 + ((hp * 4 + r) * 192 + pw * 96) * 8, 8, tag, a);
                            s0 += __uint_as_float(a[0]); s1 += __uint_as_float(a[1]);
                        }
                        unsigned c[1];
                        sweep<1>(rsWS, l * 8, WS_G0 + (rt * 128 + pw * 64) * 8, 0, tag, c);
                        gs[0] = c[0];
                    }
                    *(LAS f32x2*)(aw0) = (f32x2){-s0, -s1};
                    stsf(aw1, bflo(gs[0]));
                    *(LAS u16*)(aw2) = (u16)(gs[0] >> 16);
                    asm volatile("buffer_load_dwordx2 %0, %1, %2, 0 offen sc1" : "=v"(gc) : "v"(vgc), "s"(rsWS) : "memory");
                    SCHED();
                }
                if constexpr (t == 6) { if (RARE(bid * 2 + pw == 16)) ST16(sv, l < 32 ? g * 8192 + t16 * 16 : OOB, WS_PR); }
                if constexpr (t >= 8 && t < 12) {
                    if (RARE(bid * 2 + pw == 16)) {
                        u64 c0, c1; unsigned x;
                        asm volatile(".p2align 6\n\ts_memtime %0\n\ts_load_dword %2, %3, %4\n\ts_waitcnt lgkmcnt(0)\n\ts_memtime %1\n\ts_waitcnt lgkmcnt(0)"
                                     : "=&s"(c0), "=&s"(c1), "=&s"(x) : "s"(ws), "i"(WS_PR + (t & 1) * 8192 + (t & 2) * 64) : "memory");
                        lat[t & 1] = __builtin_elementwise_min(lat[t & 1], (unsigned)c1 - (unsigned)c0);
                        if constexpr (t == 11) {
                            asm volatile("s_getreg_b32 %0, hwreg(HW_REG_XCC_ID, 0, 3)" : "=s"(x));
                            __builtin_amdgcn_raw_buffer_store_b64((v2u){(lat[1] < lat[0] ? 0x44 : 0) ^ (x & 4), tag}, rsWS, l == 0 ? 0 : OOB, WS_PR + 1024, AUX_SC1);
                        }
                    }
                }
                if constexpr (t == 5) k_pre();
                if constexpr (t == 6) { wl_pre(); wo_pre(); }
                if constexpr (t == 7) { PIN(vgc); PIN(vgl); PIN(aw0); PIN(aw1); PIN(aw2); }
                clears(t < 7 ? t : 7, t < 7 ? t + 1 : 7);
                if constexpr (t == 13) {
                    waitv<0>();
                    PIN(gc);
                    if (RARE(!__all(gc[1] == tag))) { unsigned c[1]; sweep<1>(rsWS, vgc, 0, 0, tag, c); }
                    SCHED();
                    pf(0);
                }
                if constexpr (t == 14) pf(1);
            });
            tail16();
        }
        STAMP(28);
        auto perm1 = [&]() {
#pragma unroll
            for (int ip = 0; ip < 4; ip++)
#pragma unroll
                for (int c = 0; c < 4; c++) {
#pragma unroll
                    for (int m = 0; m < 2; m++) {
                        const unsigned a = wreg[m * 8 + 2 * ip][c], b = wreg[m * 8 + 2 * ip + 1][c];
                        wp1[m][ip][2 * c] = __builtin_amdgcn_perm(b, a, 0x05040100);
                        wp1[m][ip][2 * c + 1] = __builtin_amdgcn_perm(b, a, 0x07060302);
                    }
                }
#pragma unroll
            for (int ip = 0; ip < 4; ip++)
#pragma unroll
                for (int c = 0; c < 8; c++) { PIN(wp1[0][ip][c]); PIN(wp1[1][ip][c]); }
            SCHED();
            STAMP(29);
        };
        if (RARE(lw == 0 && (rt & 7) == 0)) {
            __builtin_amdgcn_s_setprio(1);
            sweepz<2, 1>([&](int k) { return LD16(kr * P2_STRIDE + (l & 7) * 16, WS_P2 + (hp * 16 + k * 8) * P2_STRIDE + 8192 + (rt >> 3) * 128); }, pp);
            ST4(red(), (l & 8) ? OOB : ((l >> 2) & 1) * 64 + ((l & 1) * 2 + (g >> 1)) * 16 + ((l >> 1) & 1) * 8 + (g & 1) * 4, WS_GZ + (hp * 2 + (rt >> 3)) * 128);
            __builtin_amdgcn_s_setprio(0);
        }
        SCHED();
        perm1();
        BAR();
        STAMP(30);
        res_loads();
        wo_rng(4, 8);
        STAMP(31);
        waitv<0>();
        PIN(xres[0]); PIN(xres[1]); PIN(wrv[0]); PIN(wrv[1]);
        place();
        STAMP(7);
    }
    prep2();
    if (w == 0) { SLEEP(4); poll_flags(rsWS, vFl, sFl, (v2u){tag ^ 0x5bd1e995u, tag}); }
    BAR();
    FRESH();
    SCHED();
    STAMP(8);
    bf16x8 bb[2][4];
    {
        auto aload = [&](int st, int m2) {
            const v4u r = LD16(m2 ? va1 : va0, so);
            if (m2 == 0) so = ((so | 8192) + 2048) & 0xDC00;
            return r;
        };
        constexpr int PD = 2;
        v4u af[PD][2];
#pragma unroll
        for (int i = 0; i < PD; i++) {
#pragma unroll
            for (int m2 = 1; m2 >= 0; m2--) af[i][m2] = aload(i, m2);
            SCHED();
        }
#pragma unroll
        for (int nt = 0; nt < 4; nt++) bb[0][nt] = cat8(tr_read(wb[nt]), tr_read(wb[nt] + 2048));
#pragma unroll
        for (int nt = 0; nt < 4; nt++) acc2[1][nt] = zero4;
        SCHED();
#pragma unroll
        for (int st = 0; st < 16; st++) {
            if (st + 1 < 16) {
#pragma unroll
                for (int nt = 0; nt < 4; nt++)
                    bb[(st + 1) % (2)][nt] = cat8(tr_read(wb[nt] + (st + 1) * 4096), tr_read(wb[nt] + (st + 1) * 4096 + 2048));
            }
#pragma unroll
            for (int mm = 0; mm < 2; mm++) {
                const int m2 = mm ^ 1;
                const bf16x8 a = __builtin_bit_cast(bf16x8, af[st % PD][m2]);
#pragma unroll
                for (int nt = 0; nt < 4; nt++) acc2[m2][nt] = MFMA(bb[st % (2)][nt], a, acc2[m2][nt]);
            }
            if (st + PD < 16) {
#pragma unroll
                for (int m2 = 1; m2 >= 0; m2--) af[st % PD][m2] = aload(PD + st, m2);
            }
#pragma unroll
            for (int i = 0; i < 4; i++) { SGB(0x8, 1); SGB(0x100, 2); }
            SCHED();
        }
    }
    STAMP(9);
#pragma unroll
    for (int nt = 0; nt < 4; nt++) stsf4(aXw + nt * 1024, acc2[1][nt]);
    SCHED();
    if (half) dloads(DN0, 8);
    SCHED();
    WAITL0();
    if (half) __builtin_amdgcn_s_setprio(1);
    BAR();
    SCHED();
    v4u hpk[2];
    {
        f32x4 pa[4];
#pragma unroll
        for (int nt = 0; nt < 4; nt++) pa[nt] = ldsf4(aXr + nt * 1024);
        SCHED();
#pragma unroll
        for (int i = 0; i < 8; i++) {
            const f32x4 a = acc2[0][i >> 1] + pa[i >> 1];
            hpk[i >> 2][i & 3] = pk2(a[2 * (i & 1)], a[2 * (i & 1) + 1]);
        }
        const bf16x8 h0 = __builtin_bit_cast(bf16x8, hpk[0]), h1 = __builtin_bit_cast(bf16x8, hpk[1]);
        f32x4 gq = MFMA(h0, h0, zero4), sd = MFMA(sop[0], h0, zero4);
        gq = MFMA(h1, h1, gq); sd = MFMA(sop[1], h1, sd);
        float q0 = (l & 1) ? gq[1] : gq[0], q1 = (l & 1) ? gq[3] : gq[2];
        PIN(q0); PIN(q1);
        const v4u pv = {f2s(sd[0]), f2s((l & 2) ? q1 : q0), f2s(sd[1]), swc};
        ST16(pv, vG5, sG5);
        asm volatile("s_nop 1" :: "v"(pv));
    }
    __builtin_amdgcn_s_setprio(0);
    SCHED();
    swap16(hpk[0]); swap16(hpk[1]);
    PIN(hpk[0]); PIN(hpk[1]);
    STAMP(10);
    unsigned wp2[4][8];
    auto perm2 = [&]() {
#pragma unroll
        for (int i = 16; i < 24; i++) PIN(wreg[i]);
#pragma unroll
        for (int ip = 0; ip < 4; ip++)
#pragma unroll
            for (int c = 0; c < 4; c++) {
                const unsigned a = wreg[16 + 2 * ip][c], b = wreg[16 + 2 * ip + 1][c];
                wp2[ip][2 * c] = __builtin_amdgcn_perm(b, a, 0x05040100);
                wp2[ip][2 * c + 1] = __builtin_amdgcn_perm(b, a, 0x07060302);
            }
#pragma unroll
        for (int ip = 0; ip < 4; ip++)
#pragma unroll
            for (int c = 0; c < 8; c++) PIN(wp2[ip][c]);
    };
    v4u outv[2];
    const rsrc_t rsO = __builtin_amdgcn_make_buffer_rsrc((void*)OUT, 0, 2048 * 1024 * 2, 0x00020000);
    unsigned vOut = (R2 & ~8u) * 2048 + (t16 >> 3) * 64 + gx * 16;
    PIN(vOut);
    unsigned aG = ldsb + L_WO + 512, vX = kr, v4 = l * 4, sX = WS_C + kq * 512 + lw * 128 + nr;
    unsigned aR1 = ldsb + L_RED + (lw * 4 + g) * 512 + t16 * 32, aT1 = ldsb + L_RED + (lw & 1) * 256 + l * 4;
    unsigned aR2 = ldsb + L_RED + 8192 + (lw * 4 + g) * 256 + (t16 & 7) * 32 + (t16 >> 3) * 16, aT2 = ldsb + L_RED + 8192 + l * 4, aB = kr * 8;
    const unsigned cx = (lw & 1) ? cg1u : cg1;
    unsigned s8w = WS_G8 + (cx >> 2) * 16384 + (lw >> 1) * 8192 + (kq * 2 + (lw & 1)) * 1024 + (cx & 3) * 256;
    unsigned s8r = WS_G8 + kc * 16384 + lw * 256 + nr, s8s = s8r + 4096;
    unsigned s9w = WS_G9 + (cg >> 2) * 16384 + (cg & 3) * 2048 + kc * 128, v9 = (l & 31) * 4 + (nh ^ (((l >> 3) ^ bid) & 4) * 2048);
    float a1[2][8];
    if (half) {
#pragma unroll
        for (int c = 0; c < 16; c++) { a1[c >> 3][c & 7] = 0.f; PIN(a1[c >> 3][c & 7]); }
        PIN(vX); PIN(v4); PIN(aR1); PIN(aT1); PIN(aR2); PIN(aT2); PIN(aB); PIN(v9);
    }
    PIN(aG);
    asm volatile("" : "+s"(sX), "+s"(s8w), "+s"(s8r), "+s"(s8s), "+s"(s9w));
    unsigned vP = t16 == rt2 ? OOB : t16 * 128, pk;
    {
        v4u pv[4];
        unsigned vK = tid < 4 ? ((ct + 4 * tid) & 15) * 2048 : OOB, vS = l * 16;
        PIN(vK); PIN(vS);
        unsigned sK = WS_GK + rt2 * 128, rk = 2047u - R2, aKy = ldsb + L_WO + 576, aKw = aKy + w * 4;
        unsigned vC = gx * 16, sC = WS_C + (~rt2 & 12) * 4096 + (~rt2 & 3) * 2048 + ct * 128;
        PIN(vC);
        asm volatile("" : "+s"(sK), "+s"(sC));
        PIN(rk); PIN(aKy); PIN(aKw); PIN(vP);
        auto st_ld = [&](int k) { return LD16(vS + k * 1024, sG5r); };
        auto st_sum = [&]() { const f32x4 ps = (f4(pv[0]) + f4(pv[1])) + (f4(pv[2]) + f4(pv[3])); return xrow_scatter<false>(ps[0], ps[1], ps[2], ps[3]); };
        SCHED();
        if (half) SLEEP(4); else SLEEP(7);
        __builtin_amdgcn_s_setprio(1);
        float ts;
        sweepn<4, 4>(st_ld, pv, [&]() { return ts = st_sum(); });
        STAMP(21);
        float T[4];
        xrow_gather(ts, T);
        const float mean = T[0] * (1.0f / 1024.0f), var = fmaxf(T[1] * (1.0f / 1024.0f) - mean * mean, 0.f);
        const unsigned sg = pk2(mean, __builtin_amdgcn_sqrtf(rbf(rbf(var) + 1e-5f)));
        const float mb2 = bflo(sg), isd2 = __builtin_amdgcn_rcpf(bfhi(sg));
        const float rsc = (T[2] - mb2 * T[3]) * isd2;
        const unsigned rb = pk2(rsc, rsc);
        const unsigned key = (((rb ^ (((int)rb >> 31) | 0x80000000u)) >> 16) << 11) | rk;
        unsigned kb = umx(key, dppu<0x128>(key));
        kb = umx(kb, dppu<0x124>(kb)); kb = umx(kb, dppu<0x122>(kb)); kb = umx(kb, dppu<0x121>(kb));
        stsu(aKw, kb);
        WAITL0();
        BAR();
        SCHED();
        pk = umx(wmax(*(const LAS v4u*)aKy), wmax(*(const LAS v4u*)(aKy + 16)));
        if (w == 0) ST4(pk, vK, sK);
        SCHED();
        const bool cand = key == pk;
        __builtin_amdgcn_s_setprio(0);
        v4u e[2];
#pragma unroll
        for (int i = 0; i < 8; i++) {
            const float h0 = bflo(hpk[i >> 2][i & 3]), h1 = bfhi(hpk[i >> 2][i & 3]);
            const unsigned d = pk2(h0 - mb2, h1 - mb2);
            e[i >> 2][i & 3] = nsent(pk2(bflo(d) * isd2, bfhi(d) * isd2));
        }
        if (cand) {
#pragma unroll
            for (int i = 0; i < 4; i++) ST16(e[i & 1], vC + (i & 1) * 64, sC + (i >> 1) * 8192);
#pragma unroll
            for (int i = 0; i < 4; i++)
                ST16(((v4u){nsent(hpk[i & 1][0]), nsent(hpk[i & 1][1]), nsent(hpk[i & 1][2]), nsent(hpk[i & 1][3])}), vC + (i & 1) * 64, sC + (WS_H - WS_C) + (i >> 1) * 8192);
        }
        SCHED();
        unsigned k1;
        if (w == 1) {
            SLEEP(3);
            asm volatile("buffer_load_dword %0, %1, %2, %3 offen sc1" : "=v"(k1) : "v"(vP), "s"(rsWS), "s"(sRep) : "memory");
        }
        SCHED();
        STAMP(22);
#pragma unroll
        for (int i = 0; i < 8; i++)
            outv[i >> 2][i & 3] = pk2(bflo(hpk[i >> 2][i & 3]) + bflo(e[i >> 2][i & 3]), bfhi(hpk[i >> 2][i & 3]) + bfhi(e[i >> 2][i & 3]));
        {
#pragma unroll
            for (int j = 0; j < 4; j++) {
                const unsigned a = outv[0][j], b = outv[1][j];
                outv[0][j] = __builtin_amdgcn_update_dpp(a, b, 0x128, 0xf, 0xc, false);
                outv[1][j] = __builtin_amdgcn_update_dpp(b, a, 0x128, 0xf, 0x3, false);
            }
        }
        PIN(outv[0]); PIN(outv[1]);
        if (w == 1) {
            poll_words(k1, rsWS, vP, sRep);
            STAMP(23);
            unsigned gk = umx(k1, pk);
            gk = umx(gk, dppu<0x128>(gk)); gk = umx(gk, dppu<0x124>(gk)); gk = umx(gk, dppu<0x122>(gk)); gk = umx(gk, dppu<0x121>(gk));
            stsu(aG, gk);
        }
    }
    if (half) __builtin_amdgcn_s_setprio(1);
    STAMP(24);
    WL0();
    SCHED();
    BAR();
    SCHED();
    STAMP(11);
    const unsigned gv = ldsu(aG);
    unsigned vo[2];
    auto store_out = [&](unsigned srow) {
#pragma unroll
        for (int i = 0; i < 2; i++) __builtin_amdgcn_raw_buffer_store_b128(outv[i], rsO, vo[i], ct * 128 + i * 16384, AUX_SC1);
    };
    if (RARE(!ffn)) {
        const unsigned gkey = rfl(gv), srow = ~gkey & 2047u;
        vo[0] = (R2 & ~8u) != srow ? vOut : OOB; vo[1] = (R2 | 8u) != srow ? vOut : OOB;
        store_out(srow);
        if (w != 4) { RDUMP(); return; }
        const unsigned c9 = (bid & 3) * 4 + (idx & 3), col = c9 * 64 + (bid & 4) * 8 + (l & 7) * 4 + g;
        unsigned hl[2];
        sweepz<2, 2>([&](int k) { return LD4((col >> 1) * 4, (k ? WS_C : WS_H) + (((gkey & 0x780) + (gkey & 0x600)) << 4) + nr); }, hl);
        const unsigned ord = gkey >> 11;
        const unsigned rb = (ord & 0x8000u) ? (ord & 0x7fffu) : (~ord & 0xffffu);
        float sig = rbf(1.0f / (1.0f + __expf(-bf2f(rb))));
        float hc = (g & 1) ? bfhi(hl[0]) : bflo(hl[0]), lc = (g & 1) ? bfhi(hl[1]) : bflo(hl[1]);
        unsigned vY = l * 16, vZ = l < 48 ? l * 16 + 1024 : OOB, vO = (l & 8) ? OOB : col * 2, sY = WS_G9 + (c9 >> 2) * 16384 + (c9 & 3) * 2048 + nh, sO = srow * 2048;
        PIN(sig); PIN(hc); PIN(lc); PIN(vY); PIN(vZ); PIN(vO);
        asm volatile("" : "+s"(sY), "+s"(sO));
        v4u yp[2];
        SCHED();
        STAMP(16);
        SLEEP(24);
        sweepz<2>([&](int k) { return LD16(k ? vZ : vY, sY); }, yp);
        const f32x4 ys = f4(yp[0]) + f4(yp[1]);
        float yt = xrow_scatter<false>(ys[0], ys[1], ys[2], ys[3]);
        yt += dppf<0x128>(yt);
        const float y = rbf(rbf(yt) * sig);
        __builtin_amdgcn_raw_buffer_store_b16((u16)pk2(hc + rbf(lc + y), 0.f), rsO, vO, sO, 0);
        STAMP(17);
        RDUMP();
        return;
    }
    if (w < 4) {
        const unsigned srow = ~rfl(gv) & 2047u;
        vo[0] = (R2 & ~8u) != srow ? vOut : OOB; vo[1] = (R2 | 8u) != srow ? vOut : OOB;
        PIN(vo[0]); PIN(vo[1]);
        BAR(); BAR(); BAR();
        store_out(srow);
        if (bid == 0 && tid == 0) __hip_atomic_store((gu32*)(ws + WS_EPOCH), tag, RLX_AGENT);
        STAMP(12);
        RDUMP();
        return;
    }

    auto tree16 = [&](unsigned a, unsigned stride) {
        f32x2 v[8];
#pragma unroll
        for (int i = 0; i < 8; i++) v[i] = (f32x2){ldsf(a + 2 * i * stride), ldsf(a + (2 * i + 1) * stride)};
        SCHED();
        const f32x2 t = ((v[0] + v[1]) + (v[2] + v[3])) + ((v[4] + v[5]) + (v[6] + v[7]));
        return t[0] + t[1];
    };
    vX = (((gv & 0x780) | vX) + (gv & 0x600)) << 4;
    v4u xq[1] = {LD16(vX, sX)};
    SCHED();
    const unsigned srow = ~rfl(gv) & 2047u;
    vo[0] = (R2 & ~8u) != srow ? vOut : OOB; vo[1] = (R2 | 8u) != srow ? vOut : OOB;
    PIN(vo[0]); PIN(vo[1]);
    SCHED();
    if (RARE(!__all(wmax(xq[0]) != SENT))) sweepz<1>([&](int) { return LD16(vX, sX); }, xq);
    STAMP(13);
#pragma unroll
    for (int ip = 0; ip < 4; ip++)
#pragma unroll
        for (int c = 0; c < 16; c++) dot2c(a1[c & 1][c >> 1], wp1[c & 1][ip][c >> 1], xq[0][ip]);
#pragma unroll
    for (int c = 0; c < 8; c++) fold8(a1[0][c], a1[1][c]);
    stsf4(aR1, (f32x4){a1[0][0], a1[0][1], a1[0][2], a1[0][3]});
    stsf4(aR1 + 16, (f32x4){a1[0][4], a1[0][5], a1[0][6], a1[0][7]});
    WL0();
    BAR();
    SCHED();
    ST4(f2s(tree16(aT1, 512)), v4, s8w);
    SCHED();
    STAMP(14);
    perm2();
    float a2[8];
#pragma unroll
    for (int c = 0; c < 8; c++) { a2[c] = 0.f; PIN(a2[c]); }
    SCHED();
    SLEEP(7);
    {
        unsigned pv[8];
        auto a_ld = [&](int k) { return LD4(v4 + (k & 3) * 1024, k < 4 ? s8r : s8s); };
        auto a_sum = [&](int m) { return (__uint_as_float(pv[m]) + __uint_as_float(pv[2 + m])) + (__uint_as_float(pv[4 + m]) + __uint_as_float(pv[6 + m])); };
        float gs, us;
        sweepn<8, 0>(a_ld, pv, [&]() { gs = a_sum(0); us = a_sum(1); return gs + us; });
        STAMP(15);
        const unsigned gu = pk2(gs, us);
        const float gg = bflo(gu);
        const float act = rbf(gg * __builtin_amdgcn_rcpf(1.0f + __expf(-gg))) * bfhi(gu);
        const unsigned apk = pk2(act, dppf<0xB1>(act));
        unsigned ap[4];
#pragma unroll
        for (int ip = 0; ip < 4; ip++) ap[ip] = __builtin_amdgcn_ds_bpermute(aB + ip * 64, apk);
        SCHED();
#pragma unroll
        for (int ip = 0; ip < 4; ip++)
#pragma unroll
            for (int c = 0; c < 8; c++) dot2c(a2[(c & 1) * 4 + (c >> 1)], wp2[ip][(c & 1) * 4 + (c >> 1)], ap[ip]);
#pragma unroll
        for (int c = 0; c < 4; c++) fold8(a2[c], a2[4 + c]);
        stsf4(aR2, (f32x4){a2[0], a2[1], a2[2], a2[3]});
    }
    WL0();
    BAR();
    SCHED();
    if (lw == 0) ST4(f2s(tree16(aT2, 256)), v9, s9w);
    SCHED();
    BAR();
    SCHED();
    STAMP(16);
    store_out(srow);
    RDUMP();
}
__device__ __attribute__((used, noinline)) void fk_tail() { asm volatile(".Lfk_end:"); }

extern "C" void kernel_launch(void* const* d_in, const int* in_sizes, int n_in, void* d_out, int out_size, void* d_ws,
                              size_t ws_size, hipStream_t stream) {
    (void)in_sizes; (void)n_in; (void)out_size; (void)ws_size;
    (void)hipFuncSetAttribute((const void*)fused_kernel, hipFuncAttributeMaxDynamicSharedMemorySize, LDS_BYTES);
    fused_kernel<<<256, 512, LDS_BYTES, stream>>>((const u16*)d_in[0], (const u16*)d_in[1], (const u16*)d_in[2], (const u16*)d_in[3],
                                                  (const u16*)d_in[5], (const u16*)d_in[6], (const u16*)d_in[7], (const u16*)d_in[8],
                                                  (u16*)d_out, (char*)d_ws + (-(size_t)d_ws & 0x1FFFFF));
}
```

```cpp
#include <hip/hip_runtime.h>
#include <stdint.h>
#include <utility>

typedef unsigned short u16;
typedef unsigned long long u64;
typedef short bf16x8 __attribute__((ext_vector_type(8)));
typedef short s16x4 __attribute__((ext_vector_type(4)));
typedef float f32x4 __attribute__((ext_vector_type(4)));
typedef float f32x2 __attribute__((ext_vector_type(2)));
typedef __bf16 bf16x2 __attribute__((ext_vector_type(2)));
typedef unsigned v4u __attribute__((ext_vector_type(4)));
typedef unsigned v2u __attribute__((ext_vector_type(2)));
#define LAS __attribute__((address_space(3)))
#define GAS __attribute__((address_space(1)))
#define CAS __attribute__((address_space(4)))
typedef GAS u64 gu64;
typedef GAS unsigned gu32;
typedef __amdgpu_buffer_rsrc_t rsrc_t;
#define DI __device__ __forceinline__
#define RLX_AGENT __ATOMIC_RELAXED, __HIP_MEMORY_SCOPE_AGENT
#define BAR() __builtin_amdgcn_s_barrier()
#define SCHED() __builtin_amdgcn_sched_barrier(0)
#define SGB(m, n) __builtin_amdgcn_sched_group_barrier((m), (n), 0)
#define WAITL0() asm volatile("s_waitcnt lgkmcnt(0)" ::: "memory")
#define WL0() __builtin_amdgcn_s_waitcnt(0xc07f)
#define MFMA(a, b, c) __builtin_amdgcn_mfma_f32_16x16x32_bf16((a), (b), (c), 0, 0, 0)
#define AUX_SC1 16
template <int N> DI void waitv() { asm volatile("s_waitcnt vmcnt(%0)" ::"i"(N) : "memory"); }

#define WS_EPOCH 0
#define WS_G0 0x1000
#define WS_G1 0x8000
#define WS_F4 0x60000
#define WS_GC 0x70000
#define WS_PR 0x74000
#define WS_G3 0x80000
#define WS_GZ 0xC0000
#define SENT 0xFFFFFFFFu
#define WS_TL 0x100000
#define WS_G5 0x100000
#define WS_G8 0x180000
#define WS_GK 0x1B8000
#define WS_G9 0x1C0000
#define WS_C 0x1E0000
#define WS_H 0x1F0000
#define WS_P2 0x200000
#define P2_STRIDE 0x2180
#define WS_P4 0x600000
#define WS_USED 0xA00000
#define WS_STAMP 0x1000000

#define STAGE_B 40960
#define LDS_BYTES 163840
#define L_WO 32768
#define L_MB 161792
#define L_WPHI (L_MB - 8192)
#define L_SD (L_MB + 512)
#define L_CS (L_MB + 1024)
#define L_V L_WO
#define L_KH (L_WO + 65536)
#define L_XB 0
#define L_RED 4096

#define SPIN_CAP 400000u
#define DN0 3
#define W_AUX 2
#define LOG2E 1.4426950408889634f

DI unsigned rfl(unsigned x) { return __builtin_amdgcn_readfirstlane(x); }
DI float bf2f(unsigned u) { return __uint_as_float(u << 16); }
DI float bflo(unsigned u) { return __uint_as_float(u << 16); }
DI float bfhi(unsigned u) { return __uint_as_float(u & 0xffff0000u); }
DI unsigned pk2(float lo, float hi) { f32x2 v = {lo, hi}; return __builtin_bit_cast(unsigned, __builtin_convertvector(v, bf16x2)); }
DI unsigned nsent(unsigned x) { return __builtin_elementwise_min(x, SENT - 1); }
DI unsigned umx(unsigned a, unsigned b) { return __builtin_elementwise_max(a, b); }
DI float rbf(float f) { return bflo(pk2(f, f) & 0xffffu); }
DI bf16x8 mk8(unsigned a, unsigned b, unsigned c, unsigned d) { v4u v = {a, b, c, d}; return __builtin_bit_cast(bf16x8, v); }
DI void dma16(unsigned ldsaddr, unsigned voff, rsrc_t rs, unsigned soff) {
    asm volatile("s_mov_b32 m0, %0\n\ts_nop 0\n\tbuffer_load_dwordx4 %1, %2, %3 offen lds" ::"s"(ldsaddr), "v"(voff), "s"(rs), "s"(soff) : "memory");
}
DI void dma16x4(unsigned ldsaddr, const unsigned* v, rsrc_t rs, unsigned soff) {
    asm volatile("s_mov_b32 m0, %0\n\ts_nop 0\n\tbuffer_load_dwordx4 %1, %5, %6 offen lds\n\tbuffer_load_dwordx4 %2, %5, %6 offen offset:1024 lds\n\t"
                 "buffer_load_dwordx4 %3, %5, %6 offen offset:2048 lds\n\tbuffer_load_dwordx4 %4, %5, %6 offen offset:3072 lds"
                 ::"s"(ldsaddr), "v"(v[0]), "v"(v[1]), "v"(v[2]), "v"(v[3]), "s"(rs), "s"(soff) : "memory");
}
DI void touch(unsigned voff, rsrc_t rs, unsigned soff) { asm volatile("buffer_load_dword v255, %0, %1, %2 offen" ::"v"(voff), "s"(rs), "s"(soff) : "memory", "v255"); }
DI v4u ld16_asm(unsigned voff, rsrc_t rs, unsigned soff) {
    v4u r; asm volatile("buffer_load_dwordx4 %0, %1, %2, %3 offen" : "=v"(r) : "v"(voff), "s"(rs), "s"(soff) : "memory"); return r;
}
#define PIN(x) asm volatile("" : "+v"(x))
DI s16x4 tr_read(unsigned a) { return __builtin_amdgcn_ds_read_tr16_b64_v4i16((LAS s16x4*)(a)); }
DI bf16x8 cat8(s16x4 a, s16x4 b) { return __builtin_shufflevector(a, b, 0, 1, 2, 3, 4, 5, 6, 7); }
DI bf16x8 lds128(unsigned a) { return *(const LAS bf16x8*)(a); }
DI f32x4 ldsf4(unsigned a) { return *(const LAS f32x4*)(a); }
DI float ldsf(unsigned a) { return *(const LAS float*)(a); }
DI unsigned ldsu(unsigned a) { return *(const LAS unsigned*)(a); }
DI void stsf4(unsigned a, f32x4 v) { *(LAS f32x4*)(a) = v; }
DI void stsf(unsigned a, float v) { *(LAS float*)(a) = v; }
DI void stsu(unsigned a, unsigned v) { *(LAS unsigned*)(a) = v; }
template <int C> DI float dppf(float x) { return __uint_as_float(__builtin_amdgcn_update_dpp(0, __float_as_uint(x), C, 0xf, 0xf, true)); }
template <int C> DI unsigned dppu(unsigned x) { return __builtin_amdgcn_update_dpp(0, x, C, 0xf, 0xf, true); }
DI float max2f(float a, float b) { float r; asm("v_max_f32_e32 %0, %1, %2" : "=v"(r) : "v"(a), "v"(b)); return r; }
DI float max3f(float a, float b, float c) { float r; asm("v_max3_f32 %0, %1, %2, %3" : "=v"(r) : "v"(a), "v"(b), "v"(c)); return r; }
DI float row_sum(float x) { x += dppf<0x128>(x); x += dppf<0x124>(x); x += dppf<0x122>(x); x += dppf<0x121>(x); return x; }
DI float xrow_sum(float x) {
    auto a = __builtin_amdgcn_permlane16_swap(__float_as_uint(x), __float_as_uint(x), false, false);
    x = __uint_as_float(a[0]) + __uint_as_float(a[1]);
    auto b = __builtin_amdgcn_permlane32_swap(__float_as_uint(x), __float_as_uint(x), false, false);
    return __uint_as_float(b[0]) + __uint_as_float(b[1]);
}
DI float wave_sum(float x) { return xrow_sum(row_sum(x)); }
DI unsigned wave_maxu(unsigned x) {
    x = umx(x, dppu<0x128>(x)); x = umx(x, dppu<0x124>(x)); x = umx(x, dppu<0x122>(x)); x = umx(x, dppu<0x121>(x));
    auto a = __builtin_amdgcn_permlane16_swap(x, x, false, false);
    x = umx(a[0], a[1]);
    auto b = __builtin_amdgcn_permlane32_swap(x, x, false, false);
    return umx(b[0], b[1]);
}
DI float nmax3f(float a, float b, float c) { float r; asm("v_max3_f32 %0, -%1, -%2, -%3" : "=v"(r) : "v"(a), "v"(b), "v"(c)); return r; }
DI float nmax2f(float a, float b) { float r; asm("v_max_f32_e64 %0, -%1, -%2" : "=v"(r) : "v"(a), "v"(b)); return r; }
DI float mx2(float a, float b) { return __builtin_amdgcn_fmed3f(a, b, __builtin_inff()); }
template <bool MAX> DI float xrow_scatter(float a, float b, float c, float d) {
    const auto s = __builtin_amdgcn_permlane16_swap(__float_as_uint(a), __float_as_uint(b), false, false);
    const auto t = __builtin_amdgcn_permlane16_swap(__float_as_uint(c), __float_as_uint(d), false, false);
    const float u = MAX ? mx2(__uint_as_float(s[0]), __uint_as_float(s[1])) : __uint_as_float(s[0]) + __uint_as_float(s[1]);
    const float v = MAX ? mx2(__uint_as_float(t[0]), __uint_as_float(t[1])) : __uint_as_float(t[0]) + __uint_as_float(t[1]);
    const auto r = __builtin_amdgcn_permlane32_swap(__float_as_uint(u), __float_as_uint(v), false, false);
    return MAX ? mx2(__uint_as_float(r[0]), __uint_as_float(r[1])) : __uint_as_float(r[0]) + __uint_as_float(r[1]);
}
DI void xrow_gather(float w, float (&o)[4]) {
    const auto r = __builtin_amdgcn_permlane32_swap(__float_as_uint(w), __float_as_uint(w), false, false);
    const auto s = __builtin_amdgcn_permlane16_swap(r[0], r[0], false, false);
    const auto t = __builtin_amdgcn_permlane16_swap(r[1], r[1], false, false);
    o[0] = __uint_as_float(s[0]); o[1] = __uint_as_float(s[1]); o[2] = __uint_as_float(t[0]); o[3] = __uint_as_float(t[1]);
}
struct NoHook { DI void operator()(int, float&) const {} DI void operator()(int, float&, float&) const {} };
template <bool MUL, class H = NoHook> DI void softmax2(const f32x4 (&p)[2][2], float rs0, float rs1, bool hi, bf16x8 (&out)[4], H hook = H()) {
    float m[4];
#pragma unroll
    for (int i = 0; i < 2; i++) {
        const f32x4 &p0 = p[i][0], &p1 = p[i][1];
        m[2 * i] = max3f(max3f(p0[0], p0[1], p0[2]), max3f(p0[3], p1[0], p1[1]), max2f(p1[2], p1[3]));
        m[2 * i + 1] = max3f(nmax3f(p0[0], p0[1], p0[2]), nmax3f(p0[3], p1[0], p1[1]), nmax2f(p1[2], p1[3]));
    }
    asm volatile("s_nop 1" : "+v"(m[0]), "+v"(m[1]), "+v"(m[2]), "+v"(m[3]));
    hook(1, m[3]);
    const float rsr = hi ? rs1 : rs0;
    float a[4];
    xrow_gather(xrow_scatter<true>(m[0], m[1], m[2], m[3]) * (rsr * -LOG2E), a);
    f32x2 e[4][4];
    float s[4];
#pragma unroll
    for (int k = 0; k < 4; k++) {
        if (k == 2) hook(2, a[2], s[1]);
        const float sc = ((k >> 1) ? rs1 : rs0) * ((k & 1) ? -LOG2E : LOG2E);
#pragma unroll
        for (int j = 0; j < 4; j++) {
            const f32x4& q = p[k >> 1][j >> 1];
            const f32x2 t = __builtin_elementwise_fma((f32x2){q[2 * (j & 1)], q[2 * (j & 1) + 1]}, (f32x2){sc, sc}, (f32x2){a[k], a[k]});
            e[k][j] = (f32x2){__builtin_amdgcn_exp2f(t[0]), __builtin_amdgcn_exp2f(t[1])};
        }
        const f32x2 h = (e[k][0] + e[k][1]) + (e[k][2] + e[k][3]);
        s[k] = h[0] + h[1];
        if (k == 3) hook(3, s[k]);
    }
    float iv = __builtin_amdgcn_rcpf(xrow_scatter<false>(s[0], s[1], s[2], s[3]));
    if (MUL) iv *= rsr;
    xrow_gather(iv, a);
#pragma unroll
    for (int k = 0; k < 4; k++) {
        if (k == 2) hook(4, a[2]);
        const f32x2 c = {a[k], a[k]};
        const f32x2 x0 = e[k][0] * c, x1 = e[k][1] * c, x2 = e[k][2] * c, x3 = e[k][3] * c;
        out[k] = mk8(pk2(x0[0], x0[1]), pk2(x1[0], x1[1]), pk2(x2[0], x2[1]), pk2(x3[0], x3[1]));
    }
}
DI void swap16(v4u& x) {
    const auto a = __builtin_amdgcn_permlane16_swap(x[0], x[2], false, false), b = __builtin_amdgcn_permlane16_swap(x[1], x[3], false, false);
    x = (v4u){a[0], b[0], a[1], b[1]};
}
DI float dot2(unsigned a, unsigned b, float c) {
    return __builtin_amdgcn_fdot2_f32_bf16(__builtin_bit_cast(bf16x2, a), __builtin_bit_cast(bf16x2, b), c, false);
}
DI void st_gran(char* ws, unsigned off, unsigned tag, unsigned val) {
    __hip_atomic_store((gu64*)(ws + off), ((u64)tag << 32) | val, RLX_AGENT);
}
DI v2u ldg(rsrc_t rs, unsigned voff, unsigned soff) { return __builtin_amdgcn_raw_buffer_load_b64(rs, voff, soff, AUX_SC1); }
#define RARE(c) __builtin_expect(!!(c), 0)
#define FRESH() asm volatile("" ::: "memory")
template <int N> DI void sweep(rsrc_t rs, unsigned voff, unsigned soff, unsigned S, unsigned tag, unsigned (&v)[N]) {
#pragma nounroll
    for (unsigned spins = 0;;) {
        unsigned bad = 0;
        FRESH();
#pragma unroll
        for (int k = 0; k < N; ++k) { const v2u x = ldg(rs, voff, soff + k * S); v[k] = x[0]; bad |= x[1] ^ tag; }
        if (__all(bad == 0)) break;
        if (++spins > SPIN_CAP) break;
        __builtin_amdgcn_s_sleep(1);
    }
}
DI unsigned wmax(unsigned x) { return x; }
DI unsigned wmax(v2u x) { return umx(x[0], x[1]); }
DI unsigned wmax(v4u x) { return umx(umx(x[0], x[1]), umx(x[2], x[3])); }
template <int N, int SL = 0, class T, class F> DI void sweepz(F ld, T (&v)[N]) {
#pragma nounroll
    for (unsigned spins = 0;;) {
        FRESH();
        unsigned m = 0;
#pragma unroll
        for (int k = 0; k < N; ++k) v[k] = ld(k);
#pragma unroll
        for (int k = 0; k < N; ++k) m = umx(m, wmax(v[k]));
        if (__all(m != SENT)) break;
        if (++spins > SPIN_CAP) break;
        if (SL) __builtin_amdgcn_s_sleep(SL);
    }
}
DI f32x4 f4(v4u x) { return __builtin_bit_cast(f32x4, x); }
DI bool anynan(float x) { return __builtin_amdgcn_ballot_w64(x != x) != 0; }
DI void dot2c(float& c, unsigned a, unsigned b) { asm volatile("v_dot2c_f32_bf16 %0, %1, %2" : "+v"(c) : "v"(a), "v"(b)); }
DI void fold8(float& a, float b) {
    asm volatile("v_add_f32_dpp %0, %0, %0 row_ror:8 row_mask:0xf bank_mask:0x3\n\tv_add_f32_dpp %0, %1, %1 row_ror:8 row_mask:0xf bank_mask:0xc" : "+v"(a) : "v"(b));
}
template <int N, int SL, class T, class F, class S> DI void sweepn(F ld, T (&v)[N], S sum) {
    FRESH();
#pragma unroll
    for (int k = 0; k < N; ++k) v[k] = ld(k);
    if (RARE(anynan(sum()))) { sweepz<N, SL>(ld, v); sum(); }
}
DI unsigned f2s(float x) { return nsent(__float_as_uint(x)); }
#define OOB 0x4000000u
#define LD16(v, s) __builtin_amdgcn_raw_buffer_load_b128(rsWS, (v), (s), AUX_SC1)
#define LD4(v, s) __builtin_amdgcn_raw_buffer_load_b32(rsWS, (v), (s), AUX_SC1)
#define ST16(x, v, s) __builtin_amdgcn_raw_buffer_store_b128((x), rsWS, (v), (s), AUX_SC1)
#define ST4(x, v, s) __builtin_amdgcn_raw_buffer_store_b32((x), rsWS, (v), (s), AUX_SC1)
#define SLEEP(n) __builtin_amdgcn_s_sleep(n)
DI void poll_flags(rsrc_t rs, unsigned v, unsigned off, v2u want) {
    v2u x; unsigned n = SPIN_CAP;
    asm volatile("1:\n\tbuffer_load_dwordx2 %0, %2, %3, %4 offen sc1\n\ts_sub_u32 %1, %1, 1\n\ts_cbranch_scc1 2f\n\ts_waitcnt vmcnt(0)\n\t"
                 "v_cmp_ne_u64 vcc, %0, %5\n\ts_cbranch_vccnz 1b\n2:\n\ts_waitcnt vmcnt(0)" : "=&v"(x), "+s"(n) : "v"(v), "s"(rs), "s"(off), "v"(want) : "vcc", "scc", "memory");
}
DI void poll_words(unsigned& x, rsrc_t rs, unsigned v, unsigned off) {
    unsigned n = SPIN_CAP;
    asm volatile("s_branch 3f\n1:\n\tbuffer_load_dword %0, %2, %3, %4 offen sc1\n\ts_sub_u32 %1, %1, 1\n\ts_cbranch_scc1 2f\n3:\n\ts_waitcnt vmcnt(0)\n\t"
                 "v_cmp_eq_u32 vcc, -1, %0\n\ts_cbranch_vccnz 1b\n2:\n\ts_waitcnt vmcnt(0)" : "+v"(x), "+s"(n) : "v"(v), "s"(rs), "s"(off) : "vcc", "scc", "memory");
}
#define STAMP(i)
#define JIT(i) STAMP(i)
#define RINIT()
#define RDUMP()
#define KATTR
template <class F, int... Is> DI void static_for_impl(F&& f, std::integer_sequence<int, Is...>) { (f(std::integral_constant<int, Is>{}), ...); }
template <int N, class F> DI void static_for(F&& f) { static_for_impl(f, std::make_integer_sequence<int, N>{}); }

DI s16x4 bq(unsigned b0, unsigned b1, int j, int i) {
    const int ni = (j + 2) % 6;
    return tr_read(((ni & 1) ? b1 : b0) + (ni >> 1) * 8192 + (i >> 1) * 4096 + (i & 1) * 512);
}
template <class F> DI void group(f32x4 (&acc)[4][6], const bf16x8 (&A)[2][4], const bf16x8 (&B)[2], int j, F gap) {
    const int ni = (j + 2) % 6;
#pragma unroll
    for (int i = 0; i < 8; i++) {
        const int mi = i >> 1, ks = i & 1;
        if (ni < 4) asm volatile("v_mfma_f32_16x16x32_bf16 %0, %1, %2, %0" : "+v"(acc[mi][ni]) : "v"(B[ks]), "v"(A[ks][mi]));
        else asm volatile("v_mfma_f32_16x16x32_bf16 %0, %1, %2, %0" : "+v"(acc[mi][ni]) : "v"(A[ks][mi]), "v"(B[ks]));
        gap(i);
        SCHED();
    }
}
DI void img_write2(const unsigned (&ia)[4], const bf16x8 (&f)[4]) {
#pragma unroll
    for (int k = 0; k < 4; k++) {
        const v4u u = __builtin_bit_cast(v4u, f[k]);
        *(LAS v2u*)(ia[2 * (k & 1)] + (k >> 1) * 2048) = (v2u){u[0], u[1]};
        *(LAS v2u*)(ia[2 * (k & 1) + 1] + (k >> 1) * 2048) = (v2u){u[2], u[3]};
    }
}
DI float lds_bf(unsigned a) { return bf2f(*(const LAS u16*)(a)); }
#define MFMA_SETTLE2(p) asm volatile("s_nop 7\n\ts_nop 1" : "+v"(p[0][0]), "+v"(p[0][1]), "+v"(p[1][0]), "+v"(p[1][1]))

__global__ KATTR __launch_bounds__(512, 2) void fused_kernel(const u16* __restrict__ X, const u16* __restrict__ WQKV, const u16* __restrict__ WPHI,
                                                       const u16* __restrict__ WO, const u16* __restrict__ WR, const u16* __restrict__ WG,
                                                       const u16* __restrict__ WU, const u16* __restrict__ WD, u16* __restrict__ OUT,
                                                       char* __restrict__ ws) {
    extern __shared__ __attribute__((aligned(16))) char lds[];
    u64 pc0; unsigned clen;
    const int tid = threadIdx.x, l = tid & 63;
    asm volatile("s_getpc_b64 %0\n.Lfk_beg:\n\ts_lshr_b32 %1, %3, 3\n\tv_and_b32 v255, 0x1e0, %2\n\tv_add_lshl_u32 v255, v255, %1, 7\n\ts_mov_b32 %1, .Lfk_end-.Lfk_beg\n\t"
                 "v_min_u32 v255, %1, v255\n\tglobal_load_dword v255, v255, %0" : "=&s"(pc0), "=&s"(clen) : "v"(tid), "s"(blockIdx.x) : "v255", "scc");
    SCHED();
    const unsigned w = rfl(tid >> 6);
    const unsigned bid = blockIdx.x, xcd = (bid & 3) * 2 + ((bid >> 2) & 1), idx = bid >> 3;
    const unsigned rt = (xcd & 1) * 8 + (idx & 7), hp = (xcd >> 1) * 4 + (idx >> 3);
    const unsigned rt2 = (bid & 6) * 2 + (idx & 3), ct = (bid & 1) * 8 + (idx >> 2);
    const unsigned hq = (hp + 4) & 15;
    const unsigned rot = (rt >> 2) * 4;
    const unsigned rot2 = (2 * rt2 + 16 * ct) & 31;
    const unsigned ldsb = (unsigned)(size_t)lds;
    const int g = l >> 4, q4 = (l & 15) >> 2, p4 = l & 3, t16 = l & 15;
    const unsigned kr = l >> 3, gx = (g & 1) * 2 + (g >> 1);
    asm volatile("" ::"s"(X), "s"(WQKV), "s"(WPHI), "s"(WO), "s"(WR), "s"(WG), "s"(WU), "s"(WD), "s"(OUT), "s"(ws));
    unsigned tag;
#define READ_TAG() do { SCHED(); tag = *(const volatile CAS unsigned*)(ws + WS_EPOCH) + 1u; asm volatile("" : "+s"(tag)); } while (0)
    const rsrc_t rsWS = __builtin_amdgcn_make_buffer_rsrc((void*)ws, 0, WS_USED, 0x00020000);
    const rsrc_t rsX = __builtin_amdgcn_make_buffer_rsrc((void*)X, 0, 2048 * 1024 * 2, 0x00020000);
    const rsrc_t rsW = __builtin_amdgcn_make_buffer_rsrc((void*)WQKV, 0, 1024 * 3072 * 2, 0x00020000);
    const unsigned vA = (kr * 1024 + (((l & 7) ^ kr) * 8)) * 2;
    const unsigned vB0 = (kr * 3072 + (((l & 7) ^ (((kr >> 1) & 1) << 1)) * 8)) * 2;
    const unsigned vB1 = (kr * 3072 + (((l & 7) ^ ((((kr >> 1) & 1) | 2) << 1)) * 8)) * 2;
    const unsigned srow1 = rt * 128 + hp * 8 + (w & 3) * 2;
    v4u xr[4];
    if (w < 4) {
#pragma unroll
        for (int i = 0; i < 4; i++) xr[i] = ld16_asm(l * 16, rsX, srow1 * 2048 + i * 1024);
    }
    {
        const unsigned vBw = (w & 1) ? vB1 : vB0;
#pragma unroll
        for (int i = 0; i < 2; i++) dma16(ldsb + (w * 2 + i) * 1024, vA, rsX, (rt * 128 + w * 16 + i * 8) * 2048 + rot * 128);
#pragma unroll
        for (int i = 0; i < 3; i++) dma16(ldsb + 16384 + i * 8192 + w * 1024, vBw, rsW, (rot * 64 + w * 8) * 6144 + (i ? hp : hq) * 128 + i * 2048);
    }
    const f32x4 zero4 = {0.f, 0.f, 0.f, 0.f};
    const bf16x8 ones = mk8(0x3F803F80u, 0x3F803F80u, 0x3F803F80u, 0x3F803F80u);
    const unsigned lw = w & 3;
    const bool ffn = (ct & 7) != 3;
    const unsigned fm = idx < 12 ? idx : idx - 4;
    const unsigned fj = fm < 19 ? fm / 3 * 4 + (fm % 3 ? fm % 3 + 1 : 0) : fm == 19 ? 27 - (bid & 1) : fm < 27 ? 4 * fm - 79 : 26 + (bid & 1);
    const unsigned fb = !ffn ? 255u : (bid & 7) * 28 + fj;
    const unsigned cg = (fb + 5) & 15, kc = fb >> 4;
    const unsigned kq = fb / 56, cg1 = fb % 56, cg1u = (cg1 + 3) % 56;
    v4u wreg[24];
    RINIT();
    STAMP(0);

    const rsrc_t rsWO = __builtin_amdgcn_make_buffer_rsrc((void*)WO, 0, 1024 * 1024 * 2, 0x00020000);
    const unsigned vWO = kr * 2048 + (((l & 7) ^ (((kr >> 1) & 3) << 1)) * 16);
    unsigned vwo[4];
    auto wo_so = [&](unsigned b) { return (((2 * b & 30) + (b >> 4) + rot2) & 31) * 65536 + ct * 128; };
    const unsigned fw = (((t16 >> 1) & 3) << 2) | (((t16 >> 3) & 1) << 1) | (t16 & 1);
    unsigned ad;
    unsigned ia[4];
    bf16x8 sdv[4];
    auto pre_addr = [&]() {
#pragma unroll
        for (int k = 0; k < 4; k++) { ia[k] = ldsb + L_XB + lw * 8192 + (w >> 2) * 4096 + t16 * 128 + (((4 * k + g) ^ fw) * 8); PIN(ia[k]); }
        const unsigned rr = 4 * g + q4, fr = (((rr >> 1) & 3) << 2) | (((rr >> 3) & 1) << 1) | (rr & 1);
        ad = ldsb + L_XB + (w & 1) * 16384 + rr * 128 + ((((w >> 1) * 4 + p4) ^ fr) * 8);
        PIN(ad);
    };
    auto sd_reads = [&]() {
        unsigned sa = ldsb + L_SD + g * 8;
        PIN(sa);
#pragma unroll
        for (int s = 0; s < 4; s++) {
            const v2u a = *(const LAS v2u*)(sa + s * 64), b = *(const LAS v2u*)(sa + s * 64 + 32);
            sdv[s] = mk8(a[0], a[1], b[0], b[1]);
        }
    };
    bf16x8 v0[4], v1[4];
    auto v_reads = [&]() {
        unsigned vd = ldsb + L_V + (w & 1) * 8192 + l * 16;
        PIN(vd);
#pragma unroll
        for (int s = 0; s < 4; s++) { v0[s] = lds128(vd + s * 2048); v1[s] = lds128(vd + s * 2048 + 1024); }
    };
    auto kv_tile = [&](auto hook6) {
        const unsigned h2 = w & 1, ft = w >> 1;
        WAITL0();
        BAR();
        SCHED();
        bf16x8 pf[4];
#pragma unroll
        for (int s = 0; s < 4; s++) pf[s] = cat8(tr_read(ad + s * 4096), tr_read(ad + s * 4096 + 2048));
        SCHED();
        hook6();
        SCHED();
        f32x4 k0 = zero4, k1 = zero4, kz = zero4;
#pragma unroll
        for (int s = 0; s < 4; s++) { k0 = MFMA(pf[s], v0[s], k0); k1 = MFMA(pf[s], v1[s], k1); }
#pragma unroll
        for (int s = 0; s < 4; s++) kz = MFMA(pf[s], sdv[s], kz);
        const unsigned pbase = WS_P2 + (hp * 16 + rt) * P2_STRIDE;
        const v4u o0 = {nsent(pk2(k0[0], k0[1])), nsent(pk2(k0[2], k0[3])), nsent(pk2(k1[0], k1[1])), nsent(pk2(k1[2], k1[3]))};
        __builtin_amdgcn_raw_buffer_store_b128(o0, rsWS, l * 16, pbase + h2 * 4096 + ft * 1024, AUX_SC1);
        asm volatile("s_nop 1" :: "v"(o0));
        __builtin_amdgcn_raw_buffer_store_b64((v2u){nsent(pk2(kz[0], kz[1])), nsent(pk2(kz[2], kz[3]))}, rsWS, t16 == 0 ? g * 8 : OOB,
                                              pbase + 8192 + h2 * 128 + ft * 32, AUX_SC1);
        SCHED();
        BAR();
    };

    v4u pp[2];
    auto red = [&]() {
        const v4u* p = pp;
        float lo = xrow_scatter<false>(bflo(p[0][0]) + bflo(p[1][0]), bflo(p[0][1]) + bflo(p[1][1]), bflo(p[0][2]) + bflo(p[1][2]), bflo(p[0][3]) + bflo(p[1][3]));
        float hi = xrow_scatter<false>(bfhi(p[0][0]) + bfhi(p[1][0]), bfhi(p[0][1]) + bfhi(p[1][1]), bfhi(p[0][2]) + bfhi(p[1][2]), bfhi(p[0][3]) + bfhi(p[1][3]));
        lo += dppf<0x128>(lo); hi += dppf<0x128>(hi);
        return nsent(pk2(lo, hi));
    };
    auto pp_so = [&](int k) { return WS_P2 + (hp * 16 + k * 8) * P2_STRIDE + (rt >> 2) * 2048 + (lw & 1) * 1024 + (rt & 3) * 256 + (lw >> 1) * 128; };
    auto red_issue = [&]() {
        const unsigned vpp = kr * P2_STRIDE + (l & 7) * 16;
#pragma unroll
        for (int k = 0; k < 2; k++) asm volatile("buffer_load_dwordx4 %0, %1, %2, %3 offen sc1" : "=v"(pp[k]) : "v"(vpp), "s"(rsWS), "s"(pp_so(k)) : "memory");
        SCHED();
    };
    auto kv_reduce = [&]() {
        waitv<5>();
        PIN(pp[0]); PIN(pp[1]);
        if (!__all(umx(wmax(pp[0]), wmax(pp[1])) != SENT)) sweepz<2, 1>([&](int k) { return LD16(kr * P2_STRIDE + (l & 7) * 16, pp_so(k)); }, pp);
        STAMP(25);
        ST4(red(), (l & 8) ? OOB : (l & 7) * 16 + g * 4, WS_G3 + (hp * 4 + (rt >> 2)) * 2048 + (lw & 1) * 1024 + (rt & 3) * 256 + (lw >> 1) * 128);
    };

    const unsigned half = w >> 2;
    const unsigned R2 = rt2 * 128 + lw * 32 + half * 16 + t16;

    auto dloads = [&](int i0, int i1) {
        const rsrc_t rsD = __builtin_amdgcn_make_buffer_rsrc((void*)WD, 0, 1024 * 3584 * 2, 0x00020000);
#pragma unroll
        for (int i = i0; i < i1; i++)
            wreg[16 + i] = __builtin_amdgcn_raw_buffer_load_b128(rsD, kr * 4096 + (l & 7) * 16, (kc * 256 + lw * 64 + (i >> 1) * 16 + (i & 1)) * 2048 + cg * 128, W_AUX);
        FRESH();
    };
    unsigned wp1[2][4][8];
    v4u xres[2], wrv[2];
    v2u pl;
    unsigned nh;
    unsigned so, va0, va1, wb[4], vFl, sFl, swc, sG5, sG5r, sRep, nr, aXw, aXr, vG5;
    f32x4 acc2[2][4];
    bf16x8 sop[2];
    unsigned pv;
    auto place = [&]() {
        SCHED();
        PIN(pl);
        if (RARE(rfl(pl[1]) != tag)) { unsigned c[1]; sweep<1>(rsWS, 0, WS_PR + 1024, 0, tag, c); pl[0] = c[0]; }
        pv = rfl(pl[0]);
        nh = ((bid ^ (pv >> 4)) & 4) * 2048;
        asm volatile("" : "+s"(pv), "+s"(nh));
    };
    auto prep2 = [&]() {
        SCHED();
        vFl = t16 * 128 + g * 8;
        sFl = WS_F4 + (rt2 & 12) * 4096 + (rt2 & 3) * 2048 + nh;
        va0 = WS_P4 + ((rt2 & 7) * 8 + lw * 2 + half) * 65536 + (nh ^ (~rt2 & 4) * 2048) + l * 16; va1 = va0 ^ 65536;
        so = (half + rot2 + (rot2 & 24)) * 1024;
#pragma unroll
        for (int nt = 0; nt < 4; nt++) { wb[nt] = ldsb + L_WO + half * 65536 + (4 * g + q4) * 128 + ((nt ^ ((2 * g + (q4 >> 1)) & 3)) * 32) + p4 * 8; PIN(wb[nt]); }
        swap16(xres[0]); swap16(xres[1]); swap16(wrv[0]); swap16(wrv[1]);
        asm volatile("s_getreg_b32 %0, hwreg(HW_REG_XCC_ID, 0, 3)" : "=s"(sRep));
        sRep ^= pv & 4;
        nr = (sRep & 4) * 2048;
        sRep = WS_GK + (sRep + (idx & 1) * 8) * 2048;
        sG5r = WS_G5 + (rt2 & 7) * 65536 + lw * 16384 + half * 4096 + nh;
        sG5 = sG5r + ct * 256;
        aXw = ldsb + L_XB + w * 4096 + l * 16; aXr = aXw ^ 16384; vG5 = g == q4 ? t16 * 16 : OOB;
        asm volatile("" : "+s"(sG5), "+s"(sG5r), "+s"(sRep), "+s"(nr), "+s"(sFl), "+s"(nh), "+s"(so));
        PIN(aXw); PIN(aXr); PIN(vG5); PIN(va0); PIN(va1); PIN(vFl);
#pragma unroll
        for (int nt = 0; nt < 4; nt++) {
            const unsigned x0 = xres[nt >> 1][2 * (nt & 1)], x1 = xres[nt >> 1][2 * (nt & 1) + 1];
            acc2[0][nt] = (f32x4){bflo(x0), bfhi(x0), bflo(x1), bfhi(x1)};
            PIN(acc2[0][nt]);
        }
        unsigned m0 = p4 == 0 ? 0x3F803F80u : 0u, m1 = p4 == 1 ? ~0u : 0u;
        PIN(m0); PIN(m1);
#pragma unroll
        for (int i = 0; i < 2; i++) {
            v4u u;
#pragma unroll
            for (int j = 0; j < 4; j++) u[j] = (wrv[i][j] & m1) | m0;
            sop[i] = __builtin_bit_cast(bf16x8, u);
            PIN(sop[i]);
        }
        float sw = 0.f;
#pragma unroll
        for (int i = 0; i < 8; i++) sw = dot2(wrv[i >> 2][i & 3], 0x3F803F80u, sw);
        swc = f2s(xrow_sum(sw));
        PIN(swc);
        SCHED();
    };
    auto res_loads = [&]() {
        pl = ldg(rsWS, 0, WS_PR + 1024);
#pragma unroll
        for (int i = 0; i < 2; i++) {
            xres[i] = *(const v4u*)(X + (size_t)R2 * 1024 + ct * 64 + i * 32 + 8 * gx);
            wrv[i] = *(const v4u*)(WR + ct * 64 + i * 32 + 8 * gx);
        }
        FRESH();
    };

    if (w < 4) {
        const unsigned wm = w & 1, hh = w >> 1;
        f32x4 acc[4][6];
        bf16x8 wf[2], wfq[2];
        float mbt[4], rst[4], csv[2];
        f32x4 mbv[4], csq[2], csk[2];
#pragma unroll
        for (int i = 0; i < 4; i++)
#pragma unroll
            for (int j = 0; j < 6; j++) { acc[i][j] = zero4; PIN(acc[i][j]); }
        {
            const unsigned aoff0 = ldsb + (wm * 64 + t16) * 128 + (((0 + g) ^ (l & 7)) * 16);
            const unsigned aoff1 = ldsb + (wm * 64 + t16) * 128 + (((4 + g) ^ (l & 7)) * 16);
            const unsigned sk = ((q4 >> 1) & 1) | ((g & 1) << 1);
            const unsigned boffA = ldsb + 16384 + (8 * g + q4) * 128 + (((2 * hh) ^ sk) * 32) + p4 * 8;
            const unsigned boffB = ldsb + 16384 + (8 * g + q4) * 128 + (((2 * hh + 1) ^ sk) * 32) + p4 * 8;
            bf16x8 A[2][4], B[3][2];
            s16x4 lo;
            unsigned a0[3], a1[3], bA[3], bB[3];
#pragma unroll
            for (int i = 0; i < 3; i++) {
                a0[i] = aoff0 + i * STAGE_B; a1[i] = aoff1 + i * STAGE_B; bA[i] = boffA + i * STAGE_B; bB[i] = boffB + i * STAGE_B;
                PIN(a0[i]); PIN(a1[i]); PIN(bA[i]); PIN(bB[i]);
            }
            unsigned aT = ldsb + L_MB + (wm * 64 + t16) * 4, aS = ldsb + L_SD + (wm * 64 + t16) * 2, aM = ldsb + L_MB + wm * 256 + g * 16;
            unsigned aC = ldsb + L_CS + hh * 128 + g * 16, aV = ldsb + L_CS + (128 + hh * 32 + t16) * 4;
            unsigned aW = ldsb + L_WPHI + hh * 2048 + (4 * g + q4) * 64 + p4 * 8, aK = ldsb + L_KH + w * 2048 + l * 16;
            PIN(aT); PIN(aS); PIN(aM); PIN(aC); PIN(aV); PIN(aW); PIN(aK);
            pre_addr();
            READ_TAG();
            waitv<5>();
            PIN(xr[0]); PIN(xr[1]); PIN(xr[2]); PIN(xr[3]);
            {
                float s[2], q[2], x0[2];
#pragma unroll
                for (int rr = 0; rr < 2; rr++) {
                    x0[rr] = bflo(rfl(xr[2 * rr][0]));
                    s[rr] = 0.f; q[rr] = 0.f;
#pragma unroll
                    for (int i = 0; i < 4; i++) {
                        const float a = bflo(xr[2 * rr][i]) - x0[rr], b = bfhi(xr[2 * rr][i]) - x0[rr];
                        const float c = bflo(xr[2 * rr + 1][i]) - x0[rr], d = bfhi(xr[2 * rr + 1][i]) - x0[rr];
                        s[rr] += (a + b) + (c + d);
                        q[rr] += (a * a + b * b) + (c * c + d * d);
                    }
                }
                const float sq = row_sum(xrow_scatter<false>(s[0], s[1], q[0], q[1]));
#define RLF(x, n) __uint_as_float(__builtin_amdgcn_readlane(__float_as_uint(x), (n)))
                const float r0 = RLF(sq, 0), r1 = RLF(sq, 16), r2 = RLF(sq, 32), r3 = RLF(sq, 48);
                const float sm = (l ? r1 : r0) * (1.0f / 1024.0f), qm = (l ? r3 : r2) * (1.0f / 1024.0f);
                const float var = fmaxf(qm - sm * sm, 0.f);
                if (l < 2) st_gran(ws, WS_G0 + (srow1 + l) * 8, tag, pk2((l ? x0[1] : x0[0]) + sm, sqrtf(rbf(rbf(var) + 1e-5f))));
            }
            waitv<1>();
            WAITL0();
            BAR();
            SCHED();
            STAMP(1);
            auto rdB = [&](int u, int j, int i) {
                if (i & 1) B[j % 3][i >> 1] = cat8(lo, bq(bA[u], bB[u], j, i)); else lo = bq(bA[u], bB[u], j, i);
            };
            auto rdA = [&](int u, int i) { A[i & 1][i >> 1] = lds128(((i & 1) ? a1[u] : a0[u]) + (i >> 1) * 2048); };
            auto head = [&](int u) {
                group(acc, A, B[0], 0, [&](int i) { rdB(u, 1 + (i >> 2), i & 3); });
                group(acc, A, B[1], 1, [&](int i) { if (i < 4) rdB(u, 3, i); });
                group(acc, A, B[2], 2, [&](int i) { if (i < 4) rdB(u, 4, i); });
                group(acc, A, B[0], 3, [&](int i) { if (i < 4) rdB(u, 5, i); });
                WL0();
            };
#pragma unroll
            for (int i = 0; i < 4; i++) rdB(0, 0, i);
#pragma unroll
            for (int i = 0; i < 8; i++) rdA(0, i);
            SCHED();
#pragma nounroll
            for (unsigned t = 0; t < 5; t++) {
#pragma unroll
                for (int u = 0; u < 3; u++) {
                    head(u);
                    BAR();
                    JIT(32 + t * 3 + u);
                    SCHED();
                    group(acc, A, B[1], 4, [&](int i) { if (i < 4) rdB((u + 1) % 3, 0, i); });
                    group(acc, A, B[2], 5, [&](int i) { rdA((u + 1) % 3, i); });
                }
            }
            unsigned kw[4][4];
            f32x2 kx;
            auto kpiece = [&](int n) {
                if (n > 0) { kw[((n - 1 >> 2) + 2) & 3][(n - 1) & 3] = pk2(kx[0], kx[1]); PIN(kw[((n - 1 >> 2) + 2) & 3][(n - 1) & 3]); }
                if (n < 16) {
                    const int mi = ((n >> 2) + 2) & 3, j = n & 3;
                    const f32x4 &c = acc[mi][2 + (j >> 1)], &s_ = csk[j >> 1];
                    float x0 = __builtin_fmaf(mbt[mi], s_[2 * (j & 1)], c[2 * (j & 1)]); PIN(x0);
                    float x1 = __builtin_fmaf(mbt[mi], s_[2 * (j & 1) + 1], c[2 * (j & 1) + 1]); PIN(x1);
                    kx = (f32x2){x0, x1};
                }
                if (n == 5 || n == 9) *(LAS v4u*)(aK + (n == 9) * 1024) = (v4u){kw[2 + (n == 9)][0], kw[2 + (n == 9)][1], kw[2 + (n == 9)][2], kw[2 + (n == 9)][3]};
            };
            group(acc, A, B[0], 0, [&](int i) { rdB(0, 1 + (i >> 2), i & 3); });
            group(acc, A, B[1], 1, [&](int i) {
                if (i < 4) { rdB(0, 3, i); mbt[(i + 2) & 3] = ldsf(aT + ((i + 2) & 3) * 64); }
                else if (i < 6) csk[i - 4] = ldsf4(aC + 256 + (i - 4) * 64);
                else wf[i - 6] = cat8(tr_read(aW + (i - 6) * 32), tr_read(aW + (i - 6) * 32 + 1024));
            });
            group(acc, A, B[2], 2, [&](int i) { if (i < 4) rdB(0, 4, i); else mbv[i - 4] = ldsf4(aM + (i - 4) * 64); kpiece(i); });
            group(acc, A, B[0], 3, [&](int i) { if (i < 4) rdB(0, 5, i); if (i == 2 || i == 3) csv[i - 2] = ldsf(aV + (i - 2) * 64); kpiece(8 + i); });
            WL0();
            BAR();
            SCHED();
            kpiece(16);
            f32x4 p[2][2];
#pragma unroll
            for (int i = 0; i < 4; i++) p[i >> 1][i & 1] = MFMA(wf[i & 1], mk8(kw[i >> 1][0], kw[i >> 1][1], kw[i >> 1][2], kw[i >> 1][3]), zero4);
            SCHED();
            STAMP(2);
#pragma unroll
            for (int mi = 0; mi < 4; mi++) rst[mi] = lds_bf(aS + mi * 32);
#pragma unroll
            for (int a = 0; a < 2; a++)
#pragma unroll
                for (int dt = 0; dt < 2; dt++) {
                    const f32x4 x0 = acc[2 * a][4 + dt] + mbv[2 * a] * csv[dt], x1 = acc[2 * a + 1][4 + dt] + mbv[2 * a + 1] * csv[dt];
                    *(LAS v4u*)(aK + (L_V - L_KH) + w * 2048 + (a * 2 + dt) * 1024) = (v4u){pk2(x0[0], x0[1]), pk2(x0[2], x0[3]), pk2(x1[0], x1[1]), pk2(x1[2], x1[3])};
                }
            SCHED();
            rst[0] = __builtin_amdgcn_rcpf(rst[0]); rst[1] = __builtin_amdgcn_rcpf(rst[1]);
            bf16x8 f[4];
            struct HookC {
                decltype(v_reads)& vr; decltype(sd_reads)& sr;
                DI void operator()(int k, float& x) const { if (k == 1) { PIN(x); WAITL0(); BAR(); PIN(x); } if (k == 3) { PIN(x); vr(); sr(); PIN(x); } }
                DI void operator()(int, float&, float&) const {}
            };
            softmax2<true>(p, rst[0], rst[1], g >= 2, f, HookC{v_reads, sd_reads});
            img_write2(ia, f);
#pragma unroll
            for (int ft = 0; ft < 2; ft++) wfq[ft] = cat8(tr_read(aW + ft * 32 + 4096), tr_read(aW + ft * 32 + 5120));
            csq[0] = ldsf4(aC); csq[1] = ldsf4(aC + 64);
            STAMP(3);
            kv_tile([]() {});
            STAMP(4);
            SCHED();
            group(acc, A, B[1], 4, [&](int i) { if (i == 2 || i == 3) { rst[i] = __builtin_amdgcn_rcpf(rst[i]); PIN(rst[i]); } });
            group(acc, A, B[2], 5, [&](int) {});
            asm volatile("s_nop 7\n\ts_nop 1");
            SCHED();
        }
        bf16x8 phq[2][4];
        v4u kg[6];
        unsigned vO = l * 16, vZ3 = g * 16, sK3 = WS_G3 + (hq * 2 + hh) * 4096, sZ3 = WS_GZ + (hq * 2 + hh) * 128;
        auto kg_ld = [&](int k) { return k < 2 ? LD16(vZ3, sZ3 + k * 64) : LD16(vO, sK3 + (k - 2) * 1024); };
        {
            f32x4 pq[2][2][2];
#pragma unroll
            for (int mi = 0; mi < 4; mi++) {
                const f32x4 x0 = acc[mi][0] + mbt[mi] * csq[0], x1 = acc[mi][1] + mbt[mi] * csq[1];
                const bf16x8 qf = mk8(pk2(x0[0], x0[1]), pk2(x0[2], x0[3]), pk2(x1[0], x1[1]), pk2(x1[2], x1[3]));
                pq[mi >> 1][mi & 1][0] = MFMA(wfq[0], qf, zero4); pq[mi >> 1][mi & 1][1] = MFMA(wfq[1], qf, zero4);
            }
            MFMA_SETTLE2(pq[0]); MFMA_SETTLE2(pq[1]);
            softmax2<false>(pq[0], rst[0], rst[1], g >= 2, phq[0]);
#pragma unroll
            for (int i = 0; i < 4; i++) PIN(phq[0][i]);
            SCHED();
            red_issue();
            res_loads();
            SCHED();
            kv_reduce();
            SCHED();
            BAR();
            SCHED();
            softmax2<false>(pq[1], rst[2], rst[3], g >= 2, phq[1]);
#pragma unroll
            for (int i = 0; i < 4; i++) { PIN(phq[0][i]); PIN(phq[1][i]); }
            SCHED();
        }
        STAMP(5);
        v2u fv = {tag ^ 0x5bd1e995u, tag};
        place();
        PIN(xres[0]); PIN(xres[1]); PIN(wrv[0]); PIN(wrv[1]);
        unsigned sF = WS_F4 + (rt & 12) * 4096 + (rt & 3) * 2048 + hq * 128 + w * 8 + nh, vF = l == 0 ? 0 : OOB;
        {
            unsigned sO[4];
#pragma unroll
            for (int mi = 0; mi < 4; mi++) sO[mi] = WS_P4 + ((rt & 7) * 8 + wm * 4 + mi) * 65536 + (hq >> 2) * 16384 + ((hq & 3) * 2 + hh) * 1024 + (nh ^ (~rt & 4) * 2048);
            asm volatile("" : "+s"(sO[0]), "+s"(sO[1]), "+s"(sO[2]), "+s"(sO[3]), "+s"(sF));
            PIN(fv); PIN(vF); PIN(vO); PIN(vZ3);
            SCHED();
            bf16x8 kvA[2][2];
            v4u ov[4];
#define PH(mi, s) phq[(mi) >> 1][2 * ((mi) & 1) + (s)]
            f32x4 n0[4], n1[4];
            float id[4];
            auto num = [&](int mi) {
                n0[mi] = MFMA(kvA[0][0], PH(mi, 0), zero4); n1[mi] = MFMA(kvA[0][1], PH(mi, 0), zero4);
                n0[mi] = MFMA(kvA[1][0], PH(mi, 1), n0[mi]); n1[mi] = MFMA(kvA[1][1], PH(mi, 1), n1[mi]);
            };
            sweepz<6, 4>(kg_ld, kg);
            STAMP(6);
            f32x4 dn[4];
#pragma unroll
            for (int mi = 0; mi < 4; mi++) dn[mi] = MFMA(__builtin_bit_cast(bf16x8, kg[0]), PH(mi, 0), zero4);
            SCHED();
#pragma unroll
            for (int mi = 0; mi < 4; mi++) {
                dn[mi] = MFMA(__builtin_bit_cast(bf16x8, kg[1]), PH(mi, 1), dn[mi]);
                SCHED();
                const int s = mi >> 1, dt = mi & 1;
                kvA[s][dt] = mk8(kg[2 + 2 * s][2 * dt], kg[2 + 2 * s][2 * dt + 1], kg[3 + 2 * s][2 * dt], kg[3 + 2 * s][2 * dt + 1]);
                PIN(kvA[s][dt]);
                SCHED();
            }
#pragma unroll
            for (int i = 0; i < 2; i++) {
                num(i);
                const unsigned d = pk2(dn[2 * i][0], dn[2 * i + 1][0]), e = pk2(bflo(d) + 1e-6f, bfhi(d) + 1e-6f);
                id[2 * i] = __builtin_amdgcn_rcpf(bflo(e)); id[2 * i + 1] = __builtin_amdgcn_rcpf(bfhi(e));
                PIN(id[2 * i]); PIN(id[2 * i + 1]);
                SGB(0x8, 1); SGB(0x2, 3); SGB(0x8, 1); SGB(0x2, 2); SGB(0x8, 1); SGB(0x2, 3); SGB(0x8, 1); SGB(0x2, 2);
                SCHED();
            }
#pragma unroll
            for (int mi = 0; mi < 4; mi++) {
                if (mi < 2) num(mi + 2);
                const f32x4 a = n0[mi] * id[mi], b = n1[mi] * id[mi];
                ov[mi] = (v4u){pk2(a[0], a[1]), pk2(a[2], a[3]), pk2(b[0], b[1]), pk2(b[2], b[3])};
                ST16(ov[mi], vO, sO[mi]);
                if (mi < 2) { SGB(0x8, 1); SGB(0x2, 2); SGB(0x8, 1); SGB(0x2, 2); SGB(0x8, 1); SGB(0x2, 2); SGB(0x8, 1); SGB(0x2, 2); }
                SCHED();
            }
#pragma unroll
            for (int mi = 0; mi < 4; mi++) PIN(ov[mi]);
        }
        waitv<0>();
        __builtin_amdgcn_raw_buffer_store_b64(fv, rsWS, vF, sF, AUX_SC1);
        STAMP(7);
    } else {
        const unsigned pw = lw & 1;
        const rsrc_t rsG = __builtin_amdgcn_make_buffer_rsrc((void*)WG, 0, 1024 * 3584 * 2, 0x00020000);
        const rsrc_t rsU = __builtin_amdgcn_make_buffer_rsrc((void*)WU, 0, 1024 * 3584 * 2, 0x00020000);
        unsigned vwl = kr * 57344 + (l & 7) * 16;
        unsigned sg = (kq * 256 + lw * 64) * 7168 + cg1 * 128, su = (kq * 256 + lw * 64) * 7168 + cg1u * 128;
        auto wloads = [&](int n0, int n1) {
#pragma unroll
            for (int n = n0; n < n1; n++)
                asm volatile("buffer_load_dwordx4 %0, %1, %2, %3 offen nt" : "=v"(wreg[n]) : "v"(vwl), "s"(n < 8 ? rsG : rsU), "s"((n < 8 ? sg : su) + (n & 7) * 7168));
        };
        auto dl_asm = [&]() {
            const rsrc_t rsD = __builtin_amdgcn_make_buffer_rsrc((void*)WD, 0, 1024 * 3584 * 2, 0x00020000);
#pragma unroll
            for (int i = 0; i < DN0; i++)
                asm volatile("buffer_load_dwordx4 %0, %1, %2, %3 offen nt" : "=v"(wreg[16 + i]) : "v"(kr * 4096 + (l & 7) * 16), "s"(rsD),
                             "s"((kc * 256 + lw * 64 + (i >> 1) * 16 + (i & 1)) * 2048 + cg * 128));
        };
        unsigned aK, aW, aS;
        auto k_pre = [&]() {
            pre_addr();
            aK = ldsb + L_KH + lw * 2048 + l * 16; aW = ldsb + L_WPHI + (lw >> 1) * 2048 + (4 * g + q4) * 64 + p4 * 8; aS = ldsb + L_SD + (pw * 64 + 32 + t16) * 2;
            PIN(aK); PIN(aW); PIN(aS);
        };
        auto kfeat = [&](auto hook) {
            bf16x8 wf[2];
            float rst[2];
#pragma unroll
            for (int ft = 0; ft < 2; ft++) {
                wf[ft] = cat8(tr_read(aW + ft * 32), tr_read(aW + ft * 32 + 1024));
                rst[ft] = __builtin_amdgcn_rcpf(lds_bf(aS + ft * 32));
            }
            sd_reads();
            PIN(wf[0]); PIN(wf[1]); PIN(rst[0]); PIN(rst[1]);
            SCHED();
            BAR();
            SCHED();
            const bf16x8 kf0 = lds128(aK), kf1 = lds128(aK + 1024);
            SCHED();
            hook(-1, rst[0]);
            hook(0, rst[0]);
            SCHED();
            f32x4 p[2][2];
            p[0][0] = MFMA(wf[0], kf0, zero4); p[0][1] = MFMA(wf[1], kf0, zero4);
            p[1][0] = MFMA(wf[0], kf1, zero4); p[1][1] = MFMA(wf[1], kf1, zero4);
            MFMA_SETTLE2(p);
            bf16x8 f[4];
            softmax2<true>(p, rst[0], rst[1], g >= 2, f, hook);
            img_write2(ia, f);
            SCHED();
            hook(5, rst[0]);
            SCHED();
            STAMP(3);
            kv_tile([&]() { hook(6, rst[0]); });
            STAMP(4);
        };
        auto wblk = [&](int k) { return k == 0 ? 22 + lw : k == 1 ? (lw < 3 ? 26 + lw : 4) : k < 6 ? 4 * k - 3 + lw : k == 6 ? lw : (lw < 3 ? 29 + lw : 21); };
        auto wo_init = [&]() {
#pragma unroll
            for (int i = 0; i < 4; i++) { vwo[i] = vWO + i * (16384 - 1024); PIN(vwo[i]); }
        };
        unsigned hs[8];
        auto wl_pre = [&]() { asm volatile("" : "+s"(sg), "+s"(su)); PIN(vwl); };
        auto wo_pre = [&]() {
            wo_init();
#pragma unroll
            for (int k = 0; k < 8; k++) { hs[k] = wo_so(wblk(k)); asm volatile("" : "+s"(hs[k])); }
        };
        auto wo_rng = [&](int k0, int k1) {
#pragma unroll
            for (int k = k0; k < k1; k++) dma16x4(ldsb + L_WO + wblk(k) * 4096, vwo, rsWO, hs[k]);
        };
        auto pf = [&](int i) {
            if (i == 0) { wloads(0, 6); SCHED(); if (lw < 2) wo_pre(); SCHED(); }
            if (i < 3 && !ffn) wo_rng(i, i + 1);
            if (i == 1 || i == 2) wloads(6 * i, i == 1 ? 12 : 16);
            if (i == 6) dl_asm();
        };
        struct Hook {
            decltype(pf)& f; decltype(v_reads)& vr;
            DI void operator()(int k, float& x) const { PIN(x); if (k == 1) BAR(); if (k == 3) vr(); f(3 + k); PIN(x); }
            DI void operator()(int k, float& x, float& y) const { PIN(y); f(3 + k); PIN(x); }
        };
        auto tail16 = [&]() {
            kfeat(Hook{pf, v_reads});
            if (ffn) wo_rng(0, 3);
            wo_rng(3, 4);
            STAMP(27);
            if (ffn) waitv<18>(); else waitv<6>();
#pragma unroll
            for (int n = 0; n < 16 + DN0; n++) PIN(wreg[n]);
        };
        auto vT23 = [&]() {
            const unsigned n = (idx * 2 + pw) * 64 + l, r = n / 12, c = n % 12;
            return idx < 24 ? ((r & 127) + (r >> 7) * 256 + 128) * 6144 + (c >> 2) * 2048 + (((xcd >> 1) * 4 + (c >> 2 ? 0 : 4) + (c & 3)) & 15) * 128 : OOB;
        };
        if (lw < 2) {
            const unsigned sA = (rt * 128 + lw * 64) * 2048, sB = lw * 32 * 6144 + hp * 128, sBq = lw * 32 * 6144 + hq * 128;
            unsigned buf = 1;
            unsigned va[4], vb[4];
#pragma unroll
            for (int i = 0; i < 4; i++) { va[i] = vA + i * (16384 - 1024); vb[i] = ((i & 1) ? vB1 : vB0) + i * (49152 - 1024); PIN(va[i]); PIN(vb[i]); }
            auto stage = [&](unsigned s, unsigned bf, int i0, int i1) {
                const unsigned kt = (s + rot) & 15;
                const unsigned lb = ldsb + bf * STAGE_B;
                const unsigned sa = sA + kt * 128, sb = kt * 393216;
                if (i0 <= 0 && 0 < i1) dma16x4(lb + lw * 8192, va, rsX, sa);
                if (i0 <= 1 && 1 < i1) dma16x4(lb + lw * 8192 + 4096, va, rsX, sa + 65536);
                if (i0 <= 2 && 2 < i1) dma16x4(lb + 16384 + 8192 + lw * 4096, vb, rsW, sb + sB + 2048);
                if (i0 <= 3 && 3 < i1) dma16x4(lb + 16384 + 16384 + lw * 4096, vb, rsW, sb + sB + 4096);
                if (i0 <= 4 && 4 < i1) dma16x4(lb + 16384 + lw * 4096, vb, rsW, sb + sBq);
            };
            stage(1, 1, 0, 4);
            SCHED();
            unsigned vt = vT23();
            PIN(vt);
            wl_pre(); k_pre();
            waitv<16>(); BAR();
            stage(1, 1, 4, 5);
            touch(vt, rsW, (xcd & 1) * 512 * 6144);
            waitv<1>(); BAR();
            stage(2, 2, 0, 5); stage(3, 0, 0, 3);
            waitv<12>(); BAR();
            stage(3, 0, 3, 5); stage(4, 1, 0, 4);
            waitv<16>(); BAR();
            stage(4, 1, 4, 5);
            buf = 2;
#pragma nounroll
            for (unsigned s = 5; s < 16; s++) {
                if (s > 5) { waitv<20>(); BAR(); }
                stage(s, buf, 0, 5);
                buf = buf == 2 ? 0 : buf + 1;
            }
            waitv<20>(); BAR();
            pf(0);
            READ_TAG();
            if (ffn) waitv<6>(); else waitv<10>();
            BAR();
            pf(1);
            tail16();
        } else {
            SCHED();
            unsigned v3;
            const unsigned h3 = (xcd >> 1) * 4 + 3, kv3 = !(xcd & 2), wv = idx * 2 + pw;
            auto line3 = [&](unsigned per, unsigned rows, auto rowf) {
                const unsigned n = wv * (kv3 ? per : (per + 2) / 3) + l, r3 = kv3 ? n / 3 : n, c3 = kv3 ? n % 3 : 0;
                return (idx < 24 && l < (kv3 ? per : (per + 2) / 3) && r3 < rows) ? rowf(r3) * 6144 + c3 * 2048 + ((c3 ? h3 : h3 + 4) & 15) * 128 : OOB;
            };
            v3 = line3(32, 512, [](unsigned r) { return r; });
            touch(line3(16, 256, [](unsigned r) { return (r & 127) + (r >> 7) * 256; }), rsW, (xcd & 1) * 512 * 6144);
            {
                const rsrc_t rsP = __builtin_amdgcn_make_buffer_rsrc((void*)WPHI, 0, 32 * 1024 * 2, 0x00020000);
#pragma unroll
                for (int i = 0; i < 4; i++) dma16(ldsb + L_WPHI + pw * 4096 + i * 1024, l * 16, rsP, (pw ? hq : hp) * 4096 + i * 1024);
            }
            v4u sv = {SENT, SENT, SENT, SENT};
            unsigned vc4 = l < 16 ? WS_G3 + (hp * 4 + (rt >> 2)) * 2048 + pw * 1024 + (rt & 3) * 256 + l * 16
                         : l < 24 ? WS_P2 + (hp * 16 + rt) * P2_STRIDE + 8192 + pw * 128 + (l - 16) * 16
                         : (l < 32 && pw == 0 && (rt & 7) == 0) ? WS_GZ + (hp * 2 + (rt >> 3)) * 128 + (l - 24) * 16
                         : l >= 56 ? WS_F4 + (rt & 12) * 4096 + (rt & 3) * 2048 + hq * 128 + pw * 8192 + (l & 7) * 16 : OOB;
            auto clears = [&](int i0, int i1) {
#pragma unroll
                for (int i = i0; i < i1; i++) {
                    if (i < 4) ST16(sv, l * 16, WS_P2 + (hp * 16 + rt) * P2_STRIDE + pw * 4096 + i * 1024);
                    if (i == 4) ST16(sv, vc4, 0);
                    if (i > 4) ST16(sv, l * 16, WS_TL + (bid * 4 + pw * 2 + i - 5) * 1024);
                }
                FRESH();
            };
            PIN(vc4); PIN(sv);
            unsigned ca = ldsb + 16384 + pw * 8192 + (8 * g + q4) * 128 + (((rt & 3) ^ (((q4 >> 1) & 1) | ((g & 1) << 1))) * 32) + p4 * 8;
            PIN(ca);
            bf16x8 cf[4];
            f32x4 cd[2] = {zero4, zero4};
            auto cs_read = [&](int st) {
#pragma unroll
                for (int i = 0; i < 4; i++) cf[i] = cat8(tr_read(ca + (st % 3) * STAGE_B + i * 4096), tr_read(ca + (st % 3) * STAGE_B + i * 4096 + 512));
                SCHED();
            };
            READ_TAG();
            waitv<5>();
            BAR();
            SCHED();
            cs_read(0);
            FRESH();
            const rsrc_t rsC = __builtin_amdgcn_make_buffer_rsrc((void*)pc0, 0, clen, 0x00020000);
            auto code_pf = [&]() { touch(l < 6 ? l * 128 : OOB, rsC, (idx * 2 + pw) * 768); };
            v4u gl[4];
            float s0 = 0.f, s1 = 0.f;
            unsigned bad;
            v2u gs, gc;
            unsigned vgc = WS_GC + (pw ? hq : hp) * 256 + (l & 31) * 8;
            unsigned lat[2] = {~0u, ~0u};
            unsigned vgl = (l < 48 ? l : 0) * 16;
            unsigned aw0 = ldsb + L_CS + pw * 384 + (vgl >> 1), aw1 = ldsb + L_MB + (pw * 64 + l) * 4, aw2 = ldsb + L_SD + (pw * 64 + l) * 2;
            static_for<15>([&](auto tc) {
                constexpr int t = decltype(tc)::value;
                WAITL0();
                JIT(32 + t);
                BAR();
                SCHED();
                if constexpr (t < 4) {
                    __builtin_amdgcn_s_setprio(2);
#pragma unroll
                    for (int i = 0; i < 4; i++) cd[i & 1] = MFMA(ones, cf[(i & 1) * 2 + (i >> 1)], cd[i & 1]);
                    SCHED();
                    __builtin_amdgcn_s_setprio(0);
                    if constexpr (t < 3) cs_read(t + 1);
                    else __builtin_amdgcn_raw_buffer_store_b64((v2u){__float_as_uint(g ? cd[1][0] : cd[0][0]), tag}, rsWS, (g == 1 || g + pw == 0) ? g * 512 + t16 * 8 : OOB,
                                                               WS_G1 + ((hp * 4 + (rt >> 2)) * 192 + pw * 64 + (rt & 3) * 16) * 8, AUX_SC1);
                }
                if constexpr (t == 0) touch(v3, rsW, (~xcd & 1) * 512 * 6144);
                if constexpr (t == 12) code_pf();
                if constexpr (t == 8) {
                    waitv<0>();
                    __builtin_amdgcn_raw_buffer_store_b64((v2u){0u, tag}, rsWS, l == 0 ? 0 : OOB, WS_GC + ((hp * 2 + pw) * 16 + rt) * 8, AUX_SC1);
                }
                if constexpr (t == 8 || t == 9) {
#pragma unroll
                    for (int r = (t - 8) * 2; r < (t - 8) * 2 + 2; r++)
                        asm volatile("buffer_load_dwordx4 %0, %1, %2, %3 offen sc1" : "=v"(gl[r]) : "v"(vgl), "s"(rsWS), "s"(WS_G1 + ((hp * 4 + r) * 192 + pw * 96) * 8) : "memory");
                }
                if constexpr (t == 6) {
                    asm volatile("buffer_load_dwordx2 %0, %1, %2, %3 offen sc1" : "=v"(gs) : "v"(l * 8), "s"(rsWS), "s"(WS_G0 + (rt * 128 + pw * 64) * 8) : "memory");
                }
                if constexpr (t == 11) {
                    waitv<0>();
                    PIN(gs);
                    bad = gs[1] ^ tag;
#pragma unroll
                    for (int r = 0; r < 4; r++) {
                        PIN(gl[r]);
                        bad |= (gl[r][1] ^ tag) | (gl[r][3] ^ tag);
                        s0 += __uint_as_float(gl[r][0]); s1 += __uint_as_float(gl[r][2]);
                    }
                    PIN(s0); PIN(s1); PIN(bad);
                }
                if constexpr (t == 12) {
                    if (RARE(!__all(bad == 0))) {
                        s0 = 0.f; s1 = 0.f;
#pragma nounroll
                        for (unsigned r = 0; r < 4; r++) {
                            unsigned a[2];
                            sweep<2>(rsWS, vgl, WS_G1 + ((hp * 4 + r) * 192 + pw * 96) * 8, 8, tag, a);
                            s0 += __uint_as_float(a[0]); s1 += __uint_as_float(a[1]);
                        }
                        unsigned c[1];
                        sweep<1>(rsWS, l * 8, WS_G0 + (rt * 128 + pw * 64) * 8, 0, tag, c);
                        gs[0] = c[0];
                    }
                    *(LAS f32x2*)(aw0) = (f32x2){-s0, -s1};
                    stsf(aw1, bflo(gs[0]));
                    *(LAS u16*)(aw2) = (u16)(gs[0] >> 16);
                    asm volatile("buffer_load_dwordx2 %0, %1, %2, 0 offen sc1" : "=v"(gc) : "v"(vgc), "s"(rsWS) : "memory");
                    SCHED();
                }
                if constexpr (t == 5) {
                    if (RARE(bid * 2 + pw == 16)) {
                        unsigned x;
                        asm volatile("s_load_dword %0, %1, %2\n\ts_load_dword %0, %1, %2+128\n\ts_load_dword %0, %1, %2+8192\n\ts_load_dword %0, %1, %2+8320\n\ts_waitcnt lgkmcnt(0)"
                                     : "=&s"(x) : "s"(ws), "i"(WS_PR) : "memory");
                    }
                }
                if constexpr (t == 6) { if (RARE(bid * 2 + pw == 16)) ST16(sv, l < 32 ? g * 8192 + t16 * 16 : OOB, WS_PR); }
                if constexpr (t >= 8 && t < 12) {
                    if (RARE(bid * 2 + pw == 16)) {
                        u64 c0, c1; unsigned x;
                        asm volatile(".p2align 6\n\ts_memtime %0\n\ts_load_dword %2, %3, %4 glc\n\ts_waitcnt lgkmcnt(0)\n\ts_memtime %1\n\ts_waitcnt lgkmcnt(0)"
                                     : "=&s"(c0), "=&s"(c1), "=&s"(x) : "s"(ws), "i"(WS_PR + (t & 1) * 8192 + (t & 2) * 64) : "memory");
                        lat[t & 1] = __builtin_elementwise_min(lat[t & 1], (unsigned)c1 - (unsigned)c0);
                        if constexpr (t == 11) {
                            asm volatile("s_getreg_b32 %0, hwreg(HW_REG_XCC_ID, 0, 3)" : "=s"(x));
                            __builtin_amdgcn_raw_buffer_store_b64((v2u){(lat[1] < lat[0] ? 0x44 : 0) ^ (x & 4), tag}, rsWS, l == 0 ? 0 : OOB, WS_PR + 1024, AUX_SC1);
                        }
                    }
                }
                if constexpr (t == 5) k_pre();
                if constexpr (t == 6) { wl_pre(); wo_pre(); }
                if constexpr (t == 7) { PIN(vgc); PIN(vgl); PIN(aw0); PIN(aw1); PIN(aw2); }
                clears(t < 7 ? t : 7, t < 7 ? t + 1 : 7);
                if constexpr (t == 13) {
                    waitv<0>();
                    PIN(gc);
                    if (RARE(!__all(gc[1] == tag))) { unsigned c[1]; sweep<1>(rsWS, vgc, 0, 0, tag, c); }
                    SCHED();
                    pf(0);
                }
                if constexpr (t == 14) pf(1);
            });
            tail16();
        }
        STAMP(28);
        auto perm1 = [&]() {
#pragma unroll
            for (int ip = 0; ip < 4; ip++)
#pragma unroll
                for (int c = 0; c < 4; c++) {
#pragma unroll
                    for (int m = 0; m < 2; m++) {
                        const unsigned a = wreg[m * 8 + 2 * ip][c], b = wreg[m * 8 + 2 * ip + 1][c];
                        wp1[m][ip][2 * c] = __builtin_amdgcn_perm(b, a, 0x05040100);
                        wp1[m][ip][2 * c + 1] = __builtin_amdgcn_perm(b, a, 0x07060302);
                    }
                }
#pragma unroll
            for (int ip = 0; ip < 4; ip++)
#pragma unroll
                for (int c = 0; c < 8; c++) { PIN(wp1[0][ip][c]); PIN(wp1[1][ip][c]); }
            SCHED();
            STAMP(29);
        };
        if (RARE(lw == 0 && (rt & 7) == 0)) {
            __builtin_amdgcn_s_setprio(1);
            sweepz<2, 1>([&](int k) { return LD16(kr * P2_STRIDE + (l & 7) * 16, WS_P2 + (hp * 16 + k * 8) * P2_STRIDE + 8192 + (rt >> 3) * 128); }, pp);
            ST4(red(), (l & 8) ? OOB : ((l >> 2) & 1) * 64 + ((l & 1) * 2 + (g >> 1)) * 16 + ((l >> 1) & 1) * 8 + (g & 1) * 4, WS_GZ + (hp * 2 + (rt >> 3)) * 128);
            __builtin_amdgcn_s_setprio(0);
        }
        SCHED();
        perm1();
        BAR();
        STAMP(30);
        res_loads();
        wo_rng(4, 8);
        STAMP(31);
        waitv<0>();
        PIN(xres[0]); PIN(xres[1]); PIN(wrv[0]); PIN(wrv[1]);
        place();
        STAMP(7);
    }
    prep2();
    if (w == 0) { SLEEP(4); poll_flags(rsWS, vFl, sFl, (v2u){tag ^ 0x5bd1e995u, tag}); }
    BAR();
    FRESH();
    SCHED();
    STAMP(8);
    bf16x8 bb[2][4];
    {
        auto aload = [&](int st, int m2) {
            const v4u r = LD16(m2 ? va1 : va0, so);
            if (m2 == 0) so = ((so | 8192) + 2048) & 0xDC00;
            return r;
        };
        constexpr int PD = 2;
        v4u af[PD][2];
#pragma unroll
        for (int i = 0; i < PD; i++) {
#pragma unroll
            for (int m2 = 1; m2 >= 0; m2--) af[i][m2] = aload(i, m2);
            SCHED();
        }
#pragma unroll
        for (int nt = 0; nt < 4; nt++) bb[0][nt] = cat8(tr_read(wb[nt]), tr_read(wb[nt] + 2048));
#pragma unroll
        for (int nt = 0; nt < 4; nt++) acc2[1][nt] = zero4;
        SCHED();
#pragma unroll
        for (int st = 0; st < 16; st++) {
            if (st + 1 < 16) {
#pragma unroll
                for (int nt = 0; nt < 4; nt++)
                    bb[(st + 1) % (2)][nt] = cat8(tr_read(wb[nt] + (st + 1) * 4096), tr_read(wb[nt] + (st + 1) * 4096 + 2048));
            }
#pragma unroll
            for (int mm = 0; mm < 2; mm++) {
                const int m2 = mm ^ 1;
                const bf16x8 a = __builtin_bit_cast(bf16x8, af[st % PD][m2]);
#pragma unroll
                for (int nt = 0; nt < 4; nt++) acc2[m2][nt] = MFMA(bb[st % (2)][nt], a, acc2[m2][nt]);
            }
            if (st + PD < 16) {
#pragma unroll
                for (int m2 = 1; m2 >= 0; m2--) af[st % PD][m2] = aload(PD + st, m2);
            }
#pragma unroll
            for (int i = 0; i < 4; i++) { SGB(0x8, 1); SGB(0x100, 2); }
            SCHED();
        }
    }
    STAMP(9);
#pragma unroll
    for (int nt = 0; nt < 4; nt++) stsf4(aXw + nt * 1024, acc2[1][nt]);
    SCHED();
    if (half) dloads(DN0, 8);
    SCHED();
    WAITL0();
    if (half) __builtin_amdgcn_s_setprio(1);
    BAR();
    SCHED();
    v4u hpk[2];
    {
        f32x4 pa[4];
#pragma unroll
        for (int nt = 0; nt < 4; nt++) pa[nt] = ldsf4(aXr + nt * 1024);
        SCHED();
#pragma unroll
        for (int i = 0; i < 8; i++) {
            const f32x4 a = acc2[0][i >> 1] + pa[i >> 1];
            hpk[i >> 2][i & 3] = pk2(a[2 * (i & 1)], a[2 * (i & 1) + 1]);
        }
        const bf16x8 h0 = __builtin_bit_cast(bf16x8, hpk[0]), h1 = __builtin_bit_cast(bf16x8, hpk[1]);
        f32x4 gq = MFMA(h0, h0, zero4), sd = MFMA(sop[0], h0, zero4);
        gq = MFMA(h1, h1, gq); sd = MFMA(sop[1], h1, sd);
        float q0 = (l & 1) ? gq[1] : gq[0], q1 = (l & 1) ? gq[3] : gq[2];
        PIN(q0); PIN(q1);
        const v4u pv = {f2s(sd[0]), f2s((l & 2) ? q1 : q0), f2s(sd[1]), swc};
        ST16(pv, vG5, sG5);
        asm volatile("s_nop 1" :: "v"(pv));
    }
    __builtin_amdgcn_s_setprio(0);
    SCHED();
    swap16(hpk[0]); swap16(hpk[1]);
    PIN(hpk[0]); PIN(hpk[1]);
    STAMP(10);
    unsigned wp2[4][8];
    auto perm2 = [&]() {
#pragma unroll
        for (int i = 16; i < 24; i++) PIN(wreg[i]);
#pragma unroll
        for (int ip = 0; ip < 4; ip++)
#pragma unroll
            for (int c = 0; c < 4; c++) {
                const unsigned a = wreg[16 + 2 * ip][c], b = wreg[16 + 2 * ip + 1][c];
                wp2[ip][2 * c] = __builtin_amdgcn_perm(b, a, 0x05040100);
                wp2[ip][2 * c + 1] = __builtin_amdgcn_perm(b, a, 0x07060302);
            }
#pragma unroll
        for (int ip = 0; ip < 4; ip++)
#pragma unroll
            for (int c = 0; c < 8; c++) PIN(wp2[ip][c]);
    };
    v4u outv[2];
    const rsrc_t rsO = __builtin_amdgcn_make_buffer_rsrc((void*)OUT, 0, 2048 * 1024 * 2, 0x00020000);
    unsigned vOut = (R2 & ~8u) * 2048 + (t16 >> 3) * 64 + gx * 16;
    PIN(vOut);
    unsigned aG = ldsb + L_WO + 512, vX = kr, v4 = l * 4, sX = WS_C + kq * 512 + lw * 128 + nr;
    unsigned aR1 = ldsb + L_RED + (lw * 4 + g) * 512 + t16 * 32, aT1 = ldsb + L_RED + (lw & 1) * 256 + l * 4;
    unsigned aR2 = ldsb + L_RED + 8192 + (lw * 4 + g) * 256 + (t16 & 7) * 32 + (t16 >> 3) * 16, aT2 = ldsb + L_RED + 8192 + l * 4, aB = kr * 8;
    const unsigned cx = (lw & 1) ? cg1u : cg1;
    unsigned s8w = WS_G8 + (cx >> 2) * 16384 + (lw >> 1) * 8192 + (kq * 2 + (lw & 1)) * 1024 + (cx & 3) * 256;
    unsigned s8r = WS_G8 + kc * 16384 + lw * 256 + nr, s8s = s8r + 4096;
    unsigned s9w = WS_G9 + (cg >> 2) * 16384 + (cg & 3) * 2048 + kc * 128, v9 = (l & 31) * 4 + (nh ^ (((l >> 3) ^ bid) & 4) * 2048);
    float a1[2][8];
    if (half) {
#pragma unroll
        for (int c = 0; c < 16; c++) { a1[c >> 3][c & 7] = 0.f; PIN(a1[c >> 3][c & 7]); }
        PIN(vX); PIN(v4); PIN(aR1); PIN(aT1); PIN(aR2); PIN(aT2); PIN(aB); PIN(v9);
    }
    PIN(aG);
    asm volatile("" : "+s"(sX), "+s"(s8w), "+s"(s8r), "+s"(s8s), "+s"(s9w));
    unsigned vP = t16 == rt2 ? OOB : t16 * 128, pk;
    {
        v4u pv[4];
        unsigned vK = tid < 4 ? ((ct + 4 * tid) & 15) * 2048 : OOB, vS = l * 16;
        PIN(vK); PIN(vS);
        unsigned sK = WS_GK + rt2 * 128, rk = 2047u - R2, aKy = ldsb + L_WO + 576, aKw = aKy + w * 4;
        unsigned vC = gx * 16, sC = WS_C + (~rt2 & 12) * 4096 + (~rt2 & 3) * 2048 + ct * 128;
        PIN(vC);
        asm volatile("" : "+s"(sK), "+s"(sC));
        PIN(rk); PIN(aKy); PIN(aKw); PIN(vP);
        auto st_ld = [&](int k) { return LD16(vS + k * 1024, sG5r); };
        auto st_sum = [&]() { const f32x4 ps = (f4(pv[0]) + f4(pv[1])) + (f4(pv[2]) + f4(pv[3])); return xrow_scatter<false>(ps[0], ps[1], ps[2], ps[3]); };
        SCHED();
        if (half) SLEEP(4); else SLEEP(7);
        __builtin_amdgcn_s_setprio(1);
        float ts;
        sweepn<4, 4>(st_ld, pv, [&]() { return ts = st_sum(); });
        STAMP(21);
        float T[4];
        xrow_gather(ts, T);
        const float mean = T[0] * (1.0f / 1024.0f), var = fmaxf(T[1] * (1.0f / 1024.0f) - mean * mean, 0.f);
        const unsigned sg = pk2(mean, __builtin_amdgcn_sqrtf(rbf(rbf(var) + 1e-5f)));
        const float mb2 = bflo(sg), isd2 = __builtin_amdgcn_rcpf(bfhi(sg));
        const float rsc = (T[2] - mb2 * T[3]) * isd2;
        const unsigned rb = pk2(rsc, rsc);
        const unsigned key = (((rb ^ (((int)rb >> 31) | 0x80000000u)) >> 16) << 11) | rk;
        unsigned kb = umx(key, dppu<0x128>(key));
        kb = umx(kb, dppu<0x124>(kb)); kb = umx(kb, dppu<0x122>(kb)); kb = umx(kb, dppu<0x121>(kb));
        stsu(aKw, kb);
        WAITL0();
        BAR();
        SCHED();
        pk = umx(wmax(*(const LAS v4u*)aKy), wmax(*(const LAS v4u*)(aKy + 16)));
        if (w == 0) ST4(pk, vK, sK);
        SCHED();
        const bool cand = key == pk;
        __builtin_amdgcn_s_setprio(0);
        v4u e[2];
#pragma unroll
        for (int i = 0; i < 8; i++) {
            const float h0 = bflo(hpk[i >> 2][i & 3]), h1 = bfhi(hpk[i >> 2][i & 3]);
            const unsigned d = pk2(h0 - mb2, h1 - mb2);
            e[i >> 2][i & 3] = nsent(pk2(bflo(d) * isd2, bfhi(d) * isd2));
        }
        if (cand) {
#pragma unroll
            for (int i = 0; i < 4; i++) ST16(e[i & 1], vC + (i & 1) * 64, sC + (i >> 1) * 8192);
#pragma unroll
            for (int i = 0; i < 4; i++)
                ST16(((v4u){nsent(hpk[i & 1][0]), nsent(hpk[i & 1][1]), nsent(hpk[i & 1][2]), nsent(hpk[i & 1][3])}), vC + (i & 1) * 64, sC + (WS_H - WS_C) + (i >> 1) * 8192);
        }
        SCHED();
        unsigned k1;
        if (w == 1) {
            SLEEP(3);
            asm volatile("buffer_load_dword %0, %1, %2, %3 offen sc1" : "=v"(k1) : "v"(vP), "s"(rsWS), "s"(sRep) : "memory");
        }
        SCHED();
        STAMP(22);
#pragma unroll
        for (int i = 0; i < 8; i++)
            outv[i >> 2][i & 3] = pk2(bflo(hpk[i >> 2][i & 3]) + bflo(e[i >> 2][i & 3]), bfhi(hpk[i >> 2][i & 3]) + bfhi(e[i >> 2][i & 3]));
        {
#pragma unroll
            for (int j = 0; j < 4; j++) {
                const unsigned a = outv[0][j], b = outv[1][j];
                outv[0][j] = __builtin_amdgcn_update_dpp(a, b, 0x128, 0xf, 0xc, false);
                outv[1][j] = __builtin_amdgcn_update_dpp(b, a, 0x128, 0xf, 0x3, false);
            }
        }
        PIN(outv[0]); PIN(outv[1]);
        if (w == 1) {
            poll_words(k1, rsWS, vP, sRep);
            STAMP(23);
            unsigned gk = umx(k1, pk);
            gk = umx(gk, dppu<0x128>(gk)); gk = umx(gk, dppu<0x124>(gk)); gk = umx(gk, dppu<0x122>(gk)); gk = umx(gk, dppu<0x121>(gk));
            stsu(aG, gk);
        }
    }
    if (half) __builtin_amdgcn_s_setprio(1);
    STAMP(24);
    WL0();
    SCHED();
    BAR();
    SCHED();
    STAMP(11);
    const unsigned gv = ldsu(aG);
    unsigned vo[2];
    auto store_out = [&](unsigned srow) {
#pragma unroll
        for (int i = 0; i < 2; i++) __builtin_amdgcn_raw_buffer_store_b128(outv[i], rsO, vo[i], ct * 128 + i * 16384, AUX_SC1);
    };
    if (RARE(!ffn)) {
        const unsigned gkey = rfl(gv), srow = ~gkey & 2047u;
        vo[0] = (R2 & ~8u) != srow ? vOut : OOB; vo[1] = (R2 | 8u) != srow ? vOut : OOB;
        store_out(srow);
        if (w != 4) { RDUMP(); return; }
        const unsigned c9 = (bid & 3) * 4 + (idx & 3), col = c9 * 64 + (bid & 4) * 8 + (l & 7) * 4 + g;
        unsigned hl[2];
        sweepz<2, 2>([&](int k) { return LD4((col >> 1) * 4, (k ? WS_C : WS_H) + (((gkey & 0x780) + (gkey & 0x600)) << 4) + nr); }, hl);
        const unsigned ord = gkey >> 11;
        const unsigned rb = (ord & 0x8000u) ? (ord & 0x7fffu) : (~ord & 0xffffu);
        float sig = rbf(1.0f / (1.0f + __expf(-bf2f(rb))));
        float hc = (g & 1) ? bfhi(hl[0]) : bflo(hl[0]), lc = (g & 1) ? bfhi(hl[1]) : bflo(hl[1]);
        unsigned vY = l * 16, vZ = l < 48 ? l * 16 + 1024 : OOB, vO = (l & 8) ? OOB : col * 2, sY = WS_G9 + (c9 >> 2) * 16384 + (c9 & 3) * 2048 + nh, sO = srow * 2048;
        PIN(sig); PIN(hc); PIN(lc); PIN(vY); PIN(vZ); PIN(vO);
        asm volatile("" : "+s"(sY), "+s"(sO));
        v4u yp[2];
        SCHED();
        STAMP(16);
        SLEEP(24);
        sweepz<2>([&](int k) { return LD16(k ? vZ : vY, sY); }, yp);
        const f32x4 ys = f4(yp[0]) + f4(yp[1]);
        float yt = xrow_scatter<false>(ys[0], ys[1], ys[2], ys[3]);
        yt += dppf<0x128>(yt);
        const float y = rbf(rbf(yt) * sig);
        __builtin_amdgcn_raw_buffer_store_b16((u16)pk2(hc + rbf(lc + y), 0.f), rsO, vO, sO, 0);
        STAMP(17);
        RDUMP();
        return;
    }
    if (w < 4) {
        const unsigned srow = ~rfl(gv) & 2047u;
        vo[0] = (R2 & ~8u) != srow ? vOut : OOB; vo[1] = (R2 | 8u) != srow ? vOut : OOB;
        PIN(vo[0]); PIN(vo[1]);
        BAR(); BAR(); BAR();
        store_out(srow);
        if (bid == 0 && tid == 0) __hip_atomic_store((gu32*)(ws + WS_EPOCH), tag, RLX_AGENT);
        STAMP(12);
        RDUMP();
        return;
    }

    auto tree16 = [&](unsigned a, unsigned stride) {
        f32x2 v[8];
#pragma unroll
        for (int i = 0; i < 8; i++) v[i] = (f32x2){ldsf(a + 2 * i * stride), ldsf(a + (2 * i + 1) * stride)};
        SCHED();
        const f32x2 t = ((v[0] + v[1]) + (v[2] + v[3])) + ((v[4] + v[5]) + (v[6] + v[7]));
        return t[0] + t[1];
    };
    vX = (((gv & 0x780) | vX) + (gv & 0x600)) << 4;
    v4u xq[1] = {LD16(vX, sX)};
    SCHED();
    const unsigned srow = ~rfl(gv) & 2047u;
    vo[0] = (R2 & ~8u) != srow ? vOut : OOB; vo[1] = (R2 | 8u) != srow ? vOut : OOB;
    PIN(vo[0]); PIN(vo[1]);
    SCHED();
    if (RARE(!__all(wmax(xq[0]) != SENT))) sweepz<1>([&](int) { return LD16(vX, sX); }, xq);
    STAMP(13);
#pragma unroll
    for (int ip = 0; ip < 4; ip++)
#pragma unroll
        for (int c = 0; c < 16; c++) dot2c(a1[c & 1][c >> 1], wp1[c & 1][ip][c >> 1], xq[0][ip]);
#pragma unroll
    for (int c = 0; c < 8; c++) fold8(a1[0][c], a1[1][c]);
    stsf4(aR1, (f32x4){a1[0][0], a1[0][1], a1[0][2], a1[0][3]});
    stsf4(aR1 + 16, (f32x4){a1[0][4], a1[0][5], a1[0][6], a1[0][7]});
    WL0();
    BAR();
    SCHED();
    ST4(f2s(tree16(aT1, 512)), v4, s8w);
    SCHED();
    STAMP(14);
    perm2();
    float a2[8];
#pragma unroll
    for (int c = 0; c < 8; c++) { a2[c] = 0.f; PIN(a2[c]); }
    SCHED();
    SLEEP(7);
    {
        unsigned pv[8];
        auto a_ld = [&](int k) { return LD4(v4 + (k & 3) * 1024, k < 4 ? s8r : s8s); };
        auto a_sum = [&](int m) { return (__uint_as_float(pv[m]) + __uint_as_float(pv[2 + m])) + (__uint_as_float(pv[4 + m]) + __uint_as_float(pv[6 + m])); };
        float gs, us;
        sweepn<8, 0>(a_ld, pv, [&]() { gs = a_sum(0); us = a_sum(1); return gs + us; });
        STAMP(15);
        const unsigned gu = pk2(gs, us);
        const float gg = bflo(gu);
        const float act = rbf(gg * __builtin_amdgcn_rcpf(1.0f + __expf(-gg))) * bfhi(gu);
        const unsigned apk = pk2(act, dppf<0xB1>(act));
        unsigned ap[4];
#pragma unroll
        for (int ip = 0; ip < 4; ip++) ap[ip] = __builtin_amdgcn_ds_bpermute(aB + ip * 64, apk);
        SCHED();
#pragma unroll
        for (int ip = 0; ip < 4; ip++)
#pragma unroll
            for (int c = 0; c < 8; c++) dot2c(a2[(c & 1) * 4 + (c >> 1)], wp2[ip][(c & 1) * 4 + (c >> 1)], ap[ip]);
#pragma unroll
        for (int c = 0; c < 4; c++) fold8(a2[c], a2[4 + c]);
        stsf4(aR2, (f32x4){a2[0], a2[1], a2[2], a2[3]});
    }
    WL0();
    BAR();
    SCHED();
    if (lw == 0) ST4(f2s(tree16(aT2, 256)), v9, s9w);
    SCHED();
    BAR();
    SCHED();
    STAMP(16);
    store_out(srow);
    RDUMP();
}
__device__ __attribute__((used, noinline)) void fk_tail() { asm volatile(".Lfk_end:"); }

extern "C" void kernel_launch(void* const* d_in, const int* in_sizes, int n_in, void* d_out, int out_size, void* d_ws,
                              size_t ws_size, hipStream_t stream) {
    (void)in_sizes; (void)n_in; (void)out_size; (void)ws_size;
    (void)hipFuncSetAttribute((const void*)fused_kernel, hipFuncAttributeMaxDynamicSharedMemorySize, LDS_BYTES);
    fused_kernel<<<256, 512, LDS_BYTES, stream>>>((const u16*)d_in[0], (const u16*)d_in[1], (const u16*)d_in[2], (const u16*)d_in[3],
                                                  (const u16*)d_in[5], (const u16*)d_in[6], (const u16*)d_in[7], (const u16*)d_in[8],
                                                  (u16*)d_out, (char*)d_ws + (-(size_t)d_ws & 0x1FFFFF));
}
```
